```python
import torch

def _softmax(x, axis=-1):
    return torch.softmax(x, dim=axis)
def _gelu(x):
    return torch.nn.functional.gelu(x, approximate="tanh")
def _silu(x):
    return torch.nn.functional.silu(x)
def _sigmoid(x):
    return torch.sigmoid(x)
def _layernorm(x, axis=-1, eps=1e-5):
    m = x.mean(dim=axis, keepdim=True)
    v = x.var(dim=axis, keepdim=True, unbiased=False)
    return (x - m) / torch.sqrt(v + eps)
def _scatter_add(tgt, idx, src, axis=0):
    return tgt.clone().index_add_(axis, idx, src)
def _segment_sum(src, seg, n):
    out = torch.zeros((n,) + src.shape[1:], dtype=src.dtype)
    return out.index_add_(0, seg, src)
def _pad(x, pw):
    flat = tuple(v for pair in reversed(pw) for v in pair)
    return torch.nn.functional.pad(x, flat)

kernel_name = 'sequential_preLN__compressed_kv_cla__glu_gated_glu__prompt'

def setup_inputs():

    _w = (lambda a: (lambda t: t.to(torch.bfloat16) if t.is_floating_point() else t)(torch.as_tensor(a)))
    def _A_setup():
        B, H, Sq, Sk, D, M = 1, 32, 2048, 2048, 128, 512
        Dc = 4 * D
        return {
            "x": _w((lambda *s: torch.randn(*s, dtype=torch.bfloat16) * 0.05)(B, Sq, M)),
            "c_kv": _w((lambda *s: torch.randn(*s, dtype=torch.bfloat16) * 0.4)(B, Sk, Dc)),
            "w_q": _w((lambda *s: torch.randn(*s, dtype=torch.bfloat16) * 3.2)(M, H, D) * (M ** -0.5)),
            "w_uk": _w((lambda *s: torch.randn(*s, dtype=torch.bfloat16) * 3.2)(Dc, H, D) * (Dc ** -0.5)),
            "w_uv": _w((lambda *s: torch.randn(*s, dtype=torch.bfloat16) * 0.4)(Dc, H, D) * (Dc ** -0.5)),
            "w_o": _w((lambda *s: torch.randn(*s, dtype=torch.bfloat16) * 0.4)(H, D, M) * ((H * D) ** -0.5)),
        }
    _A_in = _A_setup()

    def _B_setup():
        B, S, D, F = 1, 2048, 512, 1792
        return {
            "x": _w((lambda *s: torch.randn(*s, dtype=torch.bfloat16) * 0.05)(B, S, D)),
            "w_gate": _w((lambda *s: torch.randn(*s, dtype=torch.bfloat16) * 0.05)(D, F)),
            "w_up": _w((lambda *s: torch.randn(*s, dtype=torch.bfloat16) * 0.05)(D, F)),
            "w_down": _w((lambda *s: torch.randn(*s, dtype=torch.bfloat16) * 0.05)(F, D)),
        }
    _B_in = _B_setup()

    return {
        "A_x": _A_in["x"],
        "A_c_kv": _A_in["c_kv"],
        "A_w_q": _A_in["w_q"],
        "A_w_uk": _A_in["w_uk"],
        "A_w_uv": _A_in["w_uv"],
        "A_w_o": _A_in["w_o"],
        "B_x": _B_in["x"],
        "B_w_gate": _B_in["w_gate"],
        "B_w_up": _B_in["w_up"],
        "B_w_down": _B_in["w_down"],
    }

def reference(A_x, A_c_kv, A_w_q, A_w_uk, A_w_uv, A_w_o, B_x, B_w_gate, B_w_up, B_w_down):
    def _A_ref(x, c_kv, w_q, w_uk, w_uv, w_o):
        B, Sq, M = x.shape[0], x.shape[1], x.shape[2]
        Sk = c_kv.shape[1]
        H, D = w_q.shape[1], w_q.shape[2]
        q = torch.einsum("bqm,mhd->bhqd", x, w_q)
        k = torch.einsum("bkc,chd->bhkd", c_kv, w_uk)
        v = torch.einsum("bkc,chd->bhkd", c_kv, w_uv)
        scores = torch.einsum("bhqd,bhkd->bhqk", q, k) * (D ** -0.5)
        off = Sk - Sq
        qi = torch.arange(Sq)[:, None]
        kj = torch.arange(Sk)[None, :]
        cmask = kj <= (qi + off)
        scores = torch.where(cmask[None, None], scores, -1e9)
        p = _softmax(scores, axis=-1)
        ctx = torch.einsum("bhqk,bhkd->bhqd", p, v)
        return torch.einsum("bhqd,hdm->bqm", ctx, w_o)

    def _B_ref(x, w_gate, w_up, w_down):
        g = torch.einsum("bsd,df->bsf", x, w_gate)
        u = torch.einsum("bsd,df->bsf", x, w_up)
        h = _sigmoid(g) * u
        return torch.einsum("bsf,fd->bsd", h, w_down)

    x = A_x
    h = x + _A_ref(_layernorm(x, axis=-1), A_c_kv, A_w_q, A_w_uk, A_w_uv, A_w_o)
    h2 = h + _B_ref(_layernorm(h, axis=-1), B_w_gate, B_w_up, B_w_down)
    return h2
```

```cpp
#include <hip/hip_runtime.h>
#include <hip/hip_bf16.h>
#include <stdint.h>

typedef short bf16x8 __attribute__((ext_vector_type(8)));
typedef short s16x4 __attribute__((ext_vector_type(4)));
typedef float f32x16 __attribute__((ext_vector_type(16)));
typedef float f32x4 __attribute__((ext_vector_type(4)));
typedef float f32x2 __attribute__((ext_vector_type(2)));
typedef unsigned u32x4 __attribute__((ext_vector_type(4)));
typedef unsigned u32x2 __attribute__((ext_vector_type(2)));
typedef __bf16 bf16x2 __attribute__((ext_vector_type(2)));
#define DI __device__ __forceinline__
#define LDS3 __attribute__((address_space(3)))
#define MFMA(a, b, c) __builtin_amdgcn_mfma_f32_32x32x16_bf16((a), (b), (c), 0, 0, 0)

constexpr int S_ = 2048, M_ = 512, H_ = 32, D_ = 128, F_ = 1792;
constexpr size_t WS_Q = 0, WS_K = 16u << 20, WS_V = 32u << 20, WS_C = 48u << 20, WS_H = 64u << 20, WS_A = 66u << 20;

DI unsigned cvtpk(float lo, float hi) {
  f32x2 v = {lo, hi}; bf16x2 b = __builtin_convertvector(v, bf16x2); return __builtin_bit_cast(unsigned, b);
}
DI float bflo(unsigned u) { return __uint_as_float(u << 16); }
DI float bfhi(unsigned u) { return __uint_as_float(u & 0xffff0000u); }
DI bf16x8 trfrag(const char* p_lo, const char* p_hi) {
  s16x4 lo = __builtin_amdgcn_ds_read_tr16_b64_v4i16((LDS3 s16x4*)p_lo);
  s16x4 hi = __builtin_amdgcn_ds_read_tr16_b64_v4i16((LDS3 s16x4*)p_hi);
  return __builtin_shufflevector(lo, hi, 0, 1, 2, 3, 4, 5, 6, 7);
}
DI void glds16(const void* g, void* l) {
  __builtin_amdgcn_global_load_lds((const __attribute__((address_space(1))) unsigned*)g, (LDS3 unsigned*)l, 16, 0, 0);
}
DI u32x4 pack8(const f32x16& x, int s) {
  u32x4 w = {cvtpk(x[8 * s], x[8 * s + 1]), cvtpk(x[8 * s + 2], x[8 * s + 3]), cvtpk(x[8 * s + 4], x[8 * s + 5]), cvtpk(x[8 * s + 6], x[8 * s + 7])};
  return w;
}
DI float dot2(unsigned a, unsigned b, float c) {
  return __builtin_amdgcn_fdot2_f32_bf16(__builtin_bit_cast(bf16x2, a), __builtin_bit_cast(bf16x2, b), c, false);
}
DI float xhalf_add(float v) {
  auto r = __builtin_amdgcn_permlane32_swap(__float_as_uint(v), __float_as_uint(v), false, false);
  return __uint_as_float(r[0]) + __uint_as_float(r[1]);
}
DI float xhalf_max(float v) {
  auto r = __builtin_amdgcn_permlane32_swap(__float_as_uint(v), __float_as_uint(v), false, false);
  return fmaxf(__uint_as_float(r[0]), __uint_as_float(r[1]));
}
DI int tr_base(int lane) { return ((lane & 3) << 3) | (((lane >> 2) & 3) << 6) | (((lane >> 4) & 1) << 5) | (((lane >> 5) & 1) << 8); }

__global__ __launch_bounds__(512, 1) void k1_qkv(const uint16_t* __restrict__ x, const uint16_t* __restrict__ ckv,
                                                 const uint16_t* __restrict__ wq, const uint16_t* __restrict__ wuk,
                                                 const uint16_t* __restrict__ wuv, char* __restrict__ ws) {
  extern __shared__ __attribute__((aligned(16))) char lds[];
  const int tid = threadIdx.x, lane = tid & 63, w = __builtin_amdgcn_readfirstlane(tid >> 6), r = lane & 31, hi = lane >> 5;
  const int h = blockIdx.x & 31, rb = blockIdx.x >> 5;
  const int tok = rb * 256 + w * 32 + r, qt = rb * 8 + w;
  float* cs = (float*)(lds + 32768);
  size_t goff[2];
#pragma unroll
  for (int e = 0; e < 2; ++e) {
    const int i = 2 * w + e, ksh = i >> 1, db = (i & 1) * 2 + (lane >> 5), row = (lane >> 2) & 7, ch = lane & 3;
    const int ks = ksh >> 1, half = ksh & 1, k = 16 * ks + (row & 3) + 8 * (row >> 2) + 4 * half;
    goff[e] = (size_t)k * 4096 + h * 128 + db * 32 + ch * 8;
  }
  const int trb = tr_base(lane);
  auto stage = [&](const uint16_t* W, int p, int buf) {
#pragma unroll
    for (int e = 0; e < 2; ++e) glds16(W + (size_t)p * 64 * 4096 + goff[e], lds + buf * 16384 + (2 * w + e) * 1024);
  };
  bf16x8 af[32];
#pragma unroll
  for (int s = 0; s < 32; ++s) af[s] = *(const bf16x8*)(ckv + (size_t)tok * 512 + 16 * s + 8 * hi);
  stage(wuk, 0, 0);
#pragma unroll
  for (int c = 0; c < 3; ++c) {
    f32x16 acc[4] = {};
    float sx = 0.f, sxx = 0.f, wsum = 0.f;
    if (c == 2) {
#pragma unroll
      for (int s = 0; s < 32; ++s) af[s] = *(const bf16x8*)(x + (size_t)tok * 512 + 16 * s + 8 * hi);
    }
#pragma unroll
    for (int p = 0; p < 8; ++p) {
      const int gp = c * 8 + p, buf = gp & 1;
      __syncthreads();
      if (gp + 1 < 24) { const int c2 = (gp + 1) >> 3; stage(c2 == 0 ? wuk : (c2 == 1 ? wuv : wq), (gp + 1) & 7, buf ^ 1); }
      const char* base = lds + buf * 16384 + trb;
#pragma unroll
      for (int ks = 0; ks < 4; ++ks) {
        const int s = p * 4 + ks;
#pragma unroll
        for (int db = 0; db < 4; ++db) {
          bf16x8 wf = trfrag(base + ((ks * 2 + 0) * 4 + db) * 512, base + ((ks * 2 + 1) * 4 + db) * 512);
          if (c == 1) acc[db] = MFMA(af[s], wf, acc[db]); else acc[db] = MFMA(wf, af[s], acc[db]);
          if (c == 2) {
            const unsigned ones = w == db ? 0x3f803f80u : 0u;
            u32x4 u = __builtin_bit_cast(u32x4, wf);
#pragma unroll
            for (int e = 0; e < 4; ++e) wsum = dot2(u[e], ones, wsum);
          }
        }
        if (c == 2) {
          u32x4 u = __builtin_bit_cast(u32x4, af[s]);
#pragma unroll
          for (int e = 0; e < 4; ++e) { sx = dot2(u[e], 0x3f803f80u, sx); sxx = dot2(u[e], u[e], sxx); }
        }
      }
    }
    if (c == 0) {
      char* dst = ws + WS_K + ((size_t)(h * 64 + qt) * 8) * 1024 + lane * 16;
#pragma unroll
      for (int db = 0; db < 4; ++db)
#pragma unroll
        for (int s = 0; s < 2; ++s) *(u32x4*)(dst + (db * 2 + s) * 1024) = pack8(acc[db], s);
    } else if (c == 1) {
      char* dst = ws + WS_V + ((size_t)(h * 64 + qt) * 8) * 1024 + lane * 16;
#pragma unroll
      for (int db = 0; db < 4; ++db)
#pragma unroll
        for (int s = 0; s < 2; ++s) *(u32x4*)(dst + (s * 4 + db) * 1024) = pack8(acc[db], s);
    } else {
      wsum = xhalf_add(wsum);
      if (w < 4 && hi == 0) cs[w * 32 + r] = wsum;
      sx = xhalf_add(sx); sxx = xhalf_add(sxx);
      const float mu = sx * (1.f / 512.f);
      const float var = fmaxf(sxx * (1.f / 512.f) - mu * mu, 0.f);
      const float sc = rsqrtf(var + 1e-5f) * (1.4426950408889634f * 0.08838834764831845f);
      __syncthreads();
      char* dst = ws + WS_Q + ((size_t)(h * 64 + qt) * 8) * 1024 + lane * 16;
#pragma unroll
      for (int db = 0; db < 4; ++db) {
#pragma unroll
        for (int g = 0; g < 4; ++g) {
          f32x4 c4 = *(const f32x4*)(cs + db * 32 + 8 * g + 4 * hi);
#pragma unroll
          for (int e = 0; e < 4; ++e) acc[db][4 * g + e] = (acc[db][4 * g + e] - mu * c4[e]) * sc;
        }
#pragma unroll
        for (int s = 0; s < 2; ++s) *(u32x4*)(dst + (db * 2 + s) * 1024) = pack8(acc[db], s);
      }
    }
  }
}

__global__ __launch_bounds__(512, 1) void k2_attn(char* __restrict__ ws) {
  extern __shared__ __attribute__((aligned(16))) char lds[];
  const int tid = threadIdx.x, lane = tid & 63, w = __builtin_amdgcn_readfirstlane(tid >> 6), r = lane & 31, hi = lane >> 5;
  const int h = blockIdx.x & 31, pi = blockIdx.x >> 5;
  const int qs = w & 3, kh = w >> 2;
  const char* Kg = ws + WS_K + (size_t)h * 64 * 8192;
  const char* Vg = ws + WS_V + (size_t)h * 64 * 8192;
  auto stage = [&](int t, int buf) {
#pragma unroll
    for (int e = 0; e < 4; ++e) {
      glds16(Kg + (size_t)t * 32768 + (w * 4 + e) * 1024 + lane * 16, lds + buf * 65536 + (w * 4 + e) * 1024);
      glds16(Vg + (size_t)t * 32768 + (w * 4 + e) * 1024 + lane * 16, lds + buf * 65536 + 32768 + (w * 4 + e) * 1024);
    }
  };
  for (int pass = 0; pass < 2; ++pass) {
    const int b = pass ? pi : 15 - pi;
    const int qt = b * 4 + qs;
    bf16x8 qf[8];
    const char* Qg = ws + WS_Q + ((size_t)(h * 64 + qt) * 8) * 1024 + lane * 16;
#pragma unroll
    for (int d0 = 0; d0 < 8; ++d0) qf[d0] = *(const bf16x8*)(Qg + d0 * 1024);
    f32x16 o[4] = {};
    float m = -1e30f, l = 0.f;
    __syncthreads();
    stage(0, 0);
    for (int t = 0; t <= b; ++t) {
      const int buf = t & 1;
      __syncthreads();
      if (t < b) stage(t + 1, buf ^ 1);
      const char* Kl = lds + buf * 65536 + kh * 16384 + lane * 16;
      const char* Vl = lds + buf * 65536 + 32768 + kh * 16384 + lane * 16;
#pragma unroll
      for (int sub = 0; sub < 2; ++sub) {
        const int kt = t * 4 + kh * 2 + sub;
        if (kt > qt) continue;
        f32x16 s = {};
#pragma unroll
        for (int d0 = 0; d0 < 8; ++d0) s = MFMA(*(const bf16x8*)(Kl + sub * 8192 + d0 * 1024), qf[d0], s);
        if (kt == qt) {
#pragma unroll
          for (int e = 0; e < 16; ++e) { const int key = (e & 3) + 8 * (e >> 2) + 4 * hi; if (key > r) s[e] = -__builtin_inff(); }
        }
        float mx = s[0];
#pragma unroll
        for (int e = 1; e < 16; ++e) mx = fmaxf(mx, s[e]);
        mx = xhalf_max(mx);
        const float mn = fmaxf(m, mx), al = __builtin_amdgcn_exp2f(m - mn);
        m = mn;
        float ps = 0.f;
#pragma unroll
        for (int e = 0; e < 16; ++e) { s[e] = __builtin_amdgcn_exp2f(s[e] - mn); ps += s[e]; }
        l = l * al + ps;
#pragma unroll
        for (int db = 0; db < 4; ++db)
#pragma unroll
          for (int e = 0; e < 16; ++e) o[db][e] *= al;
#pragma unroll
        for (int ks = 0; ks < 2; ++ks) {
          const bf16x8 pf = __builtin_bit_cast(bf16x8, pack8(s, ks));
#pragma unroll
          for (int db = 0; db < 4; ++db) o[db] = MFMA(*(const bf16x8*)(Vl + sub * 8192 + (ks * 4 + db) * 1024), pf, o[db]);
        }
      }
    }
    l = xhalf_add(l);
    __syncthreads();
    char* mb = lds + qs * (16384 + 512);
    if (kh == 1) {
#pragma unroll
      for (int db = 0; db < 4; ++db)
#pragma unroll
        for (int g = 0; g < 4; ++g) {
          f32x4 v = {o[db][4 * g], o[db][4 * g + 1], o[db][4 * g + 2], o[db][4 * g + 3]};
          *(f32x4*)(mb + (db * 4 + g) * 1024 + lane * 16) = v;
        }
      *(f32x2*)(mb + 16384 + lane * 8) = f32x2{m, l};
    }
    __syncthreads();
    if (kh == 0) {
      const f32x2 ml = *(const f32x2*)(mb + 16384 + lane * 8);
      const float mn = fmaxf(m, ml[0]), a0 = __builtin_amdgcn_exp2f(m - mn), a1 = __builtin_amdgcn_exp2f(ml[0] - mn);
      const float inv = 1.f / (l * a0 + ml[1] * a1);
      const float f0 = a0 * inv, f1 = a1 * inv;
      char* dst = ws + WS_C + ((size_t)qt * 256 + h * 8) * 1024 + lane * 16;
#pragma unroll
      for (int db = 0; db < 4; ++db) {
#pragma unroll
        for (int g = 0; g < 4; ++g) {
          const f32x4 v = *(const f32x4*)(mb + (db * 4 + g) * 1024 + lane * 16);
#pragma unroll
          for (int e = 0; e < 4; ++e) o[db][4 * g + e] = o[db][4 * g + e] * f0 + v[e] * f1;
        }
#pragma unroll
        for (int s = 0; s < 2; ++s) *(u32x4*)(dst + (db * 2 + s) * 1024) = pack8(o[db], s);
      }
    }
  }
}

template <int MODE>
__global__ __launch_bounds__(512, 1) void k35_proj(const uint16_t* __restrict__ W, const uint16_t* __restrict__ x,
                                                   char* __restrict__ ws, uint16_t* __restrict__ out) {
  extern __shared__ __attribute__((aligned(16))) char lds[];
  constexpr int KS = MODE == 0 ? 256 : 112, KW = KS / 8, G = 2;
  const int tid = threadIdx.x, lane = tid & 63, w = __builtin_amdgcn_readfirstlane(tid >> 6), r = lane & 31, hi = lane >> 5;
  const int mt = blockIdx.x & 7, tt = blockIdx.x >> 3;
  const char* Ag = ws + (MODE == 0 ? WS_C : WS_A) + ((size_t)(tt * 2) * KS + w * KW) * 1024 + lane * 16;
  char* wl = lds + w * 16384;
  const int row = (lane >> 2) & 7, ch = lane & 3, mb_ = lane >> 5;
  const uint16_t* Wg = W + (size_t)(w * KW * 16 + row) * 512 + mt * 64 + mb_ * 32 + ch * 8;
  const int trb = tr_base(lane);
  f32x16 acc[2][2] = {};
  constexpr int NG = KW / G;
  auto stage = [&](int g, int buf) {
#pragma unroll
    for (int e = 0; e < G * 2; ++e) glds16(Wg + (size_t)(g * G * 16 + e * 8) * 512, wl + buf * (G * 2048) + e * 1024);
  };
  bf16x8 a[2][G][2];
  auto loadA = [&](int g, int buf) {
#pragma unroll
    for (int e = 0; e < G; ++e)
#pragma unroll
      for (int tb = 0; tb < 2; ++tb) a[buf][e][tb] = *(const bf16x8*)(Ag + ((size_t)tb * KS + g * G + e) * 1024);
  };
  stage(0, 0); loadA(0, 0);
#pragma unroll
  for (int g = 0; g < NG; ++g) {
    const int buf = g & 1;
    asm volatile("s_waitcnt vmcnt(0)" ::: "memory");
    if (g + 1 < NG) { stage(g + 1, buf ^ 1); loadA(g + 1, buf ^ 1); }
#pragma unroll
    for (int e = 0; e < G; ++e) {
      const char* base = wl + buf * (G * 2048) + e * 2048 + trb;
#pragma unroll
      for (int mb = 0; mb < 2; ++mb) {
        bf16x8 wf = trfrag(base + mb * 512, base + 1024 + mb * 512);
#pragma unroll
        for (int tb = 0; tb < 2; ++tb) acc[mb][tb] = MFMA(wf, a[buf][e][tb], acc[mb][tb]);
      }
    }
  }
  __syncthreads();
#pragma unroll
  for (int mb = 0; mb < 2; ++mb)
#pragma unroll
    for (int tb = 0; tb < 2; ++tb)
#pragma unroll
      for (int g = 0; g < 4; ++g) {
        f32x4 v = {acc[mb][tb][4 * g], acc[mb][tb][4 * g + 1], acc[mb][tb][4 * g + 2], acc[mb][tb][4 * g + 3]};
        *(f32x4*)(wl + ((mb * 2 + tb) * 4 + g) * 1024 + lane * 16) = v;
      }
  __syncthreads();
  const int tile = w >> 1, s = w & 1, mb = tile >> 1, tb = tile & 1;
  f32x4 v0 = {}, v1 = {};
#pragma unroll
  for (int p = 0; p < 8; ++p) {
    v0 += *(const f32x4*)(lds + p * 16384 + (tile * 4 + 2 * s) * 1024 + lane * 16);
    v1 += *(const f32x4*)(lds + p * 16384 + (tile * 4 + 2 * s + 1) * 1024 + lane * 16);
  }
  const int qt = tt * 2 + tb, ms = (mt * 2 + mb) * 2 + s;
  char* hp = ws + WS_H + ((size_t)qt * 32 + ms) * 1024 + lane * 16;
  const int tok = qt * 32 + r, m0 = ms * 16 + 4 * hi;
  if (MODE == 0) {
    const u32x2 x0 = *(const u32x2*)(x + (size_t)tok * 512 + m0), x1 = *(const u32x2*)(x + (size_t)tok * 512 + m0 + 8);
    u32x4 o = {cvtpk(v0[0] + bflo(x0[0]), v0[1] + bfhi(x0[0])), cvtpk(v0[2] + bflo(x0[1]), v0[3] + bfhi(x0[1])),
               cvtpk(v1[0] + bflo(x1[0]), v1[1] + bfhi(x1[0])), cvtpk(v1[2] + bflo(x1[1]), v1[3] + bfhi(x1[1]))};
    *(u32x4*)hp = o;
  } else {
    const u32x4 hh = *(const u32x4*)hp;
    u32x2 o0 = {cvtpk(v0[0] + bflo(hh[0]), v0[1] + bfhi(hh[0])), cvtpk(v0[2] + bflo(hh[1]), v0[3] + bfhi(hh[1]))};
    u32x2 o1 = {cvtpk(v1[0] + bflo(hh[2]), v1[1] + bfhi(hh[2])), cvtpk(v1[2] + bflo(hh[3]), v1[3] + bfhi(hh[3]))};
    *(u32x2*)(out + (size_t)tok * 512 + m0) = o0;
    *(u32x2*)(out + (size_t)tok * 512 + m0 + 8) = o1;
  }
}

template <int NF>
DI void k4_body(const char* lds, int buf, int p, int fg0, int trb, const bf16x8* hf, f32x16* acc, float* wsum, int wsel) {
  int boff = buf * 57344 + trb + fg0 * 32;
  asm volatile("" : "+v"(boff));
#pragma unroll
  for (int ks = 0; ks < 8; ++ks) {
    const char* base = lds + boff + ks * 16 * 448;
    __builtin_amdgcn_sched_barrier(0);
#pragma unroll
    for (int f = 0; f < NF; ++f) {
      bf16x8 wf = trfrag(base + f * 32, base + 8 * 448 + f * 32);
      acc[f] = MFMA(wf, hf[p * 8 + ks], acc[f]);
    }
  }
  __builtin_amdgcn_sched_barrier(0);
  if (wsel < NF) {
#pragma unroll
    for (int ks = 0; ks < 8; ++ks) {
      const char* base = lds + boff + wsel * 32 + ks * 16 * 448;
      u32x4 u = __builtin_bit_cast(u32x4, trfrag(base, base + 8 * 448));
#pragma unroll
      for (int e = 0; e < 4; ++e) wsum[0] = dot2(u[e], 0x3f803f80u, wsum[0]);
    }
  }
}
template <int NF>
DI void k4_wave(const uint16_t* __restrict__ wg, const uint16_t* __restrict__ wu, char* __restrict__ ws, char* lds, int lane, int w) {
  const int r = lane & 31, hi = lane >> 5;
  const int fb = blockIdx.x & 15, tb0 = blockIdx.x >> 4;
  const int tb = w & 3, fg0 = (w >> 2) * 4;
  const int qt = tb0 * 4 + tb;
  float* cs = (float*)(lds + 2 * 57344);
  unsigned src[7];
#pragma unroll
  for (int e = 0; e < 7; ++e) {
    const int c = (w * 7 + e) * 64 + lane, row = c / 28, cc = c % 28, mat = cc / 14, ch = cc % 14;
    src[e] = (unsigned)(row * F_ + fb * 112 + ch * 8) | ((unsigned)mat << 31);
  }
  auto stage = [&](int p, int buf) {
#pragma unroll
    for (int e = 0; e < 7; ++e) {
      unsigned sv = src[e]; asm volatile("" : "+v"(sv));
      glds16(((sv >> 31) ? wu : wg) + (size_t)p * 128 * F_ + (sv & 0x7fffffffu), lds + buf * 57344 + (w * 7 + e) * 1024);
    }
  };
  const int trb = (((lane >> 2) & 3) + 4 * hi) * 448 + 8 * (lane & 3) + 224 * ((lane >> 4) & 1);
  bf16x8 hf[32];
  const char* Hg = ws + WS_H + (size_t)qt * 32 * 1024 + lane * 16;
#pragma unroll
  for (int s = 0; s < 32; ++s) hf[s] = *(const bf16x8*)(Hg + s * 1024);
  stage(0, 0);
  f32x16 acc[NF] = {};
  float wsum = 0.f, sx = 0.f, sxx = 0.f;
#pragma unroll
  for (int p = 0; p < 4; ++p) {
    __syncthreads();
    if (p < 3) stage(p + 1, (p + 1) & 1);
    k4_body<NF>(lds, p & 1, p, fg0, trb, hf, acc, &wsum, tb);
#pragma unroll
    for (int s = p * 8; s < p * 8 + 8; ++s) {
      u32x4 u = __builtin_bit_cast(u32x4, hf[s]);
#pragma unroll
      for (int e = 0; e < 4; ++e) { sx = dot2(u[e], 0x3f803f80u, sx); sxx = dot2(u[e], u[e], sxx); }
    }
  }
  wsum = xhalf_add(wsum);
  if (tb < NF && hi == 0) cs[(fg0 + tb) * 32 + r] = wsum;
  sx = xhalf_add(sx); sxx = xhalf_add(sxx);
  const float mu = sx * (1.f / 512.f);
  const float var = fmaxf(sxx * (1.f / 512.f) - mu * mu, 0.f);
  const float rs = rsqrtf(var + 1e-5f);
  __syncthreads();
  char* dst = ws + WS_A + ((size_t)qt * 112 + fb * 7 + fg0) * 1024 + lane * 16;
#pragma unroll
  for (int f = 0; f < NF; ++f) {
    float a[8];
#pragma unroll
    for (int g = 0; g < 2; ++g) {
      const f32x4 cg = *(const f32x4*)(cs + (fg0 + f) * 32 + 8 * g + 4 * hi);
      const f32x4 cu = *(const f32x4*)(cs + (fg0 + f) * 32 + 16 + 8 * g + 4 * hi);
#pragma unroll
      for (int e = 0; e < 4; ++e) {
        const float gv = (acc[f][4 * g + e] - mu * cg[e]) * rs, uv = (acc[f][8 + 4 * g + e] - mu * cu[e]) * rs;
        a[4 * g + e] = uv / (1.f + __expf(-gv));
      }
    }
    u32x4 o = {cvtpk(a[0], a[1]), cvtpk(a[2], a[3]), cvtpk(a[4], a[5]), cvtpk(a[6], a[7])};
    *(u32x4*)(dst + f * 1024) = o;
  }
}
__global__ __launch_bounds__(512, 1) void k4_glu(const uint16_t* __restrict__ wg, const uint16_t* __restrict__ wu, char* __restrict__ ws) {
  extern __shared__ __attribute__((aligned(16))) char lds[];
  const int tid = threadIdx.x, lane = tid & 63, w = __builtin_amdgcn_readfirstlane(tid >> 6);
  if (w < 4) k4_wave<4>(wg, wu, ws, lds, lane, w); else k4_wave<3>(wg, wu, ws, lds, lane, w);
}

extern "C" void kernel_launch(void* const* d_in, const int* in_sizes, int n_in, void* d_out, int out_size, void* d_ws,
                              size_t ws_size, hipStream_t stream) {
  const uint16_t* x = (const uint16_t*)d_in[0];
  const uint16_t* ckv = (const uint16_t*)d_in[1];
  const uint16_t* wq = (const uint16_t*)d_in[2];
  const uint16_t* wuk = (const uint16_t*)d_in[3];
  const uint16_t* wuv = (const uint16_t*)d_in[4];
  const uint16_t* wo = (const uint16_t*)d_in[5];
  const uint16_t* wg = (const uint16_t*)d_in[7];
  const uint16_t* wu = (const uint16_t*)d_in[8];
  const uint16_t* wd = (const uint16_t*)d_in[9];
  char* ws = (char*)d_ws;
  uint16_t* out = (uint16_t*)d_out;
  (void)hipFuncSetAttribute((const void*)k2_attn, hipFuncAttributeMaxDynamicSharedMemorySize, 131072);
  (void)hipFuncSetAttribute((const void*)k35_proj<0>, hipFuncAttributeMaxDynamicSharedMemorySize, 131072);
  (void)hipFuncSetAttribute((const void*)k35_proj<1>, hipFuncAttributeMaxDynamicSharedMemorySize, 131072);
  (void)hipFuncSetAttribute((const void*)k4_glu, hipFuncAttributeMaxDynamicSharedMemorySize, 2 * 57344 + 1024);
  k1_qkv<<<256, 512, 32768 + 512, stream>>>(x, ckv, wq, wuk, wuv, ws);
  k2_attn<<<256, 512, 131072, stream>>>(ws);
  k35_proj<0><<<256, 512, 131072, stream>>>(wo, x, ws, out);
  k4_glu<<<256, 512, 2 * 57344 + 1024, stream>>>(wg, wu, ws);
  k35_proj<1><<<256, 512, 131072, stream>>>(wd, x, ws, out);
}
```

```cpp
#include <hip/hip_runtime.h>
#include <stdint.h>

typedef short bf16x8 __attribute__((ext_vector_type(8)));
typedef short s16x4 __attribute__((ext_vector_type(4)));
typedef float f32x2 __attribute__((ext_vector_type(2)));
typedef unsigned u32x4 __attribute__((ext_vector_type(4)));
typedef unsigned u32x2 __attribute__((ext_vector_type(2)));
typedef __bf16 bf16x2 __attribute__((ext_vector_type(2)));
#define DI __device__ __forceinline__
#define LDS3 __attribute__((address_space(3)))
constexpr int F_ = 1792;
constexpr size_t WS_X = 80u << 20, WS_WP = 88u << 20;
DI unsigned cvtpk(float lo, float hi) {
  f32x2 v = {lo, hi}; bf16x2 b = __builtin_convertvector(v, bf16x2); return __builtin_bit_cast(unsigned, b);
}
DI float bflo(unsigned u) { return __uint_as_float(u << 16); }
DI float bfhi(unsigned u) { return __uint_as_float(u & 0xffff0000u); }
DI bf16x8 trfrag(const char* p_lo, const char* p_hi) {
  s16x4 lo = __builtin_amdgcn_ds_read_tr16_b64_v4i16((LDS3 s16x4*)p_lo);
  s16x4 hi = __builtin_amdgcn_ds_read_tr16_b64_v4i16((LDS3 s16x4*)p_hi);
  return __builtin_shufflevector(lo, hi, 0, 1, 2, 3, 4, 5, 6, 7);
}
__global__ __launch_bounds__(256, 1) void k1_kv(char* __restrict__ ws, const uint16_t* __restrict__ ckv, const uint16_t* __restrict__ wuk,
    const uint16_t* __restrict__ wuv, const uint16_t* __restrict__ wq, const uint16_t* __restrict__ x) {
  asm volatile(
R"ASM(
s_mov_b32 s64, %11
s_and_b32 s65, %12, 0xffff
s_mov_b32 s66, 0x200000
s_mov_b32 s67, 0x20000
v_lshrrev_b32 v48, 1, v0
v_and_b32 v49, 1, v0
v_lshlrev_b32 v50, 5, v48
v_lshl_add_u32 v50, v49, 10, v50
v_lshrrev_b32 v51, 6, v0
v_lshl_add_u32 v50, v51, 10, v50
s_lshl_b32 s46, %8, 13
buffer_load_dwordx4 v[192:195], v50, s[64:67], s46 offen offset:0
buffer_load_dwordx4 v[196:199], v50, s[64:67], s46 offen offset:16
s_mov_b32 s20, %2
s_and_b32 s21, %3, 0xffff
s_mov_b32 s22, 0x200000
s_mov_b32 s23, 0x20000
s_mov_b32 s24, %4
s_and_b32 s25, %5, 0xffff
s_mov_b32 s26, 0x400000
s_mov_b32 s27, 0x20000
s_and_b32 s36, %8, 31
s_lshr_b32 s37, %8, 5
v_lshrrev_b32 v12, 6, v0
v_and_b32 v1, 63, v0
v_readfirstlane_b32 s38, v12
s_and_b32 s39, s38, 1
s_lshr_b32 s46, s38, 1
s_lshl_b32 s41, s38, 10
s_lshl_b32 s47, s39, 4
s_add_u32 s41, s41, s47
s_lshl_b32 s47, s46, 6
s_add_u32 s41, s41, s47
s_lshl_b32 s39, s39, 1
s_lshl_b32 s46, s46, 3
s_add_u32 s39, s39, s46
v_bfe_u32 v12, v0, 5, 1
v_bfe_u32 v13, v0, 4, 1
v_lshlrev_b32 v12, 4, v12
v_lshl_add_u32 v12, v13, 2, v12
v_bfe_u32 v13, v0, 3, 1
v_add3_u32 v12, s39, v12, v13
v_lshlrev_b32 v2, 13, v12
v_and_b32 v12, 7, v0
v_lshl_add_u32 v2, v12, 4, v2
s_lshl_b32 s40, s36, 8
v_bfe_u32 v13, v0, 3, 3
v_xor_b32 v12, v12, v13
v_lshlrev_b32 v12, 4, v12
v_lshl_add_u32 v8, v13, 10, v12
v_xor_b32 v9, 16, v8
s_lshl_b32 s39, s37, 2
s_add_u32 s39, s39, s38
s_lshl_b32 s42, s39, 16
s_mul_i32 s43, s38, 24064
s_add_u32 s43, s43, 67584
s_add_u32 m0, s43, 0
s_add_u32 s47, s42, 0
buffer_load_dwordx4 v8, s[20:23], s47 offen lds
s_add_u32 m0, s43, 1024
s_add_u32 s47, s42, 16384
buffer_load_dwordx4 v9, s[20:23], s47 offen lds
s_add_u32 m0, s43, 2048
s_add_u32 s47, s42, 8192
buffer_load_dwordx4 v8, s[20:23], s47 offen lds
s_add_u32 m0, s43, 3072
s_add_u32 s47, s42, 24576
buffer_load_dwordx4 v9, s[20:23], s47 offen lds
s_add_u32 m0, s43, 4096
s_add_u32 s47, s42, 32768
buffer_load_dwordx4 v8, s[20:23], s47 offen lds
s_add_u32 m0, s43, 5120
s_add_u32 s47, s42, 49152
buffer_load_dwordx4 v9, s[20:23], s47 offen lds
s_add_u32 m0, s43, 6144
s_add_u32 s47, s42, 40960
buffer_load_dwordx4 v8, s[20:23], s47 offen lds
s_add_u32 m0, s43, 7168
s_add_u32 s47, s42, 57344
buffer_load_dwordx4 v9, s[20:23], s47 offen lds
s_add_u32 m0, s41, 0
s_add_u32 s47, s40, 0
buffer_load_dwordx4 v2, s[24:27], s47 offen nt lds
s_add_u32 m0, s41, 4224
s_add_u32 s47, s40, 128
buffer_load_dwordx4 v2, s[24:27], s47 offen nt lds
s_add_u32 m0, s41, 8448
s_add_u32 s47, s40, 262144
buffer_load_dwordx4 v2, s[24:27], s47 offen nt lds
s_add_u32 m0, s41, 12672
s_add_u32 s47, s40, 262272
buffer_load_dwordx4 v2, s[24:27], s47 offen nt lds
s_add_u32 m0, s43, 8192
s_add_u32 s47, s42, 128
buffer_load_dwordx4 v8, s[20:23], s47 offen lds
s_add_u32 m0, s43, 9216
s_add_u32 s47, s42, 16512
buffer_load_dwordx4 v9, s[20:23], s47 offen lds
s_add_u32 m0, s43, 10240
s_add_u32 s47, s42, 8320
buffer_load_dwordx4 v8, s[20:23], s47 offen lds
s_add_u32 m0, s43, 11264
s_add_u32 s47, s42, 24704
buffer_load_dwordx4 v9, s[20:23], s47 offen lds
s_add_u32 m0, s43, 12288
s_add_u32 s47, s42, 32896
buffer_load_dwordx4 v8, s[20:23], s47 offen lds
s_add_u32 m0, s43, 13312
s_add_u32 s47, s42, 49280
buffer_load_dwordx4 v9, s[20:23], s47 offen lds
s_add_u32 m0, s43, 14336
s_add_u32 s47, s42, 41088
buffer_load_dwordx4 v8, s[20:23], s47 offen lds
s_add_u32 m0, s43, 15360
s_add_u32 s47, s42, 57472
buffer_load_dwordx4 v9, s[20:23], s47 offen lds
s_mov_b32 s16, %0
s_and_b32 s17, %1, 0xffff
s_mov_b32 s18, 0x8000000
s_mov_b32 s19, 0x20000
s_mov_b32 s28, %6
s_and_b32 s29, %7, 0xffff
s_mov_b32 s30, 0x400000
s_mov_b32 s31, 0x20000
s_mov_b32 s84, %9
s_and_b32 s85, %10, 0xffff
s_mov_b32 s86, 0x400000
s_mov_b32 s87, 0x20000
v_and_b32 v12, 7, v0
v_and_b32 v13, 8, v0
v_lshl_add_u32 v12, v13, 1, v12
v_lshrrev_b32 v13, 4, v1
v_and_b32 v14, 1, v13
v_lshlrev_b32 v14, 9, v14
v_lshl_add_u32 v14, v12, 4, v14
v_lshrrev_b32 v15, 1, v13
v_lshl_add_u32 v6, v15, 10, v14
v_lshl_add_u32 v7, v15, 12, v14
v_bfe_u32 v12, v0, 3, 1
v_and_b32 v14, 7, v0
v_xor_b32 v13, v13, v14
v_xor_b32 v13, v13, v12
v_lshlrev_b32 v13, 4, v13
v_lshl_add_u32 v13, v14, 7, v13
v_lshl_add_u32 v13, v12, 10, v13
v_add_u32 v10, s43, v13
v_xor_b32 v13, 64, v13
v_add_u32 v11, s43, v13
v_bfe_u32 v12, v0, 3, 1
v_mul_u32_u24 v3, 1040, v12
v_bfe_u32 v12, v0, 4, 1
v_mul_u32_u24 v12, 2112, v12
v_add_u32 v3, v3, v12
v_bfe_u32 v12, v0, 5, 1
v_lshl_add_u32 v3, v12, 9, v3
v_bfe_u32 v12, v0, 2, 1
v_lshl_add_u32 v3, v12, 7, v3
v_bfe_u32 v12, v0, 1, 1
v_lshl_add_u32 v3, v12, 5, v3
v_and_b32 v12, 1, v0
v_lshl_add_u32 v3, v12, 3, v3
v_add_u32 v4, 0x10000, v3
v_add_u32 v5, 0x20000, v3
v_lshlrev_b32 v1, 4, v1
s_lshl_b32 s46, s36, 19
s_lshr_b32 s39, s42, 2
s_add_u32 s39, s39, s46
s_add_u32 s44, s39, 0x1000000
s_add_u32 s45, s39, 0x2000000
s_mov_b32 s92, 0x3f803f80
v_mov_b32 v52, 0
v_mov_b32 v53, 0
s_lshl_b32 s39, %8, 2
s_add_u32 s39, s39, s38
s_and_b32 s46, s39, 15
s_lshl_b32 s46, s46, 5
s_lshr_b32 s39, s39, 4
s_lshl_b32 s39, s39, 15
s_add_u32 s39, s39, s46
s_add_u32 s80, s39, 0x5000000
v_and_b32 v48, 31, v48
v_lshlrev_b32 v48, 10, v48
v_lshl_add_u32 v48, v49, 4, v48
s_waitcnt vmcnt(21)
v_dot2_f32_bf16 v52, v192, s92, v52
v_dot2_f32_bf16 v53, v193, s92, v53
v_dot2_f32_bf16 v52, v194, s92, v52
v_dot2_f32_bf16 v53, v195, s92, v53
s_waitcnt vmcnt(20)
v_dot2_f32_bf16 v52, v196, s92, v52
v_dot2_f32_bf16 v53, v197, s92, v53
v_dot2_f32_bf16 v52, v198, s92, v52
v_dot2_f32_bf16 v53, v199, s92, v53
v_lshlrev_b32 v208, 16, v192
v_and_b32 v209, 0xffff0000, v192
v_lshlrev_b32 v210, 16, v193
v_and_b32 v211, 0xffff0000, v193
v_lshlrev_b32 v212, 16, v194
v_and_b32 v213, 0xffff0000, v194
v_lshlrev_b32 v214, 16, v195
v_and_b32 v215, 0xffff0000, v195
v_lshlrev_b32 v216, 16, v196
v_and_b32 v217, 0xffff0000, v196
v_lshlrev_b32 v218, 16, v197
v_and_b32 v219, 0xffff0000, v197
v_lshlrev_b32 v220, 16, v198
v_and_b32 v221, 0xffff0000, v198
v_lshlrev_b32 v222, 16, v199
v_and_b32 v223, 0xffff0000, v199
v_add_f32 v52, v52, v53
s_nop 1
v_add_f32_dpp v52, v52, v52 row_ror:8 row_mask:0xf bank_mask:0xf
s_nop 1
v_add_f32_dpp v52, v52, v52 row_ror:4 row_mask:0xf bank_mask:0xf
s_nop 1
v_add_f32_dpp v52, v52, v52 row_ror:2 row_mask:0xf bank_mask:0xf
v_mov_b32 v51, v52
s_nop 1
v_permlane16_swap_b32 v52, v51
s_nop 0
v_add_f32 v52, v52, v51
v_mov_b32 v51, v52
s_nop 1
v_permlane32_swap_b32 v52, v51
s_nop 0
v_add_f32 v52, v52, v51
v_mul_f32 v54, 0xbb000000, v52
v_mul_f32 v55, 0xbb000000, v52
v_mov_b32 v52, 0
v_mov_b32 v53, 0
v_mov_b32 v56, 0
v_mov_b32 v57, 0
v_pk_add_f32 v[208:209], v[208:209], v[54:55]
v_pk_add_f32 v[210:211], v[210:211], v[54:55]
v_pk_add_f32 v[212:213], v[212:213], v[54:55]
v_pk_add_f32 v[214:215], v[214:215], v[54:55]
v_pk_add_f32 v[216:217], v[216:217], v[54:55]
v_pk_add_f32 v[218:219], v[218:219], v[54:55]
v_pk_add_f32 v[220:221], v[220:221], v[54:55]
v_pk_add_f32 v[222:223], v[222:223], v[54:55]
v_pk_fma_f32 v[52:53], v[208:209], v[208:209], v[52:53]
v_pk_fma_f32 v[56:57], v[210:211], v[210:211], v[56:57]
v_pk_fma_f32 v[52:53], v[212:213], v[212:213], v[52:53]
v_pk_fma_f32 v[56:57], v[214:215], v[214:215], v[56:57]
v_pk_fma_f32 v[52:53], v[216:217], v[216:217], v[52:53]
v_pk_fma_f32 v[56:57], v[218:219], v[218:219], v[56:57]
v_pk_fma_f32 v[52:53], v[220:221], v[220:221], v[52:53]
v_pk_fma_f32 v[56:57], v[222:223], v[222:223], v[56:57]
v_pk_add_f32 v[52:53], v[52:53], v[56:57]
v_add_f32 v52, v52, v53
s_nop 1
v_add_f32_dpp v52, v52, v52 row_ror:8 row_mask:0xf bank_mask:0xf
s_nop 1
v_add_f32_dpp v52, v52, v52 row_ror:4 row_mask:0xf bank_mask:0xf
s_nop 1
v_add_f32_dpp v52, v52, v52 row_ror:2 row_mask:0xf bank_mask:0xf
v_mov_b32 v51, v52
s_nop 1
v_permlane16_swap_b32 v52, v51
s_nop 0
v_add_f32 v52, v52, v51
v_mov_b32 v51, v52
s_nop 1
v_permlane32_swap_b32 v52, v51
s_nop 0
v_add_f32 v52, v52, v51
v_mov_b32 v53, 0x3727c5ac
v_fmac_f32 v53, 0x3b000000, v52
v_rsq_f32 v53, v53
s_nop 0
v_mul_f32 v54, 0x3e0293ee, v53
v_mul_f32 v55, 0x3e0293ee, v53
v_pk_mul_f32 v[208:209], v[208:209], v[54:55]
v_pk_mul_f32 v[210:211], v[210:211], v[54:55]
v_pk_mul_f32 v[212:213], v[212:213], v[54:55]
v_pk_mul_f32 v[214:215], v[214:215], v[54:55]
v_pk_mul_f32 v[216:217], v[216:217], v[54:55]
v_pk_mul_f32 v[218:219], v[218:219], v[54:55]
v_pk_mul_f32 v[220:221], v[220:221], v[54:55]
v_pk_mul_f32 v[222:223], v[222:223], v[54:55]
v_cvt_pk_bf16_f32 v192, v208, v209
v_cvt_pk_bf16_f32 v193, v210, v211
v_cvt_pk_bf16_f32 v194, v216, v217
v_cvt_pk_bf16_f32 v195, v218, v219
buffer_store_dwordx4 v[192:195], v48, s[16:19], s80 offen offset:0
v_cvt_pk_bf16_f32 v196, v212, v213
v_cvt_pk_bf16_f32 v197, v214, v215
v_cvt_pk_bf16_f32 v198, v220, v221
v_cvt_pk_bf16_f32 v199, v222, v223
buffer_store_dwordx4 v[196:199], v48, s[16:19], s80 offen offset:512
s_lshl_b32 s39, %8, 12
s_add_u32 s39, s39, 0x6300000
s_cmp_lt_u32 %8, 8
s_cselect_b32 s39, s39, 0x10000000
v_lshlrev_b32 v58, 4, v0
v_add_u32 v58, s39, v58
v_mov_b32 v54, 0
v_mov_b32 v55, 0
v_mov_b32 v56, 0
v_mov_b32 v57, 0
buffer_store_dwordx4 v[54:57], v58, s[16:19], 0 offen
s_waitcnt vmcnt(21)
ds_read_b128 a[0:3], v10 offset:0
ds_read_b128 a[16:19], v11 offset:0
s_waitcnt vmcnt(19)
ds_read_b128 a[4:7], v10 offset:2048
ds_read_b128 a[20:23], v11 offset:2048
s_waitcnt vmcnt(17)
ds_read_b128 a[8:11], v10 offset:4096
ds_read_b128 a[24:27], v11 offset:4096
s_waitcnt vmcnt(15)
ds_read_b128 a[12:15], v10 offset:6144
ds_read_b128 a[28:31], v11 offset:6144
s_waitcnt vmcnt(11)
s_barrier
ds_read_b64_tr_b16 v[16:17], v3 offset:0
ds_read_b64_tr_b16 v[18:19], v3 offset:256
ds_read_b64_tr_b16 v[20:21], v3 offset:16
ds_read_b64_tr_b16 v[22:23], v3 offset:272
ds_read_b64_tr_b16 v[24:25], v3 offset:64
ds_read_b64_tr_b16 v[26:27], v3 offset:320
ds_read_b64_tr_b16 v[28:29], v3 offset:80
ds_read_b64_tr_b16 v[30:31], v3 offset:336
s_waitcnt lgkmcnt(6)
v_mfma_f32_16x16x32_bf16 v[64:67], v[16:19], a[0:3], 0
ds_read_b64_tr_b16 v[32:33], v3 offset:4224
v_mfma_f32_16x16x32_bf16 v[68:71], v[16:19], a[4:7], 0
v_mfma_f32_16x16x32_bf16 v[72:75], v[16:19], a[8:11], 0
ds_read_b64_tr_b16 v[34:35], v3 offset:4480
s_add_u32 m0, s41, 16896
s_add_u32 s47, s40, 524288
buffer_load_dwordx4 v2, s[24:27], s47 offen nt lds
v_mfma_f32_16x16x32_bf16 v[76:79], v[16:19], a[12:15], 0
s_waitcnt lgkmcnt(6)
v_mfma_f32_16x16x32_bf16 v[80:83], v[20:23], a[0:3], 0
ds_read_b64_tr_b16 v[36:37], v3 offset:4240
v_mfma_f32_16x16x32_bf16 v[84:87], v[20:23], a[4:7], 0
s_add_u32 m0, s41, 21120
s_add_u32 s47, s40, 524416
buffer_load_dwordx4 v2, s[24:27], s47 offen nt lds
v_mfma_f32_16x16x32_bf16 v[88:91], v[20:23], a[8:11], 0
ds_read_b64_tr_b16 v[38:39], v3 offset:4496
v_mfma_f32_16x16x32_bf16 v[92:95], v[20:23], a[12:15], 0
s_waitcnt lgkmcnt(6)
v_mfma_f32_16x16x32_bf16 v[96:99], v[24:27], a[0:3], 0
ds_read_b64_tr_b16 v[40:41], v3 offset:4288
s_add_u32 m0, s41, 25344
s_add_u32 s47, s40, 786432
buffer_load_dwordx4 v2, s[24:27], s47 offen nt lds
v_mfma_f32_16x16x32_bf16 v[100:103], v[24:27], a[4:7], 0
v_mfma_f32_16x16x32_bf16 v[104:107], v[24:27], a[8:11], 0
ds_read_b64_tr_b16 v[42:43], v3 offset:4544
v_mfma_f32_16x16x32_bf16 v[108:111], v[24:27], a[12:15], 0
s_add_u32 m0, s41, 29568
s_add_u32 s47, s40, 786560
buffer_load_dwordx4 v2, s[24:27], s47 offen nt lds
s_waitcnt lgkmcnt(6)
v_mfma_f32_16x16x32_bf16 v[112:115], v[28:31], a[0:3], 0
ds_read_b64_tr_b16 v[44:45], v3 offset:4304
v_mfma_f32_16x16x32_bf16 v[116:119], v[28:31], a[4:7], 0
v_mfma_f32_16x16x32_bf16 v[120:123], v[28:31], a[8:11], 0
ds_read_b64_tr_b16 v[46:47], v3 offset:4560
v_mfma_f32_16x16x32_bf16 v[124:127], v[28:31], a[12:15], 0
s_waitcnt lgkmcnt(6)
v_mfma_f32_16x16x32_bf16 v[128:131], v[32:35], a[0:3], 0
ds_read_b64_tr_b16 v[16:17], v3 offset:8448
v_mfma_f32_16x16x32_bf16 v[132:135], v[32:35], a[4:7], 0
v_mfma_f32_16x16x32_bf16 v[136:139], v[32:35], a[8:11], 0
ds_read_b64_tr_b16 v[18:19], v3 offset:8704
s_add_u32 m0, s43, 16384
s_add_u32 s47, s42, 256
buffer_load_dwordx4 v8, s[20:23], s47 offen lds
v_mfma_f32_16x16x32_bf16 v[140:143], v[32:35], a[12:15], 0
s_waitcnt lgkmcnt(6)
v_mfma_f32_16x16x32_bf16 v[144:147], v[36:39], a[0:3], 0
ds_read_b64_tr_b16 v[20:21], v3 offset:8464
v_mfma_f32_16x16x32_bf16 v[148:151], v[36:39], a[4:7], 0
s_add_u32 m0, s43, 17408
s_add_u32 s47, s42, 16640
buffer_load_dwordx4 v9, s[20:23], s47 offen lds
v_mfma_f32_16x16x32_bf16 v[152:155], v[36:39], a[8:11], 0
ds_read_b64_tr_b16 v[22:23], v3 offset:8720
v_mfma_f32_16x16x32_bf16 v[156:159], v[36:39], a[12:15], 0
s_waitcnt lgkmcnt(6)
v_mfma_f32_16x16x32_bf16 v[160:163], v[40:43], a[0:3], 0
ds_read_b64_tr_b16 v[24:25], v3 offset:8512
s_add_u32 m0, s43, 18432
s_add_u32 s47, s42, 8448
buffer_load_dwordx4 v8, s[20:23], s47 offen lds
v_mfma_f32_16x16x32_bf16 v[164:167], v[40:43], a[4:7], 0
v_mfma_f32_16x16x32_bf16 v[168:171], v[40:43], a[8:11], 0
ds_read_b64_tr_b16 v[26:27], v3 offset:8768
v_mfma_f32_16x16x32_bf16 v[172:175], v[40:43], a[12:15], 0
s_add_u32 m0, s43, 19456
s_add_u32 s47, s42, 24832
buffer_load_dwordx4 v9, s[20:23], s47 offen lds
s_waitcnt lgkmcnt(6)
v_mfma_f32_16x16x32_bf16 v[176:179], v[44:47], a[0:3], 0
ds_read_b64_tr_b16 v[28:29], v3 offset:8528
v_mfma_f32_16x16x32_bf16 v[180:183], v[44:47], a[4:7], 0
v_mfma_f32_16x16x32_bf16 v[184:187], v[44:47], a[8:11], 0
ds_read_b64_tr_b16 v[30:31], v3 offset:8784
v_mfma_f32_16x16x32_bf16 v[188:191], v[44:47], a[12:15], 0
s_waitcnt lgkmcnt(6)
v_mfma_f32_16x16x32_bf16 v[64:67], v[16:19], a[16:19], v[64:67]
ds_read_b64_tr_b16 v[32:33], v3 offset:12672
v_mfma_f32_16x16x32_bf16 v[68:71], v[16:19], a[20:23], v[68:71]
s_waitcnt vmcnt(17)
ds_read_b128 a[32:35], v10 offset:8192
v_mfma_f32_16x16x32_bf16 v[72:75], v[16:19], a[24:27], v[72:75]
ds_read_b64_tr_b16 v[34:35], v3 offset:12928
s_add_u32 m0, s43, 20480
s_add_u32 s47, s42, 33024
buffer_load_dwordx4 v8, s[20:23], s47 offen lds
v_mfma_f32_16x16x32_bf16 v[76:79], v[16:19], a[28:31], v[76:79]
ds_read_b128 a[48:51], v11 offset:8192
s_waitcnt lgkmcnt(8)
v_mfma_f32_16x16x32_bf16 v[80:83], v[20:23], a[16:19], v[80:83]
ds_read_b64_tr_b16 v[36:37], v3 offset:12688
v_mfma_f32_16x16x32_bf16 v[84:87], v[20:23], a[20:23], v[84:87]
s_add_u32 m0, s43, 21504
s_add_u32 s47, s42, 49408
buffer_load_dwordx4 v9, s[20:23], s47 offen lds
s_waitcnt vmcnt(17)
ds_read_b128 a[36:39], v10 offset:10240
v_mfma_f32_16x16x32_bf16 v[88:91], v[20:23], a[24:27], v[88:91]
ds_read_b64_tr_b16 v[38:39], v3 offset:12944
v_mfma_f32_16x16x32_bf16 v[92:95], v[20:23], a[28:31], v[92:95]
ds_read_b128 a[52:55], v11 offset:10240
s_waitcnt lgkmcnt(10)
v_mfma_f32_16x16x32_bf16 v[96:99], v[24:27], a[16:19], v[96:99]
ds_read_b64_tr_b16 v[40:41], v3 offset:12736
s_add_u32 m0, s43, 0
s_add_u32 s47, s42, 41216
buffer_load_dwordx4 v8, s[20:23], s47 offen lds
v_mfma_f32_16x16x32_bf16 v[100:103], v[24:27], a[20:23], v[100:103]
s_waitcnt vmcnt(16)
ds_read_b128 a[40:43], v10 offset:12288
v_mfma_f32_16x16x32_bf16 v[104:107], v[24:27], a[24:27], v[104:107]
ds_read_b64_tr_b16 v[42:43], v3 offset:12992
v_mfma_f32_16x16x32_bf16 v[108:111], v[24:27], a[28:31], v[108:111]
s_add_u32 m0, s43, 1024
s_add_u32 s47, s42, 57600
buffer_load_dwordx4 v9, s[20:23], s47 offen lds
ds_read_b128 a[56:59], v11 offset:12288
s_waitcnt lgkmcnt(12)
v_mfma_f32_16x16x32_bf16 v[112:115], v[28:31], a[16:19], v[112:115]
ds_read_b64_tr_b16 v[44:45], v3 offset:12752
v_mfma_f32_16x16x32_bf16 v[116:119], v[28:31], a[20:23], v[116:119]
s_waitcnt vmcnt(15)
ds_read_b128 a[44:47], v10 offset:14336
v_mfma_f32_16x16x32_bf16 v[120:123], v[28:31], a[24:27], v[120:123]
ds_read_b64_tr_b16 v[46:47], v3 offset:13008
v_mfma_f32_16x16x32_bf16 v[124:127], v[28:31], a[28:31], v[124:127]
ds_read_b128 a[60:63], v11 offset:14336
s_waitcnt lgkmcnt(13)
v_mfma_f32_16x16x32_bf16 v[128:131], v[32:35], a[16:19], v[128:131]
s_waitcnt vmcnt(8)
s_barrier
v_mfma_f32_16x16x32_bf16 v[132:135], v[32:35], a[20:23], v[132:135]
ds_read_b64_tr_b16 v[16:17], v3 offset:16896
v_mfma_f32_16x16x32_bf16 v[136:139], v[32:35], a[24:27], v[136:139]
s_add_u32 m0, s41, 33792
s_add_u32 s47, s40, 1048576
buffer_load_dwordx4 v2, s[24:27], s47 offen nt lds
v_mfma_f32_16x16x32_bf16 v[140:143], v[32:35], a[28:31], v[140:143]
ds_read_b64_tr_b16 v[18:19], v3 offset:17152
s_waitcnt lgkmcnt(11)
v_mfma_f32_16x16x32_bf16 v[144:147], v[36:39], a[16:19], v[144:147]
v_mfma_f32_16x16x32_bf16 v[148:151], v[36:39], a[20:23], v[148:151]
ds_read_b64_tr_b16 v[20:21], v3 offset:16912
s_add_u32 m0, s41, 38016
s_add_u32 s47, s40, 1048704
buffer_load_dwordx4 v2, s[24:27], s47 offen nt lds
v_mfma_f32_16x16x32_bf16 v[152:155], v[36:39], a[24:27], v[152:155]
v_mfma_f32_16x16x32_bf16 v[156:159], v[36:39], a[28:31], v[156:159]
ds_read_b64_tr_b16 v[22:23], v3 offset:17168
s_waitcnt lgkmcnt(9)
v_mfma_f32_16x16x32_bf16 v[160:163], v[40:43], a[16:19], v[160:163]
s_add_u32 m0, s41, 42240
s_add_u32 s47, s40, 1310720
buffer_load_dwordx4 v2, s[24:27], s47 offen nt lds
v_mfma_f32_16x16x32_bf16 v[164:167], v[40:43], a[20:23], v[164:167]
ds_read_b64_tr_b16 v[24:25], v3 offset:16960
v_mfma_f32_16x16x32_bf16 v[168:171], v[40:43], a[24:27], v[168:171]
v_mfma_f32_16x16x32_bf16 v[172:175], v[40:43], a[28:31], v[172:175]
ds_read_b64_tr_b16 v[26:27], v3 offset:17216
s_add_u32 m0, s41, 46464
s_add_u32 s47, s40, 1310848
buffer_load_dwordx4 v2, s[24:27], s47 offen nt lds
s_waitcnt lgkmcnt(7)
v_mfma_f32_16x16x32_bf16 v[176:179], v[44:47], a[16:19], v[176:179]
v_mfma_f32_16x16x32_bf16 v[180:183], v[44:47], a[20:23], v[180:183]
ds_read_b64_tr_b16 v[28:29], v3 offset:16976
v_mfma_f32_16x16x32_bf16 v[184:187], v[44:47], a[24:27], v[184:187]
v_mfma_f32_16x16x32_bf16 v[188:191], v[44:47], a[28:31], v[188:191]
ds_read_b64_tr_b16 v[30:31], v3 offset:17232
s_waitcnt lgkmcnt(6)
v_mfma_f32_16x16x32_bf16 v[64:67], v[16:19], a[32:35], v[64:67]
ds_read_b64_tr_b16 v[32:33], v3 offset:21120
v_mfma_f32_16x16x32_bf16 v[68:71], v[16:19], a[36:39], v[68:71]
v_mfma_f32_16x16x32_bf16 v[72:75], v[16:19], a[40:43], v[72:75]
ds_read_b64_tr_b16 v[34:35], v3 offset:21376
s_add_u32 m0, s43, 2048
s_add_u32 s47, s42, 384
buffer_load_dwordx4 v8, s[20:23], s47 offen lds
v_mfma_f32_16x16x32_bf16 v[76:79], v[16:19], a[44:47], v[76:79]
s_waitcnt lgkmcnt(6)
v_mfma_f32_16x16x32_bf16 v[80:83], v[20:23], a[32:35], v[80:83]
ds_read_b64_tr_b16 v[36:37], v3 offset:21136
v_mfma_f32_16x16x32_bf16 v[84:87], v[20:23], a[36:39], v[84:87]
s_add_u32 m0, s43, 3072
s_add_u32 s47, s42, 16768
buffer_load_dwordx4 v9, s[20:23], s47 offen lds
v_mfma_f32_16x16x32_bf16 v[88:91], v[20:23], a[40:43], v[88:91]
ds_read_b64_tr_b16 v[38:39], v3 offset:21392
v_mfma_f32_16x16x32_bf16 v[92:95], v[20:23], a[44:47], v[92:95]
s_waitcnt lgkmcnt(6)
v_mfma_f32_16x16x32_bf16 v[96:99], v[24:27], a[32:35], v[96:99]
ds_read_b64_tr_b16 v[40:41], v3 offset:21184
s_add_u32 m0, s43, 4096
s_add_u32 s47, s42, 8576
buffer_load_dwordx4 v8, s[20:23], s47 offen lds
v_mfma_f32_16x16x32_bf16 v[100:103], v[24:27], a[36:39], v[100:103]
v_mfma_f32_16x16x32_bf16 v[104:107], v[24:27], a[40:43], v[104:107]
ds_read_b64_tr_b16 v[42:43], v3 offset:21440
v_mfma_f32_16x16x32_bf16 v[108:111], v[24:27], a[44:47], v[108:111]
s_add_u32 m0, s43, 5120
s_add_u32 s47, s42, 24960
buffer_load_dwordx4 v9, s[20:23], s47 offen lds
s_waitcnt lgkmcnt(6)
v_mfma_f32_16x16x32_bf16 v[112:115], v[28:31], a[32:35], v[112:115]
ds_read_b64_tr_b16 v[44:45], v3 offset:21200
v_mfma_f32_16x16x32_bf16 v[116:119], v[28:31], a[36:39], v[116:119]
v_mfma_f32_16x16x32_bf16 v[120:123], v[28:31], a[40:43], v[120:123]
ds_read_b64_tr_b16 v[46:47], v3 offset:21456
v_mfma_f32_16x16x32_bf16 v[124:127], v[28:31], a[44:47], v[124:127]
s_waitcnt lgkmcnt(6)
v_mfma_f32_16x16x32_bf16 v[128:131], v[32:35], a[32:35], v[128:131]
ds_read_b64_tr_b16 v[16:17], v3 offset:25344
v_mfma_f32_16x16x32_bf16 v[132:135], v[32:35], a[36:39], v[132:135]
v_mfma_f32_16x16x32_bf16 v[136:139], v[32:35], a[40:43], v[136:139]
ds_read_b64_tr_b16 v[18:19], v3 offset:25600
s_add_u32 m0, s43, 6144
s_add_u32 s47, s42, 33152
buffer_load_dwordx4 v8, s[20:23], s47 offen lds
v_mfma_f32_16x16x32_bf16 v[140:143], v[32:35], a[44:47], v[140:143]
s_waitcnt lgkmcnt(6)
v_mfma_f32_16x16x32_bf16 v[144:147], v[36:39], a[32:35], v[144:147]
ds_read_b64_tr_b16 v[20:21], v3 offset:25360
v_mfma_f32_16x16x32_bf16 v[148:151], v[36:39], a[36:39], v[148:151]
s_add_u32 m0, s43, 7168
s_add_u32 s47, s42, 49536
buffer_load_dwordx4 v9, s[20:23], s47 offen lds
v_mfma_f32_16x16x32_bf16 v[152:155], v[36:39], a[40:43], v[152:155]
ds_read_b64_tr_b16 v[22:23], v3 offset:25616
v_mfma_f32_16x16x32_bf16 v[156:159], v[36:39], a[44:47], v[156:159]
s_waitcnt lgkmcnt(6)
v_mfma_f32_16x16x32_bf16 v[160:163], v[40:43], a[32:35], v[160:163]
ds_read_b64_tr_b16 v[24:25], v3 offset:25408
s_add_u32 m0, s43, 8192
s_add_u32 s47, s42, 41344
buffer_load_dwordx4 v8, s[20:23], s47 offen lds
v_mfma_f32_16x16x32_bf16 v[164:167], v[40:43], a[36:39], v[164:167]
v_mfma_f32_16x16x32_bf16 v[168:171], v[40:43], a[40:43], v[168:171]
ds_read_b64_tr_b16 v[26:27], v3 offset:25664
v_mfma_f32_16x16x32_bf16 v[172:175], v[40:43], a[44:47], v[172:175]
s_add_u32 m0, s43, 9216
s_add_u32 s47, s42, 57728
buffer_load_dwordx4 v9, s[20:23], s47 offen lds
s_waitcnt lgkmcnt(6)
v_mfma_f32_16x16x32_bf16 v[176:179], v[44:47], a[32:35], v[176:179]
ds_read_b64_tr_b16 v[28:29], v3 offset:25424
v_mfma_f32_16x16x32_bf16 v[180:183], v[44:47], a[36:39], v[180:183]
v_mfma_f32_16x16x32_bf16 v[184:187], v[44:47], a[40:43], v[184:187]
ds_read_b64_tr_b16 v[30:31], v3 offset:25680
v_mfma_f32_16x16x32_bf16 v[188:191], v[44:47], a[44:47], v[188:191]
s_waitcnt lgkmcnt(6)
v_mfma_f32_16x16x32_bf16 v[64:67], v[16:19], a[48:51], v[64:67]
ds_read_b64_tr_b16 v[32:33], v3 offset:29568
v_mfma_f32_16x16x32_bf16 v[68:71], v[16:19], a[52:55], v[68:71]
s_waitcnt vmcnt(18)
ds_read_b128 a[64:67], v10 offset:16384
v_mfma_f32_16x16x32_bf16 v[72:75], v[16:19], a[56:59], v[72:75]
ds_read_b64_tr_b16 v[34:35], v3 offset:29824
s_add_u32 m0, s41, 50688
s_add_u32 s47, s40, 1572864
buffer_load_dwordx4 v2, s[24:27], s47 offen nt lds
v_mfma_f32_16x16x32_bf16 v[76:79], v[16:19], a[60:63], v[76:79]
ds_read_b128 a[80:83], v11 offset:16384
s_waitcnt lgkmcnt(8)
v_mfma_f32_16x16x32_bf16 v[80:83], v[20:23], a[48:51], v[80:83]
ds_read_b64_tr_b16 v[36:37], v3 offset:29584
v_mfma_f32_16x16x32_bf16 v[84:87], v[20:23], a[52:55], v[84:87]
s_add_u32 m0, s41, 54912
s_add_u32 s47, s40, 1572992
buffer_load_dwordx4 v2, s[24:27], s47 offen nt lds
s_waitcnt vmcnt(18)
ds_read_b128 a[68:71], v10 offset:18432
v_mfma_f32_16x16x32_bf16 v[88:91], v[20:23], a[56:59], v[88:91]
ds_read_b64_tr_b16 v[38:39], v3 offset:29840
v_mfma_f32_16x16x32_bf16 v[92:95], v[20:23], a[60:63], v[92:95]
ds_read_b128 a[84:87], v11 offset:18432
s_waitcnt lgkmcnt(10)
v_mfma_f32_16x16x32_bf16 v[96:99], v[24:27], a[48:51], v[96:99]
ds_read_b64_tr_b16 v[40:41], v3 offset:29632
s_add_u32 m0, s41, 59136
s_add_u32 s47, s40, 1835008
buffer_load_dwordx4 v2, s[24:27], s47 offen nt lds
v_mfma_f32_16x16x32_bf16 v[100:103], v[24:27], a[52:55], v[100:103]
s_waitcnt vmcnt(17)
ds_read_b128 a[72:75], v10 offset:20480
v_mfma_f32_16x16x32_bf16 v[104:107], v[24:27], a[56:59], v[104:107]
ds_read_b64_tr_b16 v[42:43], v3 offset:29888
v_mfma_f32_16x16x32_bf16 v[108:111], v[24:27], a[60:63], v[108:111]
s_add_u32 m0, s41, 63360
s_add_u32 s47, s40, 1835136
buffer_load_dwordx4 v2, s[24:27], s47 offen nt lds
ds_read_b128 a[88:91], v11 offset:20480
s_waitcnt lgkmcnt(12)
v_mfma_f32_16x16x32_bf16 v[112:115], v[28:31], a[48:51], v[112:115]
ds_read_b64_tr_b16 v[44:45], v3 offset:29648
v_mfma_f32_16x16x32_bf16 v[116:119], v[28:31], a[52:55], v[116:119]
s_waitcnt vmcnt(16)
ds_read_b128 a[76:79], v10 offset:0
v_mfma_f32_16x16x32_bf16 v[120:123], v[28:31], a[56:59], v[120:123]
ds_read_b64_tr_b16 v[46:47], v3 offset:29904
v_mfma_f32_16x16x32_bf16 v[124:127], v[28:31], a[60:63], v[124:127]
ds_read_b128 a[92:95], v11 offset:0
s_waitcnt lgkmcnt(13)
v_mfma_f32_16x16x32_bf16 v[128:131], v[32:35], a[48:51], v[128:131]
s_waitcnt vmcnt(12)
s_barrier
v_mfma_f32_16x16x32_bf16 v[132:135], v[32:35], a[52:55], v[132:135]
ds_read_b64_tr_b16 v[16:17], v3 offset:33792
v_mfma_f32_16x16x32_bf16 v[136:139], v[32:35], a[56:59], v[136:139]
s_add_u32 m0, s43, 10240
s_add_u32 s47, s42, 512
buffer_load_dwordx4 v8, s[20:23], s47 offen lds
v_mfma_f32_16x16x32_bf16 v[140:143], v[32:35], a[60:63], v[140:143]
ds_read_b64_tr_b16 v[18:19], v3 offset:34048
s_waitcnt lgkmcnt(11)
v_mfma_f32_16x16x32_bf16 v[144:147], v[36:39], a[48:51], v[144:147]
v_mfma_f32_16x16x32_bf16 v[148:151], v[36:39], a[52:55], v[148:151]
ds_read_b64_tr_b16 v[20:21], v3 offset:33808
s_add_u32 m0, s43, 11264
s_add_u32 s47, s42, 16896
buffer_load_dwordx4 v9, s[20:23], s47 offen lds
v_mfma_f32_16x16x32_bf16 v[152:155], v[36:39], a[56:59], v[152:155]
v_mfma_f32_16x16x32_bf16 v[156:159], v[36:39], a[60:63], v[156:159]
ds_read_b64_tr_b16 v[22:23], v3 offset:34064
s_waitcnt lgkmcnt(9)
v_mfma_f32_16x16x32_bf16 v[160:163], v[40:43], a[48:51], v[160:163]
s_add_u32 m0, s43, 12288
s_add_u32 s47, s42, 8704
buffer_load_dwordx4 v8, s[20:23], s47 offen lds
v_mfma_f32_16x16x32_bf16 v[164:167], v[40:43], a[52:55], v[164:167]
ds_read_b64_tr_b16 v[24:25], v3 offset:33856
v_mfma_f32_16x16x32_bf16 v[168:171], v[40:43], a[56:59], v[168:171]
v_mfma_f32_16x16x32_bf16 v[172:175], v[40:43], a[60:63], v[172:175]
ds_read_b64_tr_b16 v[26:27], v3 offset:34112
s_add_u32 m0, s43, 13312
s_add_u32 s47, s42, 25088
buffer_load_dwordx4 v9, s[20:23], s47 offen lds
s_waitcnt lgkmcnt(7)
v_mfma_f32_16x16x32_bf16 v[176:179], v[44:47], a[48:51], v[176:179]
v_mfma_f32_16x16x32_bf16 v[180:183], v[44:47], a[52:55], v[180:183]
ds_read_b64_tr_b16 v[28:29], v3 offset:33872
v_mfma_f32_16x16x32_bf16 v[184:187], v[44:47], a[56:59], v[184:187]
v_mfma_f32_16x16x32_bf16 v[188:191], v[44:47], a[60:63], v[188:191]
ds_read_b64_tr_b16 v[30:31], v3 offset:34128
s_waitcnt lgkmcnt(6)
v_mfma_f32_16x16x32_bf16 v[64:67], v[16:19], a[64:67], v[64:67]
ds_read_b64_tr_b16 v[32:33], v3 offset:38016
v_mfma_f32_16x16x32_bf16 v[68:71], v[16:19], a[68:71], v[68:71]
v_mfma_f32_16x16x32_bf16 v[72:75], v[16:19], a[72:75], v[72:75]
ds_read_b64_tr_b16 v[34:35], v3 offset:38272
s_add_u32 m0, s43, 14336
s_add_u32 s47, s42, 33280
buffer_load_dwordx4 v8, s[20:23], s47 offen lds
v_mfma_f32_16x16x32_bf16 v[76:79], v[16:19], a[76:79], v[76:79]
s_waitcnt lgkmcnt(6)
v_mfma_f32_16x16x32_bf16 v[80:83], v[20:23], a[64:67], v[80:83]
ds_read_b64_tr_b16 v[36:37], v3 offset:38032
v_mfma_f32_16x16x32_bf16 v[84:87], v[20:23], a[68:71], v[84:87]
s_add_u32 m0, s43, 15360
s_add_u32 s47, s42, 49664
buffer_load_dwordx4 v9, s[20:23], s47 offen lds
v_mfma_f32_16x16x32_bf16 v[88:91], v[20:23], a[72:75], v[88:91]
ds_read_b64_tr_b16 v[38:39], v3 offset:38288
v_mfma_f32_16x16x32_bf16 v[92:95], v[20:23], a[76:79], v[92:95]
s_waitcnt lgkmcnt(6)
v_mfma_f32_16x16x32_bf16 v[96:99], v[24:27], a[64:67], v[96:99]
ds_read_b64_tr_b16 v[40:41], v3 offset:38080
s_add_u32 m0, s43, 16384
s_add_u32 s47, s42, 41472
buffer_load_dwordx4 v8, s[20:23], s47 offen lds
v_mfma_f32_16x16x32_bf16 v[100:103], v[24:27], a[68:71], v[100:103]
v_mfma_f32_16x16x32_bf16 v[104:107], v[24:27], a[72:75], v[104:107]
ds_read_b64_tr_b16 v[42:43], v3 offset:38336
v_mfma_f32_16x16x32_bf16 v[108:111], v[24:27], a[76:79], v[108:111]
s_add_u32 m0, s43, 17408
s_add_u32 s47, s42, 57856
buffer_load_dwordx4 v9, s[20:23], s47 offen lds
s_waitcnt lgkmcnt(6)
v_mfma_f32_16x16x32_bf16 v[112:115], v[28:31], a[64:67], v[112:115]
ds_read_b64_tr_b16 v[44:45], v3 offset:38096
v_mfma_f32_16x16x32_bf16 v[116:119], v[28:31], a[68:71], v[116:119]
v_mfma_f32_16x16x32_bf16 v[120:123], v[28:31], a[72:75], v[120:123]
ds_read_b64_tr_b16 v[46:47], v3 offset:38352
v_mfma_f32_16x16x32_bf16 v[124:127], v[28:31], a[76:79], v[124:127]
s_waitcnt lgkmcnt(6)
v_mfma_f32_16x16x32_bf16 v[128:131], v[32:35], a[64:67], v[128:131]
ds_read_b64_tr_b16 v[16:17], v3 offset:42240
v_mfma_f32_16x16x32_bf16 v[132:135], v[32:35], a[68:71], v[132:135]
v_mfma_f32_16x16x32_bf16 v[136:139], v[32:35], a[72:75], v[136:139]
ds_read_b64_tr_b16 v[18:19], v3 offset:42496
s_add_u32 m0, s41, 0
s_add_u32 s47, s40, 2097152
buffer_load_dwordx4 v2, s[24:27], s47 offen nt lds
v_mfma_f32_16x16x32_bf16 v[140:143], v[32:35], a[76:79], v[140:143]
s_waitcnt lgkmcnt(6)
v_mfma_f32_16x16x32_bf16 v[144:147], v[36:39], a[64:67], v[144:147]
ds_read_b64_tr_b16 v[20:21], v3 offset:42256
v_mfma_f32_16x16x32_bf16 v[148:151], v[36:39], a[68:71], v[148:151]
s_add_u32 m0, s41, 4224
s_add_u32 s47, s40, 2097280
buffer_load_dwordx4 v2, s[24:27], s47 offen nt lds
v_mfma_f32_16x16x32_bf16 v[152:155], v[36:39], a[72:75], v[152:155]
ds_read_b64_tr_b16 v[22:23], v3 offset:42512
v_mfma_f32_16x16x32_bf16 v[156:159], v[36:39], a[76:79], v[156:159]
s_waitcnt lgkmcnt(6)
v_mfma_f32_16x16x32_bf16 v[160:163], v[40:43], a[64:67], v[160:163]
ds_read_b64_tr_b16 v[24:25], v3 offset:42304
s_add_u32 m0, s41, 8448
s_add_u32 s47, s40, 2359296
buffer_load_dwordx4 v2, s[24:27], s47 offen nt lds
v_mfma_f32_16x16x32_bf16 v[164:167], v[40:43], a[68:71], v[164:167]
v_mfma_f32_16x16x32_bf16 v[168:171], v[40:43], a[72:75], v[168:171]
ds_read_b64_tr_b16 v[26:27], v3 offset:42560
v_mfma_f32_16x16x32_bf16 v[172:175], v[40:43], a[76:79], v[172:175]
s_add_u32 m0, s41, 12672
s_add_u32 s47, s40, 2359424
buffer_load_dwordx4 v2, s[24:27], s47 offen nt lds
s_waitcnt lgkmcnt(6)
v_mfma_f32_16x16x32_bf16 v[176:179], v[44:47], a[64:67], v[176:179]
ds_read_b64_tr_b16 v[28:29], v3 offset:42320
v_mfma_f32_16x16x32_bf16 v[180:183], v[44:47], a[68:71], v[180:183]
v_mfma_f32_16x16x32_bf16 v[184:187], v[44:47], a[72:75], v[184:187]
ds_read_b64_tr_b16 v[30:31], v3 offset:42576
v_mfma_f32_16x16x32_bf16 v[188:191], v[44:47], a[76:79], v[188:191]
s_waitcnt lgkmcnt(6)
v_mfma_f32_16x16x32_bf16 v[64:67], v[16:19], a[80:83], v[64:67]
ds_read_b64_tr_b16 v[32:33], v3 offset:46464
v_mfma_f32_16x16x32_bf16 v[68:71], v[16:19], a[84:87], v[68:71]
s_waitcnt vmcnt(22)
ds_read_b128 a[96:99], v10 offset:2048
v_mfma_f32_16x16x32_bf16 v[72:75], v[16:19], a[88:91], v[72:75]
ds_read_b64_tr_b16 v[34:35], v3 offset:46720
s_add_u32 m0, s43, 18432
s_add_u32 s47, s42, 640
buffer_load_dwordx4 v8, s[20:23], s47 offen lds
v_mfma_f32_16x16x32_bf16 v[76:79], v[16:19], a[92:95], v[76:79]
ds_read_b128 a[112:115], v11 offset:2048
s_waitcnt lgkmcnt(8)
v_mfma_f32_16x16x32_bf16 v[80:83], v[20:23], a[80:83], v[80:83]
ds_read_b64_tr_b16 v[36:37], v3 offset:46480
v_mfma_f32_16x16x32_bf16 v[84:87], v[20:23], a[84:87], v[84:87]
s_add_u32 m0, s43, 19456
s_add_u32 s47, s42, 17024
buffer_load_dwordx4 v9, s[20:23], s47 offen lds
s_waitcnt vmcnt(22)
ds_read_b128 a[100:103], v10 offset:4096
v_mfma_f32_16x16x32_bf16 v[88:91], v[20:23], a[88:91], v[88:91]
ds_read_b64_tr_b16 v[38:39], v3 offset:46736
v_mfma_f32_16x16x32_bf16 v[92:95], v[20:23], a[92:95], v[92:95]
ds_read_b128 a[116:119], v11 offset:4096
s_waitcnt lgkmcnt(10)
v_mfma_f32_16x16x32_bf16 v[96:99], v[24:27], a[80:83], v[96:99]
ds_read_b64_tr_b16 v[40:41], v3 offset:46528
s_add_u32 m0, s43, 20480
s_add_u32 s47, s42, 8832
buffer_load_dwordx4 v8, s[20:23], s47 offen lds
v_mfma_f32_16x16x32_bf16 v[100:103], v[24:27], a[84:87], v[100:103]
s_waitcnt vmcnt(21)
ds_read_b128 a[104:107], v10 offset:6144
v_mfma_f32_16x16x32_bf16 v[104:107], v[24:27], a[88:91], v[104:107]
ds_read_b64_tr_b16 v[42:43], v3 offset:46784
v_mfma_f32_16x16x32_bf16 v[108:111], v[24:27], a[92:95], v[108:111]
s_add_u32 m0, s43, 21504
s_add_u32 s47, s42, 25216
buffer_load_dwordx4 v9, s[20:23], s47 offen lds
ds_read_b128 a[120:123], v11 offset:6144
s_waitcnt lgkmcnt(12)
v_mfma_f32_16x16x32_bf16 v[112:115], v[28:31], a[80:83], v[112:115]
ds_read_b64_tr_b16 v[44:45], v3 offset:46544
v_mfma_f32_16x16x32_bf16 v[116:119], v[28:31], a[84:87], v[116:119]
s_waitcnt vmcnt(20)
ds_read_b128 a[108:111], v10 offset:8192
v_mfma_f32_16x16x32_bf16 v[120:123], v[28:31], a[88:91], v[120:123]
ds_read_b64_tr_b16 v[46:47], v3 offset:46800
v_mfma_f32_16x16x32_bf16 v[124:127], v[28:31], a[92:95], v[124:127]
ds_read_b128 a[124:127], v11 offset:8192
s_waitcnt lgkmcnt(13)
v_mfma_f32_16x16x32_bf16 v[128:131], v[32:35], a[80:83], v[128:131]
s_waitcnt vmcnt(16)
s_barrier
v_mfma_f32_16x16x32_bf16 v[132:135], v[32:35], a[84:87], v[132:135]
ds_read_b64_tr_b16 v[16:17], v3 offset:50688
v_mfma_f32_16x16x32_bf16 v[136:139], v[32:35], a[88:91], v[136:139]
s_add_u32 m0, s43, 0
s_add_u32 s47, s42, 33408
buffer_load_dwordx4 v8, s[20:23], s47 offen lds
v_mfma_f32_16x16x32_bf16 v[140:143], v[32:35], a[92:95], v[140:143]
ds_read_b64_tr_b16 v[18:19], v3 offset:50944
s_waitcnt lgkmcnt(11)
v_mfma_f32_16x16x32_bf16 v[144:147], v[36:39], a[80:83], v[144:147]
v_mfma_f32_16x16x32_bf16 v[148:151], v[36:39], a[84:87], v[148:151]
ds_read_b64_tr_b16 v[20:21], v3 offset:50704
s_add_u32 m0, s43, 1024
s_add_u32 s47, s42, 49792
buffer_load_dwordx4 v9, s[20:23], s47 offen lds
v_mfma_f32_16x16x32_bf16 v[152:155], v[36:39], a[88:91], v[152:155]
v_mfma_f32_16x16x32_bf16 v[156:159], v[36:39], a[92:95], v[156:159]
ds_read_b64_tr_b16 v[22:23], v3 offset:50960
s_waitcnt lgkmcnt(9)
v_mfma_f32_16x16x32_bf16 v[160:163], v[40:43], a[80:83], v[160:163]
s_add_u32 m0, s43, 2048
s_add_u32 s47, s42, 41600
buffer_load_dwordx4 v8, s[20:23], s47 offen lds
v_mfma_f32_16x16x32_bf16 v[164:167], v[40:43], a[84:87], v[164:167]
ds_read_b64_tr_b16 v[24:25], v3 offset:50752
v_mfma_f32_16x16x32_bf16 v[168:171], v[40:43], a[88:91], v[168:171]
v_mfma_f32_16x16x32_bf16 v[172:175], v[40:43], a[92:95], v[172:175]
ds_read_b64_tr_b16 v[26:27], v3 offset:51008
s_add_u32 m0, s43, 3072
s_add_u32 s47, s42, 57984
buffer_load_dwordx4 v9, s[20:23], s47 offen lds
s_waitcnt lgkmcnt(7)
v_mfma_f32_16x16x32_bf16 v[176:179], v[44:47], a[80:83], v[176:179]
v_mfma_f32_16x16x32_bf16 v[180:183], v[44:47], a[84:87], v[180:183]
ds_read_b64_tr_b16 v[28:29], v3 offset:50768
v_mfma_f32_16x16x32_bf16 v[184:187], v[44:47], a[88:91], v[184:187]
v_mfma_f32_16x16x32_bf16 v[188:191], v[44:47], a[92:95], v[188:191]
ds_read_b64_tr_b16 v[30:31], v3 offset:51024
s_waitcnt lgkmcnt(6)
v_mfma_f32_16x16x32_bf16 v[64:67], v[16:19], a[96:99], v[64:67]
ds_read_b64_tr_b16 v[32:33], v3 offset:54912
v_mfma_f32_16x16x32_bf16 v[68:71], v[16:19], a[100:103], v[68:71]
v_mfma_f32_16x16x32_bf16 v[72:75], v[16:19], a[104:107], v[72:75]
ds_read_b64_tr_b16 v[34:35], v3 offset:55168
s_add_u32 m0, s41, 16896
s_add_u32 s47, s40, 2621440
buffer_load_dwordx4 v2, s[24:27], s47 offen nt lds
v_mfma_f32_16x16x32_bf16 v[76:79], v[16:19], a[108:111], v[76:79]
s_waitcnt lgkmcnt(6)
v_mfma_f32_16x16x32_bf16 v[80:83], v[20:23], a[96:99], v[80:83]
ds_read_b64_tr_b16 v[36:37], v3 offset:54928
v_mfma_f32_16x16x32_bf16 v[84:87], v[20:23], a[100:103], v[84:87]
s_add_u32 m0, s41, 21120
s_add_u32 s47, s40, 2621568
buffer_load_dwordx4 v2, s[24:27], s47 offen nt lds
v_mfma_f32_16x16x32_bf16 v[88:91], v[20:23], a[104:107], v[88:91]
ds_read_b64_tr_b16 v[38:39], v3 offset:55184
v_mfma_f32_16x16x32_bf16 v[92:95], v[20:23], a[108:111], v[92:95]
s_waitcnt lgkmcnt(6)
v_mfma_f32_16x16x32_bf16 v[96:99], v[24:27], a[96:99], v[96:99]
ds_read_b64_tr_b16 v[40:41], v3 offset:54976
s_add_u32 m0, s41, 25344
s_add_u32 s47, s40, 2883584
buffer_load_dwordx4 v2, s[24:27], s47 offen nt lds
v_mfma_f32_16x16x32_bf16 v[100:103], v[24:27], a[100:103], v[100:103]
v_mfma_f32_16x16x32_bf16 v[104:107], v[24:27], a[104:107], v[104:107]
ds_read_b64_tr_b16 v[42:43], v3 offset:55232
v_mfma_f32_16x16x32_bf16 v[108:111], v[24:27], a[108:111], v[108:111]
s_add_u32 m0, s41, 29568
s_add_u32 s47, s40, 2883712
buffer_load_dwordx4 v2, s[24:27], s47 offen nt lds
s_waitcnt lgkmcnt(6)
v_mfma_f32_16x16x32_bf16 v[112:115], v[28:31], a[96:99], v[112:115]
ds_read_b64_tr_b16 v[44:45], v3 offset:54992
v_mfma_f32_16x16x32_bf16 v[116:119], v[28:31], a[100:103], v[116:119]
v_mfma_f32_16x16x32_bf16 v[120:123], v[28:31], a[104:107], v[120:123]
ds_read_b64_tr_b16 v[46:47], v3 offset:55248
v_mfma_f32_16x16x32_bf16 v[124:127], v[28:31], a[108:111], v[124:127]
s_waitcnt lgkmcnt(6)
v_mfma_f32_16x16x32_bf16 v[128:131], v[32:35], a[96:99], v[128:131]
ds_read_b64_tr_b16 v[16:17], v3 offset:59136
v_mfma_f32_16x16x32_bf16 v[132:135], v[32:35], a[100:103], v[132:135]
v_mfma_f32_16x16x32_bf16 v[136:139], v[32:35], a[104:107], v[136:139]
ds_read_b64_tr_b16 v[18:19], v3 offset:59392
s_add_u32 m0, s43, 4096
s_add_u32 s47, s42, 768
buffer_load_dwordx4 v8, s[20:23], s47 offen lds
v_mfma_f32_16x16x32_bf16 v[140:143], v[32:35], a[108:111], v[140:143]
s_waitcnt lgkmcnt(6)
v_mfma_f32_16x16x32_bf16 v[144:147], v[36:39], a[96:99], v[144:147]
ds_read_b64_tr_b16 v[20:21], v3 offset:59152
v_mfma_f32_16x16x32_bf16 v[148:151], v[36:39], a[100:103], v[148:151]
s_add_u32 m0, s43, 5120
s_add_u32 s47, s42, 17152
buffer_load_dwordx4 v9, s[20:23], s47 offen lds
v_mfma_f32_16x16x32_bf16 v[152:155], v[36:39], a[104:107], v[152:155]
ds_read_b64_tr_b16 v[22:23], v3 offset:59408
v_mfma_f32_16x16x32_bf16 v[156:159], v[36:39], a[108:111], v[156:159]
s_waitcnt lgkmcnt(6)
v_mfma_f32_16x16x32_bf16 v[160:163], v[40:43], a[96:99], v[160:163]
ds_read_b64_tr_b16 v[24:25], v3 offset:59200
s_add_u32 m0, s43, 6144
s_add_u32 s47, s42, 8960
buffer_load_dwordx4 v8, s[20:23], s47 offen lds
v_mfma_f32_16x16x32_bf16 v[164:167], v[40:43], a[100:103], v[164:167]
v_mfma_f32_16x16x32_bf16 v[168:171], v[40:43], a[104:107], v[168:171]
ds_read_b64_tr_b16 v[26:27], v3 offset:59456
v_mfma_f32_16x16x32_bf16 v[172:175], v[40:43], a[108:111], v[172:175]
s_add_u32 m0, s43, 7168
s_add_u32 s47, s42, 25344
buffer_load_dwordx4 v9, s[20:23], s47 offen lds
s_waitcnt lgkmcnt(6)
v_mfma_f32_16x16x32_bf16 v[176:179], v[44:47], a[96:99], v[176:179]
ds_read_b64_tr_b16 v[28:29], v3 offset:59216
v_mfma_f32_16x16x32_bf16 v[180:183], v[44:47], a[100:103], v[180:183]
v_mfma_f32_16x16x32_bf16 v[184:187], v[44:47], a[104:107], v[184:187]
ds_read_b64_tr_b16 v[30:31], v3 offset:59472
v_mfma_f32_16x16x32_bf16 v[188:191], v[44:47], a[108:111], v[188:191]
s_waitcnt lgkmcnt(6)
v_mfma_f32_16x16x32_bf16 v[64:67], v[16:19], a[112:115], v[64:67]
ds_read_b64_tr_b16 v[32:33], v3 offset:63360
v_mfma_f32_16x16x32_bf16 v[68:71], v[16:19], a[116:119], v[68:71]
s_waitcnt vmcnt(26)
ds_read_b128 a[128:131], v10 offset:10240
v_mfma_f32_16x16x32_bf16 v[72:75], v[16:19], a[120:123], v[72:75]
ds_read_b64_tr_b16 v[34:35], v3 offset:63616
s_add_u32 m0, s43, 8192
s_add_u32 s47, s42, 33536
buffer_load_dwordx4 v8, s[20:23], s47 offen lds
v_mfma_f32_16x16x32_bf16 v[76:79], v[16:19], a[124:127], v[76:79]
ds_read_b128 a[144:147], v11 offset:10240
s_waitcnt lgkmcnt(8)
v_mfma_f32_16x16x32_bf16 v[80:83], v[20:23], a[112:115], v[80:83]
ds_read_b64_tr_b16 v[36:37], v3 offset:63376
v_mfma_f32_16x16x32_bf16 v[84:87], v[20:23], a[116:119], v[84:87]
s_add_u32 m0, s43, 9216
s_add_u32 s47, s42, 49920
buffer_load_dwordx4 v9, s[20:23], s47 offen lds
s_waitcnt vmcnt(26)
ds_read_b128 a[132:135], v10 offset:12288
v_mfma_f32_16x16x32_bf16 v[88:91], v[20:23], a[120:123], v[88:91]
ds_read_b64_tr_b16 v[38:39], v3 offset:63632
v_mfma_f32_16x16x32_bf16 v[92:95], v[20:23], a[124:127], v[92:95]
ds_read_b128 a[148:151], v11 offset:12288
s_waitcnt lgkmcnt(10)
v_mfma_f32_16x16x32_bf16 v[96:99], v[24:27], a[112:115], v[96:99]
ds_read_b64_tr_b16 v[40:41], v3 offset:63424
v_mfma_f32_16x16x32_bf16 v[100:103], v[24:27], a[116:119], v[100:103]
s_waitcnt vmcnt(24)
ds_read_b128 a[136:139], v10 offset:14336
v_mfma_f32_16x16x32_bf16 v[104:107], v[24:27], a[120:123], v[104:107]
ds_read_b64_tr_b16 v[42:43], v3 offset:63680
v_mfma_f32_16x16x32_bf16 v[108:111], v[24:27], a[124:127], v[108:111]
ds_read_b128 a[152:155], v11 offset:14336
s_waitcnt lgkmcnt(12)
v_mfma_f32_16x16x32_bf16 v[112:115], v[28:31], a[112:115], v[112:115]
ds_read_b64_tr_b16 v[44:45], v3 offset:63440
v_mfma_f32_16x16x32_bf16 v[116:119], v[28:31], a[116:119], v[116:119]
s_waitcnt vmcnt(22)
ds_read_b128 a[140:143], v10 offset:16384
v_mfma_f32_16x16x32_bf16 v[120:123], v[28:31], a[120:123], v[120:123]
ds_read_b64_tr_b16 v[46:47], v3 offset:63696
v_mfma_f32_16x16x32_bf16 v[124:127], v[28:31], a[124:127], v[124:127]
ds_read_b128 a[156:159], v11 offset:16384
s_waitcnt lgkmcnt(13)
v_mfma_f32_16x16x32_bf16 v[128:131], v[32:35], a[112:115], v[128:131]
s_waitcnt vmcnt(18)
s_barrier
v_mfma_f32_16x16x32_bf16 v[132:135], v[32:35], a[116:119], v[132:135]
ds_read_b64_tr_b16 v[16:17], v3 offset:0
v_mfma_f32_16x16x32_bf16 v[136:139], v[32:35], a[120:123], v[136:139]
s_add_u32 m0, s43, 10240
s_add_u32 s47, s42, 41728
buffer_load_dwordx4 v8, s[20:23], s47 offen lds
v_mfma_f32_16x16x32_bf16 v[140:143], v[32:35], a[124:127], v[140:143]
ds_read_b64_tr_b16 v[18:19], v3 offset:256
s_waitcnt lgkmcnt(11)
v_mfma_f32_16x16x32_bf16 v[144:147], v[36:39], a[112:115], v[144:147]
v_mfma_f32_16x16x32_bf16 v[148:151], v[36:39], a[116:119], v[148:151]
ds_read_b64_tr_b16 v[20:21], v3 offset:16
s_add_u32 m0, s43, 11264
s_add_u32 s47, s42, 58112
buffer_load_dwordx4 v9, s[20:23], s47 offen lds
v_mfma_f32_16x16x32_bf16 v[152:155], v[36:39], a[120:123], v[152:155]
v_mfma_f32_16x16x32_bf16 v[156:159], v[36:39], a[124:127], v[156:159]
ds_read_b64_tr_b16 v[22:23], v3 offset:272
s_waitcnt lgkmcnt(9)
v_mfma_f32_16x16x32_bf16 v[160:163], v[40:43], a[112:115], v[160:163]
s_add_u32 m0, s41, 33792
s_add_u32 s47, s40, 3145728
buffer_load_dwordx4 v2, s[24:27], s47 offen nt lds
v_mfma_f32_16x16x32_bf16 v[164:167], v[40:43], a[116:119], v[164:167]
ds_read_b64_tr_b16 v[24:25], v3 offset:64
v_mfma_f32_16x16x32_bf16 v[168:171], v[40:43], a[120:123], v[168:171]
v_mfma_f32_16x16x32_bf16 v[172:175], v[40:43], a[124:127], v[172:175]
ds_read_b64_tr_b16 v[26:27], v3 offset:320
s_add_u32 m0, s41, 38016
s_add_u32 s47, s40, 3145856
buffer_load_dwordx4 v2, s[24:27], s47 offen nt lds
s_waitcnt lgkmcnt(7)
v_mfma_f32_16x16x32_bf16 v[176:179], v[44:47], a[112:115], v[176:179]
v_mfma_f32_16x16x32_bf16 v[180:183], v[44:47], a[116:119], v[180:183]
ds_read_b64_tr_b16 v[28:29], v3 offset:80
v_mfma_f32_16x16x32_bf16 v[184:187], v[44:47], a[120:123], v[184:187]
v_mfma_f32_16x16x32_bf16 v[188:191], v[44:47], a[124:127], v[188:191]
ds_read_b64_tr_b16 v[30:31], v3 offset:336
s_waitcnt lgkmcnt(6)
v_mfma_f32_16x16x32_bf16 v[64:67], v[16:19], a[128:131], v[64:67]
ds_read_b64_tr_b16 v[32:33], v3 offset:4224
v_mfma_f32_16x16x32_bf16 v[68:71], v[16:19], a[132:135], v[68:71]
v_mfma_f32_16x16x32_bf16 v[72:75], v[16:19], a[136:139], v[72:75]
ds_read_b64_tr_b16 v[34:35], v3 offset:4480
s_add_u32 m0, s41, 42240
s_add_u32 s47, s40, 3407872
buffer_load_dwordx4 v2, s[24:27], s47 offen nt lds
v_mfma_f32_16x16x32_bf16 v[76:79], v[16:19], a[140:143], v[76:79]
s_waitcnt lgkmcnt(6)
v_mfma_f32_16x16x32_bf16 v[80:83], v[20:23], a[128:131], v[80:83]
ds_read_b64_tr_b16 v[36:37], v3 offset:4240
v_mfma_f32_16x16x32_bf16 v[84:87], v[20:23], a[132:135], v[84:87]
s_add_u32 m0, s41, 46464
s_add_u32 s47, s40, 3408000
buffer_load_dwordx4 v2, s[24:27], s47 offen nt lds
v_mfma_f32_16x16x32_bf16 v[88:91], v[20:23], a[136:139], v[88:91]
ds_read_b64_tr_b16 v[38:39], v3 offset:4496
v_mfma_f32_16x16x32_bf16 v[92:95], v[20:23], a[140:143], v[92:95]
s_waitcnt lgkmcnt(6)
v_mfma_f32_16x16x32_bf16 v[96:99], v[24:27], a[128:131], v[96:99]
ds_read_b64_tr_b16 v[40:41], v3 offset:4288
v_mfma_f32_16x16x32_bf16 v[100:103], v[24:27], a[132:135], v[100:103]
v_mfma_f32_16x16x32_bf16 v[104:107], v[24:27], a[136:139], v[104:107]
ds_read_b64_tr_b16 v[42:43], v3 offset:4544
v_mfma_f32_16x16x32_bf16 v[108:111], v[24:27], a[140:143], v[108:111]
s_waitcnt lgkmcnt(6)
v_mfma_f32_16x16x32_bf16 v[112:115], v[28:31], a[128:131], v[112:115]
ds_read_b64_tr_b16 v[44:45], v3 offset:4304
v_mfma_f32_16x16x32_bf16 v[116:119], v[28:31], a[132:135], v[116:119]
v_mfma_f32_16x16x32_bf16 v[120:123], v[28:31], a[136:139], v[120:123]
ds_read_b64_tr_b16 v[46:47], v3 offset:4560
v_mfma_f32_16x16x32_bf16 v[124:127], v[28:31], a[140:143], v[124:127]
s_waitcnt lgkmcnt(6)
v_mfma_f32_16x16x32_bf16 v[128:131], v[32:35], a[128:131], v[128:131]
ds_read_b64_tr_b16 v[16:17], v3 offset:8448
v_mfma_f32_16x16x32_bf16 v[132:135], v[32:35], a[132:135], v[132:135]
v_mfma_f32_16x16x32_bf16 v[136:139], v[32:35], a[136:139], v[136:139]
ds_read_b64_tr_b16 v[18:19], v3 offset:8704
s_add_u32 m0, s43, 12288
s_add_u32 s47, s42, 896
buffer_load_dwordx4 v8, s[20:23], s47 offen lds
v_mfma_f32_16x16x32_bf16 v[140:143], v[32:35], a[140:143], v[140:143]
s_waitcnt lgkmcnt(6)
v_mfma_f32_16x16x32_bf16 v[144:147], v[36:39], a[128:131], v[144:147]
ds_read_b64_tr_b16 v[20:21], v3 offset:8464
v_mfma_f32_16x16x32_bf16 v[148:151], v[36:39], a[132:135], v[148:151]
s_add_u32 m0, s43, 13312
s_add_u32 s47, s42, 17280
buffer_load_dwordx4 v9, s[20:23], s47 offen lds
v_mfma_f32_16x16x32_bf16 v[152:155], v[36:39], a[136:139], v[152:155]
ds_read_b64_tr_b16 v[22:23], v3 offset:8720
v_mfma_f32_16x16x32_bf16 v[156:159], v[36:39], a[140:143], v[156:159]
s_waitcnt lgkmcnt(6)
v_mfma_f32_16x16x32_bf16 v[160:163], v[40:43], a[128:131], v[160:163]
ds_read_b64_tr_b16 v[24:25], v3 offset:8512
s_add_u32 m0, s43, 14336
s_add_u32 s47, s42, 9088
buffer_load_dwordx4 v8, s[20:23], s47 offen lds
v_mfma_f32_16x16x32_bf16 v[164:167], v[40:43], a[132:135], v[164:167]
v_mfma_f32_16x16x32_bf16 v[168:171], v[40:43], a[136:139], v[168:171]
ds_read_b64_tr_b16 v[26:27], v3 offset:8768
v_mfma_f32_16x16x32_bf16 v[172:175], v[40:43], a[140:143], v[172:175]
s_add_u32 m0, s43, 15360
s_add_u32 s47, s42, 25472
buffer_load_dwordx4 v9, s[20:23], s47 offen lds
s_waitcnt lgkmcnt(6)
v_mfma_f32_16x16x32_bf16 v[176:179], v[44:47], a[128:131], v[176:179]
ds_read_b64_tr_b16 v[28:29], v3 offset:8528
v_mfma_f32_16x16x32_bf16 v[180:183], v[44:47], a[132:135], v[180:183]
v_mfma_f32_16x16x32_bf16 v[184:187], v[44:47], a[136:139], v[184:187]
ds_read_b64_tr_b16 v[30:31], v3 offset:8784
v_mfma_f32_16x16x32_bf16 v[188:191], v[44:47], a[140:143], v[188:191]
s_waitcnt lgkmcnt(6)
v_mfma_f32_16x16x32_bf16 v[64:67], v[16:19], a[144:147], v[64:67]
ds_read_b64_tr_b16 v[32:33], v3 offset:12672
v_mfma_f32_16x16x32_bf16 v[68:71], v[16:19], a[148:151], v[68:71]
s_waitcnt vmcnt(26)
ds_read_b128 a[160:163], v10 offset:18432
v_mfma_f32_16x16x32_bf16 v[72:75], v[16:19], a[152:155], v[72:75]
ds_read_b64_tr_b16 v[34:35], v3 offset:12928
s_add_u32 m0, s43, 16384
s_add_u32 s47, s42, 33664
buffer_load_dwordx4 v8, s[20:23], s47 offen lds
v_mfma_f32_16x16x32_bf16 v[76:79], v[16:19], a[156:159], v[76:79]
ds_read_b128 a[176:179], v11 offset:18432
s_waitcnt lgkmcnt(8)
v_mfma_f32_16x16x32_bf16 v[80:83], v[20:23], a[144:147], v[80:83]
ds_read_b64_tr_b16 v[36:37], v3 offset:12688
v_mfma_f32_16x16x32_bf16 v[84:87], v[20:23], a[148:151], v[84:87]
s_add_u32 m0, s43, 17408
s_add_u32 s47, s42, 50048
buffer_load_dwordx4 v9, s[20:23], s47 offen lds
s_waitcnt vmcnt(26)
ds_read_b128 a[164:167], v10 offset:20480
v_mfma_f32_16x16x32_bf16 v[88:91], v[20:23], a[152:155], v[88:91]
ds_read_b64_tr_b16 v[38:39], v3 offset:12944
v_mfma_f32_16x16x32_bf16 v[92:95], v[20:23], a[156:159], v[92:95]
ds_read_b128 a[180:183], v11 offset:20480
s_waitcnt lgkmcnt(10)
v_mfma_f32_16x16x32_bf16 v[96:99], v[24:27], a[144:147], v[96:99]
ds_read_b64_tr_b16 v[40:41], v3 offset:12736
v_mfma_f32_16x16x32_bf16 v[100:103], v[24:27], a[148:151], v[100:103]
s_waitcnt vmcnt(24)
ds_read_b128 a[168:171], v10 offset:0
v_mfma_f32_16x16x32_bf16 v[104:107], v[24:27], a[152:155], v[104:107]
ds_read_b64_tr_b16 v[42:43], v3 offset:12992
v_mfma_f32_16x16x32_bf16 v[108:111], v[24:27], a[156:159], v[108:111]
ds_read_b128 a[184:187], v11 offset:0
s_waitcnt lgkmcnt(12)
v_mfma_f32_16x16x32_bf16 v[112:115], v[28:31], a[144:147], v[112:115]
ds_read_b64_tr_b16 v[44:45], v3 offset:12752
v_mfma_f32_16x16x32_bf16 v[116:119], v[28:31], a[148:151], v[116:119]
s_waitcnt vmcnt(22)
ds_read_b128 a[172:175], v10 offset:2048
v_mfma_f32_16x16x32_bf16 v[120:123], v[28:31], a[152:155], v[120:123]
ds_read_b64_tr_b16 v[46:47], v3 offset:13008
v_mfma_f32_16x16x32_bf16 v[124:127], v[28:31], a[156:159], v[124:127]
ds_read_b128 a[188:191], v11 offset:2048
s_waitcnt lgkmcnt(13)
v_mfma_f32_16x16x32_bf16 v[128:131], v[32:35], a[144:147], v[128:131]
s_waitcnt vmcnt(18)
s_barrier
v_mfma_f32_16x16x32_bf16 v[132:135], v[32:35], a[148:151], v[132:135]
ds_read_b64_tr_b16 v[16:17], v3 offset:16896
v_mfma_f32_16x16x32_bf16 v[136:139], v[32:35], a[152:155], v[136:139]
s_add_u32 m0, s43, 18432
s_add_u32 s47, s42, 41856
buffer_load_dwordx4 v8, s[20:23], s47 offen lds
v_mfma_f32_16x16x32_bf16 v[140:143], v[32:35], a[156:159], v[140:143]
ds_read_b64_tr_b16 v[18:19], v3 offset:17152
s_waitcnt lgkmcnt(11)
v_mfma_f32_16x16x32_bf16 v[144:147], v[36:39], a[144:147], v[144:147]
v_mfma_f32_16x16x32_bf16 v[148:151], v[36:39], a[148:151], v[148:151]
ds_read_b64_tr_b16 v[20:21], v3 offset:16912
s_add_u32 m0, s43, 19456
s_add_u32 s47, s42, 58240
buffer_load_dwordx4 v9, s[20:23], s47 offen lds
v_mfma_f32_16x16x32_bf16 v[152:155], v[36:39], a[152:155], v[152:155]
v_mfma_f32_16x16x32_bf16 v[156:159], v[36:39], a[156:159], v[156:159]
ds_read_b64_tr_b16 v[22:23], v3 offset:17168
s_waitcnt lgkmcnt(9)
v_mfma_f32_16x16x32_bf16 v[160:163], v[40:43], a[144:147], v[160:163]
s_add_u32 m0, s41, 50688
s_add_u32 s47, s40, 3670016
buffer_load_dwordx4 v2, s[24:27], s47 offen nt lds
v_mfma_f32_16x16x32_bf16 v[164:167], v[40:43], a[148:151], v[164:167]
ds_read_b64_tr_b16 v[24:25], v3 offset:16960
v_mfma_f32_16x16x32_bf16 v[168:171], v[40:43], a[152:155], v[168:171]
v_mfma_f32_16x16x32_bf16 v[172:175], v[40:43], a[156:159], v[172:175]
ds_read_b64_tr_b16 v[26:27], v3 offset:17216
s_add_u32 m0, s41, 54912
s_add_u32 s47, s40, 3670144
buffer_load_dwordx4 v2, s[24:27], s47 offen nt lds
s_waitcnt lgkmcnt(7)
v_mfma_f32_16x16x32_bf16 v[176:179], v[44:47], a[144:147], v[176:179]
v_mfma_f32_16x16x32_bf16 v[180:183], v[44:47], a[148:151], v[180:183]
ds_read_b64_tr_b16 v[28:29], v3 offset:16976
v_mfma_f32_16x16x32_bf16 v[184:187], v[44:47], a[152:155], v[184:187]
v_mfma_f32_16x16x32_bf16 v[188:191], v[44:47], a[156:159], v[188:191]
ds_read_b64_tr_b16 v[30:31], v3 offset:17232
s_waitcnt lgkmcnt(6)
v_mfma_f32_16x16x32_bf16 v[64:67], v[16:19], a[160:163], v[64:67]
ds_read_b64_tr_b16 v[32:33], v3 offset:21120
v_mfma_f32_16x16x32_bf16 v[68:71], v[16:19], a[164:167], v[68:71]
v_mfma_f32_16x16x32_bf16 v[72:75], v[16:19], a[168:171], v[72:75]
ds_read_b64_tr_b16 v[34:35], v3 offset:21376
s_add_u32 m0, s41, 59136
s_add_u32 s47, s40, 3932160
buffer_load_dwordx4 v2, s[24:27], s47 offen nt lds
v_mfma_f32_16x16x32_bf16 v[76:79], v[16:19], a[172:175], v[76:79]
s_waitcnt lgkmcnt(6)
v_mfma_f32_16x16x32_bf16 v[80:83], v[20:23], a[160:163], v[80:83]
ds_read_b64_tr_b16 v[36:37], v3 offset:21136
v_mfma_f32_16x16x32_bf16 v[84:87], v[20:23], a[164:167], v[84:87]
s_add_u32 m0, s41, 63360
s_add_u32 s47, s40, 3932288
buffer_load_dwordx4 v2, s[24:27], s47 offen nt lds
v_mfma_f32_16x16x32_bf16 v[88:91], v[20:23], a[168:171], v[88:91]
ds_read_b64_tr_b16 v[38:39], v3 offset:21392
v_mfma_f32_16x16x32_bf16 v[92:95], v[20:23], a[172:175], v[92:95]
s_lshl_b32 s92, %8, 2
s_add_u32 s92, s92, s38
s_mul_i32 s93, s92, 1792
s_lshl_b32 s92, s92, 12
s_waitcnt lgkmcnt(6)
v_mfma_f32_16x16x32_bf16 v[96:99], v[24:27], a[160:163], v[96:99]
ds_read_b64_tr_b16 v[40:41], v3 offset:21184
v_mfma_f32_16x16x32_bf16 v[100:103], v[24:27], a[164:167], v[100:103]
buffer_load_dwordx4 v[48:51], v1, s[84:87], s92 offen offset:0
v_mfma_f32_16x16x32_bf16 v[104:107], v[24:27], a[168:171], v[104:107]
ds_read_b64_tr_b16 v[42:43], v3 offset:21440
v_mfma_f32_16x16x32_bf16 v[108:111], v[24:27], a[172:175], v[108:111]
s_waitcnt lgkmcnt(6)
v_mfma_f32_16x16x32_bf16 v[112:115], v[28:31], a[160:163], v[112:115]
ds_read_b64_tr_b16 v[44:45], v3 offset:21200
v_mfma_f32_16x16x32_bf16 v[116:119], v[28:31], a[164:167], v[116:119]
v_mfma_f32_16x16x32_bf16 v[120:123], v[28:31], a[168:171], v[120:123]
ds_read_b64_tr_b16 v[46:47], v3 offset:21456
v_mfma_f32_16x16x32_bf16 v[124:127], v[28:31], a[172:175], v[124:127]
s_waitcnt lgkmcnt(6)
v_mfma_f32_16x16x32_bf16 v[128:131], v[32:35], a[160:163], v[128:131]
ds_read_b64_tr_b16 v[16:17], v3 offset:25344
v_mfma_f32_16x16x32_bf16 v[132:135], v[32:35], a[164:167], v[132:135]
v_mfma_f32_16x16x32_bf16 v[136:139], v[32:35], a[168:171], v[136:139]
ds_read_b64_tr_b16 v[18:19], v3 offset:25600
v_mfma_f32_16x16x32_bf16 v[140:143], v[32:35], a[172:175], v[140:143]
s_waitcnt lgkmcnt(6)
v_mfma_f32_16x16x32_bf16 v[144:147], v[36:39], a[160:163], v[144:147]
ds_read_b64_tr_b16 v[20:21], v3 offset:25360
v_mfma_f32_16x16x32_bf16 v[148:151], v[36:39], a[164:167], v[148:151]
v_mfma_f32_16x16x32_bf16 v[152:155], v[36:39], a[168:171], v[152:155]
ds_read_b64_tr_b16 v[22:23], v3 offset:25616
v_mfma_f32_16x16x32_bf16 v[156:159], v[36:39], a[172:175], v[156:159]
s_waitcnt lgkmcnt(6)
v_mfma_f32_16x16x32_bf16 v[160:163], v[40:43], a[160:163], v[160:163]
ds_read_b64_tr_b16 v[24:25], v3 offset:25408
v_mfma_f32_16x16x32_bf16 v[164:167], v[40:43], a[164:167], v[164:167]
v_mfma_f32_16x16x32_bf16 v[168:171], v[40:43], a[168:171], v[168:171]
ds_read_b64_tr_b16 v[26:27], v3 offset:25664
v_mfma_f32_16x16x32_bf16 v[172:175], v[40:43], a[172:175], v[172:175]
s_waitcnt lgkmcnt(6)
v_mfma_f32_16x16x32_bf16 v[176:179], v[44:47], a[160:163], v[176:179]
ds_read_b64_tr_b16 v[28:29], v3 offset:25424
v_mfma_f32_16x16x32_bf16 v[180:183], v[44:47], a[164:167], v[180:183]
v_mfma_f32_16x16x32_bf16 v[184:187], v[44:47], a[168:171], v[184:187]
ds_read_b64_tr_b16 v[30:31], v3 offset:25680
v_mfma_f32_16x16x32_bf16 v[188:191], v[44:47], a[172:175], v[188:191]
s_waitcnt lgkmcnt(6)
v_mfma_f32_16x16x32_bf16 v[64:67], v[16:19], a[176:179], v[64:67]
ds_read_b64_tr_b16 v[32:33], v3 offset:29568
v_mfma_f32_16x16x32_bf16 v[68:71], v[16:19], a[180:183], v[68:71]
s_waitcnt vmcnt(23)
ds_read_b128 a[192:195], v10 offset:4096
v_mfma_f32_16x16x32_bf16 v[72:75], v[16:19], a[184:187], v[72:75]
ds_read_b64_tr_b16 v[34:35], v3 offset:29824
v_mfma_f32_16x16x32_bf16 v[76:79], v[16:19], a[188:191], v[76:79]
ds_read_b128 a[208:211], v11 offset:4096
s_waitcnt lgkmcnt(8)
v_mfma_f32_16x16x32_bf16 v[80:83], v[20:23], a[176:179], v[80:83]
ds_read_b64_tr_b16 v[36:37], v3 offset:29584
v_mfma_f32_16x16x32_bf16 v[84:87], v[20:23], a[180:183], v[84:87]
s_waitcnt vmcnt(21)
ds_read_b128 a[196:199], v10 offset:6144
v_mfma_f32_16x16x32_bf16 v[88:91], v[20:23], a[184:187], v[88:91]
ds_read_b64_tr_b16 v[38:39], v3 offset:29840
v_mfma_f32_16x16x32_bf16 v[92:95], v[20:23], a[188:191], v[92:95]
ds_read_b128 a[212:215], v11 offset:6144
s_waitcnt lgkmcnt(10)
v_mfma_f32_16x16x32_bf16 v[96:99], v[24:27], a[176:179], v[96:99]
ds_read_b64_tr_b16 v[40:41], v3 offset:29632
v_mfma_f32_16x16x32_bf16 v[100:103], v[24:27], a[180:183], v[100:103]
s_waitcnt vmcnt(19)
ds_read_b128 a[200:203], v10 offset:8192
buffer_load_dwordx4 v[52:55], v1, s[84:87], s92 offen offset:1024
v_mfma_f32_16x16x32_bf16 v[104:107], v[24:27], a[184:187], v[104:107]
ds_read_b64_tr_b16 v[42:43], v3 offset:29888
v_mfma_f32_16x16x32_bf16 v[108:111], v[24:27], a[188:191], v[108:111]
ds_read_b128 a[216:219], v11 offset:8192
s_waitcnt lgkmcnt(12)
v_mfma_f32_16x16x32_bf16 v[112:115], v[28:31], a[176:179], v[112:115]
ds_read_b64_tr_b16 v[44:45], v3 offset:29648
v_mfma_f32_16x16x32_bf16 v[116:119], v[28:31], a[180:183], v[116:119]
s_waitcnt vmcnt(18)
ds_read_b128 a[204:207], v10 offset:10240
v_mfma_f32_16x16x32_bf16 v[120:123], v[28:31], a[184:187], v[120:123]
ds_read_b64_tr_b16 v[46:47], v3 offset:29904
v_mfma_f32_16x16x32_bf16 v[124:127], v[28:31], a[188:191], v[124:127]
ds_read_b128 a[220:223], v11 offset:10240
s_waitcnt lgkmcnt(13)
v_mfma_f32_16x16x32_bf16 v[128:131], v[32:35], a[176:179], v[128:131]
s_waitcnt vmcnt(14)
s_barrier
v_mfma_f32_16x16x32_bf16 v[132:135], v[32:35], a[180:183], v[132:135]
ds_read_b64_tr_b16 v[16:17], v3 offset:33792
v_mfma_f32_16x16x32_bf16 v[136:139], v[32:35], a[184:187], v[136:139]
s_add_u32 m0, s41, 0
s_add_u32 s47, s40, 0
buffer_load_dwordx4 v2, s[28:31], s47 offen nt lds
v_mfma_f32_16x16x32_bf16 v[140:143], v[32:35], a[188:191], v[140:143]
ds_read_b64_tr_b16 v[18:19], v3 offset:34048
s_waitcnt lgkmcnt(11)
v_mfma_f32_16x16x32_bf16 v[144:147], v[36:39], a[176:179], v[144:147]
v_mfma_f32_16x16x32_bf16 v[148:151], v[36:39], a[180:183], v[148:151]
ds_read_b64_tr_b16 v[20:21], v3 offset:33808
s_add_u32 m0, s41, 4224
s_add_u32 s47, s40, 262144
buffer_load_dwordx4 v2, s[28:31], s47 offen nt lds
v_mfma_f32_16x16x32_bf16 v[152:155], v[36:39], a[184:187], v[152:155]
v_mfma_f32_16x16x32_bf16 v[156:159], v[36:39], a[188:191], v[156:159]
ds_read_b64_tr_b16 v[22:23], v3 offset:34064
s_waitcnt lgkmcnt(9)
v_mfma_f32_16x16x32_bf16 v[160:163], v[40:43], a[176:179], v[160:163]
s_add_u32 m0, s41, 8448
s_add_u32 s47, s40, 524288
buffer_load_dwordx4 v2, s[28:31], s47 offen nt lds
v_mfma_f32_16x16x32_bf16 v[164:167], v[40:43], a[180:183], v[164:167]
ds_read_b64_tr_b16 v[24:25], v3 offset:33856
v_mfma_f32_16x16x32_bf16 v[168:171], v[40:43], a[184:187], v[168:171]
v_mfma_f32_16x16x32_bf16 v[172:175], v[40:43], a[188:191], v[172:175]
ds_read_b64_tr_b16 v[26:27], v3 offset:34112
s_add_u32 m0, s41, 12672
s_add_u32 s47, s40, 786432
buffer_load_dwordx4 v2, s[28:31], s47 offen nt lds
s_waitcnt lgkmcnt(7)
v_mfma_f32_16x16x32_bf16 v[176:179], v[44:47], a[176:179], v[176:179]
v_mfma_f32_16x16x32_bf16 v[180:183], v[44:47], a[180:183], v[180:183]
ds_read_b64_tr_b16 v[28:29], v3 offset:33872
v_mfma_f32_16x16x32_bf16 v[184:187], v[44:47], a[184:187], v[184:187]
v_mfma_f32_16x16x32_bf16 v[188:191], v[44:47], a[188:191], v[188:191]
ds_read_b64_tr_b16 v[30:31], v3 offset:34128
s_waitcnt lgkmcnt(6)
v_mfma_f32_16x16x32_bf16 v[64:67], v[16:19], a[192:195], v[64:67]
ds_read_b64_tr_b16 v[32:33], v3 offset:38016
v_mfma_f32_16x16x32_bf16 v[68:71], v[16:19], a[196:199], v[68:71]
v_mfma_f32_16x16x32_bf16 v[72:75], v[16:19], a[200:203], v[72:75]
ds_read_b64_tr_b16 v[34:35], v3 offset:38272
v_mfma_f32_16x16x32_bf16 v[76:79], v[16:19], a[204:207], v[76:79]
s_waitcnt lgkmcnt(6)
v_mfma_f32_16x16x32_bf16 v[80:83], v[20:23], a[192:195], v[80:83]
ds_read_b64_tr_b16 v[36:37], v3 offset:38032
v_mfma_f32_16x16x32_bf16 v[84:87], v[20:23], a[196:199], v[84:87]
v_mfma_f32_16x16x32_bf16 v[88:91], v[20:23], a[200:203], v[88:91]
ds_read_b64_tr_b16 v[38:39], v3 offset:38288
v_mfma_f32_16x16x32_bf16 v[92:95], v[20:23], a[204:207], v[92:95]
s_waitcnt lgkmcnt(6)
v_mfma_f32_16x16x32_bf16 v[96:99], v[24:27], a[192:195], v[96:99]
ds_read_b64_tr_b16 v[40:41], v3 offset:38080
v_mfma_f32_16x16x32_bf16 v[100:103], v[24:27], a[196:199], v[100:103]
buffer_load_dwordx4 v[56:59], v1, s[84:87], s92 offen offset:2048
v_mfma_f32_16x16x32_bf16 v[104:107], v[24:27], a[200:203], v[104:107]
ds_read_b64_tr_b16 v[42:43], v3 offset:38336
v_mfma_f32_16x16x32_bf16 v[108:111], v[24:27], a[204:207], v[108:111]
s_waitcnt lgkmcnt(6)
v_mfma_f32_16x16x32_bf16 v[112:115], v[28:31], a[192:195], v[112:115]
ds_read_b64_tr_b16 v[44:45], v3 offset:38096
v_mfma_f32_16x16x32_bf16 v[116:119], v[28:31], a[196:199], v[116:119]
v_mfma_f32_16x16x32_bf16 v[120:123], v[28:31], a[200:203], v[120:123]
ds_read_b64_tr_b16 v[46:47], v3 offset:38352
v_mfma_f32_16x16x32_bf16 v[124:127], v[28:31], a[204:207], v[124:127]
s_waitcnt lgkmcnt(6)
v_mfma_f32_16x16x32_bf16 v[128:131], v[32:35], a[192:195], v[128:131]
ds_read_b64_tr_b16 v[16:17], v3 offset:42240
v_mfma_f32_16x16x32_bf16 v[132:135], v[32:35], a[196:199], v[132:135]
v_mfma_f32_16x16x32_bf16 v[136:139], v[32:35], a[200:203], v[136:139]
ds_read_b64_tr_b16 v[18:19], v3 offset:42496
v_mfma_f32_16x16x32_bf16 v[140:143], v[32:35], a[204:207], v[140:143]
s_waitcnt lgkmcnt(6)
v_mfma_f32_16x16x32_bf16 v[144:147], v[36:39], a[192:195], v[144:147]
ds_read_b64_tr_b16 v[20:21], v3 offset:42256
v_mfma_f32_16x16x32_bf16 v[148:151], v[36:39], a[196:199], v[148:151]
v_mfma_f32_16x16x32_bf16 v[152:155], v[36:39], a[200:203], v[152:155]
ds_read_b64_tr_b16 v[22:23], v3 offset:42512
v_mfma_f32_16x16x32_bf16 v[156:159], v[36:39], a[204:207], v[156:159]
s_waitcnt lgkmcnt(6)
v_mfma_f32_16x16x32_bf16 v[160:163], v[40:43], a[192:195], v[160:163]
ds_read_b64_tr_b16 v[24:25], v3 offset:42304
v_mfma_f32_16x16x32_bf16 v[164:167], v[40:43], a[196:199], v[164:167]
v_mfma_f32_16x16x32_bf16 v[168:171], v[40:43], a[200:203], v[168:171]
ds_read_b64_tr_b16 v[26:27], v3 offset:42560
v_mfma_f32_16x16x32_bf16 v[172:175], v[40:43], a[204:207], v[172:175]
s_waitcnt lgkmcnt(6)
v_mfma_f32_16x16x32_bf16 v[176:179], v[44:47], a[192:195], v[176:179]
ds_read_b64_tr_b16 v[28:29], v3 offset:42320
v_mfma_f32_16x16x32_bf16 v[180:183], v[44:47], a[196:199], v[180:183]
v_mfma_f32_16x16x32_bf16 v[184:187], v[44:47], a[200:203], v[184:187]
ds_read_b64_tr_b16 v[30:31], v3 offset:42576
v_mfma_f32_16x16x32_bf16 v[188:191], v[44:47], a[204:207], v[188:191]
s_waitcnt lgkmcnt(6)
v_mfma_f32_16x16x32_bf16 v[64:67], v[16:19], a[208:211], v[64:67]
ds_read_b64_tr_b16 v[32:33], v3 offset:46464
v_mfma_f32_16x16x32_bf16 v[68:71], v[16:19], a[212:215], v[68:71]
s_waitcnt vmcnt(17)
ds_read_b128 a[224:227], v10 offset:12288
v_mfma_f32_16x16x32_bf16 v[72:75], v[16:19], a[216:219], v[72:75]
ds_read_b64_tr_b16 v[34:35], v3 offset:46720
v_mfma_f32_16x16x32_bf16 v[76:79], v[16:19], a[220:223], v[76:79]
ds_read_b128 a[240:243], v11 offset:12288
s_waitcnt lgkmcnt(8)
v_mfma_f32_16x16x32_bf16 v[80:83], v[20:23], a[208:211], v[80:83]
ds_read_b64_tr_b16 v[36:37], v3 offset:46480
v_mfma_f32_16x16x32_bf16 v[84:87], v[20:23], a[212:215], v[84:87]
s_waitcnt vmcnt(15)
ds_read_b128 a[228:231], v10 offset:14336
v_mfma_f32_16x16x32_bf16 v[88:91], v[20:23], a[216:219], v[88:91]
ds_read_b64_tr_b16 v[38:39], v3 offset:46736
v_mfma_f32_16x16x32_bf16 v[92:95], v[20:23], a[220:223], v[92:95]
ds_read_b128 a[244:247], v11 offset:14336
s_waitcnt lgkmcnt(10)
v_mfma_f32_16x16x32_bf16 v[96:99], v[24:27], a[208:211], v[96:99]
ds_read_b64_tr_b16 v[40:41], v3 offset:46528
v_mfma_f32_16x16x32_bf16 v[100:103], v[24:27], a[212:215], v[100:103]
s_waitcnt vmcnt(13)
ds_read_b128 a[232:235], v10 offset:16384
buffer_load_dwordx4 v[248:251], v1, s[84:87], s92 offen offset:3072
v_mfma_f32_16x16x32_bf16 v[104:107], v[24:27], a[216:219], v[104:107]
ds_read_b64_tr_b16 v[42:43], v3 offset:46784
v_mfma_f32_16x16x32_bf16 v[108:111], v[24:27], a[220:223], v[108:111]
ds_read_b128 a[248:251], v11 offset:16384
s_waitcnt lgkmcnt(12)
v_mfma_f32_16x16x32_bf16 v[112:115], v[28:31], a[208:211], v[112:115]
ds_read_b64_tr_b16 v[44:45], v3 offset:46544
v_mfma_f32_16x16x32_bf16 v[116:119], v[28:31], a[212:215], v[116:119]
s_waitcnt vmcnt(12)
ds_read_b128 a[236:239], v10 offset:18432
v_mfma_f32_16x16x32_bf16 v[120:123], v[28:31], a[216:219], v[120:123]
ds_read_b64_tr_b16 v[46:47], v3 offset:46800
v_mfma_f32_16x16x32_bf16 v[124:127], v[28:31], a[220:223], v[124:127]
ds_read_b128 a[252:255], v11 offset:18432
s_waitcnt lgkmcnt(13)
v_mfma_f32_16x16x32_bf16 v[128:131], v[32:35], a[208:211], v[128:131]
s_waitcnt vmcnt(8)
s_barrier
v_mfma_f32_16x16x32_bf16 v[132:135], v[32:35], a[212:215], v[132:135]
ds_read_b64_tr_b16 v[16:17], v3 offset:50688
v_mfma_f32_16x16x32_bf16 v[136:139], v[32:35], a[216:219], v[136:139]
s_add_u32 m0, s41, 16896
s_add_u32 s47, s40, 1048576
buffer_load_dwordx4 v2, s[28:31], s47 offen nt lds
v_mfma_f32_16x16x32_bf16 v[140:143], v[32:35], a[220:223], v[140:143]
ds_read_b64_tr_b16 v[18:19], v3 offset:50944
s_waitcnt lgkmcnt(11)
v_mfma_f32_16x16x32_bf16 v[144:147], v[36:39], a[208:211], v[144:147]
v_mfma_f32_16x16x32_bf16 v[148:151], v[36:39], a[212:215], v[148:151]
ds_read_b64_tr_b16 v[20:21], v3 offset:50704
s_add_u32 m0, s41, 21120
s_add_u32 s47, s40, 1310720
buffer_load_dwordx4 v2, s[28:31], s47 offen nt lds
v_mfma_f32_16x16x32_bf16 v[152:155], v[36:39], a[216:219], v[152:155]
v_mfma_f32_16x16x32_bf16 v[156:159], v[36:39], a[220:223], v[156:159]
ds_read_b64_tr_b16 v[22:23], v3 offset:50960
s_waitcnt lgkmcnt(9)
v_mfma_f32_16x16x32_bf16 v[160:163], v[40:43], a[208:211], v[160:163]
s_add_u32 m0, s41, 25344
s_add_u32 s47, s40, 1572864
buffer_load_dwordx4 v2, s[28:31], s47 offen nt lds
v_mfma_f32_16x16x32_bf16 v[164:167], v[40:43], a[212:215], v[164:167]
ds_read_b64_tr_b16 v[24:25], v3 offset:50752
v_mfma_f32_16x16x32_bf16 v[168:171], v[40:43], a[216:219], v[168:171]
v_mfma_f32_16x16x32_bf16 v[172:175], v[40:43], a[220:223], v[172:175]
ds_read_b64_tr_b16 v[26:27], v3 offset:51008
s_add_u32 m0, s41, 29568
s_add_u32 s47, s40, 1835008
buffer_load_dwordx4 v2, s[28:31], s47 offen nt lds
s_waitcnt lgkmcnt(7)
v_mfma_f32_16x16x32_bf16 v[176:179], v[44:47], a[208:211], v[176:179]
v_mfma_f32_16x16x32_bf16 v[180:183], v[44:47], a[212:215], v[180:183]
ds_read_b64_tr_b16 v[28:29], v3 offset:50768
v_mfma_f32_16x16x32_bf16 v[184:187], v[44:47], a[216:219], v[184:187]
v_mfma_f32_16x16x32_bf16 v[188:191], v[44:47], a[220:223], v[188:191]
ds_read_b64_tr_b16 v[30:31], v3 offset:51024
s_waitcnt lgkmcnt(6)
v_mfma_f32_16x16x32_bf16 v[64:67], v[16:19], a[224:227], v[64:67]
ds_read_b64_tr_b16 v[32:33], v3 offset:54912
v_mfma_f32_16x16x32_bf16 v[68:71], v[16:19], a[228:231], v[68:71]
v_mfma_f32_16x16x32_bf16 v[72:75], v[16:19], a[232:235], v[72:75]
ds_read_b64_tr_b16 v[34:35], v3 offset:55168
v_mfma_f32_16x16x32_bf16 v[76:79], v[16:19], a[236:239], v[76:79]
s_waitcnt lgkmcnt(6)
v_mfma_f32_16x16x32_bf16 v[80:83], v[20:23], a[224:227], v[80:83]
ds_read_b64_tr_b16 v[36:37], v3 offset:54928
v_mfma_f32_16x16x32_bf16 v[84:87], v[20:23], a[228:231], v[84:87]
v_mfma_f32_16x16x32_bf16 v[88:91], v[20:23], a[232:235], v[88:91]
ds_read_b64_tr_b16 v[38:39], v3 offset:55184
v_mfma_f32_16x16x32_bf16 v[92:95], v[20:23], a[236:239], v[92:95]
s_waitcnt lgkmcnt(6)
v_mfma_f32_16x16x32_bf16 v[96:99], v[24:27], a[224:227], v[96:99]
ds_read_b64_tr_b16 v[40:41], v3 offset:54976
v_mfma_f32_16x16x32_bf16 v[100:103], v[24:27], a[228:231], v[100:103]
v_mfma_f32_16x16x32_bf16 v[104:107], v[24:27], a[232:235], v[104:107]
ds_read_b64_tr_b16 v[42:43], v3 offset:55232
v_mfma_f32_16x16x32_bf16 v[108:111], v[24:27], a[236:239], v[108:111]
s_waitcnt lgkmcnt(6)
v_mfma_f32_16x16x32_bf16 v[112:115], v[28:31], a[224:227], v[112:115]
ds_read_b64_tr_b16 v[44:45], v3 offset:54992
v_mfma_f32_16x16x32_bf16 v[116:119], v[28:31], a[228:231], v[116:119]
v_mfma_f32_16x16x32_bf16 v[120:123], v[28:31], a[232:235], v[120:123]
ds_read_b64_tr_b16 v[46:47], v3 offset:55248
v_mfma_f32_16x16x32_bf16 v[124:127], v[28:31], a[236:239], v[124:127]
s_waitcnt lgkmcnt(6)
v_mfma_f32_16x16x32_bf16 v[128:131], v[32:35], a[224:227], v[128:131]
ds_read_b64_tr_b16 v[16:17], v3 offset:59136
v_mfma_f32_16x16x32_bf16 v[132:135], v[32:35], a[228:231], v[132:135]
v_mfma_f32_16x16x32_bf16 v[136:139], v[32:35], a[232:235], v[136:139]
ds_read_b64_tr_b16 v[18:19], v3 offset:59392
v_mfma_f32_16x16x32_bf16 v[140:143], v[32:35], a[236:239], v[140:143]
s_waitcnt lgkmcnt(6)
v_mfma_f32_16x16x32_bf16 v[144:147], v[36:39], a[224:227], v[144:147]
ds_read_b64_tr_b16 v[20:21], v3 offset:59152
v_mfma_f32_16x16x32_bf16 v[148:151], v[36:39], a[228:231], v[148:151]
v_mfma_f32_16x16x32_bf16 v[152:155], v[36:39], a[232:235], v[152:155]
ds_read_b64_tr_b16 v[22:23], v3 offset:59408
v_mfma_f32_16x16x32_bf16 v[156:159], v[36:39], a[236:239], v[156:159]
s_waitcnt lgkmcnt(6)
v_mfma_f32_16x16x32_bf16 v[160:163], v[40:43], a[224:227], v[160:163]
ds_read_b64_tr_b16 v[24:25], v3 offset:59200
v_mfma_f32_16x16x32_bf16 v[164:167], v[40:43], a[228:231], v[164:167]
v_mfma_f32_16x16x32_bf16 v[168:171], v[40:43], a[232:235], v[168:171]
ds_read_b64_tr_b16 v[26:27], v3 offset:59456
v_mfma_f32_16x16x32_bf16 v[172:175], v[40:43], a[236:239], v[172:175]
s_waitcnt lgkmcnt(6)
v_mfma_f32_16x16x32_bf16 v[176:179], v[44:47], a[224:227], v[176:179]
ds_read_b64_tr_b16 v[28:29], v3 offset:59216
v_mfma_f32_16x16x32_bf16 v[180:183], v[44:47], a[228:231], v[180:183]
v_mfma_f32_16x16x32_bf16 v[184:187], v[44:47], a[232:235], v[184:187]
ds_read_b64_tr_b16 v[30:31], v3 offset:59472
v_mfma_f32_16x16x32_bf16 v[188:191], v[44:47], a[236:239], v[188:191]
s_waitcnt lgkmcnt(6)
v_mfma_f32_16x16x32_bf16 v[64:67], v[16:19], a[240:243], v[64:67]
ds_read_b64_tr_b16 v[32:33], v3 offset:63360
v_mfma_f32_16x16x32_bf16 v[68:71], v[16:19], a[244:247], v[68:71]
v_mfma_f32_16x16x32_bf16 v[72:75], v[16:19], a[248:251], v[72:75]
ds_read_b64_tr_b16 v[34:35], v3 offset:63616
v_mfma_f32_16x16x32_bf16 v[76:79], v[16:19], a[252:255], v[76:79]
s_waitcnt lgkmcnt(6)
v_mfma_f32_16x16x32_bf16 v[80:83], v[20:23], a[240:243], v[80:83]
ds_read_b64_tr_b16 v[36:37], v3 offset:63376
v_mfma_f32_16x16x32_bf16 v[84:87], v[20:23], a[244:247], v[84:87]
v_mfma_f32_16x16x32_bf16 v[88:91], v[20:23], a[248:251], v[88:91]
ds_read_b64_tr_b16 v[38:39], v3 offset:63632
v_mfma_f32_16x16x32_bf16 v[92:95], v[20:23], a[252:255], v[92:95]
s_waitcnt lgkmcnt(6)
v_mfma_f32_16x16x32_bf16 v[96:99], v[24:27], a[240:243], v[96:99]
ds_read_b64_tr_b16 v[40:41], v3 offset:63424
v_mfma_f32_16x16x32_bf16 v[100:103], v[24:27], a[244:247], v[100:103]
v_mfma_f32_16x16x32_bf16 v[104:107], v[24:27], a[248:251], v[104:107]
ds_read_b64_tr_b16 v[42:43], v3 offset:63680
v_mfma_f32_16x16x32_bf16 v[108:111], v[24:27], a[252:255], v[108:111]
s_waitcnt lgkmcnt(6)
v_mfma_f32_16x16x32_bf16 v[112:115], v[28:31], a[240:243], v[112:115]
ds_read_b64_tr_b16 v[44:45], v3 offset:63440
v_mfma_f32_16x16x32_bf16 v[116:119], v[28:31], a[244:247], v[116:119]
v_mfma_f32_16x16x32_bf16 v[120:123], v[28:31], a[248:251], v[120:123]
ds_read_b64_tr_b16 v[46:47], v3 offset:63696
v_mfma_f32_16x16x32_bf16 v[124:127], v[28:31], a[252:255], v[124:127]
s_waitcnt lgkmcnt(6)
v_mfma_f32_16x16x32_bf16 v[128:131], v[32:35], a[240:243], v[128:131]
s_waitcnt vmcnt(6)
s_barrier
v_cvt_pk_bf16_f32 v64, v64, v65
v_mfma_f32_16x16x32_bf16 v[132:135], v[32:35], a[244:247], v[132:135]
ds_read_b64_tr_b16 v[16:17], v3 offset:0
v_cvt_pk_bf16_f32 v65, v66, v67
v_mfma_f32_16x16x32_bf16 v[136:139], v[32:35], a[248:251], v[136:139]
s_add_u32 m0, s41, 33792
s_add_u32 s47, s40, 2097152
buffer_load_dwordx4 v2, s[28:31], s47 offen nt lds
v_mfma_f32_16x16x32_bf16 v[140:143], v[32:35], a[252:255], v[140:143]
ds_read_b64_tr_b16 v[18:19], v3 offset:256
v_cvt_pk_bf16_f32 v66, v80, v81
s_waitcnt lgkmcnt(6)
v_mfma_f32_16x16x32_bf16 v[144:147], v[36:39], a[240:243], v[144:147]
v_cvt_pk_bf16_f32 v67, v82, v83
v_mfma_f32_16x16x32_bf16 v[148:151], v[36:39], a[244:247], v[148:151]
ds_read_b64_tr_b16 v[20:21], v3 offset:16
s_add_u32 m0, s41, 38016
s_add_u32 s47, s40, 2359296
buffer_load_dwordx4 v2, s[28:31], s47 offen nt lds
v_mfma_f32_16x16x32_bf16 v[152:155], v[36:39], a[248:251], v[152:155]
v_mfma_f32_16x16x32_bf16 v[156:159], v[36:39], a[252:255], v[156:159]
ds_read_b64_tr_b16 v[22:23], v3 offset:272
s_waitcnt lgkmcnt(6)
v_mfma_f32_16x16x32_bf16 v[160:163], v[40:43], a[240:243], v[160:163]
s_add_u32 m0, s41, 42240
s_add_u32 s47, s40, 2621440
buffer_load_dwordx4 v2, s[28:31], s47 offen nt lds
v_mfma_f32_16x16x32_bf16 v[164:167], v[40:43], a[244:247], v[164:167]
ds_read_b64_tr_b16 v[24:25], v3 offset:64
s_add_u32 s46, s44, 0
buffer_store_dwordx4 v[64:67], v6, s[16:19], s46 offen offset:0 sc1
v_mfma_f32_16x16x32_bf16 v[168:171], v[40:43], a[248:251], v[168:171]
v_mfma_f32_16x16x32_bf16 v[172:175], v[40:43], a[252:255], v[172:175]
ds_read_b64_tr_b16 v[26:27], v3 offset:320
s_add_u32 m0, s41, 46464
s_add_u32 s47, s40, 2883584
buffer_load_dwordx4 v2, s[28:31], s47 offen nt lds
s_waitcnt lgkmcnt(6)
v_mfma_f32_16x16x32_bf16 v[176:179], v[44:47], a[240:243], v[176:179]
v_mfma_f32_16x16x32_bf16 v[180:183], v[44:47], a[244:247], v[180:183]
ds_read_b64_tr_b16 v[28:29], v3 offset:80
v_mfma_f32_16x16x32_bf16 v[184:187], v[44:47], a[248:251], v[184:187]
v_mfma_f32_16x16x32_bf16 v[188:191], v[44:47], a[252:255], v[188:191]
ds_read_b64_tr_b16 v[30:31], v3 offset:336
s_waitcnt lgkmcnt(6)
v_mfma_f32_16x16x32_bf16 v[192:195], a[0:3], v[16:19], 0
ds_read_b64_tr_b16 v[32:33], v3 offset:4224
v_mfma_f32_16x16x32_bf16 v[196:199], a[4:7], v[16:19], 0
v_mfma_f32_16x16x32_bf16 v[200:203], a[8:11], v[16:19], 0
ds_read_b64_tr_b16 v[34:35], v3 offset:4480
v_mfma_f32_16x16x32_bf16 v[204:207], a[12:15], v[16:19], 0
v_cvt_pk_bf16_f32 v68, v68, v69
v_cvt_pk_bf16_f32 v69, v70, v71
s_waitcnt lgkmcnt(6)
v_mfma_f32_16x16x32_bf16 v[208:211], a[0:3], v[20:23], 0
ds_read_b64_tr_b16 v[36:37], v3 offset:4240
v_cvt_pk_bf16_f32 v70, v84, v85
v_mfma_f32_16x16x32_bf16 v[212:215], a[4:7], v[20:23], 0
v_cvt_pk_bf16_f32 v71, v86, v87
v_mfma_f32_16x16x32_bf16 v[216:219], a[8:11], v[20:23], 0
ds_read_b64_tr_b16 v[38:39], v3 offset:4496
v_mfma_f32_16x16x32_bf16 v[220:223], a[12:15], v[20:23], 0
s_waitcnt lgkmcnt(6)
v_mfma_f32_16x16x32_bf16 v[224:227], a[0:3], v[24:27], 0
ds_read_b64_tr_b16 v[40:41], v3 offset:4288
v_mfma_f32_16x16x32_bf16 v[228:231], a[4:7], v[24:27], 0
v_mfma_f32_16x16x32_bf16 v[232:235], a[8:11], v[24:27], 0
ds_read_b64_tr_b16 v[42:43], v3 offset:4544
v_mfma_f32_16x16x32_bf16 v[236:239], a[12:15], v[24:27], 0
s_add_u32 s46, s44, 0
buffer_store_dwordx4 v[68:71], v6, s[16:19], s46 offen offset:128 sc1
s_waitcnt lgkmcnt(6)
v_mfma_f32_16x16x32_bf16 v[240:243], a[0:3], v[28:31], 0
ds_read_b64_tr_b16 v[44:45], v3 offset:4304
v_mfma_f32_16x16x32_bf16 v[244:247], a[4:7], v[28:31], 0
v_mfma_f32_16x16x32_bf16 v[80:83], a[8:11], v[28:31], 0
ds_read_b64_tr_b16 v[46:47], v3 offset:4560
v_mfma_f32_16x16x32_bf16 v[64:67], a[12:15], v[28:31], 0
s_waitcnt lgkmcnt(6)
v_mfma_f32_16x16x32_bf16 v[192:195], a[16:19], v[32:35], v[192:195]
ds_read_b64_tr_b16 v[16:17], v3 offset:8448
v_mfma_f32_16x16x32_bf16 v[196:199], a[20:23], v[32:35], v[196:199]
v_mfma_f32_16x16x32_bf16 v[200:203], a[24:27], v[32:35], v[200:203]
ds_read_b64_tr_b16 v[18:19], v3 offset:8704
v_mfma_f32_16x16x32_bf16 v[204:207], a[28:31], v[32:35], v[204:207]
s_waitcnt lgkmcnt(6)
v_mfma_f32_16x16x32_bf16 v[208:211], a[16:19], v[36:39], v[208:211]
ds_read_b64_tr_b16 v[20:21], v3 offset:8464
v_mfma_f32_16x16x32_bf16 v[212:215], a[20:23], v[36:39], v[212:215]
v_mfma_f32_16x16x32_bf16 v[216:219], a[24:27], v[36:39], v[216:219]
ds_read_b64_tr_b16 v[22:23], v3 offset:8720
v_cvt_pk_bf16_f32 v72, v72, v73
v_mfma_f32_16x16x32_bf16 v[220:223], a[28:31], v[36:39], v[220:223]
v_cvt_pk_bf16_f32 v73, v74, v75
v_cvt_pk_bf16_f32 v74, v88, v89
s_waitcnt lgkmcnt(6)
v_mfma_f32_16x16x32_bf16 v[224:227], a[16:19], v[40:43], v[224:227]
ds_read_b64_tr_b16 v[24:25], v3 offset:8512
v_cvt_pk_bf16_f32 v75, v90, v91
v_mfma_f32_16x16x32_bf16 v[228:231], a[20:23], v[40:43], v[228:231]
v_mfma_f32_16x16x32_bf16 v[232:235], a[24:27], v[40:43], v[232:235]
ds_read_b64_tr_b16 v[26:27], v3 offset:8768
v_mfma_f32_16x16x32_bf16 v[236:239], a[28:31], v[40:43], v[236:239]
s_waitcnt lgkmcnt(6)
v_mfma_f32_16x16x32_bf16 v[240:243], a[16:19], v[44:47], v[240:243]
ds_read_b64_tr_b16 v[28:29], v3 offset:8528
v_mfma_f32_16x16x32_bf16 v[244:247], a[20:23], v[44:47], v[244:247]
v_mfma_f32_16x16x32_bf16 v[80:83], a[24:27], v[44:47], v[80:83]
ds_read_b64_tr_b16 v[30:31], v3 offset:8784
s_add_u32 s46, s44, 8192
buffer_store_dwordx4 v[72:75], v6, s[16:19], s46 offen offset:0 sc1
v_mfma_f32_16x16x32_bf16 v[64:67], a[28:31], v[44:47], v[64:67]
s_waitcnt lgkmcnt(6)
v_mfma_f32_16x16x32_bf16 v[192:195], a[32:35], v[16:19], v[192:195]
ds_read_b64_tr_b16 v[32:33], v3 offset:12672
v_mfma_f32_16x16x32_bf16 v[196:199], a[36:39], v[16:19], v[196:199]
v_mfma_f32_16x16x32_bf16 v[200:203], a[40:43], v[16:19], v[200:203]
ds_read_b64_tr_b16 v[34:35], v3 offset:12928
v_mfma_f32_16x16x32_bf16 v[204:207], a[44:47], v[16:19], v[204:207]
s_waitcnt lgkmcnt(6)
v_mfma_f32_16x16x32_bf16 v[208:211], a[32:35], v[20:23], v[208:211]
ds_read_b64_tr_b16 v[36:37], v3 offset:12688
v_mfma_f32_16x16x32_bf16 v[212:215], a[36:39], v[20:23], v[212:215]
v_mfma_f32_16x16x32_bf16 v[216:219], a[40:43], v[20:23], v[216:219]
ds_read_b64_tr_b16 v[38:39], v3 offset:12944
v_mfma_f32_16x16x32_bf16 v[220:223], a[44:47], v[20:23], v[220:223]
s_waitcnt lgkmcnt(6)
v_mfma_f32_16x16x32_bf16 v[224:227], a[32:35], v[24:27], v[224:227]
ds_read_b64_tr_b16 v[40:41], v3 offset:12736
v_cvt_pk_bf16_f32 v76, v76, v77
v_mfma_f32_16x16x32_bf16 v[228:231], a[36:39], v[24:27], v[228:231]
v_cvt_pk_bf16_f32 v77, v78, v79
v_cvt_pk_bf16_f32 v78, v92, v93
v_mfma_f32_16x16x32_bf16 v[232:235], a[40:43], v[24:27], v[232:235]
ds_read_b64_tr_b16 v[42:43], v3 offset:12992
v_cvt_pk_bf16_f32 v79, v94, v95
v_mfma_f32_16x16x32_bf16 v[236:239], a[44:47], v[24:27], v[236:239]
s_waitcnt lgkmcnt(6)
v_mfma_f32_16x16x32_bf16 v[240:243], a[32:35], v[28:31], v[240:243]
ds_read_b64_tr_b16 v[44:45], v3 offset:12752
v_mfma_f32_16x16x32_bf16 v[244:247], a[36:39], v[28:31], v[244:247]
v_mfma_f32_16x16x32_bf16 v[80:83], a[40:43], v[28:31], v[80:83]
ds_read_b64_tr_b16 v[46:47], v3 offset:13008
v_mfma_f32_16x16x32_bf16 v[64:67], a[44:47], v[28:31], v[64:67]
s_waitcnt lgkmcnt(6)
v_mfma_f32_16x16x32_bf16 v[192:195], a[48:51], v[32:35], v[192:195]
s_waitcnt vmcnt(7)
s_barrier
s_add_u32 s46, s44, 8192
buffer_store_dwordx4 v[76:79], v6, s[16:19], s46 offen offset:128 sc1
v_mfma_f32_16x16x32_bf16 v[196:199], a[52:55], v[32:35], v[196:199]
ds_read_b64_tr_b16 v[16:17], v3 offset:16896
v_mfma_f32_16x16x32_bf16 v[200:203], a[56:59], v[32:35], v[200:203]
s_add_u32 m0, s41, 50688
s_add_u32 s47, s40, 3145728
buffer_load_dwordx4 v2, s[28:31], s47 offen nt lds
v_mfma_f32_16x16x32_bf16 v[204:207], a[60:63], v[32:35], v[204:207]
ds_read_b64_tr_b16 v[18:19], v3 offset:17152
s_waitcnt lgkmcnt(6)
v_mfma_f32_16x16x32_bf16 v[208:211], a[48:51], v[36:39], v[208:211]
v_mfma_f32_16x16x32_bf16 v[212:215], a[52:55], v[36:39], v[212:215]
ds_read_b64_tr_b16 v[20:21], v3 offset:16912
s_add_u32 m0, s41, 54912
s_add_u32 s47, s40, 3407872
buffer_load_dwordx4 v2, s[28:31], s47 offen nt lds
v_mfma_f32_16x16x32_bf16 v[216:219], a[56:59], v[36:39], v[216:219]
v_mfma_f32_16x16x32_bf16 v[220:223], a[60:63], v[36:39], v[220:223]
ds_read_b64_tr_b16 v[22:23], v3 offset:17168
s_waitcnt lgkmcnt(6)
v_mfma_f32_16x16x32_bf16 v[224:227], a[48:51], v[40:43], v[224:227]
s_add_u32 m0, s41, 59136
s_add_u32 s47, s40, 3670016
buffer_load_dwordx4 v2, s[28:31], s47 offen nt lds
v_mfma_f32_16x16x32_bf16 v[228:231], a[52:55], v[40:43], v[228:231]
ds_read_b64_tr_b16 v[24:25], v3 offset:16960
v_mfma_f32_16x16x32_bf16 v[232:235], a[56:59], v[40:43], v[232:235]
v_mfma_f32_16x16x32_bf16 v[236:239], a[60:63], v[40:43], v[236:239]
ds_read_b64_tr_b16 v[26:27], v3 offset:17216
s_add_u32 m0, s41, 63360
s_add_u32 s47, s40, 3932160
buffer_load_dwordx4 v2, s[28:31], s47 offen nt lds
s_waitcnt lgkmcnt(6)
v_mfma_f32_16x16x32_bf16 v[240:243], a[48:51], v[44:47], v[240:243]
v_cvt_pk_bf16_f32 v96, v96, v97
v_cvt_pk_bf16_f32 v97, v98, v99
v_mfma_f32_16x16x32_bf16 v[244:247], a[52:55], v[44:47], v[244:247]
ds_read_b64_tr_b16 v[28:29], v3 offset:16976
v_cvt_pk_bf16_f32 v98, v112, v113
v_mfma_f32_16x16x32_bf16 v[80:83], a[56:59], v[44:47], v[80:83]
v_cvt_pk_bf16_f32 v99, v114, v115
v_mfma_f32_16x16x32_bf16 v[64:67], a[60:63], v[44:47], v[64:67]
ds_read_b64_tr_b16 v[30:31], v3 offset:17232
s_waitcnt lgkmcnt(6)
v_mfma_f32_16x16x32_bf16 v[192:195], a[64:67], v[16:19], v[192:195]
ds_read_b64_tr_b16 v[32:33], v3 offset:21120
v_mfma_f32_16x16x32_bf16 v[196:199], a[68:71], v[16:19], v[196:199]
v_mfma_f32_16x16x32_bf16 v[200:203], a[72:75], v[16:19], v[200:203]
ds_read_b64_tr_b16 v[34:35], v3 offset:21376
v_mfma_f32_16x16x32_bf16 v[204:207], a[76:79], v[16:19], v[204:207]
s_add_u32 s46, s44, 0
buffer_store_dwordx4 v[96:99], v6, s[16:19], s46 offen offset:2048 sc1
s_waitcnt lgkmcnt(6)
v_mfma_f32_16x16x32_bf16 v[208:211], a[64:67], v[20:23], v[208:211]
ds_read_b64_tr_b16 v[36:37], v3 offset:21136
v_mfma_f32_16x16x32_bf16 v[212:215], a[68:71], v[20:23], v[212:215]
v_mfma_f32_16x16x32_bf16 v[216:219], a[72:75], v[20:23], v[216:219]
ds_read_b64_tr_b16 v[38:39], v3 offset:21392
v_mfma_f32_16x16x32_bf16 v[220:223], a[76:79], v[20:23], v[220:223]
s_waitcnt lgkmcnt(6)
v_mfma_f32_16x16x32_bf16 v[224:227], a[64:67], v[24:27], v[224:227]
ds_read_b64_tr_b16 v[40:41], v3 offset:21184
v_mfma_f32_16x16x32_bf16 v[228:231], a[68:71], v[24:27], v[228:231]
v_mfma_f32_16x16x32_bf16 v[232:235], a[72:75], v[24:27], v[232:235]
ds_read_b64_tr_b16 v[42:43], v3 offset:21440
v_mfma_f32_16x16x32_bf16 v[236:239], a[76:79], v[24:27], v[236:239]
s_waitcnt lgkmcnt(6)
v_mfma_f32_16x16x32_bf16 v[240:243], a[64:67], v[28:31], v[240:243]
ds_read_b64_tr_b16 v[44:45], v3 offset:21200
v_mfma_f32_16x16x32_bf16 v[244:247], a[68:71], v[28:31], v[244:247]
v_cvt_pk_bf16_f32 v100, v100, v101
v_cvt_pk_bf16_f32 v101, v102, v103
v_mfma_f32_16x16x32_bf16 v[80:83], a[72:75], v[28:31], v[80:83]
ds_read_b64_tr_b16 v[46:47], v3 offset:21456
v_cvt_pk_bf16_f32 v102, v116, v117
v_mfma_f32_16x16x32_bf16 v[64:67], a[76:79], v[28:31], v[64:67]
v_cvt_pk_bf16_f32 v103, v118, v119
s_waitcnt lgkmcnt(6)
v_mfma_f32_16x16x32_bf16 v[192:195], a[80:83], v[32:35], v[192:195]
ds_read_b64_tr_b16 v[16:17], v3 offset:25344
v_mfma_f32_16x16x32_bf16 v[196:199], a[84:87], v[32:35], v[196:199]
v_mfma_f32_16x16x32_bf16 v[200:203], a[88:91], v[32:35], v[200:203]
ds_read_b64_tr_b16 v[18:19], v3 offset:25600
v_mfma_f32_16x16x32_bf16 v[204:207], a[92:95], v[32:35], v[204:207]
s_waitcnt lgkmcnt(6)
v_mfma_f32_16x16x32_bf16 v[208:211], a[80:83], v[36:39], v[208:211]
ds_read_b64_tr_b16 v[20:21], v3 offset:25360
v_mfma_f32_16x16x32_bf16 v[212:215], a[84:87], v[36:39], v[212:215]
s_add_u32 s46, s44, 0
buffer_store_dwordx4 v[100:103], v6, s[16:19], s46 offen offset:2176 sc1
v_mfma_f32_16x16x32_bf16 v[216:219], a[88:91], v[36:39], v[216:219]
ds_read_b64_tr_b16 v[22:23], v3 offset:25616
v_mfma_f32_16x16x32_bf16 v[220:223], a[92:95], v[36:39], v[220:223]
s_waitcnt lgkmcnt(6)
v_mfma_f32_16x16x32_bf16 v[224:227], a[80:83], v[40:43], v[224:227]
ds_read_b64_tr_b16 v[24:25], v3 offset:25408
v_mfma_f32_16x16x32_bf16 v[228:231], a[84:87], v[40:43], v[228:231]
v_mfma_f32_16x16x32_bf16 v[232:235], a[88:91], v[40:43], v[232:235]
ds_read_b64_tr_b16 v[26:27], v3 offset:25664
v_mfma_f32_16x16x32_bf16 v[236:239], a[92:95], v[40:43], v[236:239]
s_waitcnt lgkmcnt(6)
v_mfma_f32_16x16x32_bf16 v[240:243], a[80:83], v[44:47], v[240:243]
ds_read_b64_tr_b16 v[28:29], v3 offset:25424
v_mfma_f32_16x16x32_bf16 v[244:247], a[84:87], v[44:47], v[244:247]
v_mfma_f32_16x16x32_bf16 v[80:83], a[88:91], v[44:47], v[80:83]
ds_read_b64_tr_b16 v[30:31], v3 offset:25680
v_mfma_f32_16x16x32_bf16 v[64:67], a[92:95], v[44:47], v[64:67]
s_waitcnt lgkmcnt(6)
v_mfma_f32_16x16x32_bf16 v[192:195], a[96:99], v[16:19], v[192:195]
ds_read_b64_tr_b16 v[32:33], v3 offset:29568
v_cvt_pk_bf16_f32 v104, v104, v105
v_mfma_f32_16x16x32_bf16 v[196:199], a[100:103], v[16:19], v[196:199]
v_cvt_pk_bf16_f32 v105, v106, v107
v_cvt_pk_bf16_f32 v106, v120, v121
v_mfma_f32_16x16x32_bf16 v[200:203], a[104:107], v[16:19], v[200:203]
ds_read_b64_tr_b16 v[34:35], v3 offset:29824
s_add_u32 m0, s41, 67584
s_add_u32 s47, s40, 128
buffer_load_dwordx4 v2, s[28:31], s47 offen nt lds
v_mfma_f32_16x16x32_bf16 v[204:207], a[108:111], v[16:19], v[204:207]
v_cvt_pk_bf16_f32 v107, v122, v123
s_waitcnt lgkmcnt(6)
v_mfma_f32_16x16x32_bf16 v[208:211], a[96:99], v[20:23], v[208:211]
ds_read_b64_tr_b16 v[36:37], v3 offset:29584
v_mfma_f32_16x16x32_bf16 v[212:215], a[100:103], v[20:23], v[212:215]
s_add_u32 m0, s41, 71808
s_add_u32 s47, s40, 262272
buffer_load_dwordx4 v2, s[28:31], s47 offen nt lds
v_mfma_f32_16x16x32_bf16 v[216:219], a[104:107], v[20:23], v[216:219]
ds_read_b64_tr_b16 v[38:39], v3 offset:29840
v_mfma_f32_16x16x32_bf16 v[220:223], a[108:111], v[20:23], v[220:223]
s_waitcnt lgkmcnt(6)
v_mfma_f32_16x16x32_bf16 v[224:227], a[96:99], v[24:27], v[224:227]
ds_read_b64_tr_b16 v[40:41], v3 offset:29632
s_add_u32 m0, s41, 76032
s_add_u32 s47, s40, 524416
buffer_load_dwordx4 v2, s[28:31], s47 offen nt lds
v_mfma_f32_16x16x32_bf16 v[228:231], a[100:103], v[24:27], v[228:231]
s_add_u32 s46, s44, 8192
buffer_store_dwordx4 v[104:107], v6, s[16:19], s46 offen offset:2048 sc1
v_mfma_f32_16x16x32_bf16 v[232:235], a[104:107], v[24:27], v[232:235]
ds_read_b64_tr_b16 v[42:43], v3 offset:29888
v_mfma_f32_16x16x32_bf16 v[236:239], a[108:111], v[24:27], v[236:239]
s_add_u32 m0, s41, 80256
s_add_u32 s47, s40, 786560
buffer_load_dwordx4 v2, s[28:31], s47 offen nt lds
s_waitcnt lgkmcnt(6)
v_mfma_f32_16x16x32_bf16 v[240:243], a[96:99], v[28:31], v[240:243]
ds_read_b64_tr_b16 v[44:45], v3 offset:29648
v_mfma_f32_16x16x32_bf16 v[244:247], a[100:103], v[28:31], v[244:247]
v_mfma_f32_16x16x32_bf16 v[80:83], a[104:107], v[28:31], v[80:83]
ds_read_b64_tr_b16 v[46:47], v3 offset:29904
v_mfma_f32_16x16x32_bf16 v[64:67], a[108:111], v[28:31], v[64:67]
s_waitcnt lgkmcnt(6)
v_mfma_f32_16x16x32_bf16 v[192:195], a[112:115], v[32:35], v[192:195]
s_waitcnt vmcnt(14)
s_barrier
v_mfma_f32_16x16x32_bf16 v[196:199], a[116:119], v[32:35], v[196:199]
ds_read_b64_tr_b16 v[16:17], v3 offset:33792
v_mfma_f32_16x16x32_bf16 v[200:203], a[120:123], v[32:35], v[200:203]
v_cvt_pk_bf16_f32 v108, v108, v109
v_cvt_pk_bf16_f32 v109, v110, v111
v_mfma_f32_16x16x32_bf16 v[204:207], a[124:127], v[32:35], v[204:207]
ds_read_b64_tr_b16 v[18:19], v3 offset:34048
v_cvt_pk_bf16_f32 v110, v124, v125
s_waitcnt lgkmcnt(6)
v_mfma_f32_16x16x32_bf16 v[208:211], a[112:115], v[36:39], v[208:211]
v_cvt_pk_bf16_f32 v111, v126, v127
v_mfma_f32_16x16x32_bf16 v[212:215], a[116:119], v[36:39], v[212:215]
ds_read_b64_tr_b16 v[20:21], v3 offset:33808
v_mfma_f32_16x16x32_bf16 v[216:219], a[120:123], v[36:39], v[216:219]
v_mfma_f32_16x16x32_bf16 v[220:223], a[124:127], v[36:39], v[220:223]
ds_read_b64_tr_b16 v[22:23], v3 offset:34064
s_waitcnt lgkmcnt(6)
v_mfma_f32_16x16x32_bf16 v[224:227], a[112:115], v[40:43], v[224:227]
v_mfma_f32_16x16x32_bf16 v[228:231], a[116:119], v[40:43], v[228:231]
ds_read_b64_tr_b16 v[24:25], v3 offset:33856
v_mfma_f32_16x16x32_bf16 v[232:235], a[120:123], v[40:43], v[232:235]
s_add_u32 s46, s44, 8192
buffer_store_dwordx4 v[108:111], v6, s[16:19], s46 offen offset:2176 sc1
v_mfma_f32_16x16x32_bf16 v[236:239], a[124:127], v[40:43], v[236:239]
ds_read_b64_tr_b16 v[26:27], v3 offset:34112
s_waitcnt lgkmcnt(6)
v_mfma_f32_16x16x32_bf16 v[240:243], a[112:115], v[44:47], v[240:243]
v_mfma_f32_16x16x32_bf16 v[244:247], a[116:119], v[44:47], v[244:247]
ds_read_b64_tr_b16 v[28:29], v3 offset:33872
v_mfma_f32_16x16x32_bf16 v[80:83], a[120:123], v[44:47], v[80:83]
v_mfma_f32_16x16x32_bf16 v[64:67], a[124:127], v[44:47], v[64:67]
ds_read_b64_tr_b16 v[30:31], v3 offset:34128
s_waitcnt lgkmcnt(6)
v_mfma_f32_16x16x32_bf16 v[192:195], a[128:131], v[16:19], v[192:195]
ds_read_b64_tr_b16 v[32:33], v3 offset:38016
v_mfma_f32_16x16x32_bf16 v[196:199], a[132:135], v[16:19], v[196:199]
v_mfma_f32_16x16x32_bf16 v[200:203], a[136:139], v[16:19], v[200:203]
ds_read_b64_tr_b16 v[34:35], v3 offset:38272
s_add_u32 m0, s41, 84480
s_add_u32 s47, s40, 1048704
buffer_load_dwordx4 v2, s[28:31], s47 offen nt lds
v_mfma_f32_16x16x32_bf16 v[204:207], a[140:143], v[16:19], v[204:207]
s_waitcnt lgkmcnt(6)
v_mfma_f32_16x16x32_bf16 v[208:211], a[128:131], v[20:23], v[208:211]
ds_read_b64_tr_b16 v[36:37], v3 offset:38032
v_mfma_f32_16x16x32_bf16 v[212:215], a[132:135], v[20:23], v[212:215]
s_add_u32 m0, s41, 88704
s_add_u32 s47, s40, 1310848
buffer_load_dwordx4 v2, s[28:31], s47 offen nt lds
v_mfma_f32_16x16x32_bf16 v[216:219], a[136:139], v[20:23], v[216:219]
ds_read_b64_tr_b16 v[38:39], v3 offset:38288
v_cvt_pk_bf16_f32 v128, v128, v129
v_mfma_f32_16x16x32_bf16 v[220:223], a[140:143], v[20:23], v[220:223]
v_cvt_pk_bf16_f32 v129, v130, v131
v_cvt_pk_bf16_f32 v130, v144, v145
s_waitcnt lgkmcnt(6)
v_mfma_f32_16x16x32_bf16 v[224:227], a[128:131], v[24:27], v[224:227]
ds_read_b64_tr_b16 v[40:41], v3 offset:38080
s_add_u32 m0, s41, 92928
s_add_u32 s47, s40, 1572992
buffer_load_dwordx4 v2, s[28:31], s47 offen nt lds
v_mfma_f32_16x16x32_bf16 v[228:231], a[132:135], v[24:27], v[228:231]
v_cvt_pk_bf16_f32 v131, v146, v147
v_mfma_f32_16x16x32_bf16 v[232:235], a[136:139], v[24:27], v[232:235]
ds_read_b64_tr_b16 v[42:43], v3 offset:38336
v_mfma_f32_16x16x32_bf16 v[236:239], a[140:143], v[24:27], v[236:239]
s_add_u32 m0, s41, 97152
s_add_u32 s47, s40, 1835136
buffer_load_dwordx4 v2, s[28:31], s47 offen nt lds
s_waitcnt lgkmcnt(6)
v_mfma_f32_16x16x32_bf16 v[240:243], a[128:131], v[28:31], v[240:243]
ds_read_b64_tr_b16 v[44:45], v3 offset:38096
v_mfma_f32_16x16x32_bf16 v[244:247], a[132:135], v[28:31], v[244:247]
s_add_u32 s46, s44, 4096
buffer_store_dwordx4 v[128:131], v6, s[16:19], s46 offen offset:0 sc1
v_mfma_f32_16x16x32_bf16 v[80:83], a[136:139], v[28:31], v[80:83]
ds_read_b64_tr_b16 v[46:47], v3 offset:38352
v_mfma_f32_16x16x32_bf16 v[64:67], a[140:143], v[28:31], v[64:67]
s_waitcnt lgkmcnt(6)
v_mfma_f32_16x16x32_bf16 v[192:195], a[144:147], v[32:35], v[192:195]
ds_read_b64_tr_b16 v[16:17], v3 offset:42240
v_mfma_f32_16x16x32_bf16 v[196:199], a[148:151], v[32:35], v[196:199]
v_mfma_f32_16x16x32_bf16 v[200:203], a[152:155], v[32:35], v[200:203]
ds_read_b64_tr_b16 v[18:19], v3 offset:42496
v_mfma_f32_16x16x32_bf16 v[204:207], a[156:159], v[32:35], v[204:207]
s_waitcnt lgkmcnt(6)
v_mfma_f32_16x16x32_bf16 v[208:211], a[144:147], v[36:39], v[208:211]
ds_read_b64_tr_b16 v[20:21], v3 offset:42256
v_mfma_f32_16x16x32_bf16 v[212:215], a[148:151], v[36:39], v[212:215]
v_mfma_f32_16x16x32_bf16 v[216:219], a[152:155], v[36:39], v[216:219]
ds_read_b64_tr_b16 v[22:23], v3 offset:42512
v_mfma_f32_16x16x32_bf16 v[220:223], a[156:159], v[36:39], v[220:223]
s_waitcnt lgkmcnt(6)
v_mfma_f32_16x16x32_bf16 v[224:227], a[144:147], v[40:43], v[224:227]
ds_read_b64_tr_b16 v[24:25], v3 offset:42304
v_cvt_pk_bf16_f32 v132, v132, v133
v_mfma_f32_16x16x32_bf16 v[228:231], a[148:151], v[40:43], v[228:231]
v_cvt_pk_bf16_f32 v133, v134, v135
v_cvt_pk_bf16_f32 v134, v148, v149
v_mfma_f32_16x16x32_bf16 v[232:235], a[152:155], v[40:43], v[232:235]
ds_read_b64_tr_b16 v[26:27], v3 offset:42560
v_cvt_pk_bf16_f32 v135, v150, v151
v_mfma_f32_16x16x32_bf16 v[236:239], a[156:159], v[40:43], v[236:239]
s_waitcnt lgkmcnt(6)
v_mfma_f32_16x16x32_bf16 v[240:243], a[144:147], v[44:47], v[240:243]
ds_read_b64_tr_b16 v[28:29], v3 offset:42320
v_mfma_f32_16x16x32_bf16 v[244:247], a[148:151], v[44:47], v[244:247]
v_mfma_f32_16x16x32_bf16 v[80:83], a[152:155], v[44:47], v[80:83]
ds_read_b64_tr_b16 v[30:31], v3 offset:42576
v_mfma_f32_16x16x32_bf16 v[64:67], a[156:159], v[44:47], v[64:67]
s_waitcnt lgkmcnt(6)
v_mfma_f32_16x16x32_bf16 v[192:195], a[160:163], v[16:19], v[192:195]
ds_read_b64_tr_b16 v[32:33], v3 offset:46464
s_add_u32 s46, s44, 4096
buffer_store_dwordx4 v[132:135], v6, s[16:19], s46 offen offset:128 sc1
v_mfma_f32_16x16x32_bf16 v[196:199], a[164:167], v[16:19], v[196:199]
v_mfma_f32_16x16x32_bf16 v[200:203], a[168:171], v[16:19], v[200:203]
ds_read_b64_tr_b16 v[34:35], v3 offset:46720
s_add_u32 m0, s41, 101376
s_add_u32 s47, s40, 2097280
buffer_load_dwordx4 v2, s[28:31], s47 offen nt lds
v_mfma_f32_16x16x32_bf16 v[204:207], a[172:175], v[16:19], v[204:207]
s_waitcnt lgkmcnt(6)
v_mfma_f32_16x16x32_bf16 v[208:211], a[160:163], v[20:23], v[208:211]
ds_read_b64_tr_b16 v[36:37], v3 offset:46480
v_mfma_f32_16x16x32_bf16 v[212:215], a[164:167], v[20:23], v[212:215]
s_add_u32 m0, s41, 105600
s_add_u32 s47, s40, 2359424
buffer_load_dwordx4 v2, s[28:31], s47 offen nt lds
v_mfma_f32_16x16x32_bf16 v[216:219], a[168:171], v[20:23], v[216:219]
ds_read_b64_tr_b16 v[38:39], v3 offset:46736
v_mfma_f32_16x16x32_bf16 v[220:223], a[172:175], v[20:23], v[220:223]
s_waitcnt lgkmcnt(6)
v_mfma_f32_16x16x32_bf16 v[224:227], a[160:163], v[24:27], v[224:227]
ds_read_b64_tr_b16 v[40:41], v3 offset:46528
s_add_u32 m0, s41, 109824
s_add_u32 s47, s40, 2621568
buffer_load_dwordx4 v2, s[28:31], s47 offen nt lds
v_mfma_f32_16x16x32_bf16 v[228:231], a[164:167], v[24:27], v[228:231]
v_mfma_f32_16x16x32_bf16 v[232:235], a[168:171], v[24:27], v[232:235]
ds_read_b64_tr_b16 v[42:43], v3 offset:46784
v_cvt_pk_bf16_f32 v136, v136, v137
v_mfma_f32_16x16x32_bf16 v[236:239], a[172:175], v[24:27], v[236:239]
s_add_u32 m0, s41, 114048
s_add_u32 s47, s40, 2883712
buffer_load_dwordx4 v2, s[28:31], s47 offen nt lds
s_waitcnt lgkmcnt(6)
v_mfma_f32_16x16x32_bf16 v[240:243], a[160:163], v[28:31], v[240:243]
ds_read_b64_tr_b16 v[44:45], v3 offset:46544
v_cvt_pk_bf16_f32 v137, v138, v139
v_mfma_f32_16x16x32_bf16 v[244:247], a[164:167], v[28:31], v[244:247]
v_cvt_pk_bf16_f32 v138, v152, v153
v_cvt_pk_bf16_f32 v139, v154, v155
v_mfma_f32_16x16x32_bf16 v[80:83], a[168:171], v[28:31], v[80:83]
ds_read_b64_tr_b16 v[46:47], v3 offset:46800
v_mfma_f32_16x16x32_bf16 v[64:67], a[172:175], v[28:31], v[64:67]
s_waitcnt lgkmcnt(6)
v_mfma_f32_16x16x32_bf16 v[192:195], a[176:179], v[32:35], v[192:195]
s_waitcnt vmcnt(18)
s_barrier
v_mfma_f32_16x16x32_bf16 v[196:199], a[180:183], v[32:35], v[196:199]
ds_read_b64_tr_b16 v[16:17], v3 offset:50688
v_mfma_f32_16x16x32_bf16 v[200:203], a[184:187], v[32:35], v[200:203]
s_add_u32 s46, s44, 12288
buffer_store_dwordx4 v[136:139], v6, s[16:19], s46 offen offset:0 sc1
v_mfma_f32_16x16x32_bf16 v[204:207], a[188:191], v[32:35], v[204:207]
ds_read_b64_tr_b16 v[18:19], v3 offset:50944
s_waitcnt lgkmcnt(6)
v_mfma_f32_16x16x32_bf16 v[208:211], a[176:179], v[36:39], v[208:211]
v_mfma_f32_16x16x32_bf16 v[212:215], a[180:183], v[36:39], v[212:215]
ds_read_b64_tr_b16 v[20:21], v3 offset:50704
v_mfma_f32_16x16x32_bf16 v[216:219], a[184:187], v[36:39], v[216:219]
v_mfma_f32_16x16x32_bf16 v[220:223], a[188:191], v[36:39], v[220:223]
ds_read_b64_tr_b16 v[22:23], v3 offset:50960
s_waitcnt lgkmcnt(6)
v_mfma_f32_16x16x32_bf16 v[224:227], a[176:179], v[40:43], v[224:227]
v_mfma_f32_16x16x32_bf16 v[228:231], a[180:183], v[40:43], v[228:231]
ds_read_b64_tr_b16 v[24:25], v3 offset:50752
v_mfma_f32_16x16x32_bf16 v[232:235], a[184:187], v[40:43], v[232:235]
v_mfma_f32_16x16x32_bf16 v[236:239], a[188:191], v[40:43], v[236:239]
ds_read_b64_tr_b16 v[26:27], v3 offset:51008
s_waitcnt lgkmcnt(6)
v_mfma_f32_16x16x32_bf16 v[240:243], a[176:179], v[44:47], v[240:243]
v_mfma_f32_16x16x32_bf16 v[244:247], a[180:183], v[44:47], v[244:247]
ds_read_b64_tr_b16 v[28:29], v3 offset:50768
v_cvt_pk_bf16_f32 v140, v140, v141
v_mfma_f32_16x16x32_bf16 v[80:83], a[184:187], v[44:47], v[80:83]
v_cvt_pk_bf16_f32 v141, v142, v143
v_cvt_pk_bf16_f32 v142, v156, v157
v_mfma_f32_16x16x32_bf16 v[64:67], a[188:191], v[44:47], v[64:67]
ds_read_b64_tr_b16 v[30:31], v3 offset:51024
v_cvt_pk_bf16_f32 v143, v158, v159
s_waitcnt lgkmcnt(6)
v_mfma_f32_16x16x32_bf16 v[192:195], a[192:195], v[16:19], v[192:195]
ds_read_b64_tr_b16 v[32:33], v3 offset:54912
v_mfma_f32_16x16x32_bf16 v[196:199], a[196:199], v[16:19], v[196:199]
v_mfma_f32_16x16x32_bf16 v[200:203], a[200:203], v[16:19], v[200:203]
ds_read_b64_tr_b16 v[34:35], v3 offset:55168
s_add_u32 m0, s41, 118272
s_add_u32 s47, s40, 3145856
buffer_load_dwordx4 v2, s[28:31], s47 offen nt lds
v_mfma_f32_16x16x32_bf16 v[204:207], a[204:207], v[16:19], v[204:207]
s_waitcnt lgkmcnt(6)
v_mfma_f32_16x16x32_bf16 v[208:211], a[192:195], v[20:23], v[208:211]
ds_read_b64_tr_b16 v[36:37], v3 offset:54928
v_mfma_f32_16x16x32_bf16 v[212:215], a[196:199], v[20:23], v[212:215]
s_add_u32 m0, s41, 122496
s_add_u32 s47, s40, 3408000
buffer_load_dwordx4 v2, s[28:31], s47 offen nt lds
v_mfma_f32_16x16x32_bf16 v[216:219], a[200:203], v[20:23], v[216:219]
ds_read_b64_tr_b16 v[38:39], v3 offset:55184
s_add_u32 s46, s44, 12288
buffer_store_dwordx4 v[140:143], v6, s[16:19], s46 offen offset:128 sc1
v_mfma_f32_16x16x32_bf16 v[220:223], a[204:207], v[20:23], v[220:223]
s_waitcnt lgkmcnt(6)
v_mfma_f32_16x16x32_bf16 v[224:227], a[192:195], v[24:27], v[224:227]
ds_read_b64_tr_b16 v[40:41], v3 offset:54976
s_add_u32 m0, s41, 126720
s_add_u32 s47, s40, 3670144
buffer_load_dwordx4 v2, s[28:31], s47 offen nt lds
v_mfma_f32_16x16x32_bf16 v[228:231], a[196:199], v[24:27], v[228:231]
v_mfma_f32_16x16x32_bf16 v[232:235], a[200:203], v[24:27], v[232:235]
ds_read_b64_tr_b16 v[42:43], v3 offset:55232
v_mfma_f32_16x16x32_bf16 v[236:239], a[204:207], v[24:27], v[236:239]
s_add_u32 m0, s41, 130944
s_add_u32 s47, s40, 3932288
buffer_load_dwordx4 v2, s[28:31], s47 offen nt lds
s_waitcnt lgkmcnt(6)
v_mfma_f32_16x16x32_bf16 v[240:243], a[192:195], v[28:31], v[240:243]
ds_read_b64_tr_b16 v[44:45], v3 offset:54992
v_mfma_f32_16x16x32_bf16 v[244:247], a[196:199], v[28:31], v[244:247]
v_mfma_f32_16x16x32_bf16 v[80:83], a[200:203], v[28:31], v[80:83]
ds_read_b64_tr_b16 v[46:47], v3 offset:55248
v_mfma_f32_16x16x32_bf16 v[64:67], a[204:207], v[28:31], v[64:67]
v_cvt_pk_bf16_f32 v160, v160, v161
v_cvt_pk_bf16_f32 v161, v162, v163
s_waitcnt lgkmcnt(6)
v_mfma_f32_16x16x32_bf16 v[192:195], a[208:211], v[32:35], v[192:195]
ds_read_b64_tr_b16 v[16:17], v3 offset:59136
v_cvt_pk_bf16_f32 v162, v176, v177
v_mfma_f32_16x16x32_bf16 v[196:199], a[212:215], v[32:35], v[196:199]
v_cvt_pk_bf16_f32 v163, v178, v179
v_mfma_f32_16x16x32_bf16 v[200:203], a[216:219], v[32:35], v[200:203]
ds_read_b64_tr_b16 v[18:19], v3 offset:59392
v_mfma_f32_16x16x32_bf16 v[204:207], a[220:223], v[32:35], v[204:207]
s_waitcnt lgkmcnt(6)
v_mfma_f32_16x16x32_bf16 v[208:211], a[208:211], v[36:39], v[208:211]
ds_read_b64_tr_b16 v[20:21], v3 offset:59152
v_mfma_f32_16x16x32_bf16 v[212:215], a[212:215], v[36:39], v[212:215]
v_mfma_f32_16x16x32_bf16 v[216:219], a[216:219], v[36:39], v[216:219]
ds_read_b64_tr_b16 v[22:23], v3 offset:59408
v_mfma_f32_16x16x32_bf16 v[220:223], a[220:223], v[36:39], v[220:223]
s_add_u32 s46, s44, 4096
buffer_store_dwordx4 v[160:163], v6, s[16:19], s46 offen offset:2048 sc1
s_waitcnt lgkmcnt(6)
v_mfma_f32_16x16x32_bf16 v[224:227], a[208:211], v[40:43], v[224:227]
ds_read_b64_tr_b16 v[24:25], v3 offset:59200
v_mfma_f32_16x16x32_bf16 v[228:231], a[212:215], v[40:43], v[228:231]
v_mfma_f32_16x16x32_bf16 v[232:235], a[216:219], v[40:43], v[232:235]
ds_read_b64_tr_b16 v[26:27], v3 offset:59456
v_mfma_f32_16x16x32_bf16 v[236:239], a[220:223], v[40:43], v[236:239]
s_waitcnt lgkmcnt(6)
v_mfma_f32_16x16x32_bf16 v[240:243], a[208:211], v[44:47], v[240:243]
ds_read_b64_tr_b16 v[28:29], v3 offset:59216
v_mfma_f32_16x16x32_bf16 v[244:247], a[212:215], v[44:47], v[244:247]
v_mfma_f32_16x16x32_bf16 v[80:83], a[216:219], v[44:47], v[80:83]
ds_read_b64_tr_b16 v[30:31], v3 offset:59472
v_mfma_f32_16x16x32_bf16 v[64:67], a[220:223], v[44:47], v[64:67]
s_waitcnt lgkmcnt(6)
v_mfma_f32_16x16x32_bf16 v[192:195], a[224:227], v[16:19], v[192:195]
ds_read_b64_tr_b16 v[32:33], v3 offset:63360
v_mfma_f32_16x16x32_bf16 v[196:199], a[228:231], v[16:19], v[196:199]
v_mfma_f32_16x16x32_bf16 v[200:203], a[232:235], v[16:19], v[200:203]
ds_read_b64_tr_b16 v[34:35], v3 offset:63616
v_cvt_pk_bf16_f32 v164, v164, v165
v_mfma_f32_16x16x32_bf16 v[204:207], a[236:239], v[16:19], v[204:207]
v_cvt_pk_bf16_f32 v165, v166, v167
v_cvt_pk_bf16_f32 v166, v180, v181
s_waitcnt lgkmcnt(6)
v_mfma_f32_16x16x32_bf16 v[208:211], a[224:227], v[20:23], v[208:211]
ds_read_b64_tr_b16 v[36:37], v3 offset:63376
v_cvt_pk_bf16_f32 v167, v182, v183
v_mfma_f32_16x16x32_bf16 v[212:215], a[228:231], v[20:23], v[212:215]
v_mfma_f32_16x16x32_bf16 v[216:219], a[232:235], v[20:23], v[216:219]
ds_read_b64_tr_b16 v[38:39], v3 offset:63632
v_mfma_f32_16x16x32_bf16 v[220:223], a[236:239], v[20:23], v[220:223]
s_waitcnt lgkmcnt(6)
v_mfma_f32_16x16x32_bf16 v[224:227], a[224:227], v[24:27], v[224:227]
ds_read_b64_tr_b16 v[40:41], v3 offset:63424
v_mfma_f32_16x16x32_bf16 v[228:231], a[228:231], v[24:27], v[228:231]
v_mfma_f32_16x16x32_bf16 v[232:235], a[232:235], v[24:27], v[232:235]
ds_read_b64_tr_b16 v[42:43], v3 offset:63680
s_add_u32 s46, s44, 4096
buffer_store_dwordx4 v[164:167], v6, s[16:19], s46 offen offset:2176 sc1
v_mfma_f32_16x16x32_bf16 v[236:239], a[236:239], v[24:27], v[236:239]
s_waitcnt lgkmcnt(6)
v_mfma_f32_16x16x32_bf16 v[240:243], a[224:227], v[28:31], v[240:243]
ds_read_b64_tr_b16 v[44:45], v3 offset:63440
v_mfma_f32_16x16x32_bf16 v[244:247], a[228:231], v[28:31], v[244:247]
v_mfma_f32_16x16x32_bf16 v[80:83], a[232:235], v[28:31], v[80:83]
ds_read_b64_tr_b16 v[46:47], v3 offset:63696
v_mfma_f32_16x16x32_bf16 v[64:67], a[236:239], v[28:31], v[64:67]
s_waitcnt lgkmcnt(6)
v_mfma_f32_16x16x32_bf16 v[192:195], a[240:243], v[32:35], v[192:195]
s_waitcnt vmcnt(19)
s_barrier
v_mfma_f32_16x16x32_bf16 v[196:199], a[244:247], v[32:35], v[196:199]
ds_read_b64_tr_b16 v[16:17], v4 offset:2048
v_mfma_f32_16x16x32_bf16 v[200:203], a[248:251], v[32:35], v[200:203]
v_mfma_f32_16x16x32_bf16 v[204:207], a[252:255], v[32:35], v[204:207]
ds_read_b64_tr_b16 v[18:19], v4 offset:2304
s_waitcnt lgkmcnt(6)
v_mfma_f32_16x16x32_bf16 v[208:211], a[240:243], v[36:39], v[208:211]
v_cvt_pk_bf16_f32 v168, v168, v169
v_cvt_pk_bf16_f32 v169, v170, v171
v_mfma_f32_16x16x32_bf16 v[212:215], a[244:247], v[36:39], v[212:215]
ds_read_b64_tr_b16 v[20:21], v4 offset:2064
v_cvt_pk_bf16_f32 v170, v184, v185
v_mfma_f32_16x16x32_bf16 v[216:219], a[248:251], v[36:39], v[216:219]
v_cvt_pk_bf16_f32 v171, v186, v187
v_mfma_f32_16x16x32_bf16 v[220:223], a[252:255], v[36:39], v[220:223]
ds_read_b64_tr_b16 v[22:23], v4 offset:2320
s_waitcnt lgkmcnt(6)
v_mfma_f32_16x16x32_bf16 v[224:227], a[240:243], v[40:43], v[224:227]
v_mfma_f32_16x16x32_bf16 v[228:231], a[244:247], v[40:43], v[228:231]
ds_read_b64_tr_b16 v[24:25], v4 offset:6272
v_mfma_f32_16x16x32_bf16 v[232:235], a[248:251], v[40:43], v[232:235]
v_mfma_f32_16x16x32_bf16 v[236:239], a[252:255], v[40:43], v[236:239]
ds_read_b64_tr_b16 v[26:27], v4 offset:6528
s_waitcnt lgkmcnt(6)
v_mfma_f32_16x16x32_bf16 v[240:243], a[240:243], v[44:47], v[240:243]
s_add_u32 s46, s44, 12288
buffer_store_dwordx4 v[168:171], v6, s[16:19], s46 offen offset:2048 sc1
v_mfma_f32_16x16x32_bf16 v[244:247], a[244:247], v[44:47], v[244:247]
ds_read_b64_tr_b16 v[28:29], v4 offset:6288
v_mfma_f32_16x16x32_bf16 v[80:83], a[248:251], v[44:47], v[80:83]
v_mfma_f32_16x16x32_bf16 v[64:67], a[252:255], v[44:47], v[64:67]
ds_read_b64_tr_b16 v[30:31], v4 offset:6544
s_waitcnt lgkmcnt(6)
v_mfma_f32_16x16x32_bf16 v[84:87], a[0:3], v[16:19], 0
ds_read_b64_tr_b16 v[32:33], v4 offset:10496
v_mfma_f32_16x16x32_bf16 v[68:71], a[4:7], v[16:19], 0
v_mfma_f32_16x16x32_bf16 v[88:91], a[8:11], v[16:19], 0
ds_read_b64_tr_b16 v[34:35], v4 offset:10752
v_mfma_f32_16x16x32_bf16 v[72:75], a[12:15], v[16:19], 0
s_waitcnt lgkmcnt(6)
v_mfma_f32_16x16x32_bf16 v[92:95], a[0:3], v[20:23], 0
ds_read_b64_tr_b16 v[36:37], v4 offset:10512
v_mfma_f32_16x16x32_bf16 v[76:79], a[4:7], v[20:23], 0
v_mfma_f32_16x16x32_bf16 v[112:115], a[8:11], v[20:23], 0
ds_read_b64_tr_b16 v[38:39], v4 offset:10768
v_mfma_f32_16x16x32_bf16 v[96:99], a[12:15], v[20:23], 0
v_cvt_pk_bf16_f32 v172, v172, v173
v_cvt_pk_bf16_f32 v173, v174, v175
s_waitcnt lgkmcnt(6)
v_mfma_f32_16x16x32_bf16 v[84:87], a[16:19], v[24:27], v[84:87]
ds_read_b64_tr_b16 v[40:41], v4 offset:14720
v_cvt_pk_bf16_f32 v174, v188, v189
v_mfma_f32_16x16x32_bf16 v[68:71], a[20:23], v[24:27], v[68:71]
v_cvt_pk_bf16_f32 v175, v190, v191
v_mfma_f32_16x16x32_bf16 v[88:91], a[24:27], v[24:27], v[88:91]
ds_read_b64_tr_b16 v[42:43], v4 offset:14976
v_mfma_f32_16x16x32_bf16 v[72:75], a[28:31], v[24:27], v[72:75]
s_waitcnt lgkmcnt(6)
v_mfma_f32_16x16x32_bf16 v[92:95], a[16:19], v[28:31], v[92:95]
ds_read_b64_tr_b16 v[44:45], v4 offset:14736
v_mfma_f32_16x16x32_bf16 v[76:79], a[20:23], v[28:31], v[76:79]
v_mfma_f32_16x16x32_bf16 v[112:115], a[24:27], v[28:31], v[112:115]
ds_read_b64_tr_b16 v[46:47], v4 offset:14992
v_mfma_f32_16x16x32_bf16 v[96:99], a[28:31], v[28:31], v[96:99]
s_add_u32 s46, s44, 12288
buffer_store_dwordx4 v[172:175], v6, s[16:19], s46 offen offset:2176 sc1
s_waitcnt lgkmcnt(6)
v_mfma_f32_16x16x32_bf16 v[84:87], a[32:35], v[32:35], v[84:87]
s_waitcnt vmcnt(16)
s_barrier
v_mfma_f32_16x16x32_bf16 v[68:71], a[36:39], v[32:35], v[68:71]
ds_read_b64_tr_b16 v[16:17], v4 offset:18944
v_mfma_f32_16x16x32_bf16 v[88:91], a[40:43], v[32:35], v[88:91]
v_mfma_f32_16x16x32_bf16 v[72:75], a[44:47], v[32:35], v[72:75]
ds_read_b64_tr_b16 v[18:19], v4 offset:19200
s_waitcnt lgkmcnt(6)
v_mfma_f32_16x16x32_bf16 v[92:95], a[32:35], v[36:39], v[92:95]
v_mfma_f32_16x16x32_bf16 v[76:79], a[36:39], v[36:39], v[76:79]
ds_read_b64_tr_b16 v[20:21], v4 offset:18960
v_mfma_f32_16x16x32_bf16 v[112:115], a[40:43], v[36:39], v[112:115]
v_mfma_f32_16x16x32_bf16 v[96:99], a[44:47], v[36:39], v[96:99]
ds_read_b64_tr_b16 v[22:23], v4 offset:19216
s_waitcnt lgkmcnt(6)
v_mfma_f32_16x16x32_bf16 v[84:87], a[48:51], v[40:43], v[84:87]
v_mfma_f32_16x16x32_bf16 v[68:71], a[52:55], v[40:43], v[68:71]
ds_read_b64_tr_b16 v[24:25], v4 offset:23168
v_mfma_f32_16x16x32_bf16 v[88:91], a[56:59], v[40:43], v[88:91]
v_cvt_pk_bf16_f32 v192, v192, v193
v_cvt_pk_bf16_f32 v193, v194, v195
v_mfma_f32_16x16x32_bf16 v[72:75], a[60:63], v[40:43], v[72:75]
ds_read_b64_tr_b16 v[26:27], v4 offset:23424
v_cvt_pk_bf16_f32 v194, v196, v197
s_waitcnt lgkmcnt(6)
v_mfma_f32_16x16x32_bf16 v[92:95], a[48:51], v[44:47], v[92:95]
v_cvt_pk_bf16_f32 v195, v198, v199
v_mfma_f32_16x16x32_bf16 v[76:79], a[52:55], v[44:47], v[76:79]
ds_read_b64_tr_b16 v[28:29], v4 offset:23184
v_mfma_f32_16x16x32_bf16 v[112:115], a[56:59], v[44:47], v[112:115]
v_mfma_f32_16x16x32_bf16 v[96:99], a[60:63], v[44:47], v[96:99]
ds_read_b64_tr_b16 v[30:31], v4 offset:23440
s_waitcnt lgkmcnt(6)
v_mfma_f32_16x16x32_bf16 v[84:87], a[64:67], v[16:19], v[84:87]
ds_read_b64_tr_b16 v[32:33], v4 offset:27392
v_mfma_f32_16x16x32_bf16 v[68:71], a[68:71], v[16:19], v[68:71]
v_mfma_f32_16x16x32_bf16 v[88:91], a[72:75], v[16:19], v[88:91]
ds_read_b64_tr_b16 v[34:35], v4 offset:27648
s_add_u32 s46, s45, 0
buffer_store_dwordx4 v[192:195], v7, s[16:19], s46 offen offset:0 sc1
v_mfma_f32_16x16x32_bf16 v[72:75], a[76:79], v[16:19], v[72:75]
s_waitcnt lgkmcnt(6)
v_mfma_f32_16x16x32_bf16 v[92:95], a[64:67], v[20:23], v[92:95]
ds_read_b64_tr_b16 v[36:37], v4 offset:27408
v_mfma_f32_16x16x32_bf16 v[76:79], a[68:71], v[20:23], v[76:79]
v_mfma_f32_16x16x32_bf16 v[112:115], a[72:75], v[20:23], v[112:115]
ds_read_b64_tr_b16 v[38:39], v4 offset:27664
v_mfma_f32_16x16x32_bf16 v[96:99], a[76:79], v[20:23], v[96:99]
s_waitcnt lgkmcnt(6)
v_mfma_f32_16x16x32_bf16 v[84:87], a[80:83], v[24:27], v[84:87]
ds_read_b64_tr_b16 v[40:41], v4 offset:31616
v_mfma_f32_16x16x32_bf16 v[68:71], a[84:87], v[24:27], v[68:71]
v_mfma_f32_16x16x32_bf16 v[88:91], a[88:91], v[24:27], v[88:91]
ds_read_b64_tr_b16 v[42:43], v4 offset:31872
v_mfma_f32_16x16x32_bf16 v[72:75], a[92:95], v[24:27], v[72:75]
s_waitcnt lgkmcnt(6)
v_mfma_f32_16x16x32_bf16 v[92:95], a[80:83], v[28:31], v[92:95]
ds_read_b64_tr_b16 v[44:45], v4 offset:31632
v_cvt_pk_bf16_f32 v200, v200, v201
v_mfma_f32_16x16x32_bf16 v[76:79], a[84:87], v[28:31], v[76:79]
v_cvt_pk_bf16_f32 v201, v202, v203
v_cvt_pk_bf16_f32 v202, v204, v205
v_mfma_f32_16x16x32_bf16 v[112:115], a[88:91], v[28:31], v[112:115]
ds_read_b64_tr_b16 v[46:47], v4 offset:31888
v_cvt_pk_bf16_f32 v203, v206, v207
v_mfma_f32_16x16x32_bf16 v[96:99], a[92:95], v[28:31], v[96:99]
s_waitcnt lgkmcnt(6)
v_mfma_f32_16x16x32_bf16 v[84:87], a[96:99], v[32:35], v[84:87]
s_waitcnt vmcnt(11)
s_barrier
v_mfma_f32_16x16x32_bf16 v[68:71], a[100:103], v[32:35], v[68:71]
ds_read_b64_tr_b16 v[16:17], v4 offset:35840
v_mfma_f32_16x16x32_bf16 v[88:91], a[104:107], v[32:35], v[88:91]
v_mfma_f32_16x16x32_bf16 v[72:75], a[108:111], v[32:35], v[72:75]
ds_read_b64_tr_b16 v[18:19], v4 offset:36096
s_waitcnt lgkmcnt(6)
v_mfma_f32_16x16x32_bf16 v[92:95], a[96:99], v[36:39], v[92:95]
s_add_u32 s46, s45, 8192
buffer_store_dwordx4 v[200:203], v7, s[16:19], s46 offen offset:0 sc1
v_mfma_f32_16x16x32_bf16 v[76:79], a[100:103], v[36:39], v[76:79]
ds_read_b64_tr_b16 v[20:21], v4 offset:35856
v_mfma_f32_16x16x32_bf16 v[112:115], a[104:107], v[36:39], v[112:115]
v_mfma_f32_16x16x32_bf16 v[96:99], a[108:111], v[36:39], v[96:99]
ds_read_b64_tr_b16 v[22:23], v4 offset:36112
s_waitcnt lgkmcnt(6)
v_mfma_f32_16x16x32_bf16 v[84:87], a[112:115], v[40:43], v[84:87]
v_mfma_f32_16x16x32_bf16 v[68:71], a[116:119], v[40:43], v[68:71]
ds_read_b64_tr_b16 v[24:25], v4 offset:40064
v_mfma_f32_16x16x32_bf16 v[88:91], a[120:123], v[40:43], v[88:91]
v_mfma_f32_16x16x32_bf16 v[72:75], a[124:127], v[40:43], v[72:75]
ds_read_b64_tr_b16 v[26:27], v4 offset:40320
s_waitcnt lgkmcnt(6)
v_mfma_f32_16x16x32_bf16 v[92:95], a[112:115], v[44:47], v[92:95]
v_mfma_f32_16x16x32_bf16 v[76:79], a[116:119], v[44:47], v[76:79]
ds_read_b64_tr_b16 v[28:29], v4 offset:40080
v_mfma_f32_16x16x32_bf16 v[112:115], a[120:123], v[44:47], v[112:115]
v_mfma_f32_16x16x32_bf16 v[96:99], a[124:127], v[44:47], v[96:99]
ds_read_b64_tr_b16 v[30:31], v4 offset:40336
v_cvt_pk_bf16_f32 v208, v208, v209
s_waitcnt lgkmcnt(6)
v_mfma_f32_16x16x32_bf16 v[84:87], a[128:131], v[16:19], v[84:87]
ds_read_b64_tr_b16 v[32:33], v4 offset:44288
v_cvt_pk_bf16_f32 v209, v210, v211
v_mfma_f32_16x16x32_bf16 v[68:71], a[132:135], v[16:19], v[68:71]
v_cvt_pk_bf16_f32 v210, v212, v213
v_cvt_pk_bf16_f32 v211, v214, v215
v_mfma_f32_16x16x32_bf16 v[88:91], a[136:139], v[16:19], v[88:91]
ds_read_b64_tr_b16 v[34:35], v4 offset:44544
v_mfma_f32_16x16x32_bf16 v[72:75], a[140:143], v[16:19], v[72:75]
s_waitcnt lgkmcnt(6)
v_mfma_f32_16x16x32_bf16 v[92:95], a[128:131], v[20:23], v[92:95]
ds_read_b64_tr_b16 v[36:37], v4 offset:44304
v_mfma_f32_16x16x32_bf16 v[76:79], a[132:135], v[20:23], v[76:79]
v_mfma_f32_16x16x32_bf16 v[112:115], a[136:139], v[20:23], v[112:115]
ds_read_b64_tr_b16 v[38:39], v4 offset:44560
v_mfma_f32_16x16x32_bf16 v[96:99], a[140:143], v[20:23], v[96:99]
s_add_u32 s46, s45, 0
buffer_store_dwordx4 v[208:211], v7, s[16:19], s46 offen offset:128 sc1
s_waitcnt lgkmcnt(6)
v_mfma_f32_16x16x32_bf16 v[84:87], a[144:147], v[24:27], v[84:87]
ds_read_b64_tr_b16 v[40:41], v4 offset:48512
v_mfma_f32_16x16x32_bf16 v[68:71], a[148:151], v[24:27], v[68:71]
v_mfma_f32_16x16x32_bf16 v[88:91], a[152:155], v[24:27], v[88:91]
ds_read_b64_tr_b16 v[42:43], v4 offset:48768
v_mfma_f32_16x16x32_bf16 v[72:75], a[156:159], v[24:27], v[72:75]
s_waitcnt lgkmcnt(6)
v_mfma_f32_16x16x32_bf16 v[92:95], a[144:147], v[28:31], v[92:95]
ds_read_b64_tr_b16 v[44:45], v4 offset:48528
v_mfma_f32_16x16x32_bf16 v[76:79], a[148:151], v[28:31], v[76:79]
v_mfma_f32_16x16x32_bf16 v[112:115], a[152:155], v[28:31], v[112:115]
ds_read_b64_tr_b16 v[46:47], v4 offset:48784
v_mfma_f32_16x16x32_bf16 v[96:99], a[156:159], v[28:31], v[96:99]
s_waitcnt lgkmcnt(6)
v_mfma_f32_16x16x32_bf16 v[84:87], a[160:163], v[32:35], v[84:87]
s_waitcnt vmcnt(7)
s_barrier
v_mfma_f32_16x16x32_bf16 v[68:71], a[164:167], v[32:35], v[68:71]
ds_read_b64_tr_b16 v[16:17], v4 offset:52736
v_cvt_pk_bf16_f32 v216, v216, v217
v_mfma_f32_16x16x32_bf16 v[88:91], a[168:171], v[32:35], v[88:91]
v_cvt_pk_bf16_f32 v217, v218, v219
v_cvt_pk_bf16_f32 v218, v220, v221
v_mfma_f32_16x16x32_bf16 v[72:75], a[172:175], v[32:35], v[72:75]
ds_read_b64_tr_b16 v[18:19], v4 offset:52992
v_cvt_pk_bf16_f32 v219, v222, v223
s_waitcnt lgkmcnt(6)
v_mfma_f32_16x16x32_bf16 v[92:95], a[160:163], v[36:39], v[92:95]
v_mfma_f32_16x16x32_bf16 v[76:79], a[164:167], v[36:39], v[76:79]
ds_read_b64_tr_b16 v[20:21], v4 offset:52752
v_mfma_f32_16x16x32_bf16 v[112:115], a[168:171], v[36:39], v[112:115]
v_mfma_f32_16x16x32_bf16 v[96:99], a[172:175], v[36:39], v[96:99]
ds_read_b64_tr_b16 v[22:23], v4 offset:53008
s_waitcnt lgkmcnt(6)
v_mfma_f32_16x16x32_bf16 v[84:87], a[176:179], v[40:43], v[84:87]
v_mfma_f32_16x16x32_bf16 v[68:71], a[180:183], v[40:43], v[68:71]
ds_read_b64_tr_b16 v[24:25], v4 offset:56960
s_add_u32 s46, s45, 8192
buffer_store_dwordx4 v[216:219], v7, s[16:19], s46 offen offset:128 sc1
v_mfma_f32_16x16x32_bf16 v[88:91], a[184:187], v[40:43], v[88:91]
v_mfma_f32_16x16x32_bf16 v[72:75], a[188:191], v[40:43], v[72:75]
ds_read_b64_tr_b16 v[26:27], v4 offset:57216
s_waitcnt lgkmcnt(6)
v_mfma_f32_16x16x32_bf16 v[92:95], a[176:179], v[44:47], v[92:95]
v_mfma_f32_16x16x32_bf16 v[76:79], a[180:183], v[44:47], v[76:79]
ds_read_b64_tr_b16 v[28:29], v4 offset:56976
v_mfma_f32_16x16x32_bf16 v[112:115], a[184:187], v[44:47], v[112:115]
v_mfma_f32_16x16x32_bf16 v[96:99], a[188:191], v[44:47], v[96:99]
ds_read_b64_tr_b16 v[30:31], v4 offset:57232
s_waitcnt lgkmcnt(6)
v_mfma_f32_16x16x32_bf16 v[84:87], a[192:195], v[16:19], v[84:87]
ds_read_b64_tr_b16 v[32:33], v4 offset:61184
v_mfma_f32_16x16x32_bf16 v[68:71], a[196:199], v[16:19], v[68:71]
v_mfma_f32_16x16x32_bf16 v[88:91], a[200:203], v[16:19], v[88:91]
ds_read_b64_tr_b16 v[34:35], v4 offset:61440
v_mfma_f32_16x16x32_bf16 v[72:75], a[204:207], v[16:19], v[72:75]
s_waitcnt lgkmcnt(6)
v_mfma_f32_16x16x32_bf16 v[92:95], a[192:195], v[20:23], v[92:95]
ds_read_b64_tr_b16 v[36:37], v4 offset:61200
v_cvt_pk_bf16_f32 v224, v224, v225
v_mfma_f32_16x16x32_bf16 v[76:79], a[196:199], v[20:23], v[76:79]
v_cvt_pk_bf16_f32 v225, v226, v227
v_cvt_pk_bf16_f32 v226, v228, v229
v_mfma_f32_16x16x32_bf16 v[112:115], a[200:203], v[20:23], v[112:115]
ds_read_b64_tr_b16 v[38:39], v4 offset:61456
v_cvt_pk_bf16_f32 v227, v230, v231
v_mfma_f32_16x16x32_bf16 v[96:99], a[204:207], v[20:23], v[96:99]
s_waitcnt lgkmcnt(6)
v_mfma_f32_16x16x32_bf16 v[84:87], a[208:211], v[24:27], v[84:87]
ds_read_b64_tr_b16 v[40:41], v4 offset:65408
v_mfma_f32_16x16x32_bf16 v[68:71], a[212:215], v[24:27], v[68:71]
v_mfma_f32_16x16x32_bf16 v[88:91], a[216:219], v[24:27], v[88:91]
ds_read_b64_tr_b16 v[42:43], v5 offset:128
v_mfma_f32_16x16x32_bf16 v[72:75], a[220:223], v[24:27], v[72:75]
s_waitcnt lgkmcnt(6)
v_mfma_f32_16x16x32_bf16 v[92:95], a[208:211], v[28:31], v[92:95]
ds_read_b64_tr_b16 v[44:45], v4 offset:65424
s_add_u32 s46, s45, 0
buffer_store_dwordx4 v[224:227], v7, s[16:19], s46 offen offset:1024 sc1
v_mfma_f32_16x16x32_bf16 v[76:79], a[212:215], v[28:31], v[76:79]
v_mfma_f32_16x16x32_bf16 v[112:115], a[216:219], v[28:31], v[112:115]
ds_read_b64_tr_b16 v[46:47], v5 offset:144
v_mfma_f32_16x16x32_bf16 v[96:99], a[220:223], v[28:31], v[96:99]
s_waitcnt lgkmcnt(6)
v_mfma_f32_16x16x32_bf16 v[84:87], a[224:227], v[32:35], v[84:87]
ds_read_b64_tr_b16 v[16:17], v4 offset:2112
v_mfma_f32_16x16x32_bf16 v[68:71], a[228:231], v[32:35], v[68:71]
v_mfma_f32_16x16x32_bf16 v[88:91], a[232:235], v[32:35], v[88:91]
ds_read_b64_tr_b16 v[18:19], v4 offset:2368
v_mfma_f32_16x16x32_bf16 v[72:75], a[236:239], v[32:35], v[72:75]
s_waitcnt lgkmcnt(6)
v_mfma_f32_16x16x32_bf16 v[92:95], a[224:227], v[36:39], v[92:95]
ds_read_b64_tr_b16 v[20:21], v4 offset:2128
v_mfma_f32_16x16x32_bf16 v[76:79], a[228:231], v[36:39], v[76:79]
v_mfma_f32_16x16x32_bf16 v[112:115], a[232:235], v[36:39], v[112:115]
ds_read_b64_tr_b16 v[22:23], v4 offset:2384
v_cvt_pk_bf16_f32 v232, v232, v233
v_mfma_f32_16x16x32_bf16 v[96:99], a[236:239], v[36:39], v[96:99]
v_cvt_pk_bf16_f32 v233, v234, v235
v_cvt_pk_bf16_f32 v234, v236, v237
s_waitcnt lgkmcnt(6)
v_mfma_f32_16x16x32_bf16 v[84:87], a[240:243], v[40:43], v[84:87]
ds_read_b64_tr_b16 v[24:25], v4 offset:6336
v_cvt_pk_bf16_f32 v235, v238, v239
v_mfma_f32_16x16x32_bf16 v[68:71], a[244:247], v[40:43], v[68:71]
v_mfma_f32_16x16x32_bf16 v[88:91], a[248:251], v[40:43], v[88:91]
ds_read_b64_tr_b16 v[26:27], v4 offset:6592
v_mfma_f32_16x16x32_bf16 v[72:75], a[252:255], v[40:43], v[72:75]
s_waitcnt lgkmcnt(6)
v_mfma_f32_16x16x32_bf16 v[92:95], a[240:243], v[44:47], v[92:95]
ds_read_b64_tr_b16 v[28:29], v4 offset:6352
v_mfma_f32_16x16x32_bf16 v[76:79], a[244:247], v[44:47], v[76:79]
v_mfma_f32_16x16x32_bf16 v[112:115], a[248:251], v[44:47], v[112:115]
ds_read_b64_tr_b16 v[30:31], v4 offset:6608
s_add_u32 s46, s45, 8192
buffer_store_dwordx4 v[232:235], v7, s[16:19], s46 offen offset:1024 sc1
v_mfma_f32_16x16x32_bf16 v[96:99], a[252:255], v[44:47], v[96:99]
s_waitcnt lgkmcnt(6)
v_mfma_f32_16x16x32_bf16 v[116:119], a[0:3], v[16:19], 0
ds_read_b64_tr_b16 v[32:33], v4 offset:10560
v_mfma_f32_16x16x32_bf16 v[100:103], a[4:7], v[16:19], 0
v_mfma_f32_16x16x32_bf16 v[120:123], a[8:11], v[16:19], 0
ds_read_b64_tr_b16 v[34:35], v4 offset:10816
v_mfma_f32_16x16x32_bf16 v[104:107], a[12:15], v[16:19], 0
s_waitcnt lgkmcnt(6)
v_mfma_f32_16x16x32_bf16 v[124:127], a[0:3], v[20:23], 0
ds_read_b64_tr_b16 v[36:37], v4 offset:10576
v_mfma_f32_16x16x32_bf16 v[108:111], a[4:7], v[20:23], 0
v_mfma_f32_16x16x32_bf16 v[144:147], a[8:11], v[20:23], 0
ds_read_b64_tr_b16 v[38:39], v4 offset:10832
v_mfma_f32_16x16x32_bf16 v[128:131], a[12:15], v[20:23], 0
s_waitcnt lgkmcnt(6)
v_mfma_f32_16x16x32_bf16 v[116:119], a[16:19], v[24:27], v[116:119]
ds_read_b64_tr_b16 v[40:41], v4 offset:14784
v_mfma_f32_16x16x32_bf16 v[100:103], a[20:23], v[24:27], v[100:103]
v_cvt_pk_bf16_f32 v240, v240, v241
v_cvt_pk_bf16_f32 v241, v242, v243
v_mfma_f32_16x16x32_bf16 v[120:123], a[24:27], v[24:27], v[120:123]
ds_read_b64_tr_b16 v[42:43], v4 offset:15040
v_cvt_pk_bf16_f32 v242, v244, v245
v_mfma_f32_16x16x32_bf16 v[104:107], a[28:31], v[24:27], v[104:107]
v_cvt_pk_bf16_f32 v243, v246, v247
s_waitcnt lgkmcnt(6)
v_mfma_f32_16x16x32_bf16 v[124:127], a[16:19], v[28:31], v[124:127]
ds_read_b64_tr_b16 v[44:45], v4 offset:14800
v_mfma_f32_16x16x32_bf16 v[108:111], a[20:23], v[28:31], v[108:111]
v_mfma_f32_16x16x32_bf16 v[144:147], a[24:27], v[28:31], v[144:147]
ds_read_b64_tr_b16 v[46:47], v4 offset:15056
v_mfma_f32_16x16x32_bf16 v[128:131], a[28:31], v[28:31], v[128:131]
s_waitcnt lgkmcnt(6)
v_mfma_f32_16x16x32_bf16 v[116:119], a[32:35], v[32:35], v[116:119]
ds_read_b64_tr_b16 v[16:17], v4 offset:19008
v_mfma_f32_16x16x32_bf16 v[100:103], a[36:39], v[32:35], v[100:103]
s_add_u32 s46, s45, 0
buffer_store_dwordx4 v[240:243], v7, s[16:19], s46 offen offset:1152 sc1
v_mfma_f32_16x16x32_bf16 v[120:123], a[40:43], v[32:35], v[120:123]
ds_read_b64_tr_b16 v[18:19], v4 offset:19264
v_mfma_f32_16x16x32_bf16 v[104:107], a[44:47], v[32:35], v[104:107]
s_waitcnt lgkmcnt(6)
v_mfma_f32_16x16x32_bf16 v[124:127], a[32:35], v[36:39], v[124:127]
ds_read_b64_tr_b16 v[20:21], v4 offset:19024
v_mfma_f32_16x16x32_bf16 v[108:111], a[36:39], v[36:39], v[108:111]
v_mfma_f32_16x16x32_bf16 v[144:147], a[40:43], v[36:39], v[144:147]
ds_read_b64_tr_b16 v[22:23], v4 offset:19280
v_mfma_f32_16x16x32_bf16 v[128:131], a[44:47], v[36:39], v[128:131]
s_waitcnt lgkmcnt(6)
v_mfma_f32_16x16x32_bf16 v[116:119], a[48:51], v[40:43], v[116:119]
ds_read_b64_tr_b16 v[24:25], v4 offset:23232
v_mfma_f32_16x16x32_bf16 v[100:103], a[52:55], v[40:43], v[100:103]
v_mfma_f32_16x16x32_bf16 v[120:123], a[56:59], v[40:43], v[120:123]
ds_read_b64_tr_b16 v[26:27], v4 offset:23488
v_mfma_f32_16x16x32_bf16 v[104:107], a[60:63], v[40:43], v[104:107]
s_waitcnt lgkmcnt(6)
v_mfma_f32_16x16x32_bf16 v[124:127], a[48:51], v[44:47], v[124:127]
ds_read_b64_tr_b16 v[28:29], v4 offset:23248
v_cvt_pk_bf16_f32 v80, v80, v81
v_mfma_f32_16x16x32_bf16 v[108:111], a[52:55], v[44:47], v[108:111]
v_cvt_pk_bf16_f32 v81, v82, v83
v_cvt_pk_bf16_f32 v82, v64, v65
v_mfma_f32_16x16x32_bf16 v[144:147], a[56:59], v[44:47], v[144:147]
ds_read_b64_tr_b16 v[30:31], v4 offset:23504
v_cvt_pk_bf16_f32 v83, v66, v67
v_mfma_f32_16x16x32_bf16 v[128:131], a[60:63], v[44:47], v[128:131]
s_waitcnt lgkmcnt(6)
v_mfma_f32_16x16x32_bf16 v[116:119], a[64:67], v[16:19], v[116:119]
ds_read_b64_tr_b16 v[32:33], v4 offset:27456
v_mfma_f32_16x16x32_bf16 v[100:103], a[68:71], v[16:19], v[100:103]
v_mfma_f32_16x16x32_bf16 v[120:123], a[72:75], v[16:19], v[120:123]
ds_read_b64_tr_b16 v[34:35], v4 offset:27712
v_mfma_f32_16x16x32_bf16 v[104:107], a[76:79], v[16:19], v[104:107]
s_waitcnt lgkmcnt(6)
v_mfma_f32_16x16x32_bf16 v[124:127], a[64:67], v[20:23], v[124:127]
ds_read_b64_tr_b16 v[36:37], v4 offset:27472
s_add_u32 s46, s45, 8192
buffer_store_dwordx4 v[80:83], v7, s[16:19], s46 offen offset:1152 sc1
v_mfma_f32_16x16x32_bf16 v[108:111], a[68:71], v[20:23], v[108:111]
v_mfma_f32_16x16x32_bf16 v[144:147], a[72:75], v[20:23], v[144:147]
ds_read_b64_tr_b16 v[38:39], v4 offset:27728
v_mfma_f32_16x16x32_bf16 v[128:131], a[76:79], v[20:23], v[128:131]
s_waitcnt lgkmcnt(6)
v_mfma_f32_16x16x32_bf16 v[116:119], a[80:83], v[24:27], v[116:119]
ds_read_b64_tr_b16 v[40:41], v4 offset:31680
v_mfma_f32_16x16x32_bf16 v[100:103], a[84:87], v[24:27], v[100:103]
v_mfma_f32_16x16x32_bf16 v[120:123], a[88:91], v[24:27], v[120:123]
ds_read_b64_tr_b16 v[42:43], v4 offset:31936
v_mfma_f32_16x16x32_bf16 v[104:107], a[92:95], v[24:27], v[104:107]
s_waitcnt lgkmcnt(6)
v_mfma_f32_16x16x32_bf16 v[124:127], a[80:83], v[28:31], v[124:127]
ds_read_b64_tr_b16 v[44:45], v4 offset:31696
v_mfma_f32_16x16x32_bf16 v[108:111], a[84:87], v[28:31], v[108:111]
v_mfma_f32_16x16x32_bf16 v[144:147], a[88:91], v[28:31], v[144:147]
ds_read_b64_tr_b16 v[46:47], v4 offset:31952
v_cvt_pk_bf16_f32 v84, v84, v85
v_mfma_f32_16x16x32_bf16 v[128:131], a[92:95], v[28:31], v[128:131]
v_cvt_pk_bf16_f32 v85, v86, v87
v_cvt_pk_bf16_f32 v86, v68, v69
s_waitcnt lgkmcnt(6)
v_mfma_f32_16x16x32_bf16 v[116:119], a[96:99], v[32:35], v[116:119]
ds_read_b64_tr_b16 v[16:17], v4 offset:35904
v_cvt_pk_bf16_f32 v87, v70, v71
v_mfma_f32_16x16x32_bf16 v[100:103], a[100:103], v[32:35], v[100:103]
v_mfma_f32_16x16x32_bf16 v[120:123], a[104:107], v[32:35], v[120:123]
ds_read_b64_tr_b16 v[18:19], v4 offset:36160
v_mfma_f32_16x16x32_bf16 v[104:107], a[108:111], v[32:35], v[104:107]
s_waitcnt lgkmcnt(6)
v_mfma_f32_16x16x32_bf16 v[124:127], a[96:99], v[36:39], v[124:127]
ds_read_b64_tr_b16 v[20:21], v4 offset:35920
v_mfma_f32_16x16x32_bf16 v[108:111], a[100:103], v[36:39], v[108:111]
v_mfma_f32_16x16x32_bf16 v[144:147], a[104:107], v[36:39], v[144:147]
ds_read_b64_tr_b16 v[22:23], v4 offset:36176
s_add_u32 s46, s45, 0
buffer_store_dwordx4 v[84:87], v7, s[16:19], s46 offen offset:2048 sc1
v_mfma_f32_16x16x32_bf16 v[128:131], a[108:111], v[36:39], v[128:131]
s_waitcnt lgkmcnt(6)
v_mfma_f32_16x16x32_bf16 v[116:119], a[112:115], v[40:43], v[116:119]
ds_read_b64_tr_b16 v[24:25], v4 offset:40128
v_mfma_f32_16x16x32_bf16 v[100:103], a[116:119], v[40:43], v[100:103]
v_mfma_f32_16x16x32_bf16 v[120:123], a[120:123], v[40:43], v[120:123]
ds_read_b64_tr_b16 v[26:27], v4 offset:40384
v_mfma_f32_16x16x32_bf16 v[104:107], a[124:127], v[40:43], v[104:107]
s_waitcnt lgkmcnt(6)
v_mfma_f32_16x16x32_bf16 v[124:127], a[112:115], v[44:47], v[124:127]
ds_read_b64_tr_b16 v[28:29], v4 offset:40144
v_mfma_f32_16x16x32_bf16 v[108:111], a[116:119], v[44:47], v[108:111]
v_mfma_f32_16x16x32_bf16 v[144:147], a[120:123], v[44:47], v[144:147]
ds_read_b64_tr_b16 v[30:31], v4 offset:40400
v_mfma_f32_16x16x32_bf16 v[128:131], a[124:127], v[44:47], v[128:131]
s_waitcnt lgkmcnt(6)
v_mfma_f32_16x16x32_bf16 v[116:119], a[128:131], v[16:19], v[116:119]
ds_read_b64_tr_b16 v[32:33], v4 offset:44352
v_mfma_f32_16x16x32_bf16 v[100:103], a[132:135], v[16:19], v[100:103]
v_cvt_pk_bf16_f32 v88, v88, v89
v_cvt_pk_bf16_f32 v89, v90, v91
v_mfma_f32_16x16x32_bf16 v[120:123], a[136:139], v[16:19], v[120:123]
ds_read_b64_tr_b16 v[34:35], v4 offset:44608
v_cvt_pk_bf16_f32 v90, v72, v73
v_mfma_f32_16x16x32_bf16 v[104:107], a[140:143], v[16:19], v[104:107]
v_cvt_pk_bf16_f32 v91, v74, v75
s_waitcnt lgkmcnt(6)
v_mfma_f32_16x16x32_bf16 v[124:127], a[128:131], v[20:23], v[124:127]
ds_read_b64_tr_b16 v[36:37], v4 offset:44368
v_mfma_f32_16x16x32_bf16 v[108:111], a[132:135], v[20:23], v[108:111]
v_mfma_f32_16x16x32_bf16 v[144:147], a[136:139], v[20:23], v[144:147]
ds_read_b64_tr_b16 v[38:39], v4 offset:44624
v_mfma_f32_16x16x32_bf16 v[128:131], a[140:143], v[20:23], v[128:131]
s_waitcnt lgkmcnt(6)
v_mfma_f32_16x16x32_bf16 v[116:119], a[144:147], v[24:27], v[116:119]
ds_read_b64_tr_b16 v[40:41], v4 offset:48576
v_mfma_f32_16x16x32_bf16 v[100:103], a[148:151], v[24:27], v[100:103]
s_add_u32 s46, s45, 8192
buffer_store_dwordx4 v[88:91], v7, s[16:19], s46 offen offset:2048 sc1
v_mfma_f32_16x16x32_bf16 v[120:123], a[152:155], v[24:27], v[120:123]
ds_read_b64_tr_b16 v[42:43], v4 offset:48832
v_mfma_f32_16x16x32_bf16 v[104:107], a[156:159], v[24:27], v[104:107]
s_waitcnt lgkmcnt(6)
v_mfma_f32_16x16x32_bf16 v[124:127], a[144:147], v[28:31], v[124:127]
ds_read_b64_tr_b16 v[44:45], v4 offset:48592
v_mfma_f32_16x16x32_bf16 v[108:111], a[148:151], v[28:31], v[108:111]
v_mfma_f32_16x16x32_bf16 v[144:147], a[152:155], v[28:31], v[144:147]
ds_read_b64_tr_b16 v[46:47], v4 offset:48848
v_mfma_f32_16x16x32_bf16 v[128:131], a[156:159], v[28:31], v[128:131]
s_waitcnt lgkmcnt(6)
v_mfma_f32_16x16x32_bf16 v[116:119], a[160:163], v[32:35], v[116:119]
ds_read_b64_tr_b16 v[16:17], v4 offset:52800
v_mfma_f32_16x16x32_bf16 v[100:103], a[164:167], v[32:35], v[100:103]
v_mfma_f32_16x16x32_bf16 v[120:123], a[168:171], v[32:35], v[120:123]
ds_read_b64_tr_b16 v[18:19], v4 offset:53056
v_mfma_f32_16x16x32_bf16 v[104:107], a[172:175], v[32:35], v[104:107]
v_cvt_pk_bf16_f32 v92, v92, v93
v_cvt_pk_bf16_f32 v93, v94, v95
s_waitcnt lgkmcnt(6)
v_mfma_f32_16x16x32_bf16 v[124:127], a[160:163], v[36:39], v[124:127]
ds_read_b64_tr_b16 v[20:21], v4 offset:52816
v_cvt_pk_bf16_f32 v94, v76, v77
v_mfma_f32_16x16x32_bf16 v[108:111], a[164:167], v[36:39], v[108:111]
v_cvt_pk_bf16_f32 v95, v78, v79
v_mfma_f32_16x16x32_bf16 v[144:147], a[168:171], v[36:39], v[144:147]
ds_read_b64_tr_b16 v[22:23], v4 offset:53072
v_mfma_f32_16x16x32_bf16 v[128:131], a[172:175], v[36:39], v[128:131]
s_waitcnt lgkmcnt(6)
v_mfma_f32_16x16x32_bf16 v[116:119], a[176:179], v[40:43], v[116:119]
ds_read_b64_tr_b16 v[24:25], v4 offset:57024
v_mfma_f32_16x16x32_bf16 v[100:103], a[180:183], v[40:43], v[100:103]
v_mfma_f32_16x16x32_bf16 v[120:123], a[184:187], v[40:43], v[120:123]
ds_read_b64_tr_b16 v[26:27], v4 offset:57280
v_mfma_f32_16x16x32_bf16 v[104:107], a[188:191], v[40:43], v[104:107]
s_add_u32 s46, s45, 0
buffer_store_dwordx4 v[92:95], v7, s[16:19], s46 offen offset:2176 sc1
s_waitcnt lgkmcnt(6)
v_mfma_f32_16x16x32_bf16 v[124:127], a[176:179], v[44:47], v[124:127]
ds_read_b64_tr_b16 v[28:29], v4 offset:57040
v_mfma_f32_16x16x32_bf16 v[108:111], a[180:183], v[44:47], v[108:111]
v_mfma_f32_16x16x32_bf16 v[144:147], a[184:187], v[44:47], v[144:147]
ds_read_b64_tr_b16 v[30:31], v4 offset:57296
v_mfma_f32_16x16x32_bf16 v[128:131], a[188:191], v[44:47], v[128:131]
s_waitcnt lgkmcnt(6)
v_mfma_f32_16x16x32_bf16 v[116:119], a[192:195], v[16:19], v[116:119]
ds_read_b64_tr_b16 v[32:33], v4 offset:61248
v_mfma_f32_16x16x32_bf16 v[100:103], a[196:199], v[16:19], v[100:103]
v_mfma_f32_16x16x32_bf16 v[120:123], a[200:203], v[16:19], v[120:123]
ds_read_b64_tr_b16 v[34:35], v4 offset:61504
v_mfma_f32_16x16x32_bf16 v[104:107], a[204:207], v[16:19], v[104:107]
s_waitcnt lgkmcnt(6)
v_mfma_f32_16x16x32_bf16 v[124:127], a[192:195], v[20:23], v[124:127]
ds_read_b64_tr_b16 v[36:37], v4 offset:61264
v_mfma_f32_16x16x32_bf16 v[108:111], a[196:199], v[20:23], v[108:111]
v_mfma_f32_16x16x32_bf16 v[144:147], a[200:203], v[20:23], v[144:147]
ds_read_b64_tr_b16 v[38:39], v4 offset:61520
v_cvt_pk_bf16_f32 v112, v112, v113
v_mfma_f32_16x16x32_bf16 v[128:131], a[204:207], v[20:23], v[128:131]
v_cvt_pk_bf16_f32 v113, v114, v115
v_cvt_pk_bf16_f32 v114, v96, v97
s_waitcnt lgkmcnt(6)
v_mfma_f32_16x16x32_bf16 v[116:119], a[208:211], v[24:27], v[116:119]
ds_read_b64_tr_b16 v[40:41], v4 offset:65472
v_cvt_pk_bf16_f32 v115, v98, v99
v_mfma_f32_16x16x32_bf16 v[100:103], a[212:215], v[24:27], v[100:103]
v_mfma_f32_16x16x32_bf16 v[120:123], a[216:219], v[24:27], v[120:123]
ds_read_b64_tr_b16 v[42:43], v5 offset:192
v_mfma_f32_16x16x32_bf16 v[104:107], a[220:223], v[24:27], v[104:107]
s_waitcnt lgkmcnt(6)
v_mfma_f32_16x16x32_bf16 v[124:127], a[208:211], v[28:31], v[124:127]
ds_read_b64_tr_b16 v[44:45], v4 offset:65488
v_mfma_f32_16x16x32_bf16 v[108:111], a[212:215], v[28:31], v[108:111]
v_mfma_f32_16x16x32_bf16 v[144:147], a[216:219], v[28:31], v[144:147]
ds_read_b64_tr_b16 v[46:47], v5 offset:208
s_add_u32 s46, s45, 8192
buffer_store_dwordx4 v[112:115], v7, s[16:19], s46 offen offset:2176 sc1
v_mfma_f32_16x16x32_bf16 v[128:131], a[220:223], v[28:31], v[128:131]
s_waitcnt lgkmcnt(6)
v_mfma_f32_16x16x32_bf16 v[116:119], a[224:227], v[32:35], v[116:119]
v_mfma_f32_16x16x32_bf16 v[100:103], a[228:231], v[32:35], v[100:103]
v_mfma_f32_16x16x32_bf16 v[120:123], a[232:235], v[32:35], v[120:123]
v_mfma_f32_16x16x32_bf16 v[104:107], a[236:239], v[32:35], v[104:107]
s_waitcnt lgkmcnt(4)
v_mfma_f32_16x16x32_bf16 v[124:127], a[224:227], v[36:39], v[124:127]
v_mfma_f32_16x16x32_bf16 v[108:111], a[228:231], v[36:39], v[108:111]
v_mfma_f32_16x16x32_bf16 v[144:147], a[232:235], v[36:39], v[144:147]
v_mfma_f32_16x16x32_bf16 v[128:131], a[236:239], v[36:39], v[128:131]
s_waitcnt lgkmcnt(2)
v_mfma_f32_16x16x32_bf16 v[116:119], a[240:243], v[40:43], v[116:119]
v_mfma_f32_16x16x32_bf16 v[100:103], a[244:247], v[40:43], v[100:103]
v_mfma_f32_16x16x32_bf16 v[120:123], a[248:251], v[40:43], v[120:123]
v_mfma_f32_16x16x32_bf16 v[104:107], a[252:255], v[40:43], v[104:107]
s_waitcnt lgkmcnt(0)
v_mfma_f32_16x16x32_bf16 v[124:127], a[240:243], v[44:47], v[124:127]
v_mfma_f32_16x16x32_bf16 v[108:111], a[244:247], v[44:47], v[108:111]
v_mfma_f32_16x16x32_bf16 v[144:147], a[248:251], v[44:47], v[144:147]
v_mfma_f32_16x16x32_bf16 v[128:131], a[252:255], v[44:47], v[128:131]
v_cvt_pk_bf16_f32 v116, v116, v117
v_cvt_pk_bf16_f32 v117, v118, v119
s_nop 7
s_nop 3
v_cvt_pk_bf16_f32 v118, v100, v101
v_cvt_pk_bf16_f32 v119, v102, v103
s_add_u32 s46, s45, 0
buffer_store_dwordx4 v[116:119], v7, s[16:19], s46 offen offset:3072 sc1
v_cvt_pk_bf16_f32 v120, v120, v121
v_cvt_pk_bf16_f32 v121, v122, v123
v_cvt_pk_bf16_f32 v122, v104, v105
v_cvt_pk_bf16_f32 v123, v106, v107
s_add_u32 s46, s45, 8192
buffer_store_dwordx4 v[120:123], v7, s[16:19], s46 offen offset:3072 sc1
v_cvt_pk_bf16_f32 v124, v124, v125
v_cvt_pk_bf16_f32 v125, v126, v127
v_cvt_pk_bf16_f32 v126, v108, v109
v_cvt_pk_bf16_f32 v127, v110, v111
s_add_u32 s46, s45, 0
buffer_store_dwordx4 v[124:127], v7, s[16:19], s46 offen offset:3200 sc1
v_cvt_pk_bf16_f32 v144, v144, v145
v_cvt_pk_bf16_f32 v145, v146, v147
v_cvt_pk_bf16_f32 v146, v128, v129
v_cvt_pk_bf16_f32 v147, v130, v131
s_add_u32 s46, s45, 8192
buffer_store_dwordx4 v[144:147], v7, s[16:19], s46 offen offset:3200 sc1
.Lk1e:
)ASM"
    :: "s"((unsigned)(size_t)ws), "s"((unsigned)((size_t)ws >> 32)), "s"((unsigned)(size_t)ckv), "s"((unsigned)((size_t)ckv >> 32)), "s"((unsigned)(size_t)wuk), "s"((unsigned)((size_t)wuk >> 32)), "s"((unsigned)(size_t)wuv), "s"((unsigned)((size_t)wuv >> 32)), "s"(blockIdx.x), "s"((unsigned)(size_t)wq), "s"((unsigned)((size_t)wq >> 32)), "s"((unsigned)(size_t)x), "s"((unsigned)((size_t)x >> 32)), "v"(threadIdx.x) :
      "v1", "v2", "v3", "v4", "v5", "v6", "v7", "v8", "v9", "v10", "v11", "v12", "v13", "v14", "v15", "v16",
      "v17", "v18", "v19", "v20", "v21", "v22", "v23", "v24", "v25", "v26", "v27", "v28", "v29", "v30", "v31", "v32",
      "v33", "v34", "v35", "v36", "v37", "v38", "v39", "v40", "v41", "v42", "v43", "v44", "v45", "v46", "v47", "v48",
      "v49", "v50", "v51", "v52", "v53", "v54", "v55", "v56", "v57", "v58", "v59", "v60", "v61", "v62", "v63", "v64",
      "v65", "v66", "v67", "v68", "v69", "v70", "v71", "v72", "v73", "v74", "v75", "v76", "v77", "v78", "v79", "v80",
      "v81", "v82", "v83", "v84", "v85", "v86", "v87", "v88", "v89", "v90", "v91", "v92", "v93", "v94", "v95", "v96",
      "v97", "v98", "v99", "v100", "v101", "v102", "v103", "v104", "v105", "v106", "v107", "v108", "v109", "v110", "v111", "v112",
      "v113", "v114", "v115", "v116", "v117", "v118", "v119", "v120", "v121", "v122", "v123", "v124", "v125", "v126", "v127", "v128",
      "v129", "v130", "v131", "v132", "v133", "v134", "v135", "v136", "v137", "v138", "v139", "v140", "v141", "v142", "v143", "v144",
      "v145", "v146", "v147", "v148", "v149", "v150", "v151", "v152", "v153", "v154", "v155", "v156", "v157", "v158", "v159", "v160",
      "v161", "v162", "v163", "v164", "v165", "v166", "v167", "v168", "v169", "v170", "v171", "v172", "v173", "v174", "v175", "v176",
      "v177", "v178", "v179", "v180", "v181", "v182", "v183", "v184", "v185", "v186", "v187", "v188", "v189", "v190", "v191", "v192",
      "v193", "v194", "v195", "v196", "v197", "v198", "v199", "v200", "v201", "v202", "v203", "v204", "v205", "v206", "v207", "v208",
      "v209", "v210", "v211", "v212", "v213", "v214", "v215", "v216", "v217", "v218", "v219", "v220", "v221", "v222", "v223", "v224",
      "v225", "v226", "v227", "v228", "v229", "v230", "v231", "v232", "v233", "v234", "v235", "v236", "v237", "v238", "v239", "v240",
      "v241", "v242", "v243", "v244", "v245", "v246", "v247", "v248", "v249", "v250", "v251", "v252", "v253", "v254", "v255", "a0",
      "a1", "a2", "a3", "a4", "a5", "a6", "a7", "a8", "a9", "a10", "a11", "a12", "a13", "a14", "a15", "a16",
      "a17", "a18", "a19", "a20", "a21", "a22", "a23", "a24", "a25", "a26", "a27", "a28", "a29", "a30", "a31", "a32",
      "a33", "a34", "a35", "a36", "a37", "a38", "a39", "a40", "a41", "a42", "a43", "a44", "a45", "a46", "a47", "a48",
      "a49", "a50", "a51", "a52", "a53", "a54", "a55", "a56", "a57", "a58", "a59", "a60", "a61", "a62", "a63", "a64",
      "a65", "a66", "a67", "a68", "a69", "a70", "a71", "a72", "a73", "a74", "a75", "a76", "a77", "a78", "a79", "a80",
      "a81", "a82", "a83", "a84", "a85", "a86", "a87", "a88", "a89", "a90", "a91", "a92", "a93", "a94", "a95", "a96",
      "a97", "a98", "a99", "a100", "a101", "a102", "a103", "a104", "a105", "a106", "a107", "a108", "a109", "a110", "a111", "a112",
      "a113", "a114", "a115", "a116", "a117", "a118", "a119", "a120", "a121", "a122", "a123", "a124", "a125", "a126", "a127", "a128",
      "a129", "a130", "a131", "a132", "a133", "a134", "a135", "a136", "a137", "a138", "a139", "a140", "a141", "a142", "a143", "a144",
      "a145", "a146", "a147", "a148", "a149", "a150", "a151", "a152", "a153", "a154", "a155", "a156", "a157", "a158", "a159", "a160",
      "a161", "a162", "a163", "a164", "a165", "a166", "a167", "a168", "a169", "a170", "a171", "a172", "a173", "a174", "a175", "a176",
      "a177", "a178", "a179", "a180", "a181", "a182", "a183", "a184", "a185", "a186", "a187", "a188", "a189", "a190", "a191", "a192",
      "a193", "a194", "a195", "a196", "a197", "a198", "a199", "a200", "a201", "a202", "a203", "a204", "a205", "a206", "a207", "a208",
      "a209", "a210", "a211", "a212", "a213", "a214", "a215", "a216", "a217", "a218", "a219", "a220", "a221", "a222", "a223", "a224",
      "a225", "a226", "a227", "a228", "a229", "a230", "a231", "a232", "a233", "a234", "a235", "a236", "a237", "a238", "a239", "a240",
      "a241", "a242", "a243", "a244", "a245", "a246", "a247", "a248", "a249", "a250", "a251", "a252", "a253", "a254", "a255", "s16",
      "s17", "s18", "s19", "s20", "s21", "s22", "s23", "s24", "s25", "s26", "s27", "s28", "s29", "s30", "s31", "s32",
      "s33", "s34", "s35", "s36", "s37", "s38", "s39", "s40", "s41", "s42", "s43", "s44", "s45", "s46", "s47", "s48",
      "s49", "s50", "s51", "s52", "s53", "s54", "s55", "s56", "s57", "s58", "s59", "s60", "s61", "s62", "s63", "s64",
      "s65", "s66", "s67", "s68", "s69", "s70", "s71", "s72", "s73", "s74", "s75", "s76", "s77", "s78", "s79", "s80",
      "s81", "s82", "s83", "s84", "s85", "s86", "s87", "s88", "s89", "s90", "s91", "s92", "s93", "s94", "s95", "s96",
      "s97", "s98", "s99", "s100", "s101", "vcc", "scc", "m0", "memory");
}
__global__ __launch_bounds__(256, 1) void k2_attn(char* __restrict__ ws, const uint16_t* __restrict__ wq, const uint16_t* __restrict__ wo,
    const uint16_t* __restrict__ wd, const uint16_t* __restrict__ wg, const uint16_t* __restrict__ wu, const uint16_t* __restrict__ x, uint16_t* __restrict__ out) {
  asm volatile(
R"ASM(
.Lk2s:
s_mov_b32 s16, %0
s_and_b32 s17, %1, 0xffff
s_mov_b32 s52, 0x8000000
s_mov_b32 s18, s52
s_mov_b32 s19, 0x20000
s_and_b32 s49, %2, 31
s_lshr_b32 s50, %2, 5
v_lshrrev_b32 v16, 6, v0
v_and_b32 v1, 63, v0
v_readfirstlane_b32 s51, v16
v_lshlrev_b32 v1, 4, v1
s_lshl_b32 s29, s49, 19
s_and_b32 s47, s51, 1
s_lshl_b32 s48, s50, 2
s_add_u32 s48, s48, 4
s_lshl_b32 s36, s47, 12
s_add_u32 s24, s36, s29
s_lshl_b32 s47, s47, 1
s_cmp_lt_u32 s51, 2
s_cbranch_scc1 .Lpro01
s_add_u32 s24, s24, 0x1008000
s_sub_u32 s44, 64, s48
s_add_u32 s44, s44, s47
s_mov_b32 s22, s52
s_cmp_ge_u32 s48, 20
s_cselect_b32 s40, 33, 21
s_lshl_b32 s40, s40, 13
s_add_u32 s40, s40, s24
s_lshl_b32 s42, s44, 13
s_add_u32 s42, s42, s24
s_sub_u32 s42, s42, 0x2000
s_add_u32 s43, s42, 0x2000
s_branch .Lproc
.Lpro01:
s_add_u32 s36, s36, 65536
s_sub_u32 s72, 38, s48
s_lshl_b32 s72, s72, 13
s_add_u32 s24, s24, s72
s_add_u32 s24, s24, 0x1000000
s_sub_u32 s44, s48, 4
s_add_u32 s44, s44, s47
s_add_u32 s40, s24, 237568
s_cmp_eq_u32 s48, 4
s_cselect_b32 s22, s52, 0
.Lproc:
s_mov_b32 s76, %3
s_and_b32 s77, %4, 0xffff
s_mov_b32 s78, 4194304
s_mov_b32 s79, 0x20000
s_lshl_b32 s80, s44, 15
s_add_u32 s80, s80, 0x5000000
s_add_u32 s81, s80, 0x8000
s_and_b32 s82, s51, 1
s_lshl_b32 s82, s82, 16
s_lshl_b32 s72, s49, 8
s_add_u32 s82, s82, s72
s_lshr_b32 s72, s51, 1
s_lshl_b32 s72, s72, 7
s_add_u32 s82, s82, s72
s_lshl_b32 s83, s51, 10
s_add_u32 s83, s83, 65536
v_bfe_u32 v16, v0, 3, 3
v_lshlrev_b32 v120, 13, v16
v_bfe_u32 v16, v0, 4, 1
v_lshlrev_b32 v16, 2, v16
v_and_b32 v17, 7, v0
v_xor_b32 v16, v16, v17
v_lshl_add_u32 v120, v16, 4, v120
s_add_u32 m0, s83, 0
s_add_u32 s72, s82, 0
buffer_load_dwordx4 v120, s[76:79], s72 offen lds
s_add_u32 m0, s83, 4096
s_add_u32 s72, s82, 131072
buffer_load_dwordx4 v120, s[76:79], s72 offen lds
s_add_u32 m0, s83, 8192
s_add_u32 s72, s82, 262144
buffer_load_dwordx4 v120, s[76:79], s72 offen lds
s_add_u32 m0, s83, 12288
s_add_u32 s72, s82, 393216
buffer_load_dwordx4 v120, s[76:79], s72 offen lds
buffer_load_dwordx4 a[0:3], v1, s[16:19], s80 offen offset:0
buffer_load_dwordx4 a[4:7], v1, s[16:19], s81 offen offset:0
buffer_load_dwordx4 a[8:11], v1, s[16:19], s80 offen offset:1024
buffer_load_dwordx4 a[12:15], v1, s[16:19], s81 offen offset:1024
buffer_load_dwordx4 a[16:19], v1, s[16:19], s80 offen offset:2048
buffer_load_dwordx4 a[20:23], v1, s[16:19], s81 offen offset:2048
s_add_u32 m0, s83, 16384
s_add_u32 s72, s82, 524288
buffer_load_dwordx4 v120, s[76:79], s72 offen lds
s_add_u32 m0, s83, 20480
s_add_u32 s72, s82, 655360
buffer_load_dwordx4 v120, s[76:79], s72 offen lds
v_bfe_u32 v16, v0, 2, 2
v_lshlrev_b32 v121, 7, v16
v_bfe_u32 v16, v0, 5, 1
v_lshl_add_u32 v121, v16, 9, v121
v_bfe_u32 v16, v0, 3, 1
v_lshl_add_u32 v121, v16, 6, v121
v_bfe_u32 v16, v0, 4, 1
v_lshl_add_u32 v121, v16, 5, v121
v_and_b32 v16, 3, v0
v_lshl_add_u32 v121, v16, 3, v121
v_add_u32 v121, 0x10000, v121
v_xor_b32 v122, 64, v121
v_add_u32 v123, 0x10000, v121
v_add_u32 v124, 0x10000, v122
s_lshl_b32 s72, s51, 14
s_add_u32 s72, s72, 98304
v_add_u32 v125, s72, v1
s_lshl_b32 s74, s51, 11
s_add_u32 s75, s74, s29
s_bfe_u32 s26, %2, 0x50003
s_getpc_b64 s[30:31]
.Lpfk2:
s_sub_u32 s30, s30, .Lpfk2-.Lproc
s_subb_u32 s31, s31, 0
s_mul_i32 s26, s26, 24
v_min_u32 v32, 23, v0
v_add_lshl_u32 v32, v32, s26, 7
v_min_u32 v32, .Lk3e-.Lproc-4, v32
global_load_dword v40, v32, s[30:31]
s_waitcnt vmcnt(9)
s_barrier
ds_read_b64_tr_b16 a[224:225], v121 offset:0
ds_read_b64_tr_b16 a[226:227], v121 offset:1024
ds_read_b64_tr_b16 a[228:229], v122 offset:0
ds_read_b64_tr_b16 a[230:231], v122 offset:1024
ds_read_b64_tr_b16 a[232:233], v121 offset:2048
ds_read_b64_tr_b16 a[234:235], v121 offset:3072
ds_read_b64_tr_b16 a[236:237], v122 offset:2048
ds_read_b64_tr_b16 a[238:239], v122 offset:3072
s_waitcnt vmcnt(8) lgkmcnt(6)
v_mfma_f32_32x32x16_bf16 v[128:143], a[224:227], a[0:3], 0
ds_read_b64_tr_b16 a[240:241], v121 offset:4096
s_waitcnt vmcnt(7)
v_mfma_f32_32x32x16_bf16 v[192:207], a[224:227], a[4:7], 0
ds_read_b64_tr_b16 a[242:243], v121 offset:5120
s_add_u32 m0, s83, 24576
s_add_u32 s72, s82, 786432
buffer_load_dwordx4 v120, s[76:79], s72 offen lds
s_waitcnt lgkmcnt(6)
v_mfma_f32_32x32x16_bf16 v[144:159], a[228:231], a[0:3], 0
ds_read_b64_tr_b16 a[244:245], v122 offset:4096
s_add_u32 m0, s83, 28672
s_add_u32 s72, s82, 917504
buffer_load_dwordx4 v120, s[76:79], s72 offen lds
v_mfma_f32_32x32x16_bf16 v[208:223], a[228:231], a[4:7], 0
ds_read_b64_tr_b16 a[246:247], v122 offset:5120
buffer_load_dwordx4 a[24:27], v1, s[16:19], s80 offen offset:3072
s_add_u32 s80, s80, 4096
s_waitcnt lgkmcnt(6)
v_mfma_f32_32x32x16_bf16 v[160:175], a[232:235], a[0:3], 0
ds_read_b64_tr_b16 a[248:249], v121 offset:6144
buffer_load_dwordx4 a[28:31], v1, s[16:19], s81 offen offset:3072
s_add_u32 s81, s81, 4096
v_mfma_f32_32x32x16_bf16 v[224:239], a[232:235], a[4:7], 0
ds_read_b64_tr_b16 a[250:251], v121 offset:7168
s_waitcnt lgkmcnt(6)
v_mfma_f32_32x32x16_bf16 v[176:191], a[236:239], a[0:3], 0
ds_read_b64_tr_b16 a[252:253], v122 offset:6144
v_mfma_f32_32x32x16_bf16 v[240:255], a[236:239], a[4:7], 0
ds_read_b64_tr_b16 a[254:255], v122 offset:7168
s_waitcnt vmcnt(10) lgkmcnt(6)
v_mfma_f32_32x32x16_bf16 v[128:143], a[240:243], a[8:11], v[128:143]
ds_read_b64_tr_b16 a[224:225], v121 offset:8192
s_waitcnt vmcnt(9)
v_mfma_f32_32x32x16_bf16 v[192:207], a[240:243], a[12:15], v[192:207]
ds_read_b64_tr_b16 a[226:227], v121 offset:9216
buffer_load_dwordx4 a[32:35], v1, s[16:19], s80 offen offset:0
s_waitcnt lgkmcnt(6)
v_mfma_f32_32x32x16_bf16 v[144:159], a[244:247], a[8:11], v[144:159]
ds_read_b64_tr_b16 a[228:229], v122 offset:8192
buffer_load_dwordx4 a[36:39], v1, s[16:19], s81 offen offset:0
v_mfma_f32_32x32x16_bf16 v[208:223], a[244:247], a[12:15], v[208:223]
ds_read_b64_tr_b16 a[230:231], v122 offset:9216
buffer_load_dwordx4 a[40:43], v1, s[16:19], s80 offen offset:1024
s_waitcnt lgkmcnt(6)
v_mfma_f32_32x32x16_bf16 v[160:175], a[248:251], a[8:11], v[160:175]
ds_read_b64_tr_b16 a[232:233], v121 offset:10240
buffer_load_dwordx4 a[44:47], v1, s[16:19], s81 offen offset:1024
v_mfma_f32_32x32x16_bf16 v[224:239], a[248:251], a[12:15], v[224:239]
ds_read_b64_tr_b16 a[234:235], v121 offset:11264
s_waitcnt lgkmcnt(6)
v_mfma_f32_32x32x16_bf16 v[176:191], a[252:255], a[8:11], v[176:191]
ds_read_b64_tr_b16 a[236:237], v122 offset:10240
v_mfma_f32_32x32x16_bf16 v[240:255], a[252:255], a[12:15], v[240:255]
ds_read_b64_tr_b16 a[238:239], v122 offset:11264
s_waitcnt vmcnt(12) lgkmcnt(6)
v_mfma_f32_32x32x16_bf16 v[128:143], a[224:227], a[16:19], v[128:143]
ds_read_b64_tr_b16 a[240:241], v121 offset:12288
s_add_u32 s45, s44, 1
s_add_u32 s25, s24, 0x2000
s_mov_b32 s20, s16
s_mov_b32 s21, s17
s_waitcnt vmcnt(11)
v_mfma_f32_32x32x16_bf16 v[192:207], a[224:227], a[20:23], v[192:207]
ds_read_b64_tr_b16 a[242:243], v121 offset:13312
buffer_load_dwordx4 a[48:51], v1, s[16:19], s80 offen offset:2048
s_mov_b32 s23, 0x20000
s_lshl_b32 s70, s51, 14
s_waitcnt lgkmcnt(6)
v_mfma_f32_32x32x16_bf16 v[144:159], a[228:231], a[16:19], v[144:159]
ds_read_b64_tr_b16 a[244:245], v122 offset:12288
buffer_load_dwordx4 a[52:55], v1, s[16:19], s81 offen offset:2048
v_mfma_f32_32x32x16_bf16 v[208:223], a[228:231], a[20:23], v[208:223]
ds_read_b64_tr_b16 a[246:247], v122 offset:13312
s_add_u32 m0, s83, 32768
s_add_u32 s72, s82, 1048576
buffer_load_dwordx4 v120, s[76:79], s72 offen lds
s_waitcnt lgkmcnt(6)
v_mfma_f32_32x32x16_bf16 v[160:175], a[232:235], a[16:19], v[160:175]
ds_read_b64_tr_b16 a[248:249], v121 offset:14336
s_add_u32 m0, s83, 36864
s_add_u32 s72, s82, 1179648
buffer_load_dwordx4 v120, s[76:79], s72 offen lds
v_mfma_f32_32x32x16_bf16 v[224:239], a[232:235], a[20:23], v[224:239]
ds_read_b64_tr_b16 a[250:251], v121 offset:15360
s_waitcnt lgkmcnt(6)
v_mfma_f32_32x32x16_bf16 v[176:191], a[236:239], a[16:19], v[176:191]
ds_read_b64_tr_b16 a[252:253], v122 offset:14336
v_mfma_f32_32x32x16_bf16 v[240:255], a[236:239], a[20:23], v[240:255]
ds_read_b64_tr_b16 a[254:255], v122 offset:15360
s_waitcnt vmcnt(9) lgkmcnt(6)
v_mfma_f32_32x32x16_bf16 v[128:143], a[240:243], a[24:27], v[128:143]
s_barrier
s_add_u32 s70, s70, 131072
s_lshl_b32 s73, s48, 13
s_add_u32 s73, s73, s24
s_waitcnt vmcnt(8)
v_mfma_f32_32x32x16_bf16 v[192:207], a[240:243], a[28:31], v[192:207]
ds_read_b64_tr_b16 a[224:225], v121 offset:16384
s_add_u32 m0, s83, 40960
s_add_u32 s72, s82, 1310720
buffer_load_dwordx4 v120, s[76:79], s72 offen lds
s_sub_u32 s73, s73, 40960
s_waitcnt lgkmcnt(5)
v_mfma_f32_32x32x16_bf16 v[144:159], a[244:247], a[24:27], v[144:159]
ds_read_b64_tr_b16 a[226:227], v121 offset:17408
s_add_u32 m0, s83, 45056
s_add_u32 s72, s82, 1441792
buffer_load_dwordx4 v120, s[76:79], s72 offen lds
s_add_u32 s27, s24, 253952
v_mfma_f32_32x32x16_bf16 v[208:223], a[244:247], a[28:31], v[208:223]
ds_read_b64_tr_b16 a[228:229], v122 offset:16384
buffer_load_dwordx4 a[56:59], v1, s[16:19], s80 offen offset:3072
s_add_u32 s80, s80, 4096
s_lshl_b32 s37, s44, 18
s_waitcnt lgkmcnt(5)
v_mfma_f32_32x32x16_bf16 v[160:175], a[248:251], a[24:27], v[160:175]
ds_read_b64_tr_b16 a[230:231], v122 offset:17408
buffer_load_dwordx4 a[60:63], v1, s[16:19], s81 offen offset:3072
s_add_u32 s81, s81, 4096
v_mfma_f32_32x32x16_bf16 v[224:239], a[248:251], a[28:31], v[224:239]
ds_read_b64_tr_b16 a[232:233], v121 offset:18432
s_waitcnt lgkmcnt(5)
v_mfma_f32_32x32x16_bf16 v[176:191], a[252:255], a[24:27], v[176:191]
ds_read_b64_tr_b16 a[234:235], v121 offset:19456
ds_read_b64_tr_b16 a[236:237], v122 offset:18432
v_mfma_f32_32x32x16_bf16 v[240:255], a[252:255], a[28:31], v[240:255]
ds_read_b64_tr_b16 a[238:239], v122 offset:19456
s_waitcnt vmcnt(11) lgkmcnt(6)
v_mfma_f32_32x32x16_bf16 v[128:143], a[224:227], a[32:35], v[128:143]
ds_read_b64_tr_b16 a[240:241], v121 offset:20480
s_lshl_b32 s41, s49, 13
s_add_u32 s37, s37, s41
s_add_u32 s37, s37, 0x3000000
s_add_u32 s38, s37, 4096
s_waitcnt vmcnt(10)
v_mfma_f32_32x32x16_bf16 v[192:207], a[224:227], a[36:39], v[192:207]
ds_read_b64_tr_b16 a[242:243], v121 offset:21504
buffer_load_dwordx4 a[64:67], v1, s[16:19], s80 offen offset:0
s_add_u32 s39, s37, 262144
s_add_u32 s53, s37, 266240
s_waitcnt lgkmcnt(6)
v_mfma_f32_32x32x16_bf16 v[144:159], a[228:231], a[32:35], v[144:159]
ds_read_b64_tr_b16 a[244:245], v122 offset:20480
buffer_load_dwordx4 a[68:71], v1, s[16:19], s81 offen offset:0
v_mfma_f32_32x32x16_bf16 v[208:223], a[228:231], a[36:39], v[208:223]
ds_read_b64_tr_b16 a[246:247], v122 offset:21504
buffer_load_dwordx4 a[72:75], v1, s[16:19], s80 offen offset:1024
s_waitcnt lgkmcnt(6)
v_mfma_f32_32x32x16_bf16 v[160:175], a[232:235], a[32:35], v[160:175]
ds_read_b64_tr_b16 a[248:249], v121 offset:22528
buffer_load_dwordx4 a[76:79], v1, s[16:19], s81 offen offset:1024
v_mfma_f32_32x32x16_bf16 v[224:239], a[232:235], a[36:39], v[224:239]
ds_read_b64_tr_b16 a[250:251], v121 offset:23552
s_waitcnt lgkmcnt(6)
v_mfma_f32_32x32x16_bf16 v[176:191], a[236:239], a[32:35], v[176:191]
ds_read_b64_tr_b16 a[252:253], v122 offset:22528
v_mfma_f32_32x32x16_bf16 v[240:255], a[236:239], a[36:39], v[240:255]
ds_read_b64_tr_b16 a[254:255], v122 offset:23552
s_waitcnt vmcnt(13) lgkmcnt(6)
v_mfma_f32_32x32x16_bf16 v[128:143], a[240:243], a[40:43], v[128:143]
ds_read_b64_tr_b16 a[224:225], v121 offset:24576
s_mov_b32 s71, 0x5d800000
v_mov_b32 v5, 0xff800000
v_mov_b32 v2, v1
v_add_u32 v3, 0xffe000, v1
s_waitcnt vmcnt(12)
v_mfma_f32_32x32x16_bf16 v[192:207], a[240:243], a[44:47], v[192:207]
ds_read_b64_tr_b16 a[226:227], v121 offset:25600
buffer_load_dwordx4 a[80:83], v1, s[16:19], s80 offen offset:2048
v_add_u32 v6, s70, v1
v_mov_b32 v7, 0
s_waitcnt lgkmcnt(6)
v_mfma_f32_32x32x16_bf16 v[144:159], a[244:247], a[40:43], v[144:159]
ds_read_b64_tr_b16 a[228:229], v122 offset:24576
buffer_load_dwordx4 a[84:87], v1, s[16:19], s81 offen offset:2048
v_mfma_f32_32x32x16_bf16 v[208:223], a[244:247], a[44:47], v[208:223]
ds_read_b64_tr_b16 a[230:231], v122 offset:25600
s_add_u32 m0, s83, 49152
s_add_u32 s72, s82, 1572864
buffer_load_dwordx4 v120, s[76:79], s72 offen lds
s_waitcnt lgkmcnt(6)
v_mfma_f32_32x32x16_bf16 v[160:175], a[248:251], a[40:43], v[160:175]
ds_read_b64_tr_b16 a[232:233], v121 offset:26624
s_add_u32 m0, s83, 53248
s_add_u32 s72, s82, 1703936
buffer_load_dwordx4 v120, s[76:79], s72 offen lds
v_mfma_f32_32x32x16_bf16 v[224:239], a[248:251], a[44:47], v[224:239]
ds_read_b64_tr_b16 a[234:235], v121 offset:27648
s_waitcnt lgkmcnt(6)
v_mfma_f32_32x32x16_bf16 v[176:191], a[252:255], a[40:43], v[176:191]
ds_read_b64_tr_b16 a[236:237], v122 offset:26624
v_mfma_f32_32x32x16_bf16 v[240:255], a[252:255], a[44:47], v[240:255]
ds_read_b64_tr_b16 a[238:239], v122 offset:27648
s_waitcnt vmcnt(15) lgkmcnt(6)
v_mfma_f32_32x32x16_bf16 v[128:143], a[224:227], a[48:51], v[128:143]
ds_read_b64_tr_b16 a[240:241], v121 offset:28672
v_mov_b32 v8, 0
v_mov_b32 v9, 0
v_mov_b32 v10, 0
v_mov_b32 v11, 0
s_waitcnt vmcnt(14)
v_mfma_f32_32x32x16_bf16 v[192:207], a[224:227], a[52:55], v[192:207]
ds_read_b64_tr_b16 a[242:243], v121 offset:29696
s_add_u32 m0, s83, 57344
s_add_u32 s72, s82, 1835008
buffer_load_dwordx4 v120, s[76:79], s72 offen lds
v_mov_b32 v12, 0
s_waitcnt lgkmcnt(6)
v_mfma_f32_32x32x16_bf16 v[144:159], a[228:231], a[48:51], v[144:159]
ds_read_b64_tr_b16 a[244:245], v122 offset:28672
s_add_u32 m0, s83, 61440
s_add_u32 s72, s82, 1966080
buffer_load_dwordx4 v120, s[76:79], s72 offen lds
v_mov_b32 v13, 0
v_mfma_f32_32x32x16_bf16 v[208:223], a[228:231], a[52:55], v[208:223]
ds_read_b64_tr_b16 a[246:247], v122 offset:29696
buffer_load_dwordx4 a[88:91], v1, s[16:19], s80 offen offset:3072
s_add_u32 s80, s80, 4096
s_waitcnt lgkmcnt(6)
v_mfma_f32_32x32x16_bf16 v[160:175], a[232:235], a[48:51], v[160:175]
ds_read_b64_tr_b16 a[248:249], v121 offset:30720
buffer_load_dwordx4 a[92:95], v1, s[16:19], s81 offen offset:3072
s_add_u32 s81, s81, 4096
v_mfma_f32_32x32x16_bf16 v[224:239], a[232:235], a[52:55], v[224:239]
ds_read_b64_tr_b16 a[250:251], v121 offset:31744
s_waitcnt lgkmcnt(6)
v_mfma_f32_32x32x16_bf16 v[176:191], a[236:239], a[48:51], v[176:191]
ds_read_b64_tr_b16 a[252:253], v122 offset:30720
v_mfma_f32_32x32x16_bf16 v[240:255], a[236:239], a[52:55], v[240:255]
ds_read_b64_tr_b16 a[254:255], v122 offset:31744
s_waitcnt vmcnt(13) lgkmcnt(6)
v_mfma_f32_32x32x16_bf16 v[128:143], a[240:243], a[56:59], v[128:143]
s_barrier
v_mov_b32 v14, 0
v_mov_b32 v15, 0
v_mov_b32 v64, 0
s_waitcnt vmcnt(12)
v_mfma_f32_32x32x16_bf16 v[192:207], a[240:243], a[60:63], v[192:207]
ds_read_b64_tr_b16 a[224:225], v121 offset:32768
buffer_load_dwordx4 a[96:99], v1, s[16:19], s80 offen offset:0
v_mov_b32 v65, 0
v_mov_b32 v66, 0
v_mov_b32 v67, 0
s_waitcnt lgkmcnt(5)
v_mfma_f32_32x32x16_bf16 v[144:159], a[244:247], a[56:59], v[144:159]
ds_read_b64_tr_b16 a[226:227], v121 offset:33792
buffer_load_dwordx4 a[100:103], v1, s[16:19], s81 offen offset:0
v_mfma_f32_32x32x16_bf16 v[208:223], a[244:247], a[60:63], v[208:223]
ds_read_b64_tr_b16 a[228:229], v122 offset:32768
buffer_load_dwordx4 a[104:107], v1, s[16:19], s80 offen offset:1024
s_waitcnt lgkmcnt(5)
v_mfma_f32_32x32x16_bf16 v[160:175], a[248:251], a[56:59], v[160:175]
ds_read_b64_tr_b16 a[230:231], v122 offset:33792
buffer_load_dwordx4 a[108:111], v1, s[16:19], s81 offen offset:1024
v_mfma_f32_32x32x16_bf16 v[224:239], a[248:251], a[60:63], v[224:239]
ds_read_b64_tr_b16 a[232:233], v121 offset:34816
s_waitcnt lgkmcnt(5)
v_mfma_f32_32x32x16_bf16 v[176:191], a[252:255], a[56:59], v[176:191]
ds_read_b64_tr_b16 a[234:235], v121 offset:35840
ds_read_b64_tr_b16 a[236:237], v122 offset:34816
v_mfma_f32_32x32x16_bf16 v[240:255], a[252:255], a[60:63], v[240:255]
ds_read_b64_tr_b16 a[238:239], v122 offset:35840
s_waitcnt vmcnt(15) lgkmcnt(6)
v_mfma_f32_32x32x16_bf16 v[128:143], a[224:227], a[64:67], v[128:143]
ds_read_b64_tr_b16 a[240:241], v121 offset:36864
v_mov_b32 v68, 0
v_mov_b32 v69, 0
v_mov_b32 v70, 0
v_mov_b32 v71, 0
s_waitcnt vmcnt(14)
v_mfma_f32_32x32x16_bf16 v[192:207], a[224:227], a[68:71], v[192:207]
ds_read_b64_tr_b16 a[242:243], v121 offset:37888
buffer_load_dwordx4 a[112:115], v1, s[16:19], s80 offen offset:2048
v_mov_b32 v72, 0
v_mov_b32 v73, 0
s_waitcnt lgkmcnt(6)
v_mfma_f32_32x32x16_bf16 v[144:159], a[228:231], a[64:67], v[144:159]
ds_read_b64_tr_b16 a[244:245], v122 offset:36864
buffer_load_dwordx4 a[116:119], v1, s[16:19], s81 offen offset:2048
v_mfma_f32_32x32x16_bf16 v[208:223], a[228:231], a[68:71], v[208:223]
ds_read_b64_tr_b16 a[246:247], v122 offset:37888
s_add_u32 m0, s83, 65536
s_add_u32 s72, s82, 2097152
buffer_load_dwordx4 v120, s[76:79], s72 offen lds
s_waitcnt lgkmcnt(6)
v_mfma_f32_32x32x16_bf16 v[160:175], a[232:235], a[64:67], v[160:175]
ds_read_b64_tr_b16 a[248:249], v121 offset:38912
s_add_u32 m0, s83, 69632
s_add_u32 s72, s82, 2228224
buffer_load_dwordx4 v120, s[76:79], s72 offen lds
v_mfma_f32_32x32x16_bf16 v[224:239], a[232:235], a[68:71], v[224:239]
ds_read_b64_tr_b16 a[250:251], v121 offset:39936
s_waitcnt lgkmcnt(6)
v_mfma_f32_32x32x16_bf16 v[176:191], a[236:239], a[64:67], v[176:191]
ds_read_b64_tr_b16 a[252:253], v122 offset:38912
v_mfma_f32_32x32x16_bf16 v[240:255], a[236:239], a[68:71], v[240:255]
ds_read_b64_tr_b16 a[254:255], v122 offset:39936
s_waitcnt vmcnt(17) lgkmcnt(6)
v_mfma_f32_32x32x16_bf16 v[128:143], a[240:243], a[72:75], v[128:143]
ds_read_b64_tr_b16 a[224:225], v121 offset:40960
v_mov_b32 v74, 0
v_mov_b32 v75, 0
v_mov_b32 v76, 0
v_mov_b32 v77, 0
s_waitcnt vmcnt(16)
v_mfma_f32_32x32x16_bf16 v[192:207], a[240:243], a[76:79], v[192:207]
ds_read_b64_tr_b16 a[226:227], v121 offset:41984
s_add_u32 m0, s83, 73728
s_add_u32 s72, s82, 2359296
buffer_load_dwordx4 v120, s[76:79], s72 offen lds
v_mov_b32 v78, 0
s_waitcnt lgkmcnt(6)
v_mfma_f32_32x32x16_bf16 v[144:159], a[244:247], a[72:75], v[144:159]
ds_read_b64_tr_b16 a[228:229], v122 offset:40960
s_add_u32 m0, s83, 77824
s_add_u32 s72, s82, 2490368
buffer_load_dwordx4 v120, s[76:79], s72 offen lds
v_mov_b32 v79, 0
v_mfma_f32_32x32x16_bf16 v[208:223], a[244:247], a[76:79], v[208:223]
ds_read_b64_tr_b16 a[230:231], v122 offset:41984
buffer_load_dwordx4 a[120:123], v1, s[16:19], s80 offen offset:3072
s_add_u32 s80, s80, 4096
s_waitcnt lgkmcnt(6)
v_mfma_f32_32x32x16_bf16 v[160:175], a[248:251], a[72:75], v[160:175]
ds_read_b64_tr_b16 a[232:233], v121 offset:43008
buffer_load_dwordx4 a[124:127], v1, s[16:19], s81 offen offset:3072
s_add_u32 s81, s81, 4096
v_mfma_f32_32x32x16_bf16 v[224:239], a[248:251], a[76:79], v[224:239]
ds_read_b64_tr_b16 a[234:235], v121 offset:44032
s_waitcnt lgkmcnt(6)
v_mfma_f32_32x32x16_bf16 v[176:191], a[252:255], a[72:75], v[176:191]
ds_read_b64_tr_b16 a[236:237], v122 offset:43008
v_mfma_f32_32x32x16_bf16 v[240:255], a[252:255], a[76:79], v[240:255]
ds_read_b64_tr_b16 a[238:239], v122 offset:44032
s_waitcnt vmcnt(19) lgkmcnt(6)
v_mfma_f32_32x32x16_bf16 v[128:143], a[224:227], a[80:83], v[128:143]
ds_read_b64_tr_b16 a[240:241], v121 offset:45056
v_mov_b32 v80, 0
v_mov_b32 v81, 0
v_mov_b32 v82, 0
v_mov_b32 v83, 0
s_waitcnt vmcnt(18)
v_mfma_f32_32x32x16_bf16 v[192:207], a[224:227], a[84:87], v[192:207]
ds_read_b64_tr_b16 a[242:243], v121 offset:46080
buffer_load_dwordx4 a[0:3], v1, s[16:19], s80 offen offset:0
v_mov_b32 v84, 0
v_mov_b32 v85, 0
s_waitcnt lgkmcnt(6)
v_mfma_f32_32x32x16_bf16 v[144:159], a[228:231], a[80:83], v[144:159]
ds_read_b64_tr_b16 a[244:245], v122 offset:45056
buffer_load_dwordx4 a[4:7], v1, s[16:19], s81 offen offset:0
v_mfma_f32_32x32x16_bf16 v[208:223], a[228:231], a[84:87], v[208:223]
ds_read_b64_tr_b16 a[246:247], v122 offset:46080
buffer_load_dwordx4 a[8:11], v1, s[16:19], s80 offen offset:1024
s_waitcnt lgkmcnt(6)
v_mfma_f32_32x32x16_bf16 v[160:175], a[232:235], a[80:83], v[160:175]
ds_read_b64_tr_b16 a[248:249], v121 offset:47104
buffer_load_dwordx4 a[12:15], v1, s[16:19], s81 offen offset:1024
v_mfma_f32_32x32x16_bf16 v[224:239], a[232:235], a[84:87], v[224:239]
ds_read_b64_tr_b16 a[250:251], v121 offset:48128
s_waitcnt lgkmcnt(6)
v_mfma_f32_32x32x16_bf16 v[176:191], a[236:239], a[80:83], v[176:191]
ds_read_b64_tr_b16 a[252:253], v122 offset:47104
v_mfma_f32_32x32x16_bf16 v[240:255], a[236:239], a[84:87], v[240:255]
ds_read_b64_tr_b16 a[254:255], v122 offset:48128
s_waitcnt vmcnt(17) lgkmcnt(6)
v_mfma_f32_32x32x16_bf16 v[128:143], a[240:243], a[88:91], v[128:143]
s_barrier
v_mov_b32 v86, 0
v_mov_b32 v87, 0
v_mov_b32 v88, 0
s_waitcnt vmcnt(16)
v_mfma_f32_32x32x16_bf16 v[192:207], a[240:243], a[92:95], v[192:207]
ds_read_b64_tr_b16 a[224:225], v121 offset:49152
buffer_load_dwordx4 a[16:19], v1, s[16:19], s80 offen offset:2048
v_mov_b32 v89, 0
v_mov_b32 v90, 0
v_mov_b32 v91, 0
s_waitcnt lgkmcnt(5)
v_mfma_f32_32x32x16_bf16 v[144:159], a[244:247], a[88:91], v[144:159]
ds_read_b64_tr_b16 a[226:227], v121 offset:50176
buffer_load_dwordx4 a[20:23], v1, s[16:19], s81 offen offset:2048
v_mfma_f32_32x32x16_bf16 v[208:223], a[244:247], a[92:95], v[208:223]
ds_read_b64_tr_b16 a[228:229], v122 offset:49152
s_add_u32 m0, s83, 81920
s_add_u32 s72, s82, 2621440
buffer_load_dwordx4 v120, s[76:79], s72 offen lds
s_waitcnt lgkmcnt(5)
v_mfma_f32_32x32x16_bf16 v[160:175], a[248:251], a[88:91], v[160:175]
ds_read_b64_tr_b16 a[230:231], v122 offset:50176
s_add_u32 m0, s83, 86016
s_add_u32 s72, s82, 2752512
buffer_load_dwordx4 v120, s[76:79], s72 offen lds
v_mfma_f32_32x32x16_bf16 v[224:239], a[248:251], a[92:95], v[224:239]
ds_read_b64_tr_b16 a[232:233], v121 offset:51200
s_waitcnt lgkmcnt(5)
v_mfma_f32_32x32x16_bf16 v[176:191], a[252:255], a[88:91], v[176:191]
ds_read_b64_tr_b16 a[234:235], v121 offset:52224
ds_read_b64_tr_b16 a[236:237], v122 offset:51200
v_mfma_f32_32x32x16_bf16 v[240:255], a[252:255], a[92:95], v[240:255]
ds_read_b64_tr_b16 a[238:239], v122 offset:52224
s_waitcnt vmcnt(19) lgkmcnt(6)
v_mfma_f32_32x32x16_bf16 v[128:143], a[224:227], a[96:99], v[128:143]
ds_read_b64_tr_b16 a[240:241], v121 offset:53248
v_mov_b32 v92, 0
v_mov_b32 v93, 0
v_mov_b32 v94, 0
v_mov_b32 v95, 0
s_waitcnt vmcnt(18)
v_mfma_f32_32x32x16_bf16 v[192:207], a[224:227], a[100:103], v[192:207]
ds_read_b64_tr_b16 a[242:243], v121 offset:54272
s_add_u32 m0, s83, 90112
s_add_u32 s72, s82, 2883584
buffer_load_dwordx4 v120, s[76:79], s72 offen lds
v_lshrrev_b32 v24, 2, v0
s_waitcnt lgkmcnt(6)
v_mfma_f32_32x32x16_bf16 v[144:159], a[228:231], a[96:99], v[144:159]
ds_read_b64_tr_b16 a[244:245], v122 offset:53248
s_add_u32 m0, s83, 94208
s_add_u32 s72, s82, 3014656
buffer_load_dwordx4 v120, s[76:79], s72 offen lds
v_xor_b32 v24, v24, v0
v_mfma_f32_32x32x16_bf16 v[208:223], a[228:231], a[100:103], v[208:223]
ds_read_b64_tr_b16 a[246:247], v122 offset:54272
buffer_load_dwordx4 a[24:27], v1, s[16:19], s80 offen offset:3072
s_add_u32 s80, s80, 4096
s_waitcnt lgkmcnt(6)
v_mfma_f32_32x32x16_bf16 v[160:175], a[232:235], a[96:99], v[160:175]
ds_read_b64_tr_b16 a[248:249], v121 offset:55296
buffer_load_dwordx4 a[28:31], v1, s[16:19], s81 offen offset:3072
s_add_u32 s81, s81, 4096
v_mfma_f32_32x32x16_bf16 v[224:239], a[232:235], a[100:103], v[224:239]
ds_read_b64_tr_b16 a[250:251], v121 offset:56320
s_waitcnt lgkmcnt(6)
v_mfma_f32_32x32x16_bf16 v[176:191], a[236:239], a[96:99], v[176:191]
ds_read_b64_tr_b16 a[252:253], v122 offset:55296
v_mfma_f32_32x32x16_bf16 v[240:255], a[236:239], a[100:103], v[240:255]
ds_read_b64_tr_b16 a[254:255], v122 offset:56320
s_waitcnt vmcnt(21) lgkmcnt(6)
v_mfma_f32_32x32x16_bf16 v[128:143], a[240:243], a[104:107], v[128:143]
ds_read_b64_tr_b16 a[224:225], v121 offset:57344
v_bfe_u32 v24, v24, 2, 1
v_subrev_u32 v24, 1, v24
v_and_b32 v28, 0x3f803f80, v24
v_and_b32 v29, 0x3f803f80, v24
s_waitcnt vmcnt(20)
v_mfma_f32_32x32x16_bf16 v[192:207], a[240:243], a[108:111], v[192:207]
ds_read_b64_tr_b16 a[226:227], v121 offset:58368
buffer_load_dwordx4 a[32:35], v1, s[16:19], s80 offen offset:0
v_and_b32 v30, 0x3f803f80, v24
v_and_b32 v31, 0x3f803f80, v24
s_waitcnt lgkmcnt(6)
v_mfma_f32_32x32x16_bf16 v[144:159], a[244:247], a[104:107], v[144:159]
ds_read_b64_tr_b16 a[228:229], v122 offset:57344
buffer_load_dwordx4 a[36:39], v1, s[16:19], s81 offen offset:0
v_mfma_f32_32x32x16_bf16 v[208:223], a[244:247], a[108:111], v[208:223]
ds_read_b64_tr_b16 a[230:231], v122 offset:58368
s_add_u32 m0, s74, 0
s_add_u32 s72, s75, 16777216
buffer_load_dwordx4 v1, s[16:19], s72 offen offset:0 lds
buffer_load_dwordx4 v1, s[16:19], s72 offen offset:1024 lds
s_waitcnt lgkmcnt(6)
v_mfma_f32_32x32x16_bf16 v[160:175], a[248:251], a[104:107], v[160:175]
ds_read_b64_tr_b16 a[232:233], v121 offset:59392
s_add_u32 m0, s74, 8192
s_add_u32 s72, s75, 16785408
buffer_load_dwordx4 v1, s[16:19], s72 offen offset:0 lds
buffer_load_dwordx4 v1, s[16:19], s72 offen offset:1024 lds
v_mfma_f32_32x32x16_bf16 v[224:239], a[248:251], a[108:111], v[224:239]
ds_read_b64_tr_b16 a[234:235], v121 offset:60416
s_waitcnt lgkmcnt(6)
v_mfma_f32_32x32x16_bf16 v[176:191], a[252:255], a[104:107], v[176:191]
ds_read_b64_tr_b16 a[236:237], v122 offset:59392
v_mfma_f32_32x32x16_bf16 v[240:255], a[252:255], a[108:111], v[240:255]
ds_read_b64_tr_b16 a[238:239], v122 offset:60416
s_waitcnt vmcnt(25) lgkmcnt(6)
v_mfma_f32_32x32x16_bf16 v[128:143], a[224:227], a[112:115], v[128:143]
ds_read_b64_tr_b16 a[240:241], v121 offset:61440
s_mov_b32 s54, 0x0
s_mov_b32 s55, 0xf
s_mov_b32 s56, 0x1
s_mov_b32 s57, 0x1f
s_waitcnt vmcnt(24)
v_mfma_f32_32x32x16_bf16 v[192:207], a[224:227], a[116:119], v[192:207]
ds_read_b64_tr_b16 a[242:243], v121 offset:62464
s_add_u32 m0, s74, 32768
s_add_u32 s72, s75, 33554432
buffer_load_dwordx4 v1, s[16:19], s72 offen offset:0 lds
buffer_load_dwordx4 v1, s[16:19], s72 offen offset:1024 lds
s_waitcnt lgkmcnt(6)
v_mfma_f32_32x32x16_bf16 v[144:159], a[228:231], a[112:115], v[144:159]
ds_read_b64_tr_b16 a[244:245], v122 offset:61440
buffer_load_dwordx4 a[40:43], v1, s[16:19], s80 offen offset:1024
s_mov_b32 s58, 0x3
s_mov_b32 s59, 0x3f
v_mfma_f32_32x32x16_bf16 v[208:223], a[228:231], a[116:119], v[208:223]
ds_read_b64_tr_b16 a[246:247], v122 offset:62464
buffer_load_dwordx4 a[44:47], v1, s[16:19], s81 offen offset:1024
s_waitcnt lgkmcnt(6)
v_mfma_f32_32x32x16_bf16 v[160:175], a[232:235], a[112:115], v[160:175]
ds_read_b64_tr_b16 a[248:249], v121 offset:63488
buffer_load_dwordx4 a[48:51], v1, s[16:19], s80 offen offset:2048
v_mfma_f32_32x32x16_bf16 v[224:239], a[232:235], a[116:119], v[224:239]
ds_read_b64_tr_b16 a[250:251], v121 offset:64512
s_waitcnt lgkmcnt(6)
v_mfma_f32_32x32x16_bf16 v[176:191], a[236:239], a[112:115], v[176:191]
ds_read_b64_tr_b16 a[252:253], v122 offset:63488
v_mfma_f32_32x32x16_bf16 v[240:255], a[236:239], a[116:119], v[240:255]
ds_read_b64_tr_b16 a[254:255], v122 offset:64512
s_waitcnt vmcnt(24) lgkmcnt(6)
v_mfma_f32_32x32x16_bf16 v[128:143], a[240:243], a[120:123], v[128:143]
s_barrier
s_mov_b32 s60, 0x7
s_mov_b32 s61, 0x7f
s_mov_b32 s62, 0xff
s_waitcnt vmcnt(23)
v_mfma_f32_32x32x16_bf16 v[192:207], a[240:243], a[124:127], v[192:207]
ds_read_b64_tr_b16 a[224:225], v123 offset:0
buffer_load_dwordx4 a[52:55], v1, s[16:19], s81 offen offset:2048
s_mov_b32 s63, 0xfff
s_mov_b32 s64, 0x1ff
s_mov_b32 s65, 0x1fff
s_waitcnt lgkmcnt(5)
v_mfma_f32_32x32x16_bf16 v[144:159], a[244:247], a[120:123], v[144:159]
ds_read_b64_tr_b16 a[226:227], v123 offset:1024
s_add_u32 m0, s83, 0
s_add_u32 s72, s82, 3145728
buffer_load_dwordx4 v120, s[76:79], s72 offen lds
v_mfma_f32_32x32x16_bf16 v[208:223], a[244:247], a[124:127], v[208:223]
ds_read_b64_tr_b16 a[228:229], v124 offset:0
s_add_u32 m0, s83, 4096
s_add_u32 s72, s82, 3276800
buffer_load_dwordx4 v120, s[76:79], s72 offen lds
s_waitcnt lgkmcnt(5)
v_mfma_f32_32x32x16_bf16 v[160:175], a[248:251], a[120:123], v[160:175]
ds_read_b64_tr_b16 a[230:231], v124 offset:1024
s_add_u32 m0, s83, 8192
s_add_u32 s72, s82, 3407872
buffer_load_dwordx4 v120, s[76:79], s72 offen lds
v_mfma_f32_32x32x16_bf16 v[224:239], a[248:251], a[124:127], v[224:239]
ds_read_b64_tr_b16 a[232:233], v123 offset:2048
s_waitcnt lgkmcnt(5)
v_mfma_f32_32x32x16_bf16 v[176:191], a[252:255], a[120:123], v[176:191]
ds_read_b64_tr_b16 a[234:235], v123 offset:3072
ds_read_b64_tr_b16 a[236:237], v124 offset:2048
v_mfma_f32_32x32x16_bf16 v[240:255], a[252:255], a[124:127], v[240:255]
ds_read_b64_tr_b16 a[238:239], v124 offset:3072
s_waitcnt vmcnt(26) lgkmcnt(6)
v_mfma_f32_32x32x16_bf16 v[128:143], a[224:227], a[0:3], v[128:143]
ds_read_b64_tr_b16 a[240:241], v123 offset:4096
s_mov_b32 s66, 0x3ff
s_mov_b32 s67, 0x3fff
s_mov_b32 s68, 0x7ff
s_mov_b32 s69, 0x7fff
s_waitcnt vmcnt(25)
v_mfma_f32_32x32x16_bf16 v[192:207], a[224:227], a[4:7], v[192:207]
ds_read_b64_tr_b16 a[242:243], v123 offset:5120
s_add_u32 m0, s83, 12288
s_add_u32 s72, s82, 3538944
buffer_load_dwordx4 v120, s[76:79], s72 offen lds
s_mov_b32 s84, 0xffff
s_waitcnt lgkmcnt(6)
v_mfma_f32_32x32x16_bf16 v[144:159], a[228:231], a[0:3], v[144:159]
ds_read_b64_tr_b16 a[244:245], v124 offset:4096
buffer_load_dwordx4 a[56:59], v1, s[16:19], s80 offen offset:3072
s_add_u32 s80, s80, 4096
s_mov_b32 s85, 0xfffff
v_mfma_f32_32x32x16_bf16 v[208:223], a[228:231], a[4:7], v[208:223]
ds_read_b64_tr_b16 a[246:247], v124 offset:5120
buffer_load_dwordx4 a[60:63], v1, s[16:19], s81 offen offset:3072
s_add_u32 s81, s81, 4096
s_waitcnt lgkmcnt(6)
v_mfma_f32_32x32x16_bf16 v[160:175], a[232:235], a[0:3], v[160:175]
ds_read_b64_tr_b16 a[248:249], v123 offset:6144
s_add_u32 m0, s83, 16384
s_add_u32 s72, s82, 3670016
buffer_load_dwordx4 v120, s[76:79], s72 offen lds
v_mfma_f32_32x32x16_bf16 v[224:239], a[232:235], a[4:7], v[224:239]
ds_read_b64_tr_b16 a[250:251], v123 offset:7168
s_waitcnt lgkmcnt(6)
v_mfma_f32_32x32x16_bf16 v[176:191], a[236:239], a[0:3], v[176:191]
ds_read_b64_tr_b16 a[252:253], v124 offset:6144
v_mfma_f32_32x32x16_bf16 v[240:255], a[236:239], a[4:7], v[240:255]
ds_read_b64_tr_b16 a[254:255], v124 offset:7168
s_waitcnt vmcnt(28) lgkmcnt(6)
v_mfma_f32_32x32x16_bf16 v[128:143], a[240:243], a[8:11], v[128:143]
ds_read_b64_tr_b16 a[224:225], v123 offset:8192
s_mov_b32 s86, 0x1ffff
s_mov_b32 s87, 0x1fffff
s_mov_b32 s88, 0x3ffff
s_mov_b32 s89, 0x3fffff
s_waitcnt vmcnt(27)
v_mfma_f32_32x32x16_bf16 v[192:207], a[240:243], a[12:15], v[192:207]
ds_read_b64_tr_b16 a[226:227], v123 offset:9216
s_add_u32 m0, s83, 20480
s_add_u32 s72, s82, 3801088
buffer_load_dwordx4 v120, s[76:79], s72 offen lds
s_mov_b32 s90, 0x7ffff
s_waitcnt lgkmcnt(6)
v_mfma_f32_32x32x16_bf16 v[144:159], a[244:247], a[8:11], v[144:159]
ds_read_b64_tr_b16 a[228:229], v124 offset:8192
s_add_u32 m0, s83, 24576
s_add_u32 s72, s82, 3932160
buffer_load_dwordx4 v120, s[76:79], s72 offen lds
s_mov_b32 s91, 0x7fffff
v_mfma_f32_32x32x16_bf16 v[208:223], a[244:247], a[12:15], v[208:223]
ds_read_b64_tr_b16 a[230:231], v124 offset:9216
s_add_u32 m0, s83, 28672
s_add_u32 s72, s82, 4063232
buffer_load_dwordx4 v120, s[76:79], s72 offen lds
s_waitcnt lgkmcnt(6)
v_mfma_f32_32x32x16_bf16 v[160:175], a[248:251], a[8:11], v[160:175]
ds_read_b64_tr_b16 a[232:233], v123 offset:10240
buffer_load_dwordx4 a[64:67], v1, s[16:19], s80 offen offset:0
v_mfma_f32_32x32x16_bf16 v[224:239], a[248:251], a[12:15], v[224:239]
ds_read_b64_tr_b16 a[234:235], v123 offset:11264
s_waitcnt lgkmcnt(6)
v_mfma_f32_32x32x16_bf16 v[176:191], a[252:255], a[8:11], v[176:191]
ds_read_b64_tr_b16 a[236:237], v124 offset:10240
v_mfma_f32_32x32x16_bf16 v[240:255], a[252:255], a[12:15], v[240:255]
ds_read_b64_tr_b16 a[238:239], v124 offset:11264
s_waitcnt vmcnt(30) lgkmcnt(6)
v_mfma_f32_32x32x16_bf16 v[128:143], a[224:227], a[16:19], v[128:143]
ds_read_b64_tr_b16 a[240:241], v123 offset:12288
s_mov_b32 s92, 0xffffff
s_mov_b32 s93, 0xfffffff
s_mov_b32 s94, 0x1ffffff
s_mov_b32 s95, 0x1fffffff
s_waitcnt vmcnt(29)
v_mfma_f32_32x32x16_bf16 v[192:207], a[224:227], a[20:23], v[192:207]
ds_read_b64_tr_b16 a[242:243], v123 offset:13312
buffer_load_dwordx4 a[68:71], v1, s[16:19], s81 offen offset:0
s_mov_b32 s96, 0x3ffffff
s_mov_b32 s97, 0x3fffffff
s_waitcnt lgkmcnt(6)
v_mfma_f32_32x32x16_bf16 v[144:159], a[228:231], a[16:19], v[144:159]
ds_read_b64_tr_b16 a[244:245], v124 offset:12288
s_add_u32 m0, s74, 16384
s_add_u32 s72, s75, 16793600
buffer_load_dwordx4 v1, s[16:19], s72 offen offset:0 lds
buffer_load_dwordx4 v1, s[16:19], s72 offen offset:1024 lds
v_mfma_f32_32x32x16_bf16 v[208:223], a[228:231], a[20:23], v[208:223]
ds_read_b64_tr_b16 a[246:247], v124 offset:13312
s_add_u32 m0, s74, 40960
s_add_u32 s72, s75, 33562624
buffer_load_dwordx4 v1, s[16:19], s72 offen offset:0 lds
buffer_load_dwordx4 v1, s[16:19], s72 offen offset:1024 lds
s_waitcnt lgkmcnt(6)
v_mfma_f32_32x32x16_bf16 v[160:175], a[232:235], a[16:19], v[160:175]
ds_read_b64_tr_b16 a[248:249], v123 offset:14336
s_add_u32 m0, s74, 24576
s_add_u32 s72, s75, 16801792
buffer_load_dwordx4 v1, s[16:19], s72 offen offset:0 lds
buffer_load_dwordx4 v1, s[16:19], s72 offen offset:1024 lds
v_mfma_f32_32x32x16_bf16 v[224:239], a[232:235], a[20:23], v[224:239]
ds_read_b64_tr_b16 a[250:251], v123 offset:15360
s_waitcnt lgkmcnt(6)
v_mfma_f32_32x32x16_bf16 v[176:191], a[236:239], a[16:19], v[176:191]
ds_read_b64_tr_b16 a[252:253], v124 offset:14336
v_mfma_f32_32x32x16_bf16 v[240:255], a[236:239], a[20:23], v[240:255]
ds_read_b64_tr_b16 a[254:255], v124 offset:15360
s_waitcnt vmcnt(31) lgkmcnt(6)
v_mfma_f32_32x32x16_bf16 v[128:143], a[240:243], a[24:27], v[128:143]
s_barrier
s_mov_b32 s98, 0x7ffffff
s_mov_b32 s99, 0x7fffffff
s_waitcnt vmcnt(30)
v_mfma_f32_32x32x16_bf16 v[192:207], a[240:243], a[28:31], v[192:207]
ds_read_b64_tr_b16 a[224:225], v123 offset:16384
s_add_u32 m0, s74, 49152
s_add_u32 s72, s75, 33570816
buffer_load_dwordx4 v1, s[16:19], s72 offen offset:0 lds
buffer_load_dwordx4 v1, s[16:19], s72 offen offset:1024 lds
s_waitcnt lgkmcnt(5)
v_mfma_f32_32x32x16_bf16 v[144:159], a[244:247], a[24:27], v[144:159]
ds_read_b64_tr_b16 a[226:227], v123 offset:17408
buffer_load_dwordx4 a[72:75], v1, s[16:19], s80 offen offset:1024
v_mfma_f32_32x32x16_bf16 v[208:223], a[244:247], a[28:31], v[208:223]
ds_read_b64_tr_b16 a[228:229], v124 offset:16384
buffer_load_dwordx4 a[76:79], v1, s[16:19], s81 offen offset:1024
s_waitcnt lgkmcnt(5)
v_mfma_f32_32x32x16_bf16 v[160:175], a[248:251], a[24:27], v[160:175]
ds_read_b64_tr_b16 a[230:231], v124 offset:17408
buffer_load_dwordx4 a[80:83], v1, s[16:19], s80 offen offset:2048
v_mfma_f32_32x32x16_bf16 v[224:239], a[248:251], a[28:31], v[224:239]
ds_read_b64_tr_b16 a[232:233], v123 offset:18432
s_waitcnt lgkmcnt(5)
v_mfma_f32_32x32x16_bf16 v[176:191], a[252:255], a[24:27], v[176:191]
ds_read_b64_tr_b16 a[234:235], v123 offset:19456
ds_read_b64_tr_b16 a[236:237], v124 offset:18432
v_mfma_f32_32x32x16_bf16 v[240:255], a[252:255], a[28:31], v[240:255]
ds_read_b64_tr_b16 a[238:239], v124 offset:19456
s_waitcnt vmcnt(34) lgkmcnt(6)
v_mfma_f32_32x32x16_bf16 v[128:143], a[224:227], a[32:35], v[128:143]
ds_read_b64_tr_b16 a[240:241], v123 offset:20480
s_waitcnt vmcnt(33)
v_mfma_f32_32x32x16_bf16 v[192:207], a[224:227], a[36:39], v[192:207]
ds_read_b64_tr_b16 a[242:243], v123 offset:21504
buffer_load_dwordx4 a[84:87], v1, s[16:19], s81 offen offset:2048
s_waitcnt lgkmcnt(6)
v_mfma_f32_32x32x16_bf16 v[144:159], a[228:231], a[32:35], v[144:159]
ds_read_b64_tr_b16 a[244:245], v124 offset:20480
buffer_load_dwordx4 a[88:91], v1, s[16:19], s80 offen offset:3072
s_add_u32 s80, s80, 4096
v_mfma_f32_32x32x16_bf16 v[208:223], a[228:231], a[36:39], v[208:223]
ds_read_b64_tr_b16 a[246:247], v124 offset:21504
buffer_load_dwordx4 a[92:95], v1, s[16:19], s81 offen offset:3072
s_add_u32 s81, s81, 4096
s_waitcnt lgkmcnt(6)
v_mfma_f32_32x32x16_bf16 v[160:175], a[232:235], a[32:35], v[160:175]
ds_read_b64_tr_b16 a[248:249], v123 offset:22528
v_mfma_f32_32x32x16_bf16 v[224:239], a[232:235], a[36:39], v[224:239]
ds_read_b64_tr_b16 a[250:251], v123 offset:23552
s_waitcnt lgkmcnt(6)
v_mfma_f32_32x32x16_bf16 v[176:191], a[236:239], a[32:35], v[176:191]
ds_read_b64_tr_b16 a[252:253], v124 offset:22528
v_mfma_f32_32x32x16_bf16 v[240:255], a[236:239], a[36:39], v[240:255]
ds_read_b64_tr_b16 a[254:255], v124 offset:23552
s_waitcnt vmcnt(29) lgkmcnt(6)
v_mfma_f32_32x32x16_bf16 v[128:143], a[240:243], a[40:43], v[128:143]
ds_read_b64_tr_b16 a[224:225], v123 offset:24576
s_waitcnt vmcnt(28)
v_mfma_f32_32x32x16_bf16 v[192:207], a[240:243], a[44:47], v[192:207]
ds_read_b64_tr_b16 a[226:227], v123 offset:25600
buffer_load_dwordx4 a[96:99], v1, s[16:19], s80 offen offset:0
s_waitcnt lgkmcnt(6)
v_mfma_f32_32x32x16_bf16 v[144:159], a[244:247], a[40:43], v[144:159]
ds_read_b64_tr_b16 a[228:229], v124 offset:24576
buffer_load_dwordx4 a[104:107], v1, s[16:19], s80 offen offset:1024
v_mfma_f32_32x32x16_bf16 v[208:223], a[244:247], a[44:47], v[208:223]
ds_read_b64_tr_b16 a[230:231], v124 offset:25600
buffer_load_dwordx4 a[112:115], v1, s[16:19], s80 offen offset:2048
s_waitcnt lgkmcnt(6)
v_mfma_f32_32x32x16_bf16 v[160:175], a[248:251], a[40:43], v[160:175]
ds_read_b64_tr_b16 a[232:233], v123 offset:26624
buffer_load_dwordx4 a[120:123], v1, s[16:19], s80 offen offset:3072
s_add_u32 s80, s80, 4096
v_mfma_f32_32x32x16_bf16 v[224:239], a[248:251], a[44:47], v[224:239]
ds_read_b64_tr_b16 a[234:235], v123 offset:27648
s_waitcnt lgkmcnt(6)
v_mfma_f32_32x32x16_bf16 v[176:191], a[252:255], a[40:43], v[176:191]
ds_read_b64_tr_b16 a[236:237], v124 offset:26624
v_mfma_f32_32x32x16_bf16 v[240:255], a[252:255], a[44:47], v[240:255]
ds_read_b64_tr_b16 a[238:239], v124 offset:27648
s_waitcnt vmcnt(31) lgkmcnt(6)
v_mfma_f32_32x32x16_bf16 v[128:143], a[224:227], a[48:51], v[128:143]
ds_read_b64_tr_b16 a[240:241], v123 offset:28672
s_waitcnt vmcnt(30)
v_mfma_f32_32x32x16_bf16 v[192:207], a[224:227], a[52:55], v[192:207]
ds_read_b64_tr_b16 a[242:243], v123 offset:29696
s_waitcnt lgkmcnt(6)
v_mfma_f32_32x32x16_bf16 v[144:159], a[228:231], a[48:51], v[144:159]
ds_read_b64_tr_b16 a[244:245], v124 offset:28672
v_mfma_f32_32x32x16_bf16 v[208:223], a[228:231], a[52:55], v[208:223]
ds_read_b64_tr_b16 a[246:247], v124 offset:29696
s_waitcnt lgkmcnt(6)
v_mfma_f32_32x32x16_bf16 v[160:175], a[232:235], a[48:51], v[160:175]
ds_read_b64_tr_b16 a[248:249], v123 offset:30720
v_mfma_f32_32x32x16_bf16 v[224:239], a[232:235], a[52:55], v[224:239]
ds_read_b64_tr_b16 a[250:251], v123 offset:31744
s_waitcnt lgkmcnt(6)
v_mfma_f32_32x32x16_bf16 v[176:191], a[236:239], a[48:51], v[176:191]
ds_read_b64_tr_b16 a[252:253], v124 offset:30720
v_mfma_f32_32x32x16_bf16 v[240:255], a[236:239], a[52:55], v[240:255]
ds_read_b64_tr_b16 a[254:255], v124 offset:31744
s_waitcnt vmcnt(25) lgkmcnt(6)
v_mfma_f32_32x32x16_bf16 v[128:143], a[240:243], a[56:59], v[128:143]
s_barrier
s_waitcnt vmcnt(24)
v_mfma_f32_32x32x16_bf16 v[192:207], a[240:243], a[60:63], v[192:207]
ds_read_b64_tr_b16 a[224:225], v121 offset:0
buffer_load_dwordx4 a[100:103], v1, s[16:19], s81 offen offset:0
s_waitcnt lgkmcnt(5)
v_mfma_f32_32x32x16_bf16 v[144:159], a[244:247], a[56:59], v[144:159]
ds_read_b64_tr_b16 a[226:227], v121 offset:1024
buffer_load_dwordx4 a[108:111], v1, s[16:19], s81 offen offset:1024
v_mfma_f32_32x32x16_bf16 v[208:223], a[244:247], a[60:63], v[208:223]
ds_read_b64_tr_b16 a[228:229], v122 offset:0
buffer_load_dwordx4 a[116:119], v1, s[16:19], s81 offen offset:2048
s_waitcnt lgkmcnt(5)
v_mfma_f32_32x32x16_bf16 v[160:175], a[248:251], a[56:59], v[160:175]
ds_read_b64_tr_b16 a[230:231], v122 offset:1024
buffer_load_dwordx4 a[124:127], v1, s[16:19], s81 offen offset:3072
s_add_u32 s81, s81, 4096
v_mfma_f32_32x32x16_bf16 v[224:239], a[248:251], a[60:63], v[224:239]
ds_read_b64_tr_b16 a[232:233], v121 offset:2048
s_waitcnt lgkmcnt(5)
v_mfma_f32_32x32x16_bf16 v[176:191], a[252:255], a[56:59], v[176:191]
ds_read_b64_tr_b16 a[234:235], v121 offset:3072
ds_read_b64_tr_b16 a[236:237], v122 offset:2048
v_mfma_f32_32x32x16_bf16 v[240:255], a[252:255], a[60:63], v[240:255]
ds_read_b64_tr_b16 a[238:239], v122 offset:3072
s_waitcnt vmcnt(23) lgkmcnt(6)
v_mfma_f32_32x32x16_bf16 v[128:143], a[224:227], a[64:67], v[128:143]
ds_read_b64_tr_b16 a[240:241], v121 offset:4096
s_waitcnt vmcnt(22)
v_mfma_f32_32x32x16_bf16 v[192:207], a[224:227], a[68:71], v[192:207]
ds_read_b64_tr_b16 a[242:243], v121 offset:5120
s_waitcnt lgkmcnt(6)
v_mfma_f32_32x32x16_bf16 v[144:159], a[228:231], a[64:67], v[144:159]
ds_read_b64_tr_b16 a[244:245], v122 offset:4096
v_mfma_f32_32x32x16_bf16 v[208:223], a[228:231], a[68:71], v[208:223]
ds_read_b64_tr_b16 a[246:247], v122 offset:5120
s_waitcnt lgkmcnt(6)
v_mfma_f32_32x32x16_bf16 v[160:175], a[232:235], a[64:67], v[160:175]
ds_read_b64_tr_b16 a[248:249], v121 offset:6144
v_mfma_f32_32x32x16_bf16 v[224:239], a[232:235], a[68:71], v[224:239]
ds_read_b64_tr_b16 a[250:251], v121 offset:7168
s_waitcnt lgkmcnt(6)
v_mfma_f32_32x32x16_bf16 v[176:191], a[236:239], a[64:67], v[176:191]
ds_read_b64_tr_b16 a[252:253], v122 offset:6144
v_mfma_f32_32x32x16_bf16 v[240:255], a[236:239], a[68:71], v[240:255]
ds_read_b64_tr_b16 a[254:255], v122 offset:7168
s_waitcnt vmcnt(13) lgkmcnt(6)
v_mfma_f32_32x32x16_bf16 v[128:143], a[240:243], a[72:75], v[128:143]
s_barrier
s_waitcnt vmcnt(12)
v_mfma_f32_32x32x16_bf16 v[192:207], a[240:243], a[76:79], v[192:207]
ds_read_b64_tr_b16 a[224:225], v121 offset:8192
s_waitcnt lgkmcnt(5)
v_mfma_f32_32x32x16_bf16 v[144:159], a[244:247], a[72:75], v[144:159]
ds_read_b64_tr_b16 a[226:227], v121 offset:9216
ds_read_b64_tr_b16 a[0:1], v121 offset:16384
v_mfma_f32_32x32x16_bf16 v[208:223], a[244:247], a[76:79], v[208:223]
ds_read_b64_tr_b16 a[228:229], v122 offset:8192
s_waitcnt lgkmcnt(6)
v_mfma_f32_32x32x16_bf16 v[160:175], a[248:251], a[72:75], v[160:175]
ds_read_b64_tr_b16 a[230:231], v122 offset:9216
ds_read_b64_tr_b16 a[2:3], v121 offset:17408
v_mfma_f32_32x32x16_bf16 v[224:239], a[248:251], a[76:79], v[224:239]
ds_read_b64_tr_b16 a[232:233], v121 offset:10240
ds_read_b64_tr_b16 a[4:5], v121 offset:20480
s_waitcnt lgkmcnt(8)
v_mfma_f32_32x32x16_bf16 v[176:191], a[252:255], a[72:75], v[176:191]
ds_read_b64_tr_b16 a[234:235], v121 offset:11264
ds_read_b64_tr_b16 a[236:237], v122 offset:10240
ds_read_b64_tr_b16 a[6:7], v121 offset:21504
v_mfma_f32_32x32x16_bf16 v[240:255], a[252:255], a[76:79], v[240:255]
ds_read_b64_tr_b16 a[238:239], v122 offset:11264
s_waitcnt vmcnt(11) lgkmcnt(10)
v_mfma_f32_32x32x16_bf16 v[128:143], a[224:227], a[80:83], v[128:143]
ds_read_b64_tr_b16 a[240:241], v121 offset:12288
ds_read_b64_tr_b16 a[8:9], v121 offset:24576
s_waitcnt vmcnt(10)
v_mfma_f32_32x32x16_bf16 v[192:207], a[224:227], a[84:87], v[192:207]
ds_read_b64_tr_b16 a[242:243], v121 offset:13312
ds_read_b64_tr_b16 a[10:11], v121 offset:25600
s_waitcnt lgkmcnt(11)
v_mfma_f32_32x32x16_bf16 v[144:159], a[228:231], a[80:83], v[144:159]
ds_read_b64_tr_b16 a[244:245], v122 offset:12288
ds_read_b64_tr_b16 a[12:13], v121 offset:28672
v_mfma_f32_32x32x16_bf16 v[208:223], a[228:231], a[84:87], v[208:223]
ds_read_b64_tr_b16 a[246:247], v122 offset:13312
ds_read_b64_tr_b16 a[14:15], v121 offset:29696
s_waitcnt lgkmcnt(11)
v_mfma_f32_32x32x16_bf16 v[160:175], a[232:235], a[80:83], v[160:175]
ds_read_b64_tr_b16 a[248:249], v121 offset:14336
ds_read_b64_tr_b16 a[16:17], v122 offset:16384
v_mfma_f32_32x32x16_bf16 v[224:239], a[232:235], a[84:87], v[224:239]
ds_read_b64_tr_b16 a[250:251], v121 offset:15360
ds_read_b64_tr_b16 a[18:19], v122 offset:17408
s_waitcnt lgkmcnt(12)
v_mfma_f32_32x32x16_bf16 v[176:191], a[236:239], a[80:83], v[176:191]
ds_read_b64_tr_b16 a[252:253], v122 offset:14336
v_mfma_f32_32x32x16_bf16 v[240:255], a[236:239], a[84:87], v[240:255]
ds_read_b64_tr_b16 a[254:255], v122 offset:15360
s_waitcnt vmcnt(9) lgkmcnt(11)
v_mfma_f32_32x32x16_bf16 v[128:143], a[240:243], a[88:91], v[128:143]
ds_read_b64_tr_b16 a[20:21], v122 offset:20480
ds_read_b64_tr_b16 a[22:23], v122 offset:21504
s_waitcnt vmcnt(8)
v_mfma_f32_32x32x16_bf16 v[192:207], a[240:243], a[92:95], v[192:207]
ds_read_b64_tr_b16 a[24:25], v122 offset:24576
s_waitcnt lgkmcnt(10)
v_mfma_f32_32x32x16_bf16 v[144:159], a[244:247], a[88:91], v[144:159]
ds_read_b64_tr_b16 a[26:27], v122 offset:25600
v_mfma_f32_32x32x16_bf16 v[208:223], a[244:247], a[92:95], v[208:223]
ds_read_b64_tr_b16 a[28:29], v122 offset:28672
s_waitcnt lgkmcnt(8)
v_mfma_f32_32x32x16_bf16 v[160:175], a[248:251], a[88:91], v[160:175]
ds_read_b64_tr_b16 a[30:31], v122 offset:29696
v_mfma_f32_32x32x16_bf16 v[224:239], a[248:251], a[92:95], v[224:239]
ds_read_b64_tr_b16 a[32:33], v121 offset:18432
s_waitcnt lgkmcnt(7)
v_mfma_f32_32x32x16_bf16 v[176:191], a[252:255], a[88:91], v[176:191]
ds_read_b64_tr_b16 a[34:35], v121 offset:19456
v_mfma_f32_32x32x16_bf16 v[240:255], a[252:255], a[92:95], v[240:255]
ds_read_b64_tr_b16 a[36:37], v121 offset:22528
ds_read_b64_tr_b16 a[38:39], v121 offset:23552
s_waitcnt vmcnt(7)
v_mfma_f32_32x32x16_bf16 v[128:143], a[0:3], a[96:99], v[128:143]
ds_read_b64_tr_b16 a[40:41], v121 offset:26624
ds_read_b64_tr_b16 a[42:43], v121 offset:27648
ds_read_b64_tr_b16 a[44:45], v121 offset:30720
ds_read_b64_tr_b16 a[46:47], v121 offset:31744
ds_read_b64_tr_b16 a[48:49], v122 offset:18432
ds_read_b64_tr_b16 a[50:51], v122 offset:19456
ds_read_b64_tr_b16 a[52:53], v122 offset:22528
s_waitcnt vmcnt(6)
v_mfma_f32_32x32x16_bf16 v[128:143], a[4:7], a[104:107], v[128:143]
ds_read_b64_tr_b16 a[54:55], v122 offset:23552
ds_read_b64_tr_b16 a[56:57], v122 offset:26624
ds_read_b64_tr_b16 a[58:59], v122 offset:27648
ds_read_b64_tr_b16 a[60:61], v122 offset:30720
ds_read_b64_tr_b16 a[62:63], v122 offset:31744
s_waitcnt vmcnt(5)
v_mfma_f32_32x32x16_bf16 v[128:143], a[8:11], a[112:115], v[128:143]
s_waitcnt vmcnt(4)
v_mfma_f32_32x32x16_bf16 v[128:143], a[12:15], a[120:123], v[128:143]
v_mfma_f32_32x32x16_bf16 v[144:159], a[16:19], a[96:99], v[144:159]
s_waitcnt lgkmcnt(15)
v_mfma_f32_32x32x16_bf16 v[144:159], a[20:23], a[104:107], v[144:159]
v_cvt_pk_bf16_f32 v128, v128, v129
v_cvt_pk_bf16_f32 v129, v130, v131
v_cvt_pk_bf16_f32 v130, v132, v133
v_cvt_pk_bf16_f32 v131, v134, v135
v_cvt_pk_bf16_f32 v132, v136, v137
v_cvt_pk_bf16_f32 v133, v138, v139
v_cvt_pk_bf16_f32 v134, v140, v141
v_mfma_f32_32x32x16_bf16 v[144:159], a[24:27], a[112:115], v[144:159]
s_barrier
v_cvt_pk_bf16_f32 v135, v142, v143
ds_write_b128 v125, v[128:131] offset:0
ds_write_b128 v125, v[132:135] offset:1024
ds_read_b128 a[128:131], v125 offset:0
ds_read_b128 a[132:135], v125 offset:1024
v_mfma_f32_32x32x16_bf16 v[144:159], a[28:31], a[120:123], v[144:159]
ds_read_b128 a[192:195], v2 offset:0
ds_read_b128 a[196:199], v2 offset:1024
ds_read_b128 a[200:203], v2 offset:2048
ds_read_b128 a[204:207], v2 offset:3072
ds_read_b128 a[208:211], v2 offset:4096
ds_read_b128 a[212:215], v2 offset:5120
ds_read_b128 a[216:219], v2 offset:6144
s_waitcnt lgkmcnt(15)
v_mfma_f32_32x32x16_bf16 v[160:175], a[32:35], a[96:99], v[160:175]
ds_read_b128 a[220:223], v2 offset:7168
ds_read_b128 a[224:227], v1 offset:32768
ds_read_b128 a[228:231], v1 offset:33792
ds_read_b128 a[232:235], v1 offset:34816
ds_read_b128 a[236:239], v1 offset:35840
ds_read_b128 a[240:243], v1 offset:36864
ds_read_b128 a[244:247], v1 offset:37888
v_mfma_f32_32x32x16_bf16 v[160:175], a[36:39], a[104:107], v[160:175]
v_cvt_pk_bf16_f32 v144, v144, v145
v_cvt_pk_bf16_f32 v145, v146, v147
v_cvt_pk_bf16_f32 v146, v148, v149
v_cvt_pk_bf16_f32 v147, v150, v151
v_cvt_pk_bf16_f32 v148, v152, v153
v_cvt_pk_bf16_f32 v149, v154, v155
v_cvt_pk_bf16_f32 v150, v156, v157
v_mfma_f32_32x32x16_bf16 v[160:175], a[40:43], a[112:115], v[160:175]
v_cvt_pk_bf16_f32 v151, v158, v159
ds_write_b128 v125, v[144:147] offset:2048
ds_write_b128 v125, v[148:151] offset:3072
ds_read_b128 a[136:139], v125 offset:2048
ds_read_b128 a[140:143], v125 offset:3072
ds_read_b128 a[248:251], v1 offset:38912
ds_read_b128 a[252:255], v1 offset:39936
v_mfma_f32_32x32x16_bf16 v[160:175], a[44:47], a[120:123], v[160:175]
v_mfma_f32_32x32x16_bf16 v[176:191], a[48:51], a[96:99], v[176:191]
v_mfma_f32_32x32x16_bf16 v[176:191], a[52:55], a[104:107], v[176:191]
v_cvt_pk_bf16_f32 v160, v160, v161
v_cvt_pk_bf16_f32 v161, v162, v163
v_cvt_pk_bf16_f32 v162, v164, v165
v_cvt_pk_bf16_f32 v163, v166, v167
v_cvt_pk_bf16_f32 v164, v168, v169
v_cvt_pk_bf16_f32 v165, v170, v171
v_cvt_pk_bf16_f32 v166, v172, v173
s_waitcnt lgkmcnt(15)
v_mfma_f32_32x32x16_bf16 v[176:191], a[56:59], a[112:115], v[176:191]
v_cvt_pk_bf16_f32 v167, v174, v175
ds_write_b128 v125, v[160:163] offset:4096
ds_write_b128 v125, v[164:167] offset:5120
ds_read_b128 a[144:147], v125 offset:4096
ds_read_b128 a[148:151], v125 offset:5120
v_mfma_f32_32x32x16_bf16 v[176:191], a[60:63], a[120:123], v[176:191]
s_waitcnt vmcnt(3)
v_mfma_f32_32x32x16_bf16 v[192:207], a[0:3], a[100:103], v[192:207]
s_waitcnt vmcnt(2)
v_mfma_f32_32x32x16_bf16 v[192:207], a[4:7], a[108:111], v[192:207]
s_cmp_eq_i32 s44, 0
s_cbranch_scc1 .Lm0
.Lm0b:
v_cvt_pk_bf16_f32 v176, v176, v177
v_cvt_pk_bf16_f32 v177, v178, v179
v_cvt_pk_bf16_f32 v178, v180, v181
v_cvt_pk_bf16_f32 v179, v182, v183
v_cvt_pk_bf16_f32 v180, v184, v185
v_cvt_pk_bf16_f32 v181, v186, v187
v_cvt_pk_bf16_f32 v182, v188, v189
s_waitcnt vmcnt(1)
v_mfma_f32_32x32x16_bf16 v[192:207], a[8:11], a[116:119], v[192:207]
v_cvt_pk_bf16_f32 v183, v190, v191
ds_write_b128 v125, v[176:179] offset:6144
ds_write_b128 v125, v[180:183] offset:7168
ds_read_b128 a[152:155], v125 offset:6144
ds_read_b128 a[156:159], v125 offset:7168
s_waitcnt vmcnt(0)
v_mfma_f32_32x32x16_bf16 v[192:207], a[12:15], a[124:127], v[192:207]
v_mfma_f32_32x32x16_bf16 v[32:47], a[192:195], a[128:131], v[64:79]
v_mfma_f32_32x32x16_bf16 v[32:47], a[196:199], a[132:135], v[32:47]
v_cvt_pk_bf16_f32 v192, v192, v193
v_cvt_pk_bf16_f32 v193, v194, v195
v_cvt_pk_bf16_f32 v194, v196, v197
v_cvt_pk_bf16_f32 v195, v198, v199
v_cvt_pk_bf16_f32 v196, v200, v201
v_cvt_pk_bf16_f32 v197, v202, v203
v_cvt_pk_bf16_f32 v198, v204, v205
s_waitcnt lgkmcnt(11)
v_mfma_f32_32x32x16_bf16 v[32:47], a[200:203], a[136:139], v[32:47]
v_cvt_pk_bf16_f32 v199, v206, v207
ds_write_b128 v125, v[192:195] offset:8192
ds_write_b128 v125, v[196:199] offset:9216
ds_read_b128 a[160:163], v125 offset:8192
ds_read_b128 a[164:167], v125 offset:9216
s_waitcnt lgkmcnt(14)
v_mfma_f32_32x32x16_bf16 v[32:47], a[204:207], a[140:143], v[32:47]
s_waitcnt lgkmcnt(9)
v_mfma_f32_32x32x16_bf16 v[32:47], a[208:211], a[144:147], v[32:47]
s_waitcnt lgkmcnt(8)
v_mfma_f32_32x32x16_bf16 v[32:47], a[212:215], a[148:151], v[32:47]
s_waitcnt lgkmcnt(5)
v_mfma_f32_32x32x16_bf16 v[32:47], a[216:219], a[152:155], v[32:47]
s_waitcnt lgkmcnt(4)
v_mfma_f32_32x32x16_bf16 v[32:47], a[220:223], a[156:159], v[32:47]
v_mfma_f32_32x32x16_bf16 v[208:223], a[16:19], a[100:103], v[208:223]
v_mfma_f32_32x32x16_bf16 v[208:223], a[20:23], a[108:111], v[208:223]
v_exp_f32 v112, v32
v_exp_f32 v113, v33
v_mfma_f32_32x32x16_bf16 v[208:223], a[24:27], a[116:119], v[208:223]
v_exp_f32 v114, v34
v_exp_f32 v115, v35
v_mfma_f32_32x32x16_bf16 v[208:223], a[28:31], a[124:127], v[208:223]
v_cvt_pk_bf16_f32 v96, v112, v113
v_exp_f32 v116, v36
v_mfma_f32_32x32x16_bf16 v[224:239], a[32:35], a[100:103], v[224:239]
v_exp_f32 v117, v37
v_cvt_pk_bf16_f32 v97, v114, v115
v_mfma_f32_32x32x16_bf16 v[224:239], a[36:39], a[108:111], v[224:239]
v_exp_f32 v118, v38
v_cvt_pk_bf16_f32 v208, v208, v209
v_cvt_pk_bf16_f32 v209, v210, v211
v_cvt_pk_bf16_f32 v210, v212, v213
v_mfma_f32_32x32x16_bf16 v[224:239], a[40:43], a[116:119], v[224:239]
v_cvt_pk_bf16_f32 v211, v214, v215
v_cvt_pk_bf16_f32 v212, v216, v217
v_cvt_pk_bf16_f32 v213, v218, v219
v_cvt_pk_bf16_f32 v214, v220, v221
v_cvt_pk_bf16_f32 v215, v222, v223
ds_write_b128 v125, v[208:211] offset:10240
ds_write_b128 v125, v[212:215] offset:11264
v_mfma_f32_32x32x16_bf16 v[224:239], a[44:47], a[124:127], v[224:239]
ds_read_b128 a[168:171], v125 offset:10240
ds_read_b128 a[172:175], v125 offset:11264
v_exp_f32 v119, v39
v_cvt_pk_bf16_f32 v98, v116, v117
v_mfma_f32_32x32x16_bf16 v[240:255], a[48:51], a[100:103], v[240:255]
v_exp_f32 v112, v40
v_exp_f32 v113, v41
v_mfma_f32_32x32x16_bf16 v[240:255], a[52:55], a[108:111], v[240:255]
v_cvt_pk_bf16_f32 v224, v224, v225
v_cvt_pk_bf16_f32 v225, v226, v227
v_cvt_pk_bf16_f32 v226, v228, v229
v_cvt_pk_bf16_f32 v227, v230, v231
v_cvt_pk_bf16_f32 v228, v232, v233
v_cvt_pk_bf16_f32 v229, v234, v235
v_cvt_pk_bf16_f32 v230, v236, v237
v_mfma_f32_32x32x16_bf16 v[240:255], a[56:59], a[116:119], v[240:255]
v_cvt_pk_bf16_f32 v231, v238, v239
ds_write_b128 v125, v[224:227] offset:12288
ds_write_b128 v125, v[228:231] offset:13312
ds_read_b128 a[176:179], v125 offset:12288
ds_read_b128 a[180:183], v125 offset:13312
v_cvt_pk_bf16_f32 v99, v118, v119
v_exp_f32 v114, v42
v_exp_f32 v115, v43
v_mfma_f32_32x32x16_bf16 v[240:255], a[60:63], a[124:127], v[240:255]
v_cvt_pk_bf16_f32 v100, v112, v113
v_mfma_f32_16x16x32_bf16 v[24:27], v[28:31], v[96:99], 0
s_waitcnt lgkmcnt(9)
v_mfma_f32_32x32x16_bf16 v[48:63], a[192:195], a[160:163], v[80:95]
v_exp_f32 v116, v44
v_exp_f32 v117, v45
s_waitcnt lgkmcnt(8)
v_mfma_f32_32x32x16_bf16 v[48:63], a[196:199], a[164:167], v[48:63]
ds_read_b128 a[192:195], v2 offset:8192
v_cvt_pk_bf16_f32 v101, v114, v115
v_exp_f32 v118, v46
ds_read_b128 a[196:199], v2 offset:9216
s_waitcnt lgkmcnt(7)
v_mfma_f32_32x32x16_bf16 v[48:63], a[200:203], a[168:171], v[48:63]
v_exp_f32 v119, v47
v_cvt_pk_bf16_f32 v102, v116, v117
ds_read_b128 a[200:203], v2 offset:10240
v_cvt_pk_bf16_f32 v103, v118, v119
s_waitcnt lgkmcnt(7)
v_mfma_f32_32x32x16_bf16 v[48:63], a[204:207], a[172:175], v[48:63]
v_mfma_f32_16x16x32_bf16 v[24:27], v[28:31], v[100:103], v[24:27]
ds_read_b128 a[204:207], v2 offset:11264
v_cvt_pk_bf16_f32 v240, v240, v241
v_cvt_pk_bf16_f32 v241, v242, v243
s_waitcnt lgkmcnt(5)
v_mfma_f32_32x32x16_bf16 v[48:63], a[208:211], a[176:179], v[48:63]
v_cvt_pk_bf16_f32 v242, v244, v245
v_cvt_pk_bf16_f32 v243, v246, v247
v_accvgpr_write_b32 a184, v240
v_accvgpr_write_b32 a185, v241
v_accvgpr_write_b32 a186, v242
v_accvgpr_write_b32 a187, v243
ds_read_b128 a[208:211], v2 offset:12288
s_waitcnt lgkmcnt(5)
v_mfma_f32_32x32x16_bf16 v[48:63], a[212:215], a[180:183], v[48:63]
v_cvt_pk_bf16_f32 v244, v248, v249
v_cvt_pk_bf16_f32 v245, v250, v251
v_cvt_pk_bf16_f32 v246, v252, v253
v_cvt_pk_bf16_f32 v247, v254, v255
v_accvgpr_write_b32 a188, v244
v_accvgpr_write_b32 a189, v245
v_accvgpr_write_b32 a190, v246
v_mfma_f32_32x32x16_bf16 v[48:63], a[216:219], a[184:187], v[48:63]
v_accvgpr_write_b32 a191, v247
ds_write_b128 v125, v[240:243] offset:14336
ds_write_b128 v125, v[244:247] offset:15360
ds_read_b128 a[212:215], v2 offset:13312
v_add_u32 v23, 0xcf800000, v24
v_cmp_le_u32 vcc, 0x2d000000, v23
s_cmp_eq_i32 s44, 0
v_mfma_f32_32x32x16_bf16 v[48:63], a[220:223], a[188:191], v[48:63]
ds_read_b128 a[216:219], v2 offset:14336
ds_read_b128 a[220:223], v2 offset:15360
s_cbranch_vccnz .Lr1
.Lr1b:
s_cbranch_scc1 .Ls1
.Ls1b:
v_add_f32 v10, v10, v24
s_waitcnt lgkmcnt(9)
v_mfma_f32_32x32x16_bf16 v[32:47], a[192:195], a[128:131], v[64:79]
s_add_u32 m0, s36, 0
s_waitcnt lgkmcnt(8)
v_mfma_f32_32x32x16_bf16 v[32:47], a[196:199], a[132:135], v[32:47]
v_exp_f32 v120, v48
v_exp_f32 v121, v49
s_waitcnt lgkmcnt(7)
v_mfma_f32_32x32x16_bf16 v[32:47], a[200:203], a[136:139], v[32:47]
v_cvt_pk_bf16_f32 v104, v120, v121
v_exp_f32 v122, v50
v_exp_f32 v123, v51
s_waitcnt lgkmcnt(6)
v_mfma_f32_32x32x16_bf16 v[32:47], a[204:207], a[140:143], v[32:47]
v_cvt_pk_bf16_f32 v105, v122, v123
v_exp_f32 v124, v52
v_exp_f32 v125, v53
s_waitcnt lgkmcnt(5)
v_mfma_f32_32x32x16_bf16 v[32:47], a[208:211], a[144:147], v[32:47]
v_cvt_pk_bf16_f32 v106, v124, v125
v_exp_f32 v126, v54
v_exp_f32 v127, v55
s_waitcnt lgkmcnt(2)
v_mfma_f32_32x32x16_bf16 v[32:47], a[212:215], a[148:151], v[32:47]
v_cvt_pk_bf16_f32 v107, v126, v127
v_exp_f32 v120, v56
v_exp_f32 v121, v57
s_waitcnt lgkmcnt(1)
v_mfma_f32_32x32x16_bf16 v[32:47], a[216:219], a[152:155], v[32:47]
v_cvt_pk_bf16_f32 v108, v120, v121
v_exp_f32 v122, v58
v_exp_f32 v123, v59
v_mfma_f32_16x16x32_bf16 v[24:27], v[28:31], v[104:107], 0
s_waitcnt lgkmcnt(0)
s_barrier
v_mfma_f32_32x32x16_bf16 v[32:47], a[220:223], a[156:159], v[32:47]
v_cvt_pk_bf16_f32 v109, v122, v123
v_exp_f32 v124, v60
v_exp_f32 v125, v61
buffer_load_dwordx4 v1, s[20:23], s24 offen offset:0 lds
v_mfma_f32_32x32x16_bf16 v[128:143], a[224:227], v[96:99], 0
v_cvt_pk_bf16_f32 v110, v124, v125
v_exp_f32 v126, v62
v_exp_f32 v127, v63
buffer_load_dwordx4 v1, s[20:23], s24 offen offset:1024 lds
v_mfma_f32_32x32x16_bf16 v[144:159], a[228:231], v[96:99], 0
v_cvt_pk_bf16_f32 v111, v126, v127
v_exp_f32 v112, v32
buffer_load_dwordx4 v1, s[20:23], s24 offen offset:2048 lds
buffer_load_dwordx4 v1, s[20:23], s24 offen offset:3072 lds
s_add_u32 m0, s36, 57344
v_mfma_f32_32x32x16_bf16 v[160:175], a[232:235], v[96:99], 0
v_exp_f32 v113, v33
v_mfma_f32_16x16x32_bf16 v[24:27], v[28:31], v[108:111], v[24:27]
buffer_load_dwordx4 v3, s[20:23], s24 offen offset:0 lds
v_mfma_f32_32x32x16_bf16 v[176:191], a[236:239], v[96:99], 0
v_cvt_pk_bf16_f32 v96, v112, v113
v_exp_f32 v114, v34
buffer_load_dwordx4 v3, s[20:23], s24 offen offset:1024 lds
buffer_load_dwordx4 v3, s[20:23], s24 offen offset:2048 lds
v_mfma_f32_32x32x16_bf16 v[128:143], a[240:243], v[100:103], v[128:143]
v_exp_f32 v115, v35
v_add_u32 v23, 0xcf800000, v24
v_cmp_le_u32 vcc, 0x2d000000, v23
s_cmp_eq_i32 s44, 0
v_mfma_f32_32x32x16_bf16 v[144:159], a[244:247], v[100:103], v[144:159]
v_cvt_pk_bf16_f32 v97, v114, v115
v_exp_f32 v116, v36
s_cbranch_vccnz .Lr3
.Lr3b:
s_cbranch_scc1 .Ls3
.Ls3b:
v_mfma_f32_32x32x16_bf16 v[160:175], a[248:251], v[100:103], v[160:175]
v_exp_f32 v117, v37
v_add_f32 v11, v11, v24
buffer_load_dwordx4 v3, s[20:23], s24 offen offset:3072 lds
v_mfma_f32_32x32x16_bf16 v[176:191], a[252:255], v[100:103], v[176:191]
v_cvt_pk_bf16_f32 v98, v116, v117
v_exp_f32 v118, v38
s_barrier
s_add_u32 s24, s24, 0x2000
v_mfma_f32_32x32x16_bf16 v[48:63], a[192:195], a[160:163], v[80:95]
v_exp_f32 v119, v39
ds_read_b128 a[192:195], v2 offset:16384
v_mfma_f32_32x32x16_bf16 v[48:63], a[196:199], a[164:167], v[48:63]
v_cvt_pk_bf16_f32 v99, v118, v119
v_exp_f32 v112, v40
ds_read_b128 a[196:199], v2 offset:17408
v_mfma_f32_32x32x16_bf16 v[48:63], a[200:203], a[168:171], v[48:63]
v_exp_f32 v113, v41
v_mfma_f32_16x16x32_bf16 v[24:27], v[28:31], v[96:99], 0
ds_read_b128 a[200:203], v2 offset:18432
v_mfma_f32_32x32x16_bf16 v[48:63], a[204:207], a[172:175], v[48:63]
v_cvt_pk_bf16_f32 v100, v112, v113
v_exp_f32 v114, v42
ds_read_b128 a[204:207], v2 offset:19456
v_mfma_f32_32x32x16_bf16 v[48:63], a[208:211], a[176:179], v[48:63]
v_exp_f32 v115, v43
ds_read_b128 a[208:211], v2 offset:20480
v_mfma_f32_32x32x16_bf16 v[48:63], a[212:215], a[180:183], v[48:63]
v_cvt_pk_bf16_f32 v101, v114, v115
v_exp_f32 v116, v44
ds_read_b128 a[212:215], v2 offset:21504
v_mfma_f32_32x32x16_bf16 v[48:63], a[216:219], a[184:187], v[48:63]
v_exp_f32 v117, v45
ds_read_b128 a[216:219], v2 offset:22528
v_mfma_f32_32x32x16_bf16 v[48:63], a[220:223], a[188:191], v[48:63]
v_cvt_pk_bf16_f32 v102, v116, v117
v_exp_f32 v118, v46
ds_read_b128 a[220:223], v2 offset:23552
v_mfma_f32_32x32x16_bf16 v[192:207], a[224:227], v[104:107], 0
v_exp_f32 v119, v47
ds_read_b128 a[224:227], v1 offset:40960
v_mfma_f32_32x32x16_bf16 v[208:223], a[228:231], v[104:107], 0
v_cvt_pk_bf16_f32 v103, v118, v119
v_exp_f32 v120, v48
ds_read_b128 a[228:231], v1 offset:41984
v_mfma_f32_32x32x16_bf16 v[224:239], a[232:235], v[104:107], 0
v_mfma_f32_16x16x32_bf16 v[24:27], v[28:31], v[100:103], v[24:27]
v_exp_f32 v121, v49
ds_read_b128 a[232:235], v1 offset:43008
v_mfma_f32_32x32x16_bf16 v[240:255], a[236:239], v[104:107], 0
v_cvt_pk_bf16_f32 v104, v120, v121
v_exp_f32 v122, v50
ds_read_b128 a[236:239], v1 offset:44032
v_mfma_f32_32x32x16_bf16 v[192:207], a[240:243], v[108:111], v[192:207]
v_exp_f32 v123, v51
v_cmp_ngt_f32 vcc, s71, v24
ds_read_b128 a[240:243], v1 offset:45056
v_mfma_f32_32x32x16_bf16 v[208:223], a[244:247], v[108:111], v[208:223]
v_cvt_pk_bf16_f32 v105, v122, v123
v_exp_f32 v124, v52
s_cbranch_vccnz .Lr2
.Lr2b:
v_mfma_f32_32x32x16_bf16 v[224:239], a[248:251], v[108:111], v[224:239]
v_exp_f32 v125, v53
v_add_f32 v10, v10, v24
ds_read_b128 a[244:247], v1 offset:46080
ds_read_b128 a[248:251], v1 offset:47104
v_mfma_f32_32x32x16_bf16 v[240:255], a[252:255], v[108:111], v[240:255]
v_cvt_pk_bf16_f32 v106, v124, v125
v_exp_f32 v126, v54
ds_read_b128 a[252:255], v1 offset:48128
s_waitcnt lgkmcnt(8)
s_cmp_eq_u32 s51, 2
s_cbranch_scc1 .Len2
s_cmp_eq_u32 s51, 3
s_cbranch_scc1 .Len3
s_cmp_eq_u32 s51, 1
s_cbranch_scc1 .Lmain1
.Lmain0:
s_cmp_eq_u32 s48, 4
s_cbranch_scc1 .Lyl0_2
s_branch .Ly0_2
.Ly0_0:
v_mfma_f32_32x32x16_bf16 v[32:47], a[192:195], a[128:131], v[64:79]
v_exp_f32 v127, v55
v_mfma_f32_32x32x16_bf16 v[32:47], a[196:199], a[132:135], v[32:47]
v_cvt_pk_bf16_f32 v107, v126, v127
v_exp_f32 v120, v56
v_mfma_f32_32x32x16_bf16 v[32:47], a[200:203], a[136:139], v[32:47]
v_exp_f32 v121, v57
v_mfma_f32_16x16x32_bf16 v[24:27], v[28:31], v[104:107], 0
v_mfma_f32_32x32x16_bf16 v[32:47], a[204:207], a[140:143], v[32:47]
v_cvt_pk_bf16_f32 v108, v120, v121
v_exp_f32 v122, v58
v_mfma_f32_32x32x16_bf16 v[32:47], a[208:211], a[144:147], v[32:47]
v_exp_f32 v123, v59
v_mfma_f32_32x32x16_bf16 v[32:47], a[212:215], a[148:151], v[32:47]
v_cvt_pk_bf16_f32 v109, v122, v123
v_exp_f32 v124, v60
v_mfma_f32_32x32x16_bf16 v[32:47], a[216:219], a[152:155], v[32:47]
v_exp_f32 v125, v61
v_mfma_f32_32x32x16_bf16 v[32:47], a[220:223], a[156:159], v[32:47]
v_cvt_pk_bf16_f32 v110, v124, v125
v_exp_f32 v126, v62
s_waitcnt lgkmcnt(0)
v_mfma_f32_32x32x16_bf16 v[128:143], a[224:227], v[96:99], v[128:143]
v_exp_f32 v127, v63
v_mfma_f32_32x32x16_bf16 v[144:159], a[228:231], v[96:99], v[144:159]
v_cvt_pk_bf16_f32 v111, v126, v127
v_exp_f32 v112, v32
v_mfma_f32_32x32x16_bf16 v[160:175], a[232:235], v[96:99], v[160:175]
v_exp_f32 v113, v33
v_mfma_f32_16x16x32_bf16 v[24:27], v[28:31], v[108:111], v[24:27]
v_mfma_f32_32x32x16_bf16 v[176:191], a[236:239], v[96:99], v[176:191]
v_cvt_pk_bf16_f32 v96, v112, v113
v_exp_f32 v114, v34
v_mfma_f32_32x32x16_bf16 v[128:143], a[240:243], v[100:103], v[128:143]
v_exp_f32 v115, v35
v_cmp_ngt_f32 vcc, s71, v24
v_mfma_f32_32x32x16_bf16 v[144:159], a[244:247], v[100:103], v[144:159]
v_cvt_pk_bf16_f32 v97, v114, v115
v_exp_f32 v116, v36
s_cbranch_vccnz .Lr16
.Lr16b:
v_mfma_f32_32x32x16_bf16 v[160:175], a[248:251], v[100:103], v[160:175]
v_exp_f32 v117, v37
v_add_f32 v11, v11, v24
v_mfma_f32_32x32x16_bf16 v[176:191], a[252:255], v[100:103], v[176:191]
v_cvt_pk_bf16_f32 v98, v116, v117
v_exp_f32 v118, v38
s_waitcnt vmcnt(0)
s_barrier
s_add_u32 s24, s24, 0x2000
v_mfma_f32_32x32x16_bf16 v[48:63], a[192:195], a[160:163], v[80:95]
v_exp_f32 v119, v39
ds_read_b128 a[192:195], v2 offset:8192
v_mfma_f32_32x32x16_bf16 v[48:63], a[196:199], a[164:167], v[48:63]
v_cvt_pk_bf16_f32 v99, v118, v119
v_exp_f32 v112, v40
ds_read_b128 a[196:199], v2 offset:9216
v_mfma_f32_32x32x16_bf16 v[48:63], a[200:203], a[168:171], v[48:63]
v_exp_f32 v113, v41
v_mfma_f32_16x16x32_bf16 v[24:27], v[28:31], v[96:99], 0
ds_read_b128 a[200:203], v2 offset:10240
v_mfma_f32_32x32x16_bf16 v[48:63], a[204:207], a[172:175], v[48:63]
v_cvt_pk_bf16_f32 v100, v112, v113
v_exp_f32 v114, v42
ds_read_b128 a[204:207], v2 offset:11264
v_mfma_f32_32x32x16_bf16 v[48:63], a[208:211], a[176:179], v[48:63]
v_exp_f32 v115, v43
ds_read_b128 a[208:211], v2 offset:12288
v_mfma_f32_32x32x16_bf16 v[48:63], a[212:215], a[180:183], v[48:63]
v_cvt_pk_bf16_f32 v101, v114, v115
v_exp_f32 v116, v44
ds_read_b128 a[212:215], v2 offset:13312
v_mfma_f32_32x32x16_bf16 v[48:63], a[216:219], a[184:187], v[48:63]
v_exp_f32 v117, v45
ds_read_b128 a[216:219], v2 offset:14336
v_mfma_f32_32x32x16_bf16 v[48:63], a[220:223], a[188:191], v[48:63]
v_cvt_pk_bf16_f32 v102, v116, v117
v_exp_f32 v118, v46
ds_read_b128 a[220:223], v2 offset:15360
v_mfma_f32_32x32x16_bf16 v[192:207], a[224:227], v[104:107], v[192:207]
v_exp_f32 v119, v47
ds_read_b128 a[224:227], v1 offset:32768
v_mfma_f32_32x32x16_bf16 v[208:223], a[228:231], v[104:107], v[208:223]
v_cvt_pk_bf16_f32 v103, v118, v119
v_exp_f32 v120, v48
ds_read_b128 a[228:231], v1 offset:33792
v_mfma_f32_32x32x16_bf16 v[224:239], a[232:235], v[104:107], v[224:239]
v_mfma_f32_16x16x32_bf16 v[24:27], v[28:31], v[100:103], v[24:27]
v_exp_f32 v121, v49
ds_read_b128 a[232:235], v1 offset:34816
v_mfma_f32_32x32x16_bf16 v[240:255], a[236:239], v[104:107], v[240:255]
v_cvt_pk_bf16_f32 v104, v120, v121
v_exp_f32 v122, v50
ds_read_b128 a[236:239], v1 offset:35840
v_mfma_f32_32x32x16_bf16 v[192:207], a[240:243], v[108:111], v[192:207]
v_exp_f32 v123, v51
v_cmp_ngt_f32 vcc, s71, v24
ds_read_b128 a[240:243], v1 offset:36864
v_mfma_f32_32x32x16_bf16 v[208:223], a[244:247], v[108:111], v[208:223]
v_cvt_pk_bf16_f32 v105, v122, v123
v_exp_f32 v124, v52
s_cbranch_vccnz .Lr15
.Lr15b:
v_mfma_f32_32x32x16_bf16 v[224:239], a[248:251], v[108:111], v[224:239]
v_exp_f32 v125, v53
v_add_f32 v10, v10, v24
ds_read_b128 a[244:247], v1 offset:37888
ds_read_b128 a[248:251], v1 offset:38912
v_mfma_f32_32x32x16_bf16 v[240:255], a[252:255], v[108:111], v[240:255]
v_cvt_pk_bf16_f32 v106, v124, v125
v_exp_f32 v126, v54
ds_read_b128 a[252:255], v1 offset:39936
s_waitcnt lgkmcnt(8)
v_mfma_f32_32x32x16_bf16 v[32:47], a[192:195], a[128:131], v[64:79]
v_exp_f32 v127, v55
v_mfma_f32_32x32x16_bf16 v[32:47], a[196:199], a[132:135], v[32:47]
v_cvt_pk_bf16_f32 v107, v126, v127
v_exp_f32 v120, v56
v_mfma_f32_32x32x16_bf16 v[32:47], a[200:203], a[136:139], v[32:47]
v_exp_f32 v121, v57
v_mfma_f32_16x16x32_bf16 v[24:27], v[28:31], v[104:107], 0
v_mfma_f32_32x32x16_bf16 v[32:47], a[204:207], a[140:143], v[32:47]
v_cvt_pk_bf16_f32 v108, v120, v121
v_exp_f32 v122, v58
v_mfma_f32_32x32x16_bf16 v[32:47], a[208:211], a[144:147], v[32:47]
v_exp_f32 v123, v59
v_mfma_f32_32x32x16_bf16 v[32:47], a[212:215], a[148:151], v[32:47]
v_cvt_pk_bf16_f32 v109, v122, v123
v_exp_f32 v124, v60
v_mfma_f32_32x32x16_bf16 v[32:47], a[216:219], a[152:155], v[32:47]
v_exp_f32 v125, v61
v_mfma_f32_32x32x16_bf16 v[32:47], a[220:223], a[156:159], v[32:47]
v_cvt_pk_bf16_f32 v110, v124, v125
v_exp_f32 v126, v62
s_waitcnt lgkmcnt(0)
v_mfma_f32_32x32x16_bf16 v[128:143], a[224:227], v[96:99], v[128:143]
v_exp_f32 v127, v63
v_mfma_f32_32x32x16_bf16 v[144:159], a[228:231], v[96:99], v[144:159]
v_cvt_pk_bf16_f32 v111, v126, v127
v_exp_f32 v112, v32
v_mfma_f32_32x32x16_bf16 v[160:175], a[232:235], v[96:99], v[160:175]
v_exp_f32 v113, v33
v_mfma_f32_16x16x32_bf16 v[24:27], v[28:31], v[108:111], v[24:27]
v_mfma_f32_32x32x16_bf16 v[176:191], a[236:239], v[96:99], v[176:191]
v_cvt_pk_bf16_f32 v96, v112, v113
v_exp_f32 v114, v34
v_mfma_f32_32x32x16_bf16 v[128:143], a[240:243], v[100:103], v[128:143]
v_exp_f32 v115, v35
v_cmp_ngt_f32 vcc, s71, v24
v_mfma_f32_32x32x16_bf16 v[144:159], a[244:247], v[100:103], v[144:159]
v_cvt_pk_bf16_f32 v97, v114, v115
v_exp_f32 v116, v36
s_cbranch_vccnz .Lr18
.Lr18b:
v_mfma_f32_32x32x16_bf16 v[160:175], a[248:251], v[100:103], v[160:175]
v_exp_f32 v117, v37
v_add_f32 v11, v11, v24
v_mfma_f32_32x32x16_bf16 v[176:191], a[252:255], v[100:103], v[176:191]
v_cvt_pk_bf16_f32 v98, v116, v117
v_exp_f32 v118, v38
s_waitcnt vmcnt(0)
s_barrier
s_add_u32 s24, s24, 0x2000
v_mfma_f32_32x32x16_bf16 v[48:63], a[192:195], a[160:163], v[80:95]
v_exp_f32 v119, v39
ds_read_b128 a[192:195], v2 offset:16384
v_mfma_f32_32x32x16_bf16 v[48:63], a[196:199], a[164:167], v[48:63]
v_cvt_pk_bf16_f32 v99, v118, v119
v_exp_f32 v112, v40
ds_read_b128 a[196:199], v2 offset:17408
v_mfma_f32_32x32x16_bf16 v[48:63], a[200:203], a[168:171], v[48:63]
v_exp_f32 v113, v41
v_mfma_f32_16x16x32_bf16 v[24:27], v[28:31], v[96:99], 0
ds_read_b128 a[200:203], v2 offset:18432
v_mfma_f32_32x32x16_bf16 v[48:63], a[204:207], a[172:175], v[48:63]
v_cvt_pk_bf16_f32 v100, v112, v113
v_exp_f32 v114, v42
ds_read_b128 a[204:207], v2 offset:19456
v_mfma_f32_32x32x16_bf16 v[48:63], a[208:211], a[176:179], v[48:63]
v_exp_f32 v115, v43
ds_read_b128 a[208:211], v2 offset:20480
v_mfma_f32_32x32x16_bf16 v[48:63], a[212:215], a[180:183], v[48:63]
v_cvt_pk_bf16_f32 v101, v114, v115
v_exp_f32 v116, v44
ds_read_b128 a[212:215], v2 offset:21504
v_mfma_f32_32x32x16_bf16 v[48:63], a[216:219], a[184:187], v[48:63]
v_exp_f32 v117, v45
ds_read_b128 a[216:219], v2 offset:22528
v_mfma_f32_32x32x16_bf16 v[48:63], a[220:223], a[188:191], v[48:63]
v_cvt_pk_bf16_f32 v102, v116, v117
v_exp_f32 v118, v46
ds_read_b128 a[220:223], v2 offset:23552
v_mfma_f32_32x32x16_bf16 v[192:207], a[224:227], v[104:107], v[192:207]
v_exp_f32 v119, v47
ds_read_b128 a[224:227], v1 offset:40960
v_mfma_f32_32x32x16_bf16 v[208:223], a[228:231], v[104:107], v[208:223]
v_cvt_pk_bf16_f32 v103, v118, v119
v_exp_f32 v120, v48
ds_read_b128 a[228:231], v1 offset:41984
v_mfma_f32_32x32x16_bf16 v[224:239], a[232:235], v[104:107], v[224:239]
v_mfma_f32_16x16x32_bf16 v[24:27], v[28:31], v[100:103], v[24:27]
v_exp_f32 v121, v49
ds_read_b128 a[232:235], v1 offset:43008
v_mfma_f32_32x32x16_bf16 v[240:255], a[236:239], v[104:107], v[240:255]
v_cvt_pk_bf16_f32 v104, v120, v121
v_exp_f32 v122, v50
ds_read_b128 a[236:239], v1 offset:44032
v_mfma_f32_32x32x16_bf16 v[192:207], a[240:243], v[108:111], v[192:207]
v_exp_f32 v123, v51
v_cmp_ngt_f32 vcc, s71, v24
ds_read_b128 a[240:243], v1 offset:45056
v_mfma_f32_32x32x16_bf16 v[208:223], a[244:247], v[108:111], v[208:223]
v_cvt_pk_bf16_f32 v105, v122, v123
v_exp_f32 v124, v52
s_cbranch_vccnz .Lr17
.Lr17b:
v_mfma_f32_32x32x16_bf16 v[224:239], a[248:251], v[108:111], v[224:239]
v_exp_f32 v125, v53
v_add_f32 v10, v10, v24
ds_read_b128 a[244:247], v1 offset:46080
ds_read_b128 a[248:251], v1 offset:47104
v_mfma_f32_32x32x16_bf16 v[240:255], a[252:255], v[108:111], v[240:255]
v_cvt_pk_bf16_f32 v106, v124, v125
v_exp_f32 v126, v54
ds_read_b128 a[252:255], v1 offset:48128
s_waitcnt lgkmcnt(8)
.Ly0_2:
v_mfma_f32_32x32x16_bf16 v[32:47], a[192:195], a[128:131], v[64:79]
v_exp_f32 v127, v55
v_mfma_f32_32x32x16_bf16 v[32:47], a[196:199], a[132:135], v[32:47]
v_cvt_pk_bf16_f32 v107, v126, v127
v_exp_f32 v120, v56
v_mfma_f32_32x32x16_bf16 v[32:47], a[200:203], a[136:139], v[32:47]
v_exp_f32 v121, v57
v_mfma_f32_16x16x32_bf16 v[24:27], v[28:31], v[104:107], 0
v_mfma_f32_32x32x16_bf16 v[32:47], a[204:207], a[140:143], v[32:47]
v_cvt_pk_bf16_f32 v108, v120, v121
v_exp_f32 v122, v58
v_mfma_f32_32x32x16_bf16 v[32:47], a[208:211], a[144:147], v[32:47]
v_exp_f32 v123, v59
v_mfma_f32_32x32x16_bf16 v[32:47], a[212:215], a[148:151], v[32:47]
v_cvt_pk_bf16_f32 v109, v122, v123
v_exp_f32 v124, v60
v_mfma_f32_32x32x16_bf16 v[32:47], a[216:219], a[152:155], v[32:47]
v_exp_f32 v125, v61
v_mfma_f32_32x32x16_bf16 v[32:47], a[220:223], a[156:159], v[32:47]
v_cvt_pk_bf16_f32 v110, v124, v125
v_exp_f32 v126, v62
s_waitcnt lgkmcnt(0)
v_mfma_f32_32x32x16_bf16 v[128:143], a[224:227], v[96:99], v[128:143]
v_exp_f32 v127, v63
v_mfma_f32_32x32x16_bf16 v[144:159], a[228:231], v[96:99], v[144:159]
v_cvt_pk_bf16_f32 v111, v126, v127
v_exp_f32 v112, v32
v_mfma_f32_32x32x16_bf16 v[160:175], a[232:235], v[96:99], v[160:175]
v_exp_f32 v113, v33
v_mfma_f32_16x16x32_bf16 v[24:27], v[28:31], v[108:111], v[24:27]
v_mfma_f32_32x32x16_bf16 v[176:191], a[236:239], v[96:99], v[176:191]
v_cvt_pk_bf16_f32 v96, v112, v113
v_exp_f32 v114, v34
v_mfma_f32_32x32x16_bf16 v[128:143], a[240:243], v[100:103], v[128:143]
v_exp_f32 v115, v35
v_cmp_ngt_f32 vcc, s71, v24
v_mfma_f32_32x32x16_bf16 v[144:159], a[244:247], v[100:103], v[144:159]
v_cvt_pk_bf16_f32 v97, v114, v115
v_exp_f32 v116, v36
s_cbranch_vccnz .Lr20
.Lr20b:
v_mfma_f32_32x32x16_bf16 v[160:175], a[248:251], v[100:103], v[160:175]
v_exp_f32 v117, v37
v_add_f32 v11, v11, v24
v_mfma_f32_32x32x16_bf16 v[176:191], a[252:255], v[100:103], v[176:191]
v_cvt_pk_bf16_f32 v98, v116, v117
v_exp_f32 v118, v38
s_waitcnt vmcnt(0)
s_barrier
s_add_u32 s24, s24, 0x2000
v_mfma_f32_32x32x16_bf16 v[48:63], a[192:195], a[160:163], v[80:95]
v_exp_f32 v119, v39
ds_read_b128 a[192:195], v2 offset:24576
v_mfma_f32_32x32x16_bf16 v[48:63], a[196:199], a[164:167], v[48:63]
v_cvt_pk_bf16_f32 v99, v118, v119
v_exp_f32 v112, v40
ds_read_b128 a[196:199], v2 offset:25600
v_mfma_f32_32x32x16_bf16 v[48:63], a[200:203], a[168:171], v[48:63]
v_exp_f32 v113, v41
v_mfma_f32_16x16x32_bf16 v[24:27], v[28:31], v[96:99], 0
ds_read_b128 a[200:203], v2 offset:26624
v_mfma_f32_32x32x16_bf16 v[48:63], a[204:207], a[172:175], v[48:63]
v_cvt_pk_bf16_f32 v100, v112, v113
v_exp_f32 v114, v42
ds_read_b128 a[204:207], v2 offset:27648
v_mfma_f32_32x32x16_bf16 v[48:63], a[208:211], a[176:179], v[48:63]
v_exp_f32 v115, v43
ds_read_b128 a[208:211], v2 offset:28672
v_mfma_f32_32x32x16_bf16 v[48:63], a[212:215], a[180:183], v[48:63]
v_cvt_pk_bf16_f32 v101, v114, v115
v_exp_f32 v116, v44
ds_read_b128 a[212:215], v2 offset:29696
v_mfma_f32_32x32x16_bf16 v[48:63], a[216:219], a[184:187], v[48:63]
v_exp_f32 v117, v45
ds_read_b128 a[216:219], v2 offset:30720
v_mfma_f32_32x32x16_bf16 v[48:63], a[220:223], a[188:191], v[48:63]
v_cvt_pk_bf16_f32 v102, v116, v117
v_exp_f32 v118, v46
ds_read_b128 a[220:223], v2 offset:31744
v_mfma_f32_32x32x16_bf16 v[192:207], a[224:227], v[104:107], v[192:207]
v_exp_f32 v119, v47
ds_read_b128 a[224:227], v1 offset:49152
v_mfma_f32_32x32x16_bf16 v[208:223], a[228:231], v[104:107], v[208:223]
v_cvt_pk_bf16_f32 v103, v118, v119
v_exp_f32 v120, v48
ds_read_b128 a[228:231], v1 offset:50176
v_mfma_f32_32x32x16_bf16 v[224:239], a[232:235], v[104:107], v[224:239]
v_mfma_f32_16x16x32_bf16 v[24:27], v[28:31], v[100:103], v[24:27]
v_exp_f32 v121, v49
ds_read_b128 a[232:235], v1 offset:51200
v_mfma_f32_32x32x16_bf16 v[240:255], a[236:239], v[104:107], v[240:255]
v_cvt_pk_bf16_f32 v104, v120, v121
v_exp_f32 v122, v50
ds_read_b128 a[236:239], v1 offset:52224
v_mfma_f32_32x32x16_bf16 v[192:207], a[240:243], v[108:111], v[192:207]
v_exp_f32 v123, v51
v_cmp_ngt_f32 vcc, s71, v24
ds_read_b128 a[240:243], v1 offset:53248
v_mfma_f32_32x32x16_bf16 v[208:223], a[244:247], v[108:111], v[208:223]
v_cvt_pk_bf16_f32 v105, v122, v123
v_exp_f32 v124, v52
s_cbranch_vccnz .Lr19
.Lr19b:
v_mfma_f32_32x32x16_bf16 v[224:239], a[248:251], v[108:111], v[224:239]
v_exp_f32 v125, v53
v_add_f32 v10, v10, v24
ds_read_b128 a[244:247], v1 offset:54272
ds_read_b128 a[248:251], v1 offset:55296
v_mfma_f32_32x32x16_bf16 v[240:255], a[252:255], v[108:111], v[240:255]
v_cvt_pk_bf16_f32 v106, v124, v125
v_exp_f32 v126, v54
ds_read_b128 a[252:255], v1 offset:56320
s_waitcnt lgkmcnt(8)
v_mfma_f32_32x32x16_bf16 v[32:47], a[192:195], a[128:131], v[64:79]
v_exp_f32 v127, v55
v_mfma_f32_32x32x16_bf16 v[32:47], a[196:199], a[132:135], v[32:47]
v_cvt_pk_bf16_f32 v107, v126, v127
v_exp_f32 v120, v56
v_mfma_f32_32x32x16_bf16 v[32:47], a[200:203], a[136:139], v[32:47]
v_exp_f32 v121, v57
v_mfma_f32_16x16x32_bf16 v[24:27], v[28:31], v[104:107], 0
v_mfma_f32_32x32x16_bf16 v[32:47], a[204:207], a[140:143], v[32:47]
v_cvt_pk_bf16_f32 v108, v120, v121
v_exp_f32 v122, v58
v_mfma_f32_32x32x16_bf16 v[32:47], a[208:211], a[144:147], v[32:47]
v_exp_f32 v123, v59
v_mfma_f32_32x32x16_bf16 v[32:47], a[212:215], a[148:151], v[32:47]
v_cvt_pk_bf16_f32 v109, v122, v123
v_exp_f32 v124, v60
v_mfma_f32_32x32x16_bf16 v[32:47], a[216:219], a[152:155], v[32:47]
v_exp_f32 v125, v61
v_mfma_f32_32x32x16_bf16 v[32:47], a[220:223], a[156:159], v[32:47]
v_cvt_pk_bf16_f32 v110, v124, v125
v_exp_f32 v126, v62
s_waitcnt lgkmcnt(0)
v_mfma_f32_32x32x16_bf16 v[128:143], a[224:227], v[96:99], v[128:143]
v_exp_f32 v127, v63
v_mfma_f32_32x32x16_bf16 v[144:159], a[228:231], v[96:99], v[144:159]
v_cvt_pk_bf16_f32 v111, v126, v127
v_exp_f32 v112, v32
v_mfma_f32_32x32x16_bf16 v[160:175], a[232:235], v[96:99], v[160:175]
v_exp_f32 v113, v33
v_mfma_f32_16x16x32_bf16 v[24:27], v[28:31], v[108:111], v[24:27]
v_mfma_f32_32x32x16_bf16 v[176:191], a[236:239], v[96:99], v[176:191]
v_cvt_pk_bf16_f32 v96, v112, v113
v_exp_f32 v114, v34
v_mfma_f32_32x32x16_bf16 v[128:143], a[240:243], v[100:103], v[128:143]
v_exp_f32 v115, v35
v_cmp_ngt_f32 vcc, s71, v24
v_mfma_f32_32x32x16_bf16 v[144:159], a[244:247], v[100:103], v[144:159]
v_cvt_pk_bf16_f32 v97, v114, v115
v_exp_f32 v116, v36
s_cbranch_vccnz .Lr22
.Lr22b:
v_mfma_f32_32x32x16_bf16 v[160:175], a[248:251], v[100:103], v[160:175]
v_exp_f32 v117, v37
v_add_f32 v11, v11, v24
v_mfma_f32_32x32x16_bf16 v[176:191], a[252:255], v[100:103], v[176:191]
v_cvt_pk_bf16_f32 v98, v116, v117
v_exp_f32 v118, v38
s_waitcnt vmcnt(0)
s_barrier
s_add_u32 s24, s24, 0x2000
v_mfma_f32_32x32x16_bf16 v[48:63], a[192:195], a[160:163], v[80:95]
v_exp_f32 v119, v39
ds_read_b128 a[192:195], v2 offset:0
v_mfma_f32_32x32x16_bf16 v[48:63], a[196:199], a[164:167], v[48:63]
v_cvt_pk_bf16_f32 v99, v118, v119
v_exp_f32 v112, v40
ds_read_b128 a[196:199], v2 offset:1024
v_mfma_f32_32x32x16_bf16 v[48:63], a[200:203], a[168:171], v[48:63]
v_exp_f32 v113, v41
v_mfma_f32_16x16x32_bf16 v[24:27], v[28:31], v[96:99], 0
ds_read_b128 a[200:203], v2 offset:2048
v_mfma_f32_32x32x16_bf16 v[48:63], a[204:207], a[172:175], v[48:63]
v_cvt_pk_bf16_f32 v100, v112, v113
v_exp_f32 v114, v42
ds_read_b128 a[204:207], v2 offset:3072
v_mfma_f32_32x32x16_bf16 v[48:63], a[208:211], a[176:179], v[48:63]
v_exp_f32 v115, v43
ds_read_b128 a[208:211], v2 offset:4096
v_mfma_f32_32x32x16_bf16 v[48:63], a[212:215], a[180:183], v[48:63]
v_cvt_pk_bf16_f32 v101, v114, v115
v_exp_f32 v116, v44
ds_read_b128 a[212:215], v2 offset:5120
v_mfma_f32_32x32x16_bf16 v[48:63], a[216:219], a[184:187], v[48:63]
v_exp_f32 v117, v45
ds_read_b128 a[216:219], v2 offset:6144
v_mfma_f32_32x32x16_bf16 v[48:63], a[220:223], a[188:191], v[48:63]
v_cvt_pk_bf16_f32 v102, v116, v117
v_exp_f32 v118, v46
ds_read_b128 a[220:223], v2 offset:7168
v_mfma_f32_32x32x16_bf16 v[192:207], a[224:227], v[104:107], v[192:207]
v_exp_f32 v119, v47
ds_read_b128 a[224:227], v1 offset:57344
v_mfma_f32_32x32x16_bf16 v[208:223], a[228:231], v[104:107], v[208:223]
v_cvt_pk_bf16_f32 v103, v118, v119
v_exp_f32 v120, v48
ds_read_b128 a[228:231], v1 offset:58368
v_mfma_f32_32x32x16_bf16 v[224:239], a[232:235], v[104:107], v[224:239]
v_mfma_f32_16x16x32_bf16 v[24:27], v[28:31], v[100:103], v[24:27]
v_exp_f32 v121, v49
ds_read_b128 a[232:235], v1 offset:59392
v_mfma_f32_32x32x16_bf16 v[240:255], a[236:239], v[104:107], v[240:255]
v_cvt_pk_bf16_f32 v104, v120, v121
v_exp_f32 v122, v50
ds_read_b128 a[236:239], v1 offset:60416
v_mfma_f32_32x32x16_bf16 v[192:207], a[240:243], v[108:111], v[192:207]
v_exp_f32 v123, v51
v_cmp_ngt_f32 vcc, s71, v24
ds_read_b128 a[240:243], v1 offset:61440
v_mfma_f32_32x32x16_bf16 v[208:223], a[244:247], v[108:111], v[208:223]
v_cvt_pk_bf16_f32 v105, v122, v123
v_exp_f32 v124, v52
s_cbranch_vccnz .Lr21
.Lr21b:
v_mfma_f32_32x32x16_bf16 v[224:239], a[248:251], v[108:111], v[224:239]
v_exp_f32 v125, v53
v_add_f32 v10, v10, v24
ds_read_b128 a[244:247], v1 offset:62464
ds_read_b128 a[248:251], v1 offset:63488
v_mfma_f32_32x32x16_bf16 v[240:255], a[252:255], v[108:111], v[240:255]
v_cvt_pk_bf16_f32 v106, v124, v125
v_exp_f32 v126, v54
ds_read_b128 a[252:255], v1 offset:64512
s_waitcnt lgkmcnt(8)
s_cmp_lg_u32 s24, s73
s_cbranch_scc1 .Ly0_0
v_mfma_f32_32x32x16_bf16 v[32:47], a[192:195], a[128:131], v[64:79]
v_exp_f32 v127, v55
v_mfma_f32_32x32x16_bf16 v[32:47], a[196:199], a[132:135], v[32:47]
v_cvt_pk_bf16_f32 v107, v126, v127
v_exp_f32 v120, v56
v_mfma_f32_32x32x16_bf16 v[32:47], a[200:203], a[136:139], v[32:47]
v_exp_f32 v121, v57
v_mfma_f32_16x16x32_bf16 v[24:27], v[28:31], v[104:107], 0
v_mfma_f32_32x32x16_bf16 v[32:47], a[204:207], a[140:143], v[32:47]
v_cvt_pk_bf16_f32 v108, v120, v121
v_exp_f32 v122, v58
v_mfma_f32_32x32x16_bf16 v[32:47], a[208:211], a[144:147], v[32:47]
v_exp_f32 v123, v59
v_mfma_f32_32x32x16_bf16 v[32:47], a[212:215], a[148:151], v[32:47]
v_cvt_pk_bf16_f32 v109, v122, v123
v_exp_f32 v124, v60
v_mfma_f32_32x32x16_bf16 v[32:47], a[216:219], a[152:155], v[32:47]
v_exp_f32 v125, v61
v_mfma_f32_32x32x16_bf16 v[32:47], a[220:223], a[156:159], v[32:47]
v_cvt_pk_bf16_f32 v110, v124, v125
v_exp_f32 v126, v62
s_waitcnt lgkmcnt(0)
v_mfma_f32_32x32x16_bf16 v[128:143], a[224:227], v[96:99], v[128:143]
v_exp_f32 v127, v63
v_mfma_f32_32x32x16_bf16 v[144:159], a[228:231], v[96:99], v[144:159]
v_cvt_pk_bf16_f32 v111, v126, v127
v_cndmask_b32 v32, v32, v5, s[54:55]
v_cndmask_b32 v33, v33, v5, s[56:57]
v_exp_f32 v112, v32
v_mfma_f32_32x32x16_bf16 v[160:175], a[232:235], v[96:99], v[160:175]
v_cndmask_b32 v34, v34, v5, s[58:59]
v_cndmask_b32 v35, v35, v5, s[60:61]
v_exp_f32 v113, v33
v_mfma_f32_16x16x32_bf16 v[24:27], v[28:31], v[108:111], v[24:27]
v_mfma_f32_32x32x16_bf16 v[176:191], a[236:239], v[96:99], v[176:191]
v_cvt_pk_bf16_f32 v96, v112, v113
v_cndmask_b32 v36, v36, v5, s[62:63]
v_cndmask_b32 v37, v37, v5, s[64:65]
v_exp_f32 v114, v34
v_mfma_f32_32x32x16_bf16 v[128:143], a[240:243], v[100:103], v[128:143]
v_cndmask_b32 v38, v38, v5, s[66:67]
v_cndmask_b32 v39, v39, v5, s[68:69]
v_exp_f32 v115, v35
v_cmp_ngt_f32 vcc, s71, v24
v_mfma_f32_32x32x16_bf16 v[144:159], a[244:247], v[100:103], v[144:159]
v_cvt_pk_bf16_f32 v97, v114, v115
v_cndmask_b32 v40, v40, v5, s[84:85]
v_cndmask_b32 v41, v41, v5, s[86:87]
v_exp_f32 v116, v36
s_cbranch_vccnz .Lr24
.Lr24b:
v_mfma_f32_32x32x16_bf16 v[160:175], a[248:251], v[100:103], v[160:175]
v_cndmask_b32 v42, v42, v5, s[88:89]
v_cndmask_b32 v43, v43, v5, s[90:91]
v_exp_f32 v117, v37
v_add_f32 v11, v11, v24
v_mfma_f32_32x32x16_bf16 v[176:191], a[252:255], v[100:103], v[176:191]
v_cvt_pk_bf16_f32 v98, v116, v117
v_cndmask_b32 v44, v44, v5, s[92:93]
v_cndmask_b32 v45, v45, v5, s[94:95]
v_exp_f32 v118, v38
s_waitcnt vmcnt(0)
s_barrier
s_add_u32 s24, s24, 0x2000
v_mfma_f32_32x32x16_bf16 v[48:63], a[192:195], a[160:163], v[80:95]
v_cndmask_b32 v46, v46, v5, s[96:97]
v_cndmask_b32 v47, v47, v5, s[98:99]
v_exp_f32 v119, v39
ds_read_b128 a[192:195], v2 offset:8192
v_mfma_f32_32x32x16_bf16 v[48:63], a[196:199], a[164:167], v[48:63]
v_cvt_pk_bf16_f32 v99, v118, v119
v_exp_f32 v112, v40
ds_read_b128 a[196:199], v2 offset:9216
v_mfma_f32_32x32x16_bf16 v[48:63], a[200:203], a[168:171], v[48:63]
v_exp_f32 v113, v41
v_mfma_f32_16x16x32_bf16 v[24:27], v[28:31], v[96:99], 0
ds_read_b128 a[200:203], v2 offset:10240
v_mfma_f32_32x32x16_bf16 v[48:63], a[204:207], a[172:175], v[48:63]
v_cvt_pk_bf16_f32 v100, v112, v113
v_exp_f32 v114, v42
ds_read_b128 a[204:207], v2 offset:11264
v_mfma_f32_32x32x16_bf16 v[48:63], a[208:211], a[176:179], v[48:63]
v_exp_f32 v115, v43
ds_read_b128 a[208:211], v2 offset:12288
v_mfma_f32_32x32x16_bf16 v[48:63], a[212:215], a[180:183], v[48:63]
v_cvt_pk_bf16_f32 v101, v114, v115
v_exp_f32 v116, v44
ds_read_b128 a[212:215], v2 offset:13312
v_mfma_f32_32x32x16_bf16 v[48:63], a[216:219], a[184:187], v[48:63]
v_exp_f32 v117, v45
ds_read_b128 a[216:219], v2 offset:14336
v_mfma_f32_32x32x16_bf16 v[48:63], a[220:223], a[188:191], v[48:63]
v_cvt_pk_bf16_f32 v102, v116, v117
v_exp_f32 v118, v46
ds_read_b128 a[220:223], v2 offset:15360
v_mfma_f32_32x32x16_bf16 v[192:207], a[224:227], v[104:107], v[192:207]
v_exp_f32 v119, v47
ds_read_b128 a[224:227], v1 offset:32768
v_mfma_f32_32x32x16_bf16 v[208:223], a[228:231], v[104:107], v[208:223]
v_cvt_pk_bf16_f32 v103, v118, v119
v_exp_f32 v120, v48
ds_read_b128 a[228:231], v1 offset:33792
v_mfma_f32_32x32x16_bf16 v[224:239], a[232:235], v[104:107], v[224:239]
v_mfma_f32_16x16x32_bf16 v[24:27], v[28:31], v[100:103], v[24:27]
v_exp_f32 v121, v49
ds_read_b128 a[232:235], v1 offset:34816
v_mfma_f32_32x32x16_bf16 v[240:255], a[236:239], v[104:107], v[240:255]
v_cvt_pk_bf16_f32 v104, v120, v121
v_exp_f32 v122, v50
ds_read_b128 a[236:239], v1 offset:35840
v_mfma_f32_32x32x16_bf16 v[192:207], a[240:243], v[108:111], v[192:207]
v_exp_f32 v123, v51
v_cmp_ngt_f32 vcc, s71, v24
ds_read_b128 a[240:243], v1 offset:36864
v_mfma_f32_32x32x16_bf16 v[208:223], a[244:247], v[108:111], v[208:223]
v_cvt_pk_bf16_f32 v105, v122, v123
v_exp_f32 v124, v52
s_cbranch_vccnz .Lr23
.Lr23b:
v_mfma_f32_32x32x16_bf16 v[224:239], a[248:251], v[108:111], v[224:239]
v_exp_f32 v125, v53
v_add_f32 v10, v10, v24
ds_read_b128 a[244:247], v1 offset:37888
ds_read_b128 a[248:251], v1 offset:38912
v_mfma_f32_32x32x16_bf16 v[240:255], a[252:255], v[108:111], v[240:255]
v_cvt_pk_bf16_f32 v106, v124, v125
v_exp_f32 v126, v54
ds_read_b128 a[252:255], v1 offset:39936
s_waitcnt lgkmcnt(8)
s_mov_b32 s22, s52
s_add_u32 m0, s36, 0
v_exp_f32 v127, v55
buffer_load_dwordx4 v1, s[20:23], s24 offen offset:0 lds
v_cvt_pk_bf16_f32 v107, v126, v127
v_exp_f32 v120, v56
v_exp_f32 v121, v57
v_mfma_f32_16x16x32_bf16 v[24:27], v[28:31], v[104:107], 0
buffer_load_dwordx4 v1, s[20:23], s24 offen offset:1024 lds
v_cvt_pk_bf16_f32 v108, v120, v121
v_exp_f32 v122, v58
v_exp_f32 v123, v59
buffer_load_dwordx4 v1, s[20:23], s24 offen offset:2048 lds
v_cvt_pk_bf16_f32 v109, v122, v123
v_exp_f32 v124, v60
v_exp_f32 v125, v61
buffer_load_dwordx4 v1, s[20:23], s24 offen offset:3072 lds
s_add_u32 m0, s36, 57344
v_cvt_pk_bf16_f32 v110, v124, v125
v_exp_f32 v126, v62
s_waitcnt lgkmcnt(0)
v_mfma_f32_32x32x16_bf16 v[128:143], a[224:227], v[96:99], v[128:143]
v_exp_f32 v127, v63
buffer_load_dwordx4 v3, s[20:23], s24 offen offset:0 lds
v_mfma_f32_32x32x16_bf16 v[144:159], a[228:231], v[96:99], v[144:159]
v_cvt_pk_bf16_f32 v111, v126, v127
v_mfma_f32_32x32x16_bf16 v[160:175], a[232:235], v[96:99], v[160:175]
v_mfma_f32_16x16x32_bf16 v[24:27], v[28:31], v[108:111], v[24:27]
buffer_load_dwordx4 v3, s[20:23], s24 offen offset:1024 lds
v_mfma_f32_32x32x16_bf16 v[176:191], a[236:239], v[96:99], v[176:191]
buffer_load_dwordx4 v3, s[20:23], s24 offen offset:2048 lds
v_mfma_f32_32x32x16_bf16 v[128:143], a[240:243], v[100:103], v[128:143]
v_cmp_ngt_f32 vcc, s71, v24
v_mfma_f32_32x32x16_bf16 v[144:159], a[244:247], v[100:103], v[144:159]
s_cbranch_vccnz .Lr26
.Lr26b:
v_mfma_f32_32x32x16_bf16 v[160:175], a[248:251], v[100:103], v[160:175]
v_add_f32 v11, v11, v24
buffer_load_dwordx4 v3, s[20:23], s24 offen offset:3072 lds
v_mfma_f32_32x32x16_bf16 v[176:191], a[252:255], v[100:103], v[176:191]
s_waitcnt vmcnt(16)
s_barrier
s_add_u32 s24, s24, 0x2000
v_mfma_f32_32x32x16_bf16 v[48:63], a[192:195], a[160:163], v[80:95]
ds_read_b128 a[192:195], v2 offset:16384
v_mfma_f32_32x32x16_bf16 v[48:63], a[196:199], a[164:167], v[48:63]
ds_read_b128 a[196:199], v2 offset:17408
v_mfma_f32_32x32x16_bf16 v[48:63], a[200:203], a[168:171], v[48:63]
ds_read_b128 a[200:203], v2 offset:18432
v_mfma_f32_32x32x16_bf16 v[48:63], a[204:207], a[172:175], v[48:63]
ds_read_b128 a[204:207], v2 offset:19456
v_mfma_f32_32x32x16_bf16 v[48:63], a[208:211], a[176:179], v[48:63]
ds_read_b128 a[208:211], v2 offset:20480
v_mfma_f32_32x32x16_bf16 v[48:63], a[212:215], a[180:183], v[48:63]
ds_read_b128 a[212:215], v2 offset:21504
v_mfma_f32_32x32x16_bf16 v[48:63], a[216:219], a[184:187], v[48:63]
ds_read_b128 a[216:219], v2 offset:22528
v_mfma_f32_32x32x16_bf16 v[48:63], a[220:223], a[188:191], v[48:63]
ds_read_b128 a[220:223], v2 offset:23552
v_mfma_f32_32x32x16_bf16 v[192:207], a[224:227], v[104:107], v[192:207]
ds_read_b128 a[224:227], v1 offset:40960
v_mfma_f32_32x32x16_bf16 v[208:223], a[228:231], v[104:107], v[208:223]
v_cndmask_b32 v48, v48, v5, s[54:55]
v_cndmask_b32 v49, v49, v5, s[56:57]
v_cndmask_b32 v50, v50, v5, s[58:59]
v_exp_f32 v120, v48
ds_read_b128 a[228:231], v1 offset:41984
v_mfma_f32_32x32x16_bf16 v[224:239], a[232:235], v[104:107], v[224:239]
v_cndmask_b32 v51, v51, v5, s[60:61]
v_cndmask_b32 v52, v52, v5, s[62:63]
v_cndmask_b32 v53, v53, v5, s[64:65]
v_exp_f32 v121, v49
ds_read_b128 a[232:235], v1 offset:43008
v_mfma_f32_32x32x16_bf16 v[240:255], a[236:239], v[104:107], v[240:255]
v_cvt_pk_bf16_f32 v104, v120, v121
v_cndmask_b32 v54, v54, v5, s[66:67]
v_cndmask_b32 v55, v55, v5, s[68:69]
v_exp_f32 v122, v50
ds_read_b128 a[236:239], v1 offset:44032
v_mfma_f32_32x32x16_bf16 v[192:207], a[240:243], v[108:111], v[192:207]
v_cndmask_b32 v56, v56, v5, s[84:85]
v_cndmask_b32 v57, v57, v5, s[86:87]
v_exp_f32 v123, v51
ds_read_b128 a[240:243], v1 offset:45056
v_mfma_f32_32x32x16_bf16 v[208:223], a[244:247], v[108:111], v[208:223]
v_cvt_pk_bf16_f32 v105, v122, v123
v_cndmask_b32 v58, v58, v5, s[88:89]
v_cndmask_b32 v59, v59, v5, s[90:91]
v_exp_f32 v124, v52
v_mfma_f32_32x32x16_bf16 v[224:239], a[248:251], v[108:111], v[224:239]
v_cndmask_b32 v60, v60, v5, s[92:93]
v_cndmask_b32 v61, v61, v5, s[94:95]
v_exp_f32 v125, v53
ds_read_b128 a[244:247], v1 offset:46080
ds_read_b128 a[248:251], v1 offset:47104
v_mfma_f32_32x32x16_bf16 v[240:255], a[252:255], v[108:111], v[240:255]
v_cvt_pk_bf16_f32 v106, v124, v125
v_cndmask_b32 v62, v62, v5, s[96:97]
v_cndmask_b32 v63, v63, v5, s[98:99]
v_exp_f32 v126, v54
ds_read_b128 a[252:255], v1 offset:48128
s_waitcnt lgkmcnt(8)
.Lyl0_2:
s_add_u32 m0, s36, 8192
v_exp_f32 v127, v55
buffer_load_dwordx4 v1, s[20:23], s24 offen offset:0 lds
v_cvt_pk_bf16_f32 v107, v126, v127
v_exp_f32 v120, v56
v_exp_f32 v121, v57
v_mfma_f32_16x16x32_bf16 v[24:27], v[28:31], v[104:107], 0
buffer_load_dwordx4 v1, s[20:23], s24 offen offset:1024 lds
v_cvt_pk_bf16_f32 v108, v120, v121
v_exp_f32 v122, v58
v_exp_f32 v123, v59
buffer_load_dwordx4 v1, s[20:23], s24 offen offset:2048 lds
v_cvt_pk_bf16_f32 v109, v122, v123
v_exp_f32 v124, v60
v_exp_f32 v125, v61
buffer_load_dwordx4 v1, s[20:23], s24 offen offset:3072 lds
s_add_u32 m0, s36, 32768
v_cvt_pk_bf16_f32 v110, v124, v125
v_exp_f32 v126, v62
s_waitcnt lgkmcnt(0)
v_exp_f32 v127, v63
buffer_load_dwordx4 v3, s[20:23], s24 offen offset:0 lds
v_cvt_pk_bf16_f32 v111, v126, v127
v_rcp_f32 v10, v10
s_nop 0
v_pk_mul_f32 v[128:129], v[128:129], v[10:11] op_sel:[0,0] op_sel_hi:[1,0]
v_pk_mul_f32 v[130:131], v[130:131], v[10:11] op_sel:[0,0] op_sel_hi:[1,0]
v_pk_mul_f32 v[132:133], v[132:133], v[10:11] op_sel:[0,0] op_sel_hi:[1,0]
v_mfma_f32_16x16x32_bf16 v[24:27], v[28:31], v[108:111], v[24:27]
buffer_load_dwordx4 v3, s[20:23], s24 offen offset:1024 lds
v_pk_mul_f32 v[134:135], v[134:135], v[10:11] op_sel:[0,0] op_sel_hi:[1,0]
v_pk_mul_f32 v[136:137], v[136:137], v[10:11] op_sel:[0,0] op_sel_hi:[1,0]
v_pk_mul_f32 v[138:139], v[138:139], v[10:11] op_sel:[0,0] op_sel_hi:[1,0]
buffer_load_dwordx4 v3, s[20:23], s24 offen offset:2048 lds
v_pk_mul_f32 v[140:141], v[140:141], v[10:11] op_sel:[0,0] op_sel_hi:[1,0]
v_pk_mul_f32 v[142:143], v[142:143], v[10:11] op_sel:[0,0] op_sel_hi:[1,0]
v_cvt_pk_bf16_f32 v128, v128, v129
v_cvt_pk_bf16_f32 v129, v130, v131
v_cvt_pk_bf16_f32 v130, v132, v133
v_cvt_pk_bf16_f32 v131, v134, v135
buffer_store_dwordx4 v[128:131], v1, s[16:19], s37 offen offset:0 sc1
v_cmp_ngt_f32 vcc, s71, v24
s_nop 3
s_cbranch_vccnz .Lr28
.Lr28b:
v_add_f32 v11, v11, v24
buffer_load_dwordx4 v3, s[20:23], s24 offen offset:3072 lds
v_cvt_pk_bf16_f32 v132, v136, v137
v_cvt_pk_bf16_f32 v133, v138, v139
v_cvt_pk_bf16_f32 v134, v140, v141
v_cvt_pk_bf16_f32 v135, v142, v143
buffer_store_dwordx4 v[132:135], v1, s[16:19], s37 offen offset:1024 sc1
v_pk_mul_f32 v[144:145], v[144:145], v[10:11] op_sel:[0,0] op_sel_hi:[1,0]
s_waitcnt vmcnt(18)
s_barrier
s_add_u32 s24, s24, 0x2000
ds_read_b128 a[192:195], v2 offset:24576
v_pk_mul_f32 v[146:147], v[146:147], v[10:11] op_sel:[0,0] op_sel_hi:[1,0]
v_pk_mul_f32 v[148:149], v[148:149], v[10:11] op_sel:[0,0] op_sel_hi:[1,0]
v_pk_mul_f32 v[150:151], v[150:151], v[10:11] op_sel:[0,0] op_sel_hi:[1,0]
v_pk_mul_f32 v[152:153], v[152:153], v[10:11] op_sel:[0,0] op_sel_hi:[1,0]
v_pk_mul_f32 v[154:155], v[154:155], v[10:11] op_sel:[0,0] op_sel_hi:[1,0]
v_pk_mul_f32 v[156:157], v[156:157], v[10:11] op_sel:[0,0] op_sel_hi:[1,0]
v_pk_mul_f32 v[158:159], v[158:159], v[10:11] op_sel:[0,0] op_sel_hi:[1,0]
ds_read_b128 a[196:199], v2 offset:25600
v_cvt_pk_bf16_f32 v144, v144, v145
v_cvt_pk_bf16_f32 v145, v146, v147
v_cvt_pk_bf16_f32 v146, v148, v149
v_cvt_pk_bf16_f32 v147, v150, v151
buffer_store_dwordx4 v[144:147], v1, s[16:19], s37 offen offset:2048 sc1
v_cvt_pk_bf16_f32 v148, v152, v153
v_cvt_pk_bf16_f32 v149, v154, v155
ds_read_b128 a[200:203], v2 offset:26624
v_cvt_pk_bf16_f32 v150, v156, v157
v_cvt_pk_bf16_f32 v151, v158, v159
buffer_store_dwordx4 v[148:151], v1, s[16:19], s37 offen offset:3072 sc1
v_pk_mul_f32 v[160:161], v[160:161], v[10:11] op_sel:[0,0] op_sel_hi:[1,0]
v_pk_mul_f32 v[162:163], v[162:163], v[10:11] op_sel:[0,0] op_sel_hi:[1,0]
v_pk_mul_f32 v[164:165], v[164:165], v[10:11] op_sel:[0,0] op_sel_hi:[1,0]
v_pk_mul_f32 v[166:167], v[166:167], v[10:11] op_sel:[0,0] op_sel_hi:[1,0]
ds_read_b128 a[204:207], v2 offset:27648
v_pk_mul_f32 v[168:169], v[168:169], v[10:11] op_sel:[0,0] op_sel_hi:[1,0]
v_pk_mul_f32 v[170:171], v[170:171], v[10:11] op_sel:[0,0] op_sel_hi:[1,0]
v_pk_mul_f32 v[172:173], v[172:173], v[10:11] op_sel:[0,0] op_sel_hi:[1,0]
v_pk_mul_f32 v[174:175], v[174:175], v[10:11] op_sel:[0,0] op_sel_hi:[1,0]
v_cvt_pk_bf16_f32 v160, v160, v161
v_cvt_pk_bf16_f32 v161, v162, v163
v_cvt_pk_bf16_f32 v162, v164, v165
ds_read_b128 a[208:211], v2 offset:28672
v_cvt_pk_bf16_f32 v163, v166, v167
buffer_store_dwordx4 v[160:163], v1, s[16:19], s38 offen offset:0 sc1
v_cvt_pk_bf16_f32 v164, v168, v169
v_cvt_pk_bf16_f32 v165, v170, v171
v_cvt_pk_bf16_f32 v166, v172, v173
v_cvt_pk_bf16_f32 v167, v174, v175
buffer_store_dwordx4 v[164:167], v1, s[16:19], s38 offen offset:1024 sc1
ds_read_b128 a[212:215], v2 offset:29696
v_pk_mul_f32 v[176:177], v[176:177], v[10:11] op_sel:[0,0] op_sel_hi:[1,0]
v_pk_mul_f32 v[178:179], v[178:179], v[10:11] op_sel:[0,0] op_sel_hi:[1,0]
v_pk_mul_f32 v[180:181], v[180:181], v[10:11] op_sel:[0,0] op_sel_hi:[1,0]
v_pk_mul_f32 v[182:183], v[182:183], v[10:11] op_sel:[0,0] op_sel_hi:[1,0]
v_pk_mul_f32 v[184:185], v[184:185], v[10:11] op_sel:[0,0] op_sel_hi:[1,0]
v_pk_mul_f32 v[186:187], v[186:187], v[10:11] op_sel:[0,0] op_sel_hi:[1,0]
v_pk_mul_f32 v[188:189], v[188:189], v[10:11] op_sel:[0,0] op_sel_hi:[1,0]
ds_read_b128 a[216:219], v2 offset:30720
v_pk_mul_f32 v[190:191], v[190:191], v[10:11] op_sel:[0,0] op_sel_hi:[1,0]
v_cvt_pk_bf16_f32 v176, v176, v177
v_cvt_pk_bf16_f32 v177, v178, v179
v_cvt_pk_bf16_f32 v178, v180, v181
v_cvt_pk_bf16_f32 v179, v182, v183
buffer_store_dwordx4 v[176:179], v1, s[16:19], s38 offen offset:2048 sc1
v_cvt_pk_bf16_f32 v180, v184, v185
ds_read_b128 a[220:223], v2 offset:31744
v_cvt_pk_bf16_f32 v181, v186, v187
v_cvt_pk_bf16_f32 v182, v188, v189
v_cvt_pk_bf16_f32 v183, v190, v191
buffer_store_dwordx4 v[180:183], v1, s[16:19], s38 offen offset:3072 sc1
v_mfma_f32_32x32x16_bf16 v[192:207], a[224:227], v[104:107], v[192:207]
ds_read_b128 a[224:227], v1 offset:49152
v_mfma_f32_32x32x16_bf16 v[208:223], a[228:231], v[104:107], v[208:223]
ds_read_b128 a[228:231], v1 offset:50176
v_mfma_f32_32x32x16_bf16 v[224:239], a[232:235], v[104:107], v[224:239]
ds_read_b128 a[232:235], v1 offset:51200
v_mfma_f32_32x32x16_bf16 v[240:255], a[236:239], v[104:107], v[240:255]
ds_read_b128 a[236:239], v1 offset:52224
v_mfma_f32_32x32x16_bf16 v[192:207], a[240:243], v[108:111], v[192:207]
ds_read_b128 a[240:243], v1 offset:53248
v_mfma_f32_32x32x16_bf16 v[208:223], a[244:247], v[108:111], v[208:223]
v_mfma_f32_32x32x16_bf16 v[224:239], a[248:251], v[108:111], v[224:239]
ds_read_b128 a[244:247], v1 offset:54272
ds_read_b128 a[248:251], v1 offset:55296
v_mfma_f32_32x32x16_bf16 v[240:255], a[252:255], v[108:111], v[240:255]
ds_read_b128 a[252:255], v1 offset:56320
s_waitcnt lgkmcnt(8)
s_add_u32 m0, s36, 16384
v_mov_b32 v64, 0
v_mov_b32 v65, 0
buffer_load_dwordx4 v1, s[20:23], s24 offen offset:0 lds
s_add_u32 s25, s24, 0x6000
s_sub_u32 s41, s25, 0x2000
s_sub_i32 s44, 30, s48
s_add_i32 s45, s44, 1
v_mov_b32 v66, 0
v_mov_b32 v67, 0
s_cmp_le_i32 s44, 2
s_cselect_b32 s42, s41, -1
v_mov_b32 v68, 0
v_mov_b32 v69, 0
buffer_load_dwordx4 v1, s[20:23], s24 offen offset:1024 lds
s_cmp_le_i32 s45, 2
s_cselect_b32 s43, s41, -1
v_mov_b32 v70, 0
v_mov_b32 v71, 0
v_rcp_f32 v11, v11
v_mov_b32 v72, 0
v_mov_b32 v73, 0
buffer_load_dwordx4 v1, s[20:23], s24 offen offset:2048 lds
v_pk_mul_f32 v[192:193], v[192:193], v[10:11] op_sel:[0,1] op_sel_hi:[1,1]
v_pk_mul_f32 v[194:195], v[194:195], v[10:11] op_sel:[0,1] op_sel_hi:[1,1]
v_pk_mul_f32 v[196:197], v[196:197], v[10:11] op_sel:[0,1] op_sel_hi:[1,1]
v_pk_mul_f32 v[198:199], v[198:199], v[10:11] op_sel:[0,1] op_sel_hi:[1,1]
v_pk_mul_f32 v[200:201], v[200:201], v[10:11] op_sel:[0,1] op_sel_hi:[1,1]
v_mov_b32 v74, 0
v_mov_b32 v75, 0
v_pk_mul_f32 v[202:203], v[202:203], v[10:11] op_sel:[0,1] op_sel_hi:[1,1]
v_pk_mul_f32 v[204:205], v[204:205], v[10:11] op_sel:[0,1] op_sel_hi:[1,1]
v_pk_mul_f32 v[206:207], v[206:207], v[10:11] op_sel:[0,1] op_sel_hi:[1,1]
v_cvt_pk_bf16_f32 v192, v192, v193
v_cvt_pk_bf16_f32 v193, v194, v195
v_cvt_pk_bf16_f32 v194, v196, v197
v_mov_b32 v76, 0
v_mov_b32 v77, 0
buffer_load_dwordx4 v1, s[20:23], s24 offen offset:3072 lds
s_add_u32 m0, s36, 40960
v_cvt_pk_bf16_f32 v195, v198, v199
buffer_store_dwordx4 v[192:195], v1, s[16:19], s39 offen offset:0 sc1
v_cvt_pk_bf16_f32 v196, v200, v201
v_cvt_pk_bf16_f32 v197, v202, v203
v_mov_b32 v78, 0
v_mov_b32 v79, 0
s_waitcnt lgkmcnt(0)
v_cvt_pk_bf16_f32 v198, v204, v205
v_cvt_pk_bf16_f32 v199, v206, v207
buffer_store_dwordx4 v[196:199], v1, s[16:19], s39 offen offset:1024 sc1
v_pk_mul_f32 v[208:209], v[208:209], v[10:11] op_sel:[0,1] op_sel_hi:[1,1]
v_pk_mul_f32 v[210:211], v[210:211], v[10:11] op_sel:[0,1] op_sel_hi:[1,1]
ds_read_b128 a[128:131], v6 offset:0
ds_read_b128 a[132:135], v6 offset:1024
buffer_load_dwordx4 v3, s[20:23], s24 offen offset:0 lds
v_pk_mul_f32 v[212:213], v[212:213], v[10:11] op_sel:[0,1] op_sel_hi:[1,1]
v_pk_mul_f32 v[214:215], v[214:215], v[10:11] op_sel:[0,1] op_sel_hi:[1,1]
v_pk_mul_f32 v[216:217], v[216:217], v[10:11] op_sel:[0,1] op_sel_hi:[1,1]
v_pk_mul_f32 v[218:219], v[218:219], v[10:11] op_sel:[0,1] op_sel_hi:[1,1]
v_pk_mul_f32 v[220:221], v[220:221], v[10:11] op_sel:[0,1] op_sel_hi:[1,1]
ds_read_b128 a[136:139], v6 offset:2048
ds_read_b128 a[140:143], v6 offset:3072
v_pk_mul_f32 v[222:223], v[222:223], v[10:11] op_sel:[0,1] op_sel_hi:[1,1]
v_cvt_pk_bf16_f32 v208, v208, v209
v_cvt_pk_bf16_f32 v209, v210, v211
v_cvt_pk_bf16_f32 v210, v212, v213
v_cvt_pk_bf16_f32 v211, v214, v215
buffer_store_dwordx4 v[208:211], v1, s[16:19], s39 offen offset:2048 sc1
ds_read_b128 a[144:147], v6 offset:4096
ds_read_b128 a[148:151], v6 offset:5120
buffer_load_dwordx4 v3, s[20:23], s24 offen offset:1024 lds
v_cvt_pk_bf16_f32 v212, v216, v217
v_cvt_pk_bf16_f32 v213, v218, v219
v_cvt_pk_bf16_f32 v214, v220, v221
v_cvt_pk_bf16_f32 v215, v222, v223
buffer_store_dwordx4 v[212:215], v1, s[16:19], s39 offen offset:3072 sc1
ds_read_b128 a[152:155], v6 offset:6144
ds_read_b128 a[156:159], v6 offset:7168
buffer_load_dwordx4 v3, s[20:23], s24 offen offset:2048 lds
v_pk_mul_f32 v[224:225], v[224:225], v[10:11] op_sel:[0,1] op_sel_hi:[1,1]
v_pk_mul_f32 v[226:227], v[226:227], v[10:11] op_sel:[0,1] op_sel_hi:[1,1]
v_pk_mul_f32 v[228:229], v[228:229], v[10:11] op_sel:[0,1] op_sel_hi:[1,1]
v_pk_mul_f32 v[230:231], v[230:231], v[10:11] op_sel:[0,1] op_sel_hi:[1,1]
v_pk_mul_f32 v[232:233], v[232:233], v[10:11] op_sel:[0,1] op_sel_hi:[1,1]
v_add_u32 v2, 0x10000, v1
buffer_load_dwordx4 v3, s[20:23], s24 offen offset:3072 lds
v_pk_mul_f32 v[234:235], v[234:235], v[10:11] op_sel:[0,1] op_sel_hi:[1,1]
v_pk_mul_f32 v[236:237], v[236:237], v[10:11] op_sel:[0,1] op_sel_hi:[1,1]
v_pk_mul_f32 v[238:239], v[238:239], v[10:11] op_sel:[0,1] op_sel_hi:[1,1]
v_cvt_pk_bf16_f32 v224, v224, v225
v_cvt_pk_bf16_f32 v225, v226, v227
v_cvt_pk_bf16_f32 v226, v228, v229
s_waitcnt vmcnt(28)
s_barrier
s_add_u32 s24, s24, 0x2000
ds_read_b128 a[192:195], v2 offset:0
v_cvt_pk_bf16_f32 v227, v230, v231
buffer_store_dwordx4 v[224:227], v1, s[16:19], s53 offen offset:0 sc1
v_cvt_pk_bf16_f32 v228, v232, v233
v_cvt_pk_bf16_f32 v229, v234, v235
v_cvt_pk_bf16_f32 v230, v236, v237
v_cvt_pk_bf16_f32 v231, v238, v239
buffer_store_dwordx4 v[228:231], v1, s[16:19], s53 offen offset:1024 sc1
v_mov_b32 v80, 0
v_mov_b32 v81, 0
ds_read_b128 a[196:199], v2 offset:1024
v_pk_mul_f32 v[240:241], v[240:241], v[10:11] op_sel:[0,1] op_sel_hi:[1,1]
v_pk_mul_f32 v[242:243], v[242:243], v[10:11] op_sel:[0,1] op_sel_hi:[1,1]
v_pk_mul_f32 v[244:245], v[244:245], v[10:11] op_sel:[0,1] op_sel_hi:[1,1]
v_pk_mul_f32 v[246:247], v[246:247], v[10:11] op_sel:[0,1] op_sel_hi:[1,1]
v_pk_mul_f32 v[248:249], v[248:249], v[10:11] op_sel:[0,1] op_sel_hi:[1,1]
v_mov_b32 v82, 0
v_mov_b32 v83, 0
ds_read_b128 a[200:203], v2 offset:2048
v_pk_mul_f32 v[250:251], v[250:251], v[10:11] op_sel:[0,1] op_sel_hi:[1,1]
v_pk_mul_f32 v[252:253], v[252:253], v[10:11] op_sel:[0,1] op_sel_hi:[1,1]
v_pk_mul_f32 v[254:255], v[254:255], v[10:11] op_sel:[0,1] op_sel_hi:[1,1]
v_cvt_pk_bf16_f32 v240, v240, v241
v_cvt_pk_bf16_f32 v241, v242, v243
v_mov_b32 v84, 0
v_mov_b32 v85, 0
ds_read_b128 a[204:207], v2 offset:3072
v_cvt_pk_bf16_f32 v242, v244, v245
v_cvt_pk_bf16_f32 v243, v246, v247
buffer_store_dwordx4 v[240:243], v1, s[16:19], s53 offen offset:2048 sc1
v_cvt_pk_bf16_f32 v244, v248, v249
v_cvt_pk_bf16_f32 v245, v250, v251
v_mov_b32 v86, 0
v_mov_b32 v87, 0
ds_read_b128 a[208:211], v2 offset:4096
v_cvt_pk_bf16_f32 v246, v252, v253
v_cvt_pk_bf16_f32 v247, v254, v255
buffer_store_dwordx4 v[244:247], v1, s[16:19], s53 offen offset:3072 sc1
v_mov_b32 v88, 0
v_mov_b32 v89, 0
ds_read_b128 a[212:215], v2 offset:5120
v_mov_b32 v90, 0
v_mov_b32 v91, 0
ds_read_b128 a[216:219], v2 offset:6144
v_mov_b32 v92, 0
v_mov_b32 v93, 0
ds_read_b128 a[220:223], v2 offset:7168
v_mov_b32 v94, 0
v_mov_b32 v95, 0
ds_read_b128 a[224:227], v1 offset:57344
ds_read_b128 a[228:231], v1 offset:58368
ds_read_b128 a[232:235], v1 offset:59392
ds_read_b128 a[236:239], v1 offset:60416
ds_read_b128 a[240:243], v1 offset:61440
s_cmp_lt_u32 s48, 32
s_cselect_b32 s22, s52, 0
s_cbranch_scc0 .Lt31
.Lt31b:
ds_read_b128 a[244:247], v1 offset:62464
ds_read_b128 a[248:251], v1 offset:63488
ds_read_b128 a[252:255], v1 offset:64512
s_waitcnt lgkmcnt(8)
v_mfma_f32_32x32x16_bf16 v[32:47], a[192:195], a[128:131], v[64:79]
s_add_u32 m0, s36, 24576
ds_read_b128 a[160:163], v6 offset:8192
ds_read_b128 a[164:167], v6 offset:9216
buffer_load_dwordx4 v1, s[20:23], s24 offen offset:0 lds
v_mfma_f32_32x32x16_bf16 v[32:47], a[196:199], a[132:135], v[32:47]
ds_read_b128 a[168:171], v6 offset:10240
ds_read_b128 a[172:175], v6 offset:11264
v_mfma_f32_32x32x16_bf16 v[32:47], a[200:203], a[136:139], v[32:47]
ds_read_b128 a[176:179], v6 offset:12288
ds_read_b128 a[180:183], v6 offset:13312
buffer_load_dwordx4 v1, s[20:23], s24 offen offset:1024 lds
v_mfma_f32_32x32x16_bf16 v[32:47], a[204:207], a[140:143], v[32:47]
ds_read_b128 a[184:187], v6 offset:14336
ds_read_b128 a[188:191], v6 offset:15360
v_mfma_f32_32x32x16_bf16 v[32:47], a[208:211], a[144:147], v[32:47]
buffer_load_dwordx4 v1, s[20:23], s24 offen offset:2048 lds
v_mfma_f32_32x32x16_bf16 v[32:47], a[212:215], a[148:151], v[32:47]
v_mfma_f32_32x32x16_bf16 v[32:47], a[216:219], a[152:155], v[32:47]
buffer_load_dwordx4 v1, s[20:23], s24 offen offset:3072 lds
s_add_u32 m0, s36, 49152
v_mfma_f32_32x32x16_bf16 v[32:47], a[220:223], a[156:159], v[32:47]
s_waitcnt lgkmcnt(0)
buffer_load_dwordx4 v3, s[20:23], s24 offen offset:0 lds
s_nop 7
s_nop 1
v_exp_f32 v112, v32
v_exp_f32 v113, v33
buffer_load_dwordx4 v3, s[20:23], s24 offen offset:1024 lds
v_cvt_pk_bf16_f32 v96, v112, v113
v_exp_f32 v114, v34
buffer_load_dwordx4 v3, s[20:23], s24 offen offset:2048 lds
v_exp_f32 v115, v35
s_cmp_eq_u32 s48, 32
s_cbranch_scc1 .Ls33
.Ls33b:
v_cvt_pk_bf16_f32 v97, v114, v115
v_exp_f32 v116, v36
v_exp_f32 v117, v37
buffer_load_dwordx4 v3, s[20:23], s24 offen offset:3072 lds
v_cvt_pk_bf16_f32 v98, v116, v117
v_exp_f32 v118, v38
s_waitcnt vmcnt(30)
s_barrier
s_add_u32 s24, s24, 0x2000
v_mfma_f32_32x32x16_bf16 v[48:63], a[192:195], a[160:163], v[80:95]
v_exp_f32 v119, v39
ds_read_b128 a[192:195], v2 offset:8192
v_mfma_f32_32x32x16_bf16 v[48:63], a[196:199], a[164:167], v[48:63]
v_cvt_pk_bf16_f32 v99, v118, v119
v_exp_f32 v112, v40
ds_read_b128 a[196:199], v2 offset:9216
v_mfma_f32_32x32x16_bf16 v[48:63], a[200:203], a[168:171], v[48:63]
v_exp_f32 v113, v41
v_mfma_f32_16x16x32_bf16 v[24:27], v[28:31], v[96:99], 0
ds_read_b128 a[200:203], v2 offset:10240
v_mfma_f32_32x32x16_bf16 v[48:63], a[204:207], a[172:175], v[48:63]
v_cvt_pk_bf16_f32 v100, v112, v113
v_exp_f32 v114, v42
ds_read_b128 a[204:207], v2 offset:11264
v_mfma_f32_32x32x16_bf16 v[48:63], a[208:211], a[176:179], v[48:63]
v_exp_f32 v115, v43
ds_read_b128 a[208:211], v2 offset:12288
v_mfma_f32_32x32x16_bf16 v[48:63], a[212:215], a[180:183], v[48:63]
v_cvt_pk_bf16_f32 v101, v114, v115
v_exp_f32 v116, v44
ds_read_b128 a[212:215], v2 offset:13312
v_mfma_f32_32x32x16_bf16 v[48:63], a[216:219], a[184:187], v[48:63]
v_exp_f32 v117, v45
ds_read_b128 a[216:219], v2 offset:14336
v_mfma_f32_32x32x16_bf16 v[48:63], a[220:223], a[188:191], v[48:63]
v_cvt_pk_bf16_f32 v102, v116, v117
v_exp_f32 v118, v46
ds_read_b128 a[220:223], v2 offset:15360
v_exp_f32 v119, v47
ds_read_b128 a[224:227], v2 offset:32768
v_cvt_pk_bf16_f32 v103, v118, v119
s_nop 5
v_exp_f32 v120, v48
ds_read_b128 a[228:231], v2 offset:33792
v_mfma_f32_16x16x32_bf16 v[24:27], v[28:31], v[100:103], v[24:27]
v_exp_f32 v121, v49
ds_read_b128 a[232:235], v2 offset:34816
v_cvt_pk_bf16_f32 v104, v120, v121
v_exp_f32 v122, v50
ds_read_b128 a[236:239], v2 offset:35840
v_exp_f32 v123, v51
s_nop 1
v_add_u32 v23, 0xcf800000, v24
v_cmp_le_u32 vcc, 0x2d000000, v23
s_cmp_ge_u32 s24, s42
ds_read_b128 a[240:243], v2 offset:36864
v_cvt_pk_bf16_f32 v105, v122, v123
v_exp_f32 v124, v52
s_cbranch_vccnz .Lr32
.Lr32b:
s_cbranch_scc1 .Ls32
.Ls32b:
v_exp_f32 v125, v53
v_add_f32 v8, v8, v24
ds_read_b128 a[244:247], v2 offset:37888
ds_read_b128 a[248:251], v2 offset:38912
v_cvt_pk_bf16_f32 v106, v124, v125
v_exp_f32 v126, v54
ds_read_b128 a[252:255], v2 offset:39936
s_waitcnt lgkmcnt(8)
v_mfma_f32_32x32x16_bf16 v[32:47], a[192:195], a[128:131], v[64:79]
s_add_u32 m0, s36, 0
v_exp_f32 v127, v55
buffer_load_dwordx4 v1, s[20:23], s24 offen offset:0 lds
v_mfma_f32_32x32x16_bf16 v[32:47], a[196:199], a[132:135], v[32:47]
v_cvt_pk_bf16_f32 v107, v126, v127
v_exp_f32 v120, v56
v_mfma_f32_32x32x16_bf16 v[32:47], a[200:203], a[136:139], v[32:47]
v_exp_f32 v121, v57
v_mfma_f32_16x16x32_bf16 v[24:27], v[28:31], v[104:107], 0
buffer_load_dwordx4 v1, s[20:23], s24 offen offset:1024 lds
v_mfma_f32_32x32x16_bf16 v[32:47], a[204:207], a[140:143], v[32:47]
v_cvt_pk_bf16_f32 v108, v120, v121
v_exp_f32 v122, v58
v_mfma_f32_32x32x16_bf16 v[32:47], a[208:211], a[144:147], v[32:47]
v_exp_f32 v123, v59
buffer_load_dwordx4 v1, s[20:23], s24 offen offset:2048 lds
v_mfma_f32_32x32x16_bf16 v[32:47], a[212:215], a[148:151], v[32:47]
v_cvt_pk_bf16_f32 v109, v122, v123
v_exp_f32 v124, v60
v_mfma_f32_32x32x16_bf16 v[32:47], a[216:219], a[152:155], v[32:47]
v_exp_f32 v125, v61
buffer_load_dwordx4 v1, s[20:23], s24 offen offset:3072 lds
s_add_u32 m0, s36, 57344
v_mfma_f32_32x32x16_bf16 v[32:47], a[220:223], a[156:159], v[32:47]
v_cvt_pk_bf16_f32 v110, v124, v125
v_exp_f32 v126, v62
s_waitcnt lgkmcnt(0)
v_mfma_f32_32x32x16_bf16 a[0:15], a[224:227], v[96:99], 0
v_exp_f32 v127, v63
buffer_load_dwordx4 v3, s[20:23], s24 offen offset:0 lds
v_mfma_f32_32x32x16_bf16 a[16:31], a[228:231], v[96:99], 0
v_cvt_pk_bf16_f32 v111, v126, v127
v_exp_f32 v112, v32
v_mfma_f32_32x32x16_bf16 a[32:47], a[232:235], v[96:99], 0
v_exp_f32 v113, v33
v_mfma_f32_16x16x32_bf16 v[24:27], v[28:31], v[108:111], v[24:27]
buffer_load_dwordx4 v3, s[20:23], s24 offen offset:1024 lds
v_mfma_f32_32x32x16_bf16 a[48:63], a[236:239], v[96:99], 0
v_cvt_pk_bf16_f32 v96, v112, v113
v_exp_f32 v114, v34
buffer_load_dwordx4 v3, s[20:23], s24 offen offset:2048 lds
v_mfma_f32_32x32x16_bf16 a[0:15], a[240:243], v[100:103], a[0:15]
v_exp_f32 v115, v35
v_add_u32 v23, 0xcf800000, v24
v_cmp_le_u32 vcc, 0x2d000000, v23
s_cmp_ge_u32 s24, s43
v_mfma_f32_32x32x16_bf16 a[16:31], a[244:247], v[100:103], a[16:31]
v_cvt_pk_bf16_f32 v97, v114, v115
v_exp_f32 v116, v36
s_cbranch_vccnz .Lr35
.Lr35b:
s_cbranch_scc1 .Ls35
.Ls35b:
v_mfma_f32_32x32x16_bf16 a[32:47], a[248:251], v[100:103], a[32:47]
v_exp_f32 v117, v37
v_add_f32 v9, v9, v24
buffer_load_dwordx4 v3, s[20:23], s24 offen offset:3072 lds
v_mfma_f32_32x32x16_bf16 a[48:63], a[252:255], v[100:103], a[48:63]
v_cvt_pk_bf16_f32 v98, v116, v117
v_exp_f32 v118, v38
s_waitcnt vmcnt(20)
s_barrier
s_add_u32 s24, s24, 0x2000
v_mfma_f32_32x32x16_bf16 v[48:63], a[192:195], a[160:163], v[80:95]
v_exp_f32 v119, v39
ds_read_b128 a[192:195], v2 offset:16384
v_mfma_f32_32x32x16_bf16 v[48:63], a[196:199], a[164:167], v[48:63]
v_cvt_pk_bf16_f32 v99, v118, v119
v_exp_f32 v112, v40
ds_read_b128 a[196:199], v2 offset:17408
v_mfma_f32_32x32x16_bf16 v[48:63], a[200:203], a[168:171], v[48:63]
v_exp_f32 v113, v41
v_mfma_f32_16x16x32_bf16 v[24:27], v[28:31], v[96:99], 0
ds_read_b128 a[200:203], v2 offset:18432
v_mfma_f32_32x32x16_bf16 v[48:63], a[204:207], a[172:175], v[48:63]
v_cvt_pk_bf16_f32 v100, v112, v113
v_exp_f32 v114, v42
ds_read_b128 a[204:207], v2 offset:19456
v_mfma_f32_32x32x16_bf16 v[48:63], a[208:211], a[176:179], v[48:63]
v_exp_f32 v115, v43
ds_read_b128 a[208:211], v2 offset:20480
v_mfma_f32_32x32x16_bf16 v[48:63], a[212:215], a[180:183], v[48:63]
v_cvt_pk_bf16_f32 v101, v114, v115
v_exp_f32 v116, v44
ds_read_b128 a[212:215], v2 offset:21504
v_mfma_f32_32x32x16_bf16 v[48:63], a[216:219], a[184:187], v[48:63]
v_exp_f32 v117, v45
ds_read_b128 a[216:219], v2 offset:22528
v_mfma_f32_32x32x16_bf16 v[48:63], a[220:223], a[188:191], v[48:63]
v_cvt_pk_bf16_f32 v102, v116, v117
v_exp_f32 v118, v46
ds_read_b128 a[220:223], v2 offset:23552
v_mfma_f32_32x32x16_bf16 a[64:79], a[224:227], v[104:107], 0
v_exp_f32 v119, v47
ds_read_b128 a[224:227], v2 offset:40960
v_mfma_f32_32x32x16_bf16 a[80:95], a[228:231], v[104:107], 0
v_cvt_pk_bf16_f32 v103, v118, v119
v_exp_f32 v120, v48
ds_read_b128 a[228:231], v2 offset:41984
v_mfma_f32_32x32x16_bf16 a[96:111], a[232:235], v[104:107], 0
v_mfma_f32_16x16x32_bf16 v[24:27], v[28:31], v[100:103], v[24:27]
v_exp_f32 v121, v49
ds_read_b128 a[232:235], v2 offset:43008
v_mfma_f32_32x32x16_bf16 a[112:127], a[236:239], v[104:107], 0
v_cvt_pk_bf16_f32 v104, v120, v121
v_exp_f32 v122, v50
ds_read_b128 a[236:239], v2 offset:44032
v_mfma_f32_32x32x16_bf16 a[64:79], a[240:243], v[108:111], a[64:79]
v_exp_f32 v123, v51
v_cmp_ngt_f32 vcc, s71, v24
s_cmp_ge_u32 s24, s42
ds_read_b128 a[240:243], v2 offset:45056
v_mfma_f32_32x32x16_bf16 a[80:95], a[244:247], v[108:111], a[80:95]
v_cvt_pk_bf16_f32 v105, v122, v123
v_exp_f32 v124, v52
s_cbranch_vccnz .Lr34
.Lr34b:
s_cbranch_scc1 .Ls34
.Ls34b:
v_mfma_f32_32x32x16_bf16 a[96:111], a[248:251], v[108:111], a[96:111]
v_exp_f32 v125, v53
v_add_f32 v8, v8, v24
ds_read_b128 a[244:247], v2 offset:46080
ds_read_b128 a[248:251], v2 offset:47104
v_mfma_f32_32x32x16_bf16 a[112:127], a[252:255], v[108:111], a[112:127]
v_cvt_pk_bf16_f32 v106, v124, v125
v_exp_f32 v126, v54
ds_read_b128 a[252:255], v2 offset:48128
s_waitcnt lgkmcnt(8)
s_cmp_eq_u32 s48, 32
s_cbranch_scc1 .LdA
v_mfma_f32_32x32x16_bf16 v[32:47], a[192:195], a[128:131], v[64:79]
s_add_u32 m0, s36, 8192
v_exp_f32 v127, v55
buffer_load_dwordx4 v1, s[20:23], s24 offen offset:0 lds
v_mfma_f32_32x32x16_bf16 v[32:47], a[196:199], a[132:135], v[32:47]
v_cvt_pk_bf16_f32 v107, v126, v127
v_exp_f32 v120, v56
v_mfma_f32_32x32x16_bf16 v[32:47], a[200:203], a[136:139], v[32:47]
v_exp_f32 v121, v57
v_mfma_f32_16x16x32_bf16 v[24:27], v[28:31], v[104:107], 0
buffer_load_dwordx4 v1, s[20:23], s24 offen offset:1024 lds
v_mfma_f32_32x32x16_bf16 v[32:47], a[204:207], a[140:143], v[32:47]
v_cvt_pk_bf16_f32 v108, v120, v121
v_exp_f32 v122, v58
v_mfma_f32_32x32x16_bf16 v[32:47], a[208:211], a[144:147], v[32:47]
v_exp_f32 v123, v59
buffer_load_dwordx4 v1, s[20:23], s24 offen offset:2048 lds
v_mfma_f32_32x32x16_bf16 v[32:47], a[212:215], a[148:151], v[32:47]
v_cvt_pk_bf16_f32 v109, v122, v123
v_exp_f32 v124, v60
v_mfma_f32_32x32x16_bf16 v[32:47], a[216:219], a[152:155], v[32:47]
v_exp_f32 v125, v61
buffer_load_dwordx4 v1, s[20:23], s24 offen offset:3072 lds
s_add_u32 m0, s36, 32768
v_mfma_f32_32x32x16_bf16 v[32:47], a[220:223], a[156:159], v[32:47]
v_cvt_pk_bf16_f32 v110, v124, v125
v_exp_f32 v126, v62
s_waitcnt lgkmcnt(0)
v_mfma_f32_32x32x16_bf16 a[0:15], a[224:227], v[96:99], a[0:15]
v_exp_f32 v127, v63
buffer_load_dwordx4 v3, s[20:23], s24 offen offset:0 lds
v_mfma_f32_32x32x16_bf16 a[16:31], a[228:231], v[96:99], a[16:31]
v_cvt_pk_bf16_f32 v111, v126, v127
v_exp_f32 v112, v32
v_mfma_f32_32x32x16_bf16 a[32:47], a[232:235], v[96:99], a[32:47]
v_exp_f32 v113, v33
v_mfma_f32_16x16x32_bf16 v[24:27], v[28:31], v[108:111], v[24:27]
buffer_load_dwordx4 v3, s[20:23], s24 offen offset:1024 lds
v_mfma_f32_32x32x16_bf16 a[48:63], a[236:239], v[96:99], a[48:63]
v_cvt_pk_bf16_f32 v96, v112, v113
v_exp_f32 v114, v34
buffer_load_dwordx4 v3, s[20:23], s24 offen offset:2048 lds
v_mfma_f32_32x32x16_bf16 a[0:15], a[240:243], v[100:103], a[0:15]
v_exp_f32 v115, v35
v_cmp_ngt_f32 vcc, s71, v24
v_mfma_f32_32x32x16_bf16 a[16:31], a[244:247], v[100:103], a[16:31]
v_cvt_pk_bf16_f32 v97, v114, v115
v_exp_f32 v116, v36
s_cbranch_vccnz .Lr37
.Lr37b:
v_mfma_f32_32x32x16_bf16 a[32:47], a[248:251], v[100:103], a[32:47]
v_exp_f32 v117, v37
v_add_f32 v9, v9, v24
buffer_load_dwordx4 v3, s[20:23], s24 offen offset:3072 lds
v_mfma_f32_32x32x16_bf16 a[48:63], a[252:255], v[100:103], a[48:63]
v_cvt_pk_bf16_f32 v98, v116, v117
v_exp_f32 v118, v38
s_waitcnt vmcnt(16)
s_barrier
s_add_u32 s24, s24, 0x2000
v_mfma_f32_32x32x16_bf16 v[48:63], a[192:195], a[160:163], v[80:95]
v_exp_f32 v119, v39
ds_read_b128 a[192:195], v2 offset:24576
v_mfma_f32_32x32x16_bf16 v[48:63], a[196:199], a[164:167], v[48:63]
v_cvt_pk_bf16_f32 v99, v118, v119
v_exp_f32 v112, v40
ds_read_b128 a[196:199], v2 offset:25600
v_mfma_f32_32x32x16_bf16 v[48:63], a[200:203], a[168:171], v[48:63]
v_exp_f32 v113, v41
v_mfma_f32_16x16x32_bf16 v[24:27], v[28:31], v[96:99], 0
ds_read_b128 a[200:203], v2 offset:26624
v_mfma_f32_32x32x16_bf16 v[48:63], a[204:207], a[172:175], v[48:63]
v_cvt_pk_bf16_f32 v100, v112, v113
v_exp_f32 v114, v42
ds_read_b128 a[204:207], v2 offset:27648
v_mfma_f32_32x32x16_bf16 v[48:63], a[208:211], a[176:179], v[48:63]
v_exp_f32 v115, v43
ds_read_b128 a[208:211], v2 offset:28672
v_mfma_f32_32x32x16_bf16 v[48:63], a[212:215], a[180:183], v[48:63]
v_cvt_pk_bf16_f32 v101, v114, v115
v_exp_f32 v116, v44
ds_read_b128 a[212:215], v2 offset:29696
v_mfma_f32_32x32x16_bf16 v[48:63], a[216:219], a[184:187], v[48:63]
v_exp_f32 v117, v45
ds_read_b128 a[216:219], v2 offset:30720
v_mfma_f32_32x32x16_bf16 v[48:63], a[220:223], a[188:191], v[48:63]
v_cvt_pk_bf16_f32 v102, v116, v117
v_exp_f32 v118, v46
ds_read_b128 a[220:223], v2 offset:31744
v_mfma_f32_32x32x16_bf16 a[64:79], a[224:227], v[104:107], a[64:79]
v_exp_f32 v119, v47
ds_read_b128 a[224:227], v2 offset:49152
v_mfma_f32_32x32x16_bf16 a[80:95], a[228:231], v[104:107], a[80:95]
v_cvt_pk_bf16_f32 v103, v118, v119
v_exp_f32 v120, v48
ds_read_b128 a[228:231], v2 offset:50176
v_mfma_f32_32x32x16_bf16 a[96:111], a[232:235], v[104:107], a[96:111]
v_mfma_f32_16x16x32_bf16 v[24:27], v[28:31], v[100:103], v[24:27]
v_exp_f32 v121, v49
ds_read_b128 a[232:235], v2 offset:51200
v_mfma_f32_32x32x16_bf16 a[112:127], a[236:239], v[104:107], a[112:127]
v_cvt_pk_bf16_f32 v104, v120, v121
v_exp_f32 v122, v50
ds_read_b128 a[236:239], v2 offset:52224
v_mfma_f32_32x32x16_bf16 a[64:79], a[240:243], v[108:111], a[64:79]
v_exp_f32 v123, v51
v_cmp_ngt_f32 vcc, s71, v24
ds_read_b128 a[240:243], v2 offset:53248
v_mfma_f32_32x32x16_bf16 a[80:95], a[244:247], v[108:111], a[80:95]
v_cvt_pk_bf16_f32 v105, v122, v123
v_exp_f32 v124, v52
s_cbranch_vccnz .Lr36
.Lr36b:
v_mfma_f32_32x32x16_bf16 a[96:111], a[248:251], v[108:111], a[96:111]
v_exp_f32 v125, v53
v_add_f32 v8, v8, v24
ds_read_b128 a[244:247], v2 offset:54272
ds_read_b128 a[248:251], v2 offset:55296
v_mfma_f32_32x32x16_bf16 a[112:127], a[252:255], v[108:111], a[112:127]
v_cvt_pk_bf16_f32 v106, v124, v125
v_exp_f32 v126, v54
ds_read_b128 a[252:255], v2 offset:56320
s_waitcnt lgkmcnt(8)
s_cmp_eq_u32 s48, 28
s_cbranch_scc1 .Lxl0_1
s_branch .Lx0_3
.Lx0_2:
v_mfma_f32_32x32x16_bf16 v[32:47], a[192:195], a[128:131], v[64:79]
s_add_u32 m0, s36, 8192
v_exp_f32 v127, v55
buffer_load_dwordx4 v1, s[20:23], s24 offen offset:0 lds
v_mfma_f32_32x32x16_bf16 v[32:47], a[196:199], a[132:135], v[32:47]
v_cvt_pk_bf16_f32 v107, v126, v127
v_exp_f32 v120, v56
v_mfma_f32_32x32x16_bf16 v[32:47], a[200:203], a[136:139], v[32:47]
v_exp_f32 v121, v57
v_mfma_f32_16x16x32_bf16 v[24:27], v[28:31], v[104:107], 0
buffer_load_dwordx4 v1, s[20:23], s24 offen offset:1024 lds
v_mfma_f32_32x32x16_bf16 v[32:47], a[204:207], a[140:143], v[32:47]
v_cvt_pk_bf16_f32 v108, v120, v121
v_exp_f32 v122, v58
v_mfma_f32_32x32x16_bf16 v[32:47], a[208:211], a[144:147], v[32:47]
v_exp_f32 v123, v59
buffer_load_dwordx4 v1, s[20:23], s24 offen offset:2048 lds
v_mfma_f32_32x32x16_bf16 v[32:47], a[212:215], a[148:151], v[32:47]
v_cvt_pk_bf16_f32 v109, v122, v123
v_exp_f32 v124, v60
v_mfma_f32_32x32x16_bf16 v[32:47], a[216:219], a[152:155], v[32:47]
v_exp_f32 v125, v61
buffer_load_dwordx4 v1, s[20:23], s24 offen offset:3072 lds
s_add_u32 m0, s36, 32768
v_mfma_f32_32x32x16_bf16 v[32:47], a[220:223], a[156:159], v[32:47]
v_cvt_pk_bf16_f32 v110, v124, v125
v_exp_f32 v126, v62
s_waitcnt lgkmcnt(0)
v_mfma_f32_32x32x16_bf16 a[0:15], a[224:227], v[96:99], a[0:15]
v_exp_f32 v127, v63
buffer_load_dwordx4 v3, s[20:23], s24 offen offset:0 lds
v_mfma_f32_32x32x16_bf16 a[16:31], a[228:231], v[96:99], a[16:31]
v_cvt_pk_bf16_f32 v111, v126, v127
v_exp_f32 v112, v32
v_mfma_f32_32x32x16_bf16 a[32:47], a[232:235], v[96:99], a[32:47]
v_exp_f32 v113, v33
v_mfma_f32_16x16x32_bf16 v[24:27], v[28:31], v[108:111], v[24:27]
buffer_load_dwordx4 v3, s[20:23], s24 offen offset:1024 lds
v_mfma_f32_32x32x16_bf16 a[48:63], a[236:239], v[96:99], a[48:63]
v_cvt_pk_bf16_f32 v96, v112, v113
v_exp_f32 v114, v34
buffer_load_dwordx4 v3, s[20:23], s24 offen offset:2048 lds
v_mfma_f32_32x32x16_bf16 a[0:15], a[240:243], v[100:103], a[0:15]
v_exp_f32 v115, v35
v_cmp_ngt_f32 vcc, s71, v24
v_mfma_f32_32x32x16_bf16 a[16:31], a[244:247], v[100:103], a[16:31]
v_cvt_pk_bf16_f32 v97, v114, v115
v_exp_f32 v116, v36
s_cbranch_vccnz .Lr8
.Lr8b:
v_mfma_f32_32x32x16_bf16 a[32:47], a[248:251], v[100:103], a[32:47]
v_exp_f32 v117, v37
v_add_f32 v9, v9, v24
buffer_load_dwordx4 v3, s[20:23], s24 offen offset:3072 lds
v_mfma_f32_32x32x16_bf16 a[48:63], a[252:255], v[100:103], a[48:63]
v_cvt_pk_bf16_f32 v98, v116, v117
v_exp_f32 v118, v38
s_waitcnt vmcnt(16)
s_barrier
s_add_u32 s24, s24, 0x2000
v_mfma_f32_32x32x16_bf16 v[48:63], a[192:195], a[160:163], v[80:95]
v_exp_f32 v119, v39
ds_read_b128 a[192:195], v2 offset:24576
v_mfma_f32_32x32x16_bf16 v[48:63], a[196:199], a[164:167], v[48:63]
v_cvt_pk_bf16_f32 v99, v118, v119
v_exp_f32 v112, v40
ds_read_b128 a[196:199], v2 offset:25600
v_mfma_f32_32x32x16_bf16 v[48:63], a[200:203], a[168:171], v[48:63]
v_exp_f32 v113, v41
v_mfma_f32_16x16x32_bf16 v[24:27], v[28:31], v[96:99], 0
ds_read_b128 a[200:203], v2 offset:26624
v_mfma_f32_32x32x16_bf16 v[48:63], a[204:207], a[172:175], v[48:63]
v_cvt_pk_bf16_f32 v100, v112, v113
v_exp_f32 v114, v42
ds_read_b128 a[204:207], v2 offset:27648
v_mfma_f32_32x32x16_bf16 v[48:63], a[208:211], a[176:179], v[48:63]
v_exp_f32 v115, v43
ds_read_b128 a[208:211], v2 offset:28672
v_mfma_f32_32x32x16_bf16 v[48:63], a[212:215], a[180:183], v[48:63]
v_cvt_pk_bf16_f32 v101, v114, v115
v_exp_f32 v116, v44
ds_read_b128 a[212:215], v2 offset:29696
v_mfma_f32_32x32x16_bf16 v[48:63], a[216:219], a[184:187], v[48:63]
v_exp_f32 v117, v45
ds_read_b128 a[216:219], v2 offset:30720
v_mfma_f32_32x32x16_bf16 v[48:63], a[220:223], a[188:191], v[48:63]
v_cvt_pk_bf16_f32 v102, v116, v117
v_exp_f32 v118, v46
ds_read_b128 a[220:223], v2 offset:31744
v_mfma_f32_32x32x16_bf16 a[64:79], a[224:227], v[104:107], a[64:79]
v_exp_f32 v119, v47
ds_read_b128 a[224:227], v2 offset:49152
v_mfma_f32_32x32x16_bf16 a[80:95], a[228:231], v[104:107], a[80:95]
v_cvt_pk_bf16_f32 v103, v118, v119
v_exp_f32 v120, v48
ds_read_b128 a[228:231], v2 offset:50176
v_mfma_f32_32x32x16_bf16 a[96:111], a[232:235], v[104:107], a[96:111]
v_mfma_f32_16x16x32_bf16 v[24:27], v[28:31], v[100:103], v[24:27]
v_exp_f32 v121, v49
ds_read_b128 a[232:235], v2 offset:51200
v_mfma_f32_32x32x16_bf16 a[112:127], a[236:239], v[104:107], a[112:127]
v_cvt_pk_bf16_f32 v104, v120, v121
v_exp_f32 v122, v50
ds_read_b128 a[236:239], v2 offset:52224
v_mfma_f32_32x32x16_bf16 a[64:79], a[240:243], v[108:111], a[64:79]
v_exp_f32 v123, v51
v_cmp_ngt_f32 vcc, s71, v24
ds_read_b128 a[240:243], v2 offset:53248
v_mfma_f32_32x32x16_bf16 a[80:95], a[244:247], v[108:111], a[80:95]
v_cvt_pk_bf16_f32 v105, v122, v123
v_exp_f32 v124, v52
s_cbranch_vccnz .Lr7
.Lr7b:
v_mfma_f32_32x32x16_bf16 a[96:111], a[248:251], v[108:111], a[96:111]
v_exp_f32 v125, v53
v_add_f32 v8, v8, v24
ds_read_b128 a[244:247], v2 offset:54272
ds_read_b128 a[248:251], v2 offset:55296
v_mfma_f32_32x32x16_bf16 a[112:127], a[252:255], v[108:111], a[112:127]
v_cvt_pk_bf16_f32 v106, v124, v125
v_exp_f32 v126, v54
ds_read_b128 a[252:255], v2 offset:56320
s_waitcnt lgkmcnt(8)
.Lx0_3:
v_mfma_f32_32x32x16_bf16 v[32:47], a[192:195], a[128:131], v[64:79]
s_add_u32 m0, s36, 16384
v_exp_f32 v127, v55
buffer_load_dwordx4 v1, s[20:23], s24 offen offset:0 lds
v_mfma_f32_32x32x16_bf16 v[32:47], a[196:199], a[132:135], v[32:47]
v_cvt_pk_bf16_f32 v107, v126, v127
v_exp_f32 v120, v56
v_mfma_f32_32x32x16_bf16 v[32:47], a[200:203], a[136:139], v[32:47]
v_exp_f32 v121, v57
v_mfma_f32_16x16x32_bf16 v[24:27], v[28:31], v[104:107], 0
buffer_load_dwordx4 v1, s[20:23], s24 offen offset:1024 lds
v_mfma_f32_32x32x16_bf16 v[32:47], a[204:207], a[140:143], v[32:47]
v_cvt_pk_bf16_f32 v108, v120, v121
v_exp_f32 v122, v58
v_mfma_f32_32x32x16_bf16 v[32:47], a[208:211], a[144:147], v[32:47]
v_exp_f32 v123, v59
buffer_load_dwordx4 v1, s[20:23], s24 offen offset:2048 lds
v_mfma_f32_32x32x16_bf16 v[32:47], a[212:215], a[148:151], v[32:47]
v_cvt_pk_bf16_f32 v109, v122, v123
v_exp_f32 v124, v60
v_mfma_f32_32x32x16_bf16 v[32:47], a[216:219], a[152:155], v[32:47]
v_exp_f32 v125, v61
buffer_load_dwordx4 v1, s[20:23], s24 offen offset:3072 lds
s_add_u32 m0, s36, 40960
v_mfma_f32_32x32x16_bf16 v[32:47], a[220:223], a[156:159], v[32:47]
v_cvt_pk_bf16_f32 v110, v124, v125
v_exp_f32 v126, v62
s_waitcnt lgkmcnt(0)
v_mfma_f32_32x32x16_bf16 a[0:15], a[224:227], v[96:99], a[0:15]
v_exp_f32 v127, v63
buffer_load_dwordx4 v3, s[20:23], s24 offen offset:0 lds
v_mfma_f32_32x32x16_bf16 a[16:31], a[228:231], v[96:99], a[16:31]
v_cvt_pk_bf16_f32 v111, v126, v127
v_exp_f32 v112, v32
v_mfma_f32_32x32x16_bf16 a[32:47], a[232:235], v[96:99], a[32:47]
v_exp_f32 v113, v33
v_mfma_f32_16x16x32_bf16 v[24:27], v[28:31], v[108:111], v[24:27]
buffer_load_dwordx4 v3, s[20:23], s24 offen offset:1024 lds
v_mfma_f32_32x32x16_bf16 a[48:63], a[236:239], v[96:99], a[48:63]
v_cvt_pk_bf16_f32 v96, v112, v113
v_exp_f32 v114, v34
buffer_load_dwordx4 v3, s[20:23], s24 offen offset:2048 lds
v_mfma_f32_32x32x16_bf16 a[0:15], a[240:243], v[100:103], a[0:15]
v_exp_f32 v115, v35
v_cmp_ngt_f32 vcc, s71, v24
v_mfma_f32_32x32x16_bf16 a[16:31], a[244:247], v[100:103], a[16:31]
v_cvt_pk_bf16_f32 v97, v114, v115
v_exp_f32 v116, v36
s_cbranch_vccnz .Lr10
.Lr10b:
v_mfma_f32_32x32x16_bf16 a[32:47], a[248:251], v[100:103], a[32:47]
v_exp_f32 v117, v37
v_add_f32 v9, v9, v24
buffer_load_dwordx4 v3, s[20:23], s24 offen offset:3072 lds
v_mfma_f32_32x32x16_bf16 a[48:63], a[252:255], v[100:103], a[48:63]
v_cvt_pk_bf16_f32 v98, v116, v117
v_exp_f32 v118, v38
s_waitcnt vmcnt(16)
s_barrier
s_add_u32 s24, s24, 0x2000
v_mfma_f32_32x32x16_bf16 v[48:63], a[192:195], a[160:163], v[80:95]
v_exp_f32 v119, v39
ds_read_b128 a[192:195], v2 offset:0
v_mfma_f32_32x32x16_bf16 v[48:63], a[196:199], a[164:167], v[48:63]
v_cvt_pk_bf16_f32 v99, v118, v119
v_exp_f32 v112, v40
ds_read_b128 a[196:199], v2 offset:1024
v_mfma_f32_32x32x16_bf16 v[48:63], a[200:203], a[168:171], v[48:63]
v_exp_f32 v113, v41
v_mfma_f32_16x16x32_bf16 v[24:27], v[28:31], v[96:99], 0
ds_read_b128 a[200:203], v2 offset:2048
v_mfma_f32_32x32x16_bf16 v[48:63], a[204:207], a[172:175], v[48:63]
v_cvt_pk_bf16_f32 v100, v112, v113
v_exp_f32 v114, v42
ds_read_b128 a[204:207], v2 offset:3072
v_mfma_f32_32x32x16_bf16 v[48:63], a[208:211], a[176:179], v[48:63]
v_exp_f32 v115, v43
ds_read_b128 a[208:211], v2 offset:4096
v_mfma_f32_32x32x16_bf16 v[48:63], a[212:215], a[180:183], v[48:63]
v_cvt_pk_bf16_f32 v101, v114, v115
v_exp_f32 v116, v44
ds_read_b128 a[212:215], v2 offset:5120
v_mfma_f32_32x32x16_bf16 v[48:63], a[216:219], a[184:187], v[48:63]
v_exp_f32 v117, v45
ds_read_b128 a[216:219], v2 offset:6144
v_mfma_f32_32x32x16_bf16 v[48:63], a[220:223], a[188:191], v[48:63]
v_cvt_pk_bf16_f32 v102, v116, v117
v_exp_f32 v118, v46
ds_read_b128 a[220:223], v2 offset:7168
v_mfma_f32_32x32x16_bf16 a[64:79], a[224:227], v[104:107], a[64:79]
v_exp_f32 v119, v47
ds_read_b128 a[224:227], v2 offset:57344
v_mfma_f32_32x32x16_bf16 a[80:95], a[228:231], v[104:107], a[80:95]
v_cvt_pk_bf16_f32 v103, v118, v119
v_exp_f32 v120, v48
ds_read_b128 a[228:231], v2 offset:58368
v_mfma_f32_32x32x16_bf16 a[96:111], a[232:235], v[104:107], a[96:111]
v_mfma_f32_16x16x32_bf16 v[24:27], v[28:31], v[100:103], v[24:27]
v_exp_f32 v121, v49
ds_read_b128 a[232:235], v2 offset:59392
v_mfma_f32_32x32x16_bf16 a[112:127], a[236:239], v[104:107], a[112:127]
v_cvt_pk_bf16_f32 v104, v120, v121
v_exp_f32 v122, v50
ds_read_b128 a[236:239], v2 offset:60416
v_mfma_f32_32x32x16_bf16 a[64:79], a[240:243], v[108:111], a[64:79]
v_exp_f32 v123, v51
v_cmp_ngt_f32 vcc, s71, v24
ds_read_b128 a[240:243], v2 offset:61440
v_mfma_f32_32x32x16_bf16 a[80:95], a[244:247], v[108:111], a[80:95]
v_cvt_pk_bf16_f32 v105, v122, v123
v_exp_f32 v124, v52
s_cbranch_vccnz .Lr9
.Lr9b:
v_mfma_f32_32x32x16_bf16 a[96:111], a[248:251], v[108:111], a[96:111]
v_exp_f32 v125, v53
v_add_f32 v8, v8, v24
ds_read_b128 a[244:247], v2 offset:62464
ds_read_b128 a[248:251], v2 offset:63488
v_mfma_f32_32x32x16_bf16 a[112:127], a[252:255], v[108:111], a[112:127]
v_cvt_pk_bf16_f32 v106, v124, v125
v_exp_f32 v126, v54
ds_read_b128 a[252:255], v2 offset:64512
s_waitcnt lgkmcnt(8)
.Lx0_0:
v_mfma_f32_32x32x16_bf16 v[32:47], a[192:195], a[128:131], v[64:79]
s_add_u32 m0, s36, 24576
v_exp_f32 v127, v55
buffer_load_dwordx4 v1, s[20:23], s24 offen offset:0 lds
v_mfma_f32_32x32x16_bf16 v[32:47], a[196:199], a[132:135], v[32:47]
v_cvt_pk_bf16_f32 v107, v126, v127
v_exp_f32 v120, v56
v_mfma_f32_32x32x16_bf16 v[32:47], a[200:203], a[136:139], v[32:47]
v_exp_f32 v121, v57
v_mfma_f32_16x16x32_bf16 v[24:27], v[28:31], v[104:107], 0
buffer_load_dwordx4 v1, s[20:23], s24 offen offset:1024 lds
v_mfma_f32_32x32x16_bf16 v[32:47], a[204:207], a[140:143], v[32:47]
v_cvt_pk_bf16_f32 v108, v120, v121
v_exp_f32 v122, v58
v_mfma_f32_32x32x16_bf16 v[32:47], a[208:211], a[144:147], v[32:47]
v_exp_f32 v123, v59
buffer_load_dwordx4 v1, s[20:23], s24 offen offset:2048 lds
v_mfma_f32_32x32x16_bf16 v[32:47], a[212:215], a[148:151], v[32:47]
v_cvt_pk_bf16_f32 v109, v122, v123
v_exp_f32 v124, v60
v_mfma_f32_32x32x16_bf16 v[32:47], a[216:219], a[152:155], v[32:47]
v_exp_f32 v125, v61
buffer_load_dwordx4 v1, s[20:23], s24 offen offset:3072 lds
s_add_u32 m0, s36, 49152
v_mfma_f32_32x32x16_bf16 v[32:47], a[220:223], a[156:159], v[32:47]
v_cvt_pk_bf16_f32 v110, v124, v125
v_exp_f32 v126, v62
s_waitcnt lgkmcnt(0)
v_mfma_f32_32x32x16_bf16 a[0:15], a[224:227], v[96:99], a[0:15]
v_exp_f32 v127, v63
buffer_load_dwordx4 v3, s[20:23], s24 offen offset:0 lds
v_mfma_f32_32x32x16_bf16 a[16:31], a[228:231], v[96:99], a[16:31]
v_cvt_pk_bf16_f32 v111, v126, v127
v_exp_f32 v112, v32
v_mfma_f32_32x32x16_bf16 a[32:47], a[232:235], v[96:99], a[32:47]
v_exp_f32 v113, v33
v_mfma_f32_16x16x32_bf16 v[24:27], v[28:31], v[108:111], v[24:27]
buffer_load_dwordx4 v3, s[20:23], s24 offen offset:1024 lds
v_mfma_f32_32x32x16_bf16 a[48:63], a[236:239], v[96:99], a[48:63]
v_cvt_pk_bf16_f32 v96, v112, v113
v_exp_f32 v114, v34
buffer_load_dwordx4 v3, s[20:23], s24 offen offset:2048 lds
v_mfma_f32_32x32x16_bf16 a[0:15], a[240:243], v[100:103], a[0:15]
v_exp_f32 v115, v35
v_cmp_ngt_f32 vcc, s71, v24
v_mfma_f32_32x32x16_bf16 a[16:31], a[244:247], v[100:103], a[16:31]
v_cvt_pk_bf16_f32 v97, v114, v115
v_exp_f32 v116, v36
s_cbranch_vccnz .Lr12
.Lr12b:
v_mfma_f32_32x32x16_bf16 a[32:47], a[248:251], v[100:103], a[32:47]
v_exp_f32 v117, v37
v_add_f32 v9, v9, v24
buffer_load_dwordx4 v3, s[20:23], s24 offen offset:3072 lds
v_mfma_f32_32x32x16_bf16 a[48:63], a[252:255], v[100:103], a[48:63]
v_cvt_pk_bf16_f32 v98, v116, v117
v_exp_f32 v118, v38
s_waitcnt vmcnt(16)
s_barrier
s_add_u32 s24, s24, 0x2000
v_mfma_f32_32x32x16_bf16 v[48:63], a[192:195], a[160:163], v[80:95]
v_exp_f32 v119, v39
ds_read_b128 a[192:195], v2 offset:8192
v_mfma_f32_32x32x16_bf16 v[48:63], a[196:199], a[164:167], v[48:63]
v_cvt_pk_bf16_f32 v99, v118, v119
v_exp_f32 v112, v40
ds_read_b128 a[196:199], v2 offset:9216
v_mfma_f32_32x32x16_bf16 v[48:63], a[200:203], a[168:171], v[48:63]
v_exp_f32 v113, v41
v_mfma_f32_16x16x32_bf16 v[24:27], v[28:31], v[96:99], 0
ds_read_b128 a[200:203], v2 offset:10240
v_mfma_f32_32x32x16_bf16 v[48:63], a[204:207], a[172:175], v[48:63]
v_cvt_pk_bf16_f32 v100, v112, v113
v_exp_f32 v114, v42
ds_read_b128 a[204:207], v2 offset:11264
v_mfma_f32_32x32x16_bf16 v[48:63], a[208:211], a[176:179], v[48:63]
v_exp_f32 v115, v43
ds_read_b128 a[208:211], v2 offset:12288
v_mfma_f32_32x32x16_bf16 v[48:63], a[212:215], a[180:183], v[48:63]
v_cvt_pk_bf16_f32 v101, v114, v115
v_exp_f32 v116, v44
ds_read_b128 a[212:215], v2 offset:13312
v_mfma_f32_32x32x16_bf16 v[48:63], a[216:219], a[184:187], v[48:63]
v_exp_f32 v117, v45
ds_read_b128 a[216:219], v2 offset:14336
v_mfma_f32_32x32x16_bf16 v[48:63], a[220:223], a[188:191], v[48:63]
v_cvt_pk_bf16_f32 v102, v116, v117
v_exp_f32 v118, v46
ds_read_b128 a[220:223], v2 offset:15360
v_mfma_f32_32x32x16_bf16 a[64:79], a[224:227], v[104:107], a[64:79]
v_exp_f32 v119, v47
ds_read_b128 a[224:227], v2 offset:32768
v_mfma_f32_32x32x16_bf16 a[80:95], a[228:231], v[104:107], a[80:95]
v_cvt_pk_bf16_f32 v103, v118, v119
v_exp_f32 v120, v48
ds_read_b128 a[228:231], v2 offset:33792
v_mfma_f32_32x32x16_bf16 a[96:111], a[232:235], v[104:107], a[96:111]
v_mfma_f32_16x16x32_bf16 v[24:27], v[28:31], v[100:103], v[24:27]
v_exp_f32 v121, v49
ds_read_b128 a[232:235], v2 offset:34816
v_mfma_f32_32x32x16_bf16 a[112:127], a[236:239], v[104:107], a[112:127]
v_cvt_pk_bf16_f32 v104, v120, v121
v_exp_f32 v122, v50
ds_read_b128 a[236:239], v2 offset:35840
v_mfma_f32_32x32x16_bf16 a[64:79], a[240:243], v[108:111], a[64:79]
v_exp_f32 v123, v51
v_cmp_ngt_f32 vcc, s71, v24
ds_read_b128 a[240:243], v2 offset:36864
v_mfma_f32_32x32x16_bf16 a[80:95], a[244:247], v[108:111], a[80:95]
v_cvt_pk_bf16_f32 v105, v122, v123
v_exp_f32 v124, v52
s_cbranch_vccnz .Lr11
.Lr11b:
v_mfma_f32_32x32x16_bf16 a[96:111], a[248:251], v[108:111], a[96:111]
v_exp_f32 v125, v53
v_add_f32 v8, v8, v24
ds_read_b128 a[244:247], v2 offset:37888
ds_read_b128 a[248:251], v2 offset:38912
v_mfma_f32_32x32x16_bf16 a[112:127], a[252:255], v[108:111], a[112:127]
v_cvt_pk_bf16_f32 v106, v124, v125
v_exp_f32 v126, v54
ds_read_b128 a[252:255], v2 offset:39936
s_waitcnt lgkmcnt(8)
.Lx0_1:
v_mfma_f32_32x32x16_bf16 v[32:47], a[192:195], a[128:131], v[64:79]
s_add_u32 m0, s36, 0
v_exp_f32 v127, v55
buffer_load_dwordx4 v1, s[20:23], s24 offen offset:0 lds
v_mfma_f32_32x32x16_bf16 v[32:47], a[196:199], a[132:135], v[32:47]
v_cvt_pk_bf16_f32 v107, v126, v127
v_exp_f32 v120, v56
v_mfma_f32_32x32x16_bf16 v[32:47], a[200:203], a[136:139], v[32:47]
v_exp_f32 v121, v57
v_mfma_f32_16x16x32_bf16 v[24:27], v[28:31], v[104:107], 0
buffer_load_dwordx4 v1, s[20:23], s24 offen offset:1024 lds
v_mfma_f32_32x32x16_bf16 v[32:47], a[204:207], a[140:143], v[32:47]
v_cvt_pk_bf16_f32 v108, v120, v121
v_exp_f32 v122, v58
v_mfma_f32_32x32x16_bf16 v[32:47], a[208:211], a[144:147], v[32:47]
v_exp_f32 v123, v59
buffer_load_dwordx4 v1, s[20:23], s24 offen offset:2048 lds
v_mfma_f32_32x32x16_bf16 v[32:47], a[212:215], a[148:151], v[32:47]
v_cvt_pk_bf16_f32 v109, v122, v123
v_exp_f32 v124, v60
v_mfma_f32_32x32x16_bf16 v[32:47], a[216:219], a[152:155], v[32:47]
v_exp_f32 v125, v61
buffer_load_dwordx4 v1, s[20:23], s24 offen offset:3072 lds
s_add_u32 m0, s36, 57344
v_mfma_f32_32x32x16_bf16 v[32:47], a[220:223], a[156:159], v[32:47]
v_cvt_pk_bf16_f32 v110, v124, v125
v_exp_f32 v126, v62
s_waitcnt lgkmcnt(0)
v_mfma_f32_32x32x16_bf16 a[0:15], a[224:227], v[96:99], a[0:15]
v_exp_f32 v127, v63
buffer_load_dwordx4 v3, s[20:23], s24 offen offset:0 lds
v_mfma_f32_32x32x16_bf16 a[16:31], a[228:231], v[96:99], a[16:31]
v_cvt_pk_bf16_f32 v111, v126, v127
v_exp_f32 v112, v32
v_mfma_f32_32x32x16_bf16 a[32:47], a[232:235], v[96:99], a[32:47]
v_exp_f32 v113, v33
v_mfma_f32_16x16x32_bf16 v[24:27], v[28:31], v[108:111], v[24:27]
buffer_load_dwordx4 v3, s[20:23], s24 offen offset:1024 lds
v_mfma_f32_32x32x16_bf16 a[48:63], a[236:239], v[96:99], a[48:63]
v_cvt_pk_bf16_f32 v96, v112, v113
v_exp_f32 v114, v34
buffer_load_dwordx4 v3, s[20:23], s24 offen offset:2048 lds
v_mfma_f32_32x32x16_bf16 a[0:15], a[240:243], v[100:103], a[0:15]
v_exp_f32 v115, v35
v_cmp_ngt_f32 vcc, s71, v24
v_mfma_f32_32x32x16_bf16 a[16:31], a[244:247], v[100:103], a[16:31]
v_cvt_pk_bf16_f32 v97, v114, v115
v_exp_f32 v116, v36
s_cbranch_vccnz .Lr14
.Lr14b:
v_mfma_f32_32x32x16_bf16 a[32:47], a[248:251], v[100:103], a[32:47]
v_exp_f32 v117, v37
v_add_f32 v9, v9, v24
buffer_load_dwordx4 v3, s[20:23], s24 offen offset:3072 lds
v_mfma_f32_32x32x16_bf16 a[48:63], a[252:255], v[100:103], a[48:63]
v_cvt_pk_bf16_f32 v98, v116, v117
v_exp_f32 v118, v38
s_waitcnt vmcnt(16)
s_barrier
s_add_u32 s24, s24, 0x2000
v_mfma_f32_32x32x16_bf16 v[48:63], a[192:195], a[160:163], v[80:95]
v_exp_f32 v119, v39
ds_read_b128 a[192:195], v2 offset:16384
v_mfma_f32_32x32x16_bf16 v[48:63], a[196:199], a[164:167], v[48:63]
v_cvt_pk_bf16_f32 v99, v118, v119
v_exp_f32 v112, v40
ds_read_b128 a[196:199], v2 offset:17408
v_mfma_f32_32x32x16_bf16 v[48:63], a[200:203], a[168:171], v[48:63]
v_exp_f32 v113, v41
v_mfma_f32_16x16x32_bf16 v[24:27], v[28:31], v[96:99], 0
ds_read_b128 a[200:203], v2 offset:18432
v_mfma_f32_32x32x16_bf16 v[48:63], a[204:207], a[172:175], v[48:63]
v_cvt_pk_bf16_f32 v100, v112, v113
v_exp_f32 v114, v42
ds_read_b128 a[204:207], v2 offset:19456
v_mfma_f32_32x32x16_bf16 v[48:63], a[208:211], a[176:179], v[48:63]
v_exp_f32 v115, v43
ds_read_b128 a[208:211], v2 offset:20480
v_mfma_f32_32x32x16_bf16 v[48:63], a[212:215], a[180:183], v[48:63]
v_cvt_pk_bf16_f32 v101, v114, v115
v_exp_f32 v116, v44
ds_read_b128 a[212:215], v2 offset:21504
v_mfma_f32_32x32x16_bf16 v[48:63], a[216:219], a[184:187], v[48:63]
v_exp_f32 v117, v45
ds_read_b128 a[216:219], v2 offset:22528
v_mfma_f32_32x32x16_bf16 v[48:63], a[220:223], a[188:191], v[48:63]
v_cvt_pk_bf16_f32 v102, v116, v117
v_exp_f32 v118, v46
ds_read_b128 a[220:223], v2 offset:23552
v_mfma_f32_32x32x16_bf16 a[64:79], a[224:227], v[104:107], a[64:79]
v_exp_f32 v119, v47
ds_read_b128 a[224:227], v2 offset:40960
v_mfma_f32_32x32x16_bf16 a[80:95], a[228:231], v[104:107], a[80:95]
v_cvt_pk_bf16_f32 v103, v118, v119
v_exp_f32 v120, v48
ds_read_b128 a[228:231], v2 offset:41984
v_mfma_f32_32x32x16_bf16 a[96:111], a[232:235], v[104:107], a[96:111]
v_mfma_f32_16x16x32_bf16 v[24:27], v[28:31], v[100:103], v[24:27]
v_exp_f32 v121, v49
ds_read_b128 a[232:235], v2 offset:43008
v_mfma_f32_32x32x16_bf16 a[112:127], a[236:239], v[104:107], a[112:127]
v_cvt_pk_bf16_f32 v104, v120, v121
v_exp_f32 v122, v50
ds_read_b128 a[236:239], v2 offset:44032
v_mfma_f32_32x32x16_bf16 a[64:79], a[240:243], v[108:111], a[64:79]
v_exp_f32 v123, v51
v_cmp_ngt_f32 vcc, s71, v24
ds_read_b128 a[240:243], v2 offset:45056
v_mfma_f32_32x32x16_bf16 a[80:95], a[244:247], v[108:111], a[80:95]
v_cvt_pk_bf16_f32 v105, v122, v123
v_exp_f32 v124, v52
s_cbranch_vccnz .Lr13
.Lr13b:
v_mfma_f32_32x32x16_bf16 a[96:111], a[248:251], v[108:111], a[96:111]
v_exp_f32 v125, v53
v_add_f32 v8, v8, v24
ds_read_b128 a[244:247], v2 offset:46080
ds_read_b128 a[248:251], v2 offset:47104
v_mfma_f32_32x32x16_bf16 a[112:127], a[252:255], v[108:111], a[112:127]
v_cvt_pk_bf16_f32 v106, v124, v125
v_exp_f32 v126, v54
ds_read_b128 a[252:255], v2 offset:48128
s_waitcnt lgkmcnt(8)
s_cmp_lg_u32 s24, s40
s_cbranch_scc1 .Lx0_2
v_mfma_f32_32x32x16_bf16 v[32:47], a[192:195], a[128:131], v[64:79]
s_add_u32 m0, s36, 8192
v_exp_f32 v127, v55
buffer_load_dwordx4 v1, s[20:23], s24 offen offset:0 lds
v_mfma_f32_32x32x16_bf16 v[32:47], a[196:199], a[132:135], v[32:47]
v_cvt_pk_bf16_f32 v107, v126, v127
v_exp_f32 v120, v56
v_mfma_f32_32x32x16_bf16 v[32:47], a[200:203], a[136:139], v[32:47]
v_exp_f32 v121, v57
v_mfma_f32_16x16x32_bf16 v[24:27], v[28:31], v[104:107], 0
buffer_load_dwordx4 v1, s[20:23], s24 offen offset:1024 lds
v_mfma_f32_32x32x16_bf16 v[32:47], a[204:207], a[140:143], v[32:47]
v_cvt_pk_bf16_f32 v108, v120, v121
v_exp_f32 v122, v58
v_mfma_f32_32x32x16_bf16 v[32:47], a[208:211], a[144:147], v[32:47]
v_exp_f32 v123, v59
buffer_load_dwordx4 v1, s[20:23], s24 offen offset:2048 lds
v_mfma_f32_32x32x16_bf16 v[32:47], a[212:215], a[148:151], v[32:47]
v_cvt_pk_bf16_f32 v109, v122, v123
v_exp_f32 v124, v60
v_mfma_f32_32x32x16_bf16 v[32:47], a[216:219], a[152:155], v[32:47]
v_exp_f32 v125, v61
buffer_load_dwordx4 v1, s[20:23], s24 offen offset:3072 lds
s_add_u32 m0, s36, 32768
v_mfma_f32_32x32x16_bf16 v[32:47], a[220:223], a[156:159], v[32:47]
v_cvt_pk_bf16_f32 v110, v124, v125
v_exp_f32 v126, v62
s_waitcnt lgkmcnt(0)
v_mfma_f32_32x32x16_bf16 a[0:15], a[224:227], v[96:99], a[0:15]
v_exp_f32 v127, v63
buffer_load_dwordx4 v3, s[20:23], s24 offen offset:0 lds
v_mfma_f32_32x32x16_bf16 a[16:31], a[228:231], v[96:99], a[16:31]
v_cvt_pk_bf16_f32 v111, v126, v127
v_cndmask_b32 v32, v32, v5, s[54:55]
v_cndmask_b32 v33, v33, v5, s[56:57]
v_exp_f32 v112, v32
v_mfma_f32_32x32x16_bf16 a[32:47], a[232:235], v[96:99], a[32:47]
v_cndmask_b32 v34, v34, v5, s[58:59]
v_cndmask_b32 v35, v35, v5, s[60:61]
v_exp_f32 v113, v33
v_mfma_f32_16x16x32_bf16 v[24:27], v[28:31], v[108:111], v[24:27]
buffer_load_dwordx4 v3, s[20:23], s24 offen offset:1024 lds
v_mfma_f32_32x32x16_bf16 a[48:63], a[236:239], v[96:99], a[48:63]
v_cvt_pk_bf16_f32 v96, v112, v113
v_cndmask_b32 v36, v36, v5, s[62:63]
v_cndmask_b32 v37, v37, v5, s[64:65]
v_exp_f32 v114, v34
buffer_load_dwordx4 v3, s[20:23], s24 offen offset:2048 lds
v_mfma_f32_32x32x16_bf16 a[0:15], a[240:243], v[100:103], a[0:15]
v_cndmask_b32 v38, v38, v5, s[66:67]
v_cndmask_b32 v39, v39, v5, s[68:69]
v_exp_f32 v115, v35
v_cmp_ngt_f32 vcc, s71, v24
v_mfma_f32_32x32x16_bf16 a[16:31], a[244:247], v[100:103], a[16:31]
v_cvt_pk_bf16_f32 v97, v114, v115
v_cndmask_b32 v40, v40, v5, s[84:85]
v_cndmask_b32 v41, v41, v5, s[86:87]
v_exp_f32 v116, v36
s_cbranch_vccnz .Lr39
.Lr39b:
v_mfma_f32_32x32x16_bf16 a[32:47], a[248:251], v[100:103], a[32:47]
v_cndmask_b32 v42, v42, v5, s[88:89]
v_cndmask_b32 v43, v43, v5, s[90:91]
v_exp_f32 v117, v37
v_add_f32 v9, v9, v24
buffer_load_dwordx4 v3, s[20:23], s24 offen offset:3072 lds
v_mfma_f32_32x32x16_bf16 a[48:63], a[252:255], v[100:103], a[48:63]
v_cvt_pk_bf16_f32 v98, v116, v117
v_cndmask_b32 v44, v44, v5, s[92:93]
v_cndmask_b32 v45, v45, v5, s[94:95]
v_exp_f32 v118, v38
s_waitcnt vmcnt(16)
s_barrier
s_add_u32 s24, s24, 0x2000
v_mfma_f32_32x32x16_bf16 v[48:63], a[192:195], a[160:163], v[80:95]
v_cndmask_b32 v46, v46, v5, s[96:97]
v_cndmask_b32 v47, v47, v5, s[98:99]
v_exp_f32 v119, v39
ds_read_b128 a[192:195], v2 offset:24576
v_mfma_f32_32x32x16_bf16 v[48:63], a[196:199], a[164:167], v[48:63]
v_cvt_pk_bf16_f32 v99, v118, v119
v_exp_f32 v112, v40
ds_read_b128 a[196:199], v2 offset:25600
v_mfma_f32_32x32x16_bf16 v[48:63], a[200:203], a[168:171], v[48:63]
v_exp_f32 v113, v41
v_mfma_f32_16x16x32_bf16 v[24:27], v[28:31], v[96:99], 0
ds_read_b128 a[200:203], v2 offset:26624
v_mfma_f32_32x32x16_bf16 v[48:63], a[204:207], a[172:175], v[48:63]
v_cvt_pk_bf16_f32 v100, v112, v113
v_exp_f32 v114, v42
ds_read_b128 a[204:207], v2 offset:27648
v_mfma_f32_32x32x16_bf16 v[48:63], a[208:211], a[176:179], v[48:63]
v_exp_f32 v115, v43
ds_read_b128 a[208:211], v2 offset:28672
v_mfma_f32_32x32x16_bf16 v[48:63], a[212:215], a[180:183], v[48:63]
v_cvt_pk_bf16_f32 v101, v114, v115
v_exp_f32 v116, v44
ds_read_b128 a[212:215], v2 offset:29696
v_mfma_f32_32x32x16_bf16 v[48:63], a[216:219], a[184:187], v[48:63]
v_exp_f32 v117, v45
ds_read_b128 a[216:219], v2 offset:30720
v_mfma_f32_32x32x16_bf16 v[48:63], a[220:223], a[188:191], v[48:63]
v_cvt_pk_bf16_f32 v102, v116, v117
v_exp_f32 v118, v46
ds_read_b128 a[220:223], v2 offset:31744
v_mfma_f32_32x32x16_bf16 a[64:79], a[224:227], v[104:107], a[64:79]
v_exp_f32 v119, v47
ds_read_b128 a[224:227], v2 offset:49152
v_mfma_f32_32x32x16_bf16 a[80:95], a[228:231], v[104:107], a[80:95]
v_cvt_pk_bf16_f32 v103, v118, v119
v_exp_f32 v120, v48
ds_read_b128 a[228:231], v2 offset:50176
v_mfma_f32_32x32x16_bf16 a[96:111], a[232:235], v[104:107], a[96:111]
v_mfma_f32_16x16x32_bf16 v[24:27], v[28:31], v[100:103], v[24:27]
v_exp_f32 v121, v49
ds_read_b128 a[232:235], v2 offset:51200
v_mfma_f32_32x32x16_bf16 a[112:127], a[236:239], v[104:107], a[112:127]
v_cvt_pk_bf16_f32 v104, v120, v121
v_exp_f32 v122, v50
ds_read_b128 a[236:239], v2 offset:52224
v_mfma_f32_32x32x16_bf16 a[64:79], a[240:243], v[108:111], a[64:79]
v_exp_f32 v123, v51
v_cmp_ngt_f32 vcc, s71, v24
ds_read_b128 a[240:243], v2 offset:53248
v_mfma_f32_32x32x16_bf16 a[80:95], a[244:247], v[108:111], a[80:95]
v_cvt_pk_bf16_f32 v105, v122, v123
v_exp_f32 v124, v52
s_cbranch_vccnz .Lr38
.Lr38b:
v_mfma_f32_32x32x16_bf16 a[96:111], a[248:251], v[108:111], a[96:111]
v_exp_f32 v125, v53
v_add_f32 v8, v8, v24
ds_read_b128 a[244:247], v2 offset:54272
ds_read_b128 a[248:251], v2 offset:55296
v_mfma_f32_32x32x16_bf16 a[112:127], a[252:255], v[108:111], a[112:127]
v_cvt_pk_bf16_f32 v106, v124, v125
v_exp_f32 v126, v54
ds_read_b128 a[252:255], v2 offset:56320
s_waitcnt lgkmcnt(8)
.Lxl0_1:
s_add_u32 m0, s36, 16384
v_exp_f32 v127, v55
buffer_load_dwordx4 v1, s[20:23], s24 offen offset:0 lds
v_cvt_pk_bf16_f32 v107, v126, v127
v_exp_f32 v120, v56
v_exp_f32 v121, v57
v_mfma_f32_16x16x32_bf16 v[24:27], v[28:31], v[104:107], 0
buffer_load_dwordx4 v1, s[20:23], s24 offen offset:1024 lds
v_cvt_pk_bf16_f32 v108, v120, v121
v_exp_f32 v122, v58
v_exp_f32 v123, v59
buffer_load_dwordx4 v1, s[20:23], s24 offen offset:2048 lds
v_cvt_pk_bf16_f32 v109, v122, v123
v_exp_f32 v124, v60
v_exp_f32 v125, v61
buffer_load_dwordx4 v1, s[20:23], s24 offen offset:3072 lds
s_add_u32 m0, s36, 40960
v_cvt_pk_bf16_f32 v110, v124, v125
v_exp_f32 v126, v62
s_waitcnt lgkmcnt(0)
v_mfma_f32_32x32x16_bf16 a[0:15], a[224:227], v[96:99], a[0:15]
v_exp_f32 v127, v63
buffer_load_dwordx4 v3, s[20:23], s24 offen offset:0 lds
v_mfma_f32_32x32x16_bf16 a[16:31], a[228:231], v[96:99], a[16:31]
v_cvt_pk_bf16_f32 v111, v126, v127
v_mfma_f32_32x32x16_bf16 a[32:47], a[232:235], v[96:99], a[32:47]
v_mfma_f32_16x16x32_bf16 v[24:27], v[28:31], v[108:111], v[24:27]
buffer_load_dwordx4 v3, s[20:23], s24 offen offset:1024 lds
v_mfma_f32_32x32x16_bf16 a[48:63], a[236:239], v[96:99], a[48:63]
buffer_load_dwordx4 v3, s[20:23], s24 offen offset:2048 lds
v_mfma_f32_32x32x16_bf16 a[0:15], a[240:243], v[100:103], a[0:15]
v_cmp_ngt_f32 vcc, s71, v24
v_mfma_f32_32x32x16_bf16 a[16:31], a[244:247], v[100:103], a[16:31]
s_cbranch_vccnz .Lr41
.Lr41b:
v_mfma_f32_32x32x16_bf16 a[32:47], a[248:251], v[100:103], a[32:47]
v_add_f32 v9, v9, v24
buffer_load_dwordx4 v3, s[20:23], s24 offen offset:3072 lds
v_mfma_f32_32x32x16_bf16 a[48:63], a[252:255], v[100:103], a[48:63]
s_waitcnt vmcnt(16)
s_barrier
s_add_u32 s24, s24, 0x2000
v_mfma_f32_32x32x16_bf16 v[48:63], a[192:195], a[160:163], v[80:95]
ds_read_b128 a[192:195], v2 offset:0
v_mfma_f32_32x32x16_bf16 v[48:63], a[196:199], a[164:167], v[48:63]
ds_read_b128 a[196:199], v2 offset:1024
v_mfma_f32_32x32x16_bf16 v[48:63], a[200:203], a[168:171], v[48:63]
ds_read_b128 a[200:203], v2 offset:2048
v_mfma_f32_32x32x16_bf16 v[48:63], a[204:207], a[172:175], v[48:63]
ds_read_b128 a[204:207], v2 offset:3072
v_mfma_f32_32x32x16_bf16 v[48:63], a[208:211], a[176:179], v[48:63]
ds_read_b128 a[208:211], v2 offset:4096
v_mfma_f32_32x32x16_bf16 v[48:63], a[212:215], a[180:183], v[48:63]
ds_read_b128 a[212:215], v2 offset:5120
v_mfma_f32_32x32x16_bf16 v[48:63], a[216:219], a[184:187], v[48:63]
ds_read_b128 a[216:219], v2 offset:6144
v_mfma_f32_32x32x16_bf16 v[48:63], a[220:223], a[188:191], v[48:63]
ds_read_b128 a[220:223], v2 offset:7168
v_mfma_f32_32x32x16_bf16 a[64:79], a[224:227], v[104:107], a[64:79]
ds_read_b128 a[224:227], v2 offset:57344
v_mfma_f32_32x32x16_bf16 a[80:95], a[228:231], v[104:107], a[80:95]
v_cndmask_b32 v48, v48, v5, s[54:55]
v_cndmask_b32 v49, v49, v5, s[56:57]
v_cndmask_b32 v50, v50, v5, s[58:59]
v_exp_f32 v120, v48
ds_read_b128 a[228:231], v2 offset:58368
v_mfma_f32_32x32x16_bf16 a[96:111], a[232:235], v[104:107], a[96:111]
v_cndmask_b32 v51, v51, v5, s[60:61]
v_cndmask_b32 v52, v52, v5, s[62:63]
v_cndmask_b32 v53, v53, v5, s[64:65]
v_exp_f32 v121, v49
ds_read_b128 a[232:235], v2 offset:59392
v_mfma_f32_32x32x16_bf16 a[112:127], a[236:239], v[104:107], a[112:127]
v_cvt_pk_bf16_f32 v104, v120, v121
v_cndmask_b32 v54, v54, v5, s[66:67]
v_cndmask_b32 v55, v55, v5, s[68:69]
v_exp_f32 v122, v50
ds_read_b128 a[236:239], v2 offset:60416
v_mfma_f32_32x32x16_bf16 a[64:79], a[240:243], v[108:111], a[64:79]
v_cndmask_b32 v56, v56, v5, s[84:85]
v_cndmask_b32 v57, v57, v5, s[86:87]
v_exp_f32 v123, v51
ds_read_b128 a[240:243], v2 offset:61440
v_mfma_f32_32x32x16_bf16 a[80:95], a[244:247], v[108:111], a[80:95]
v_cvt_pk_bf16_f32 v105, v122, v123
v_cndmask_b32 v58, v58, v5, s[88:89]
v_cndmask_b32 v59, v59, v5, s[90:91]
v_exp_f32 v124, v52
v_mfma_f32_32x32x16_bf16 a[96:111], a[248:251], v[108:111], a[96:111]
v_cndmask_b32 v60, v60, v5, s[92:93]
v_cndmask_b32 v61, v61, v5, s[94:95]
v_exp_f32 v125, v53
ds_read_b128 a[244:247], v2 offset:62464
ds_read_b128 a[248:251], v2 offset:63488
v_mfma_f32_32x32x16_bf16 a[112:127], a[252:255], v[108:111], a[112:127]
v_cvt_pk_bf16_f32 v106, v124, v125
v_cndmask_b32 v62, v62, v5, s[96:97]
v_cndmask_b32 v63, v63, v5, s[98:99]
v_exp_f32 v126, v54
ds_read_b128 a[252:255], v2 offset:64512
s_waitcnt lgkmcnt(8)
s_mov_b32 s22, 0
s_add_u32 m0, s36, 24576
v_exp_f32 v127, v55
buffer_load_dwordx4 v1, s[20:23], s24 offen offset:0 lds
v_cvt_pk_bf16_f32 v107, v126, v127
v_exp_f32 v120, v56
v_exp_f32 v121, v57
v_mfma_f32_16x16x32_bf16 v[24:27], v[28:31], v[104:107], 0
buffer_load_dwordx4 v1, s[20:23], s24 offen offset:1024 lds
v_cvt_pk_bf16_f32 v108, v120, v121
v_exp_f32 v122, v58
v_exp_f32 v123, v59
buffer_load_dwordx4 v1, s[20:23], s24 offen offset:2048 lds
v_cvt_pk_bf16_f32 v109, v122, v123
v_exp_f32 v124, v60
v_exp_f32 v125, v61
buffer_load_dwordx4 v1, s[20:23], s24 offen offset:3072 lds
s_add_u32 m0, s36, 49152
v_cvt_pk_bf16_f32 v110, v124, v125
v_exp_f32 v126, v62
s_waitcnt lgkmcnt(0)
v_exp_f32 v127, v63
buffer_load_dwordx4 v3, s[20:23], s24 offen offset:0 lds
v_cvt_pk_bf16_f32 v111, v126, v127
s_nop 1
v_mfma_f32_16x16x32_bf16 v[24:27], v[28:31], v[108:111], v[24:27]
buffer_load_dwordx4 v3, s[20:23], s24 offen offset:1024 lds
buffer_load_dwordx4 v3, s[20:23], s24 offen offset:2048 lds
s_nop 5
v_cmp_ngt_f32 vcc, s71, v24
s_nop 3
s_cbranch_vccnz .Lr43
.Lr43b:
v_add_f32 v9, v9, v24
buffer_load_dwordx4 v3, s[20:23], s24 offen offset:3072 lds
s_waitcnt vmcnt(16)
s_barrier
s_add_u32 s24, s24, 0x2000
ds_read_b128 a[192:195], v2 offset:8192
ds_read_b128 a[196:199], v2 offset:9216
ds_read_b128 a[200:203], v2 offset:10240
ds_read_b128 a[204:207], v2 offset:11264
ds_read_b128 a[208:211], v2 offset:12288
ds_read_b128 a[212:215], v2 offset:13312
ds_read_b128 a[216:219], v2 offset:14336
ds_read_b128 a[220:223], v2 offset:15360
v_mfma_f32_32x32x16_bf16 a[64:79], a[224:227], v[104:107], a[64:79]
ds_read_b128 a[224:227], v2 offset:32768
v_mfma_f32_32x32x16_bf16 a[80:95], a[228:231], v[104:107], a[80:95]
ds_read_b128 a[228:231], v2 offset:33792
v_mfma_f32_32x32x16_bf16 a[96:111], a[232:235], v[104:107], a[96:111]
ds_read_b128 a[232:235], v2 offset:34816
v_mfma_f32_32x32x16_bf16 a[112:127], a[236:239], v[104:107], a[112:127]
ds_read_b128 a[236:239], v2 offset:35840
v_mfma_f32_32x32x16_bf16 a[64:79], a[240:243], v[108:111], a[64:79]
ds_read_b128 a[240:243], v2 offset:36864
v_mfma_f32_32x32x16_bf16 a[80:95], a[244:247], v[108:111], a[80:95]
v_mfma_f32_32x32x16_bf16 a[96:111], a[248:251], v[108:111], a[96:111]
ds_read_b128 a[244:247], v2 offset:37888
ds_read_b128 a[248:251], v2 offset:38912
v_mfma_f32_32x32x16_bf16 a[112:127], a[252:255], v[108:111], a[112:127]
ds_read_b128 a[252:255], v2 offset:39936
s_waitcnt lgkmcnt(8)
s_add_u32 m0, s36, 0
s_nop 0
buffer_load_dwordx4 v1, s[20:23], s24 offen offset:0 lds
buffer_load_dwordx4 v1, s[20:23], s24 offen offset:1024 lds
buffer_load_dwordx4 v1, s[20:23], s24 offen offset:2048 lds
buffer_load_dwordx4 v1, s[20:23], s24 offen offset:3072 lds
s_add_u32 m0, s36, 57344
s_waitcnt lgkmcnt(0)
buffer_load_dwordx4 v3, s[20:23], s24 offen offset:0 lds
buffer_load_dwordx4 v3, s[20:23], s24 offen offset:1024 lds
buffer_load_dwordx4 v3, s[20:23], s24 offen offset:2048 lds
buffer_load_dwordx4 v3, s[20:23], s24 offen offset:3072 lds
s_waitcnt vmcnt(16)
s_barrier
s_add_u32 s24, s24, 0x2000
ds_read_b128 a[192:195], v2 offset:16384
ds_read_b128 a[196:199], v2 offset:17408
ds_read_b128 a[200:203], v2 offset:18432
ds_read_b128 a[204:207], v2 offset:19456
ds_read_b128 a[208:211], v2 offset:20480
ds_read_b128 a[212:215], v2 offset:21504
ds_read_b128 a[216:219], v2 offset:22528
ds_read_b128 a[220:223], v2 offset:23552
ds_read_b128 a[224:227], v2 offset:40960
ds_read_b128 a[228:231], v2 offset:41984
ds_read_b128 a[232:235], v2 offset:43008
ds_read_b128 a[236:239], v2 offset:44032
ds_read_b128 a[240:243], v2 offset:45056
ds_read_b128 a[244:247], v2 offset:46080
ds_read_b128 a[248:251], v2 offset:47104
ds_read_b128 a[252:255], v2 offset:48128
s_waitcnt lgkmcnt(8)
.LdA0:
s_and_b32 s47, s51, 1
s_lshl_b32 s72, s47, 14
s_add_u32 s72, s72, 131072
v_add_u32 v22, s72, v1
s_lshl_b32 s72, s47, 16
s_add_u32 s72, s72, 8192
v_add_u32 v23, s72, v1
s_lshl_b32 s72, s51, 10
s_add_u32 s72, s72, 32768
v_lshrrev_b32 v21, 1, v1
v_add_u32 v21, s72, v21
s_lshl_b32 s47, s47, 1
s_sub_u32 s29, 64, s48
s_add_u32 s29, s29, s47
s_lshl_b32 s29, s29, 18
s_lshl_b32 s72, s49, 13
s_add_u32 s29, s29, s72
s_add_u32 s29, s29, 0x3000000
s_add_u32 s72, s29, 4096
v_mov_b32 v18, v13
v_mov_b32 v19, v9
ds_write_b64 v21, v[18:19]
ds_write_b128 v23, a[64:67] offset:0
ds_write_b128 v23, a[68:71] offset:1024
ds_write_b128 v23, a[72:75] offset:2048
ds_write_b128 v23, a[76:79] offset:3072
ds_write_b128 v23, a[80:83] offset:4096
ds_write_b128 v23, a[84:87] offset:5120
ds_write_b128 v23, a[88:91] offset:6144
ds_write_b128 v23, a[92:95] offset:7168
ds_write_b128 v23, a[96:99] offset:8192
ds_write_b128 v23, a[100:103] offset:9216
ds_write_b128 v23, a[104:107] offset:10240
ds_write_b128 v23, a[108:111] offset:11264
ds_write_b128 v23, a[112:115] offset:12288
ds_write_b128 v23, a[116:119] offset:13312
ds_write_b128 v23, a[120:123] offset:14336
ds_write_b128 v23, a[124:127] offset:15360
v_accvgpr_read_b32 v128, a0
v_accvgpr_read_b32 v129, a1
v_accvgpr_read_b32 v130, a2
v_accvgpr_read_b32 v131, a3
v_accvgpr_read_b32 v132, a4
v_accvgpr_read_b32 v133, a5
v_accvgpr_read_b32 v134, a6
v_accvgpr_read_b32 v135, a7
v_accvgpr_read_b32 v136, a8
v_accvgpr_read_b32 v137, a9
v_accvgpr_read_b32 v138, a10
v_accvgpr_read_b32 v139, a11
v_accvgpr_read_b32 v140, a12
v_accvgpr_read_b32 v141, a13
v_accvgpr_read_b32 v142, a14
v_accvgpr_read_b32 v143, a15
v_accvgpr_read_b32 v144, a16
v_accvgpr_read_b32 v145, a17
v_accvgpr_read_b32 v146, a18
v_accvgpr_read_b32 v147, a19
v_accvgpr_read_b32 v148, a20
v_accvgpr_read_b32 v149, a21
v_accvgpr_read_b32 v150, a22
v_accvgpr_read_b32 v151, a23
v_accvgpr_read_b32 v152, a24
v_accvgpr_read_b32 v153, a25
v_accvgpr_read_b32 v154, a26
v_accvgpr_read_b32 v155, a27
v_accvgpr_read_b32 v156, a28
v_accvgpr_read_b32 v157, a29
v_accvgpr_read_b32 v158, a30
v_accvgpr_read_b32 v159, a31
v_accvgpr_read_b32 v160, a32
v_accvgpr_read_b32 v161, a33
v_accvgpr_read_b32 v162, a34
v_accvgpr_read_b32 v163, a35
v_accvgpr_read_b32 v164, a36
v_accvgpr_read_b32 v165, a37
v_accvgpr_read_b32 v166, a38
v_accvgpr_read_b32 v167, a39
v_accvgpr_read_b32 v168, a40
v_accvgpr_read_b32 v169, a41
v_accvgpr_read_b32 v170, a42
v_accvgpr_read_b32 v171, a43
v_accvgpr_read_b32 v172, a44
v_accvgpr_read_b32 v173, a45
v_accvgpr_read_b32 v174, a46
v_accvgpr_read_b32 v175, a47
v_accvgpr_read_b32 v176, a48
v_accvgpr_read_b32 v177, a49
v_accvgpr_read_b32 v178, a50
v_accvgpr_read_b32 v179, a51
v_accvgpr_read_b32 v180, a52
v_accvgpr_read_b32 v181, a53
v_accvgpr_read_b32 v182, a54
v_accvgpr_read_b32 v183, a55
v_accvgpr_read_b32 v184, a56
v_accvgpr_read_b32 v185, a57
v_accvgpr_read_b32 v186, a58
v_accvgpr_read_b32 v187, a59
v_accvgpr_read_b32 v188, a60
v_accvgpr_read_b32 v189, a61
v_accvgpr_read_b32 v190, a62
v_accvgpr_read_b32 v191, a63
s_waitcnt lgkmcnt(0)
s_barrier
s_branch .LfA
.Lmain1:
s_cmp_eq_u32 s48, 4
s_cbranch_scc1 .Lyl1_2
s_branch .Ly1_2
.Ly1_0:
v_mfma_f32_32x32x16_bf16 v[32:47], a[192:195], a[128:131], v[64:79]
v_exp_f32 v127, v55
v_mfma_f32_32x32x16_bf16 v[32:47], a[196:199], a[132:135], v[32:47]
v_cvt_pk_bf16_f32 v107, v126, v127
v_exp_f32 v120, v56
v_mfma_f32_32x32x16_bf16 v[32:47], a[200:203], a[136:139], v[32:47]
v_exp_f32 v121, v57
v_mfma_f32_16x16x32_bf16 v[24:27], v[28:31], v[104:107], 0
v_mfma_f32_32x32x16_bf16 v[32:47], a[204:207], a[140:143], v[32:47]
v_cvt_pk_bf16_f32 v108, v120, v121
v_exp_f32 v122, v58
v_mfma_f32_32x32x16_bf16 v[32:47], a[208:211], a[144:147], v[32:47]
v_exp_f32 v123, v59
v_mfma_f32_32x32x16_bf16 v[32:47], a[212:215], a[148:151], v[32:47]
v_cvt_pk_bf16_f32 v109, v122, v123
v_exp_f32 v124, v60
v_mfma_f32_32x32x16_bf16 v[32:47], a[216:219], a[152:155], v[32:47]
v_exp_f32 v125, v61
v_mfma_f32_32x32x16_bf16 v[32:47], a[220:223], a[156:159], v[32:47]
v_cvt_pk_bf16_f32 v110, v124, v125
v_exp_f32 v126, v62
s_waitcnt lgkmcnt(0)
v_mfma_f32_32x32x16_bf16 v[128:143], a[224:227], v[96:99], v[128:143]
v_exp_f32 v127, v63
v_mfma_f32_32x32x16_bf16 v[144:159], a[228:231], v[96:99], v[144:159]
v_cvt_pk_bf16_f32 v111, v126, v127
v_exp_f32 v112, v32
v_mfma_f32_32x32x16_bf16 v[160:175], a[232:235], v[96:99], v[160:175]
v_exp_f32 v113, v33
v_mfma_f32_16x16x32_bf16 v[24:27], v[28:31], v[108:111], v[24:27]
v_mfma_f32_32x32x16_bf16 v[176:191], a[236:239], v[96:99], v[176:191]
v_cvt_pk_bf16_f32 v96, v112, v113
v_exp_f32 v114, v34
v_mfma_f32_32x32x16_bf16 v[128:143], a[240:243], v[100:103], v[128:143]
v_exp_f32 v115, v35
v_cmp_ngt_f32 vcc, s71, v24
v_mfma_f32_32x32x16_bf16 v[144:159], a[244:247], v[100:103], v[144:159]
v_cvt_pk_bf16_f32 v97, v114, v115
v_exp_f32 v116, v36
s_cbranch_vccnz .Lr55
.Lr55b:
v_mfma_f32_32x32x16_bf16 v[160:175], a[248:251], v[100:103], v[160:175]
v_exp_f32 v117, v37
v_add_f32 v11, v11, v24
v_mfma_f32_32x32x16_bf16 v[176:191], a[252:255], v[100:103], v[176:191]
v_cvt_pk_bf16_f32 v98, v116, v117
v_exp_f32 v118, v38
s_waitcnt vmcnt(0)
s_barrier
s_add_u32 s24, s24, 0x2000
v_mfma_f32_32x32x16_bf16 v[48:63], a[192:195], a[160:163], v[80:95]
v_exp_f32 v119, v39
ds_read_b128 a[192:195], v2 offset:8192
v_mfma_f32_32x32x16_bf16 v[48:63], a[196:199], a[164:167], v[48:63]
v_cvt_pk_bf16_f32 v99, v118, v119
v_exp_f32 v112, v40
ds_read_b128 a[196:199], v2 offset:9216
v_mfma_f32_32x32x16_bf16 v[48:63], a[200:203], a[168:171], v[48:63]
v_exp_f32 v113, v41
v_mfma_f32_16x16x32_bf16 v[24:27], v[28:31], v[96:99], 0
ds_read_b128 a[200:203], v2 offset:10240
v_mfma_f32_32x32x16_bf16 v[48:63], a[204:207], a[172:175], v[48:63]
v_cvt_pk_bf16_f32 v100, v112, v113
v_exp_f32 v114, v42
ds_read_b128 a[204:207], v2 offset:11264
v_mfma_f32_32x32x16_bf16 v[48:63], a[208:211], a[176:179], v[48:63]
v_exp_f32 v115, v43
ds_read_b128 a[208:211], v2 offset:12288
v_mfma_f32_32x32x16_bf16 v[48:63], a[212:215], a[180:183], v[48:63]
v_cvt_pk_bf16_f32 v101, v114, v115
v_exp_f32 v116, v44
ds_read_b128 a[212:215], v2 offset:13312
v_mfma_f32_32x32x16_bf16 v[48:63], a[216:219], a[184:187], v[48:63]
v_exp_f32 v117, v45
ds_read_b128 a[216:219], v2 offset:14336
v_mfma_f32_32x32x16_bf16 v[48:63], a[220:223], a[188:191], v[48:63]
v_cvt_pk_bf16_f32 v102, v116, v117
v_exp_f32 v118, v46
ds_read_b128 a[220:223], v2 offset:15360
v_mfma_f32_32x32x16_bf16 v[192:207], a[224:227], v[104:107], v[192:207]
v_exp_f32 v119, v47
ds_read_b128 a[224:227], v1 offset:32768
v_mfma_f32_32x32x16_bf16 v[208:223], a[228:231], v[104:107], v[208:223]
v_cvt_pk_bf16_f32 v103, v118, v119
v_exp_f32 v120, v48
ds_read_b128 a[228:231], v1 offset:33792
v_mfma_f32_32x32x16_bf16 v[224:239], a[232:235], v[104:107], v[224:239]
v_mfma_f32_16x16x32_bf16 v[24:27], v[28:31], v[100:103], v[24:27]
v_exp_f32 v121, v49
ds_read_b128 a[232:235], v1 offset:34816
v_mfma_f32_32x32x16_bf16 v[240:255], a[236:239], v[104:107], v[240:255]
v_cvt_pk_bf16_f32 v104, v120, v121
v_exp_f32 v122, v50
ds_read_b128 a[236:239], v1 offset:35840
v_mfma_f32_32x32x16_bf16 v[192:207], a[240:243], v[108:111], v[192:207]
v_exp_f32 v123, v51
v_cmp_ngt_f32 vcc, s71, v24
ds_read_b128 a[240:243], v1 offset:36864
v_mfma_f32_32x32x16_bf16 v[208:223], a[244:247], v[108:111], v[208:223]
v_cvt_pk_bf16_f32 v105, v122, v123
v_exp_f32 v124, v52
s_cbranch_vccnz .Lr54
.Lr54b:
v_mfma_f32_32x32x16_bf16 v[224:239], a[248:251], v[108:111], v[224:239]
v_exp_f32 v125, v53
v_add_f32 v10, v10, v24
ds_read_b128 a[244:247], v1 offset:37888
ds_read_b128 a[248:251], v1 offset:38912
v_mfma_f32_32x32x16_bf16 v[240:255], a[252:255], v[108:111], v[240:255]
v_cvt_pk_bf16_f32 v106, v124, v125
v_exp_f32 v126, v54
ds_read_b128 a[252:255], v1 offset:39936
s_waitcnt lgkmcnt(8)
v_mfma_f32_32x32x16_bf16 v[32:47], a[192:195], a[128:131], v[64:79]
v_exp_f32 v127, v55
v_mfma_f32_32x32x16_bf16 v[32:47], a[196:199], a[132:135], v[32:47]
v_cvt_pk_bf16_f32 v107, v126, v127
v_exp_f32 v120, v56
v_mfma_f32_32x32x16_bf16 v[32:47], a[200:203], a[136:139], v[32:47]
v_exp_f32 v121, v57
v_mfma_f32_16x16x32_bf16 v[24:27], v[28:31], v[104:107], 0
v_mfma_f32_32x32x16_bf16 v[32:47], a[204:207], a[140:143], v[32:47]
v_cvt_pk_bf16_f32 v108, v120, v121
v_exp_f32 v122, v58
v_mfma_f32_32x32x16_bf16 v[32:47], a[208:211], a[144:147], v[32:47]
v_exp_f32 v123, v59
v_mfma_f32_32x32x16_bf16 v[32:47], a[212:215], a[148:151], v[32:47]
v_cvt_pk_bf16_f32 v109, v122, v123
v_exp_f32 v124, v60
v_mfma_f32_32x32x16_bf16 v[32:47], a[216:219], a[152:155], v[32:47]
v_exp_f32 v125, v61
v_mfma_f32_32x32x16_bf16 v[32:47], a[220:223], a[156:159], v[32:47]
v_cvt_pk_bf16_f32 v110, v124, v125
v_exp_f32 v126, v62
s_waitcnt lgkmcnt(0)
v_mfma_f32_32x32x16_bf16 v[128:143], a[224:227], v[96:99], v[128:143]
v_exp_f32 v127, v63
v_mfma_f32_32x32x16_bf16 v[144:159], a[228:231], v[96:99], v[144:159]
v_cvt_pk_bf16_f32 v111, v126, v127
v_exp_f32 v112, v32
v_mfma_f32_32x32x16_bf16 v[160:175], a[232:235], v[96:99], v[160:175]
v_exp_f32 v113, v33
v_mfma_f32_16x16x32_bf16 v[24:27], v[28:31], v[108:111], v[24:27]
v_mfma_f32_32x32x16_bf16 v[176:191], a[236:239], v[96:99], v[176:191]
v_cvt_pk_bf16_f32 v96, v112, v113
v_exp_f32 v114, v34
v_mfma_f32_32x32x16_bf16 v[128:143], a[240:243], v[100:103], v[128:143]
v_exp_f32 v115, v35
v_cmp_ngt_f32 vcc, s71, v24
v_mfma_f32_32x32x16_bf16 v[144:159], a[244:247], v[100:103], v[144:159]
v_cvt_pk_bf16_f32 v97, v114, v115
v_exp_f32 v116, v36
s_cbranch_vccnz .Lr57
.Lr57b:
v_mfma_f32_32x32x16_bf16 v[160:175], a[248:251], v[100:103], v[160:175]
v_exp_f32 v117, v37
v_add_f32 v11, v11, v24
v_mfma_f32_32x32x16_bf16 v[176:191], a[252:255], v[100:103], v[176:191]
v_cvt_pk_bf16_f32 v98, v116, v117
v_exp_f32 v118, v38
s_waitcnt vmcnt(0)
s_barrier
s_add_u32 s24, s24, 0x2000
v_mfma_f32_32x32x16_bf16 v[48:63], a[192:195], a[160:163], v[80:95]
v_exp_f32 v119, v39
ds_read_b128 a[192:195], v2 offset:16384
v_mfma_f32_32x32x16_bf16 v[48:63], a[196:199], a[164:167], v[48:63]
v_cvt_pk_bf16_f32 v99, v118, v119
v_exp_f32 v112, v40
ds_read_b128 a[196:199], v2 offset:17408
v_mfma_f32_32x32x16_bf16 v[48:63], a[200:203], a[168:171], v[48:63]
v_exp_f32 v113, v41
v_mfma_f32_16x16x32_bf16 v[24:27], v[28:31], v[96:99], 0
ds_read_b128 a[200:203], v2 offset:18432
v_mfma_f32_32x32x16_bf16 v[48:63], a[204:207], a[172:175], v[48:63]
v_cvt_pk_bf16_f32 v100, v112, v113
v_exp_f32 v114, v42
ds_read_b128 a[204:207], v2 offset:19456
v_mfma_f32_32x32x16_bf16 v[48:63], a[208:211], a[176:179], v[48:63]
v_exp_f32 v115, v43
ds_read_b128 a[208:211], v2 offset:20480
v_mfma_f32_32x32x16_bf16 v[48:63], a[212:215], a[180:183], v[48:63]
v_cvt_pk_bf16_f32 v101, v114, v115
v_exp_f32 v116, v44
ds_read_b128 a[212:215], v2 offset:21504
v_mfma_f32_32x32x16_bf16 v[48:63], a[216:219], a[184:187], v[48:63]
v_exp_f32 v117, v45
ds_read_b128 a[216:219], v2 offset:22528
v_mfma_f32_32x32x16_bf16 v[48:63], a[220:223], a[188:191], v[48:63]
v_cvt_pk_bf16_f32 v102, v116, v117
v_exp_f32 v118, v46
ds_read_b128 a[220:223], v2 offset:23552
v_mfma_f32_32x32x16_bf16 v[192:207], a[224:227], v[104:107], v[192:207]
v_exp_f32 v119, v47
ds_read_b128 a[224:227], v1 offset:40960
v_mfma_f32_32x32x16_bf16 v[208:223], a[228:231], v[104:107], v[208:223]
v_cvt_pk_bf16_f32 v103, v118, v119
v_exp_f32 v120, v48
ds_read_b128 a[228:231], v1 offset:41984
v_mfma_f32_32x32x16_bf16 v[224:239], a[232:235], v[104:107], v[224:239]
v_mfma_f32_16x16x32_bf16 v[24:27], v[28:31], v[100:103], v[24:27]
v_exp_f32 v121, v49
ds_read_b128 a[232:235], v1 offset:43008
v_mfma_f32_32x32x16_bf16 v[240:255], a[236:239], v[104:107], v[240:255]
v_cvt_pk_bf16_f32 v104, v120, v121
v_exp_f32 v122, v50
ds_read_b128 a[236:239], v1 offset:44032
v_mfma_f32_32x32x16_bf16 v[192:207], a[240:243], v[108:111], v[192:207]
v_exp_f32 v123, v51
v_cmp_ngt_f32 vcc, s71, v24
ds_read_b128 a[240:243], v1 offset:45056
v_mfma_f32_32x32x16_bf16 v[208:223], a[244:247], v[108:111], v[208:223]
v_cvt_pk_bf16_f32 v105, v122, v123
v_exp_f32 v124, v52
s_cbranch_vccnz .Lr56
.Lr56b:
v_mfma_f32_32x32x16_bf16 v[224:239], a[248:251], v[108:111], v[224:239]
v_exp_f32 v125, v53
v_add_f32 v10, v10, v24
ds_read_b128 a[244:247], v1 offset:46080
ds_read_b128 a[248:251], v1 offset:47104
v_mfma_f32_32x32x16_bf16 v[240:255], a[252:255], v[108:111], v[240:255]
v_cvt_pk_bf16_f32 v106, v124, v125
v_exp_f32 v126, v54
ds_read_b128 a[252:255], v1 offset:48128
s_waitcnt lgkmcnt(8)
.Ly1_2:
v_mfma_f32_32x32x16_bf16 v[32:47], a[192:195], a[128:131], v[64:79]
v_exp_f32 v127, v55
v_mfma_f32_32x32x16_bf16 v[32:47], a[196:199], a[132:135], v[32:47]
v_cvt_pk_bf16_f32 v107, v126, v127
v_exp_f32 v120, v56
v_mfma_f32_32x32x16_bf16 v[32:47], a[200:203], a[136:139], v[32:47]
v_exp_f32 v121, v57
v_mfma_f32_16x16x32_bf16 v[24:27], v[28:31], v[104:107], 0
v_mfma_f32_32x32x16_bf16 v[32:47], a[204:207], a[140:143], v[32:47]
v_cvt_pk_bf16_f32 v108, v120, v121
v_exp_f32 v122, v58
v_mfma_f32_32x32x16_bf16 v[32:47], a[208:211], a[144:147], v[32:47]
v_exp_f32 v123, v59
v_mfma_f32_32x32x16_bf16 v[32:47], a[212:215], a[148:151], v[32:47]
v_cvt_pk_bf16_f32 v109, v122, v123
v_exp_f32 v124, v60
v_mfma_f32_32x32x16_bf16 v[32:47], a[216:219], a[152:155], v[32:47]
v_exp_f32 v125, v61
v_mfma_f32_32x32x16_bf16 v[32:47], a[220:223], a[156:159], v[32:47]
v_cvt_pk_bf16_f32 v110, v124, v125
v_exp_f32 v126, v62
s_waitcnt lgkmcnt(0)
v_mfma_f32_32x32x16_bf16 v[128:143], a[224:227], v[96:99], v[128:143]
v_exp_f32 v127, v63
v_mfma_f32_32x32x16_bf16 v[144:159], a[228:231], v[96:99], v[144:159]
v_cvt_pk_bf16_f32 v111, v126, v127
v_exp_f32 v112, v32
v_mfma_f32_32x32x16_bf16 v[160:175], a[232:235], v[96:99], v[160:175]
v_exp_f32 v113, v33
v_mfma_f32_16x16x32_bf16 v[24:27], v[28:31], v[108:111], v[24:27]
v_mfma_f32_32x32x16_bf16 v[176:191], a[236:239], v[96:99], v[176:191]
v_cvt_pk_bf16_f32 v96, v112, v113
v_exp_f32 v114, v34
v_mfma_f32_32x32x16_bf16 v[128:143], a[240:243], v[100:103], v[128:143]
v_exp_f32 v115, v35
v_cmp_ngt_f32 vcc, s71, v24
v_mfma_f32_32x32x16_bf16 v[144:159], a[244:247], v[100:103], v[144:159]
v_cvt_pk_bf16_f32 v97, v114, v115
v_exp_f32 v116, v36
s_cbranch_vccnz .Lr59
.Lr59b:
v_mfma_f32_32x32x16_bf16 v[160:175], a[248:251], v[100:103], v[160:175]
v_exp_f32 v117, v37
v_add_f32 v11, v11, v24
v_mfma_f32_32x32x16_bf16 v[176:191], a[252:255], v[100:103], v[176:191]
v_cvt_pk_bf16_f32 v98, v116, v117
v_exp_f32 v118, v38
s_waitcnt vmcnt(0)
s_barrier
s_add_u32 s24, s24, 0x2000
v_mfma_f32_32x32x16_bf16 v[48:63], a[192:195], a[160:163], v[80:95]
v_exp_f32 v119, v39
ds_read_b128 a[192:195], v2 offset:24576
v_mfma_f32_32x32x16_bf16 v[48:63], a[196:199], a[164:167], v[48:63]
v_cvt_pk_bf16_f32 v99, v118, v119
v_exp_f32 v112, v40
ds_read_b128 a[196:199], v2 offset:25600
v_mfma_f32_32x32x16_bf16 v[48:63], a[200:203], a[168:171], v[48:63]
v_exp_f32 v113, v41
v_mfma_f32_16x16x32_bf16 v[24:27], v[28:31], v[96:99], 0
ds_read_b128 a[200:203], v2 offset:26624
v_mfma_f32_32x32x16_bf16 v[48:63], a[204:207], a[172:175], v[48:63]
v_cvt_pk_bf16_f32 v100, v112, v113
v_exp_f32 v114, v42
ds_read_b128 a[204:207], v2 offset:27648
v_mfma_f32_32x32x16_bf16 v[48:63], a[208:211], a[176:179], v[48:63]
v_exp_f32 v115, v43
ds_read_b128 a[208:211], v2 offset:28672
v_mfma_f32_32x32x16_bf16 v[48:63], a[212:215], a[180:183], v[48:63]
v_cvt_pk_bf16_f32 v101, v114, v115
v_exp_f32 v116, v44
ds_read_b128 a[212:215], v2 offset:29696
v_mfma_f32_32x32x16_bf16 v[48:63], a[216:219], a[184:187], v[48:63]
v_exp_f32 v117, v45
ds_read_b128 a[216:219], v2 offset:30720
v_mfma_f32_32x32x16_bf16 v[48:63], a[220:223], a[188:191], v[48:63]
v_cvt_pk_bf16_f32 v102, v116, v117
v_exp_f32 v118, v46
ds_read_b128 a[220:223], v2 offset:31744
v_mfma_f32_32x32x16_bf16 v[192:207], a[224:227], v[104:107], v[192:207]
v_exp_f32 v119, v47
ds_read_b128 a[224:227], v1 offset:49152
v_mfma_f32_32x32x16_bf16 v[208:223], a[228:231], v[104:107], v[208:223]
v_cvt_pk_bf16_f32 v103, v118, v119
v_exp_f32 v120, v48
ds_read_b128 a[228:231], v1 offset:50176
v_mfma_f32_32x32x16_bf16 v[224:239], a[232:235], v[104:107], v[224:239]
v_mfma_f32_16x16x32_bf16 v[24:27], v[28:31], v[100:103], v[24:27]
v_exp_f32 v121, v49
ds_read_b128 a[232:235], v1 offset:51200
v_mfma_f32_32x32x16_bf16 v[240:255], a[236:239], v[104:107], v[240:255]
v_cvt_pk_bf16_f32 v104, v120, v121
v_exp_f32 v122, v50
ds_read_b128 a[236:239], v1 offset:52224
v_mfma_f32_32x32x16_bf16 v[192:207], a[240:243], v[108:111], v[192:207]
v_exp_f32 v123, v51
v_cmp_ngt_f32 vcc, s71, v24
ds_read_b128 a[240:243], v1 offset:53248
v_mfma_f32_32x32x16_bf16 v[208:223], a[244:247], v[108:111], v[208:223]
v_cvt_pk_bf16_f32 v105, v122, v123
v_exp_f32 v124, v52
s_cbranch_vccnz .Lr58
.Lr58b:
v_mfma_f32_32x32x16_bf16 v[224:239], a[248:251], v[108:111], v[224:239]
v_exp_f32 v125, v53
v_add_f32 v10, v10, v24
ds_read_b128 a[244:247], v1 offset:54272
ds_read_b128 a[248:251], v1 offset:55296
v_mfma_f32_32x32x16_bf16 v[240:255], a[252:255], v[108:111], v[240:255]
v_cvt_pk_bf16_f32 v106, v124, v125
v_exp_f32 v126, v54
ds_read_b128 a[252:255], v1 offset:56320
s_waitcnt lgkmcnt(8)
v_mfma_f32_32x32x16_bf16 v[32:47], a[192:195], a[128:131], v[64:79]
v_exp_f32 v127, v55
v_mfma_f32_32x32x16_bf16 v[32:47], a[196:199], a[132:135], v[32:47]
v_cvt_pk_bf16_f32 v107, v126, v127
v_exp_f32 v120, v56
v_mfma_f32_32x32x16_bf16 v[32:47], a[200:203], a[136:139], v[32:47]
v_exp_f32 v121, v57
v_mfma_f32_16x16x32_bf16 v[24:27], v[28:31], v[104:107], 0
v_mfma_f32_32x32x16_bf16 v[32:47], a[204:207], a[140:143], v[32:47]
v_cvt_pk_bf16_f32 v108, v120, v121
v_exp_f32 v122, v58
v_mfma_f32_32x32x16_bf16 v[32:47], a[208:211], a[144:147], v[32:47]
v_exp_f32 v123, v59
v_mfma_f32_32x32x16_bf16 v[32:47], a[212:215], a[148:151], v[32:47]
v_cvt_pk_bf16_f32 v109, v122, v123
v_exp_f32 v124, v60
v_mfma_f32_32x32x16_bf16 v[32:47], a[216:219], a[152:155], v[32:47]
v_exp_f32 v125, v61
v_mfma_f32_32x32x16_bf16 v[32:47], a[220:223], a[156:159], v[32:47]
v_cvt_pk_bf16_f32 v110, v124, v125
v_exp_f32 v126, v62
s_waitcnt lgkmcnt(0)
v_mfma_f32_32x32x16_bf16 v[128:143], a[224:227], v[96:99], v[128:143]
v_exp_f32 v127, v63
v_mfma_f32_32x32x16_bf16 v[144:159], a[228:231], v[96:99], v[144:159]
v_cvt_pk_bf16_f32 v111, v126, v127
v_exp_f32 v112, v32
v_mfma_f32_32x32x16_bf16 v[160:175], a[232:235], v[96:99], v[160:175]
v_exp_f32 v113, v33
v_mfma_f32_16x16x32_bf16 v[24:27], v[28:31], v[108:111], v[24:27]
v_mfma_f32_32x32x16_bf16 v[176:191], a[236:239], v[96:99], v[176:191]
v_cvt_pk_bf16_f32 v96, v112, v113
v_exp_f32 v114, v34
v_mfma_f32_32x32x16_bf16 v[128:143], a[240:243], v[100:103], v[128:143]
v_exp_f32 v115, v35
v_cmp_ngt_f32 vcc, s71, v24
v_mfma_f32_32x32x16_bf16 v[144:159], a[244:247], v[100:103], v[144:159]
v_cvt_pk_bf16_f32 v97, v114, v115
v_exp_f32 v116, v36
s_cbranch_vccnz .Lr61
.Lr61b:
v_mfma_f32_32x32x16_bf16 v[160:175], a[248:251], v[100:103], v[160:175]
v_exp_f32 v117, v37
v_add_f32 v11, v11, v24
v_mfma_f32_32x32x16_bf16 v[176:191], a[252:255], v[100:103], v[176:191]
v_cvt_pk_bf16_f32 v98, v116, v117
v_exp_f32 v118, v38
s_waitcnt vmcnt(0)
s_barrier
s_add_u32 s24, s24, 0x2000
v_mfma_f32_32x32x16_bf16 v[48:63], a[192:195], a[160:163], v[80:95]
v_exp_f32 v119, v39
ds_read_b128 a[192:195], v2 offset:0
v_mfma_f32_32x32x16_bf16 v[48:63], a[196:199], a[164:167], v[48:63]
v_cvt_pk_bf16_f32 v99, v118, v119
v_exp_f32 v112, v40
ds_read_b128 a[196:199], v2 offset:1024
v_mfma_f32_32x32x16_bf16 v[48:63], a[200:203], a[168:171], v[48:63]
v_exp_f32 v113, v41
v_mfma_f32_16x16x32_bf16 v[24:27], v[28:31], v[96:99], 0
ds_read_b128 a[200:203], v2 offset:2048
v_mfma_f32_32x32x16_bf16 v[48:63], a[204:207], a[172:175], v[48:63]
v_cvt_pk_bf16_f32 v100, v112, v113
v_exp_f32 v114, v42
ds_read_b128 a[204:207], v2 offset:3072
v_mfma_f32_32x32x16_bf16 v[48:63], a[208:211], a[176:179], v[48:63]
v_exp_f32 v115, v43
ds_read_b128 a[208:211], v2 offset:4096
v_mfma_f32_32x32x16_bf16 v[48:63], a[212:215], a[180:183], v[48:63]
v_cvt_pk_bf16_f32 v101, v114, v115
v_exp_f32 v116, v44
ds_read_b128 a[212:215], v2 offset:5120
v_mfma_f32_32x32x16_bf16 v[48:63], a[216:219], a[184:187], v[48:63]
v_exp_f32 v117, v45
ds_read_b128 a[216:219], v2 offset:6144
v_mfma_f32_32x32x16_bf16 v[48:63], a[220:223], a[188:191], v[48:63]
v_cvt_pk_bf16_f32 v102, v116, v117
v_exp_f32 v118, v46
ds_read_b128 a[220:223], v2 offset:7168
v_mfma_f32_32x32x16_bf16 v[192:207], a[224:227], v[104:107], v[192:207]
v_exp_f32 v119, v47
ds_read_b128 a[224:227], v1 offset:57344
v_mfma_f32_32x32x16_bf16 v[208:223], a[228:231], v[104:107], v[208:223]
v_cvt_pk_bf16_f32 v103, v118, v119
v_exp_f32 v120, v48
ds_read_b128 a[228:231], v1 offset:58368
v_mfma_f32_32x32x16_bf16 v[224:239], a[232:235], v[104:107], v[224:239]
v_mfma_f32_16x16x32_bf16 v[24:27], v[28:31], v[100:103], v[24:27]
v_exp_f32 v121, v49
ds_read_b128 a[232:235], v1 offset:59392
v_mfma_f32_32x32x16_bf16 v[240:255], a[236:239], v[104:107], v[240:255]
v_cvt_pk_bf16_f32 v104, v120, v121
v_exp_f32 v122, v50
ds_read_b128 a[236:239], v1 offset:60416
v_mfma_f32_32x32x16_bf16 v[192:207], a[240:243], v[108:111], v[192:207]
v_exp_f32 v123, v51
v_cmp_ngt_f32 vcc, s71, v24
ds_read_b128 a[240:243], v1 offset:61440
v_mfma_f32_32x32x16_bf16 v[208:223], a[244:247], v[108:111], v[208:223]
v_cvt_pk_bf16_f32 v105, v122, v123
v_exp_f32 v124, v52
s_cbranch_vccnz .Lr60
.Lr60b:
v_mfma_f32_32x32x16_bf16 v[224:239], a[248:251], v[108:111], v[224:239]
v_exp_f32 v125, v53
v_add_f32 v10, v10, v24
ds_read_b128 a[244:247], v1 offset:62464
ds_read_b128 a[248:251], v1 offset:63488
v_mfma_f32_32x32x16_bf16 v[240:255], a[252:255], v[108:111], v[240:255]
v_cvt_pk_bf16_f32 v106, v124, v125
v_exp_f32 v126, v54
ds_read_b128 a[252:255], v1 offset:64512
s_waitcnt lgkmcnt(8)
s_cmp_lg_u32 s24, s73
s_cbranch_scc1 .Ly1_0
v_mfma_f32_32x32x16_bf16 v[32:47], a[192:195], a[128:131], v[64:79]
v_exp_f32 v127, v55
v_mfma_f32_32x32x16_bf16 v[32:47], a[196:199], a[132:135], v[32:47]
v_cvt_pk_bf16_f32 v107, v126, v127
v_exp_f32 v120, v56
v_mfma_f32_32x32x16_bf16 v[32:47], a[200:203], a[136:139], v[32:47]
v_exp_f32 v121, v57
v_mfma_f32_16x16x32_bf16 v[24:27], v[28:31], v[104:107], 0
v_mfma_f32_32x32x16_bf16 v[32:47], a[204:207], a[140:143], v[32:47]
v_cvt_pk_bf16_f32 v108, v120, v121
v_exp_f32 v122, v58
v_mfma_f32_32x32x16_bf16 v[32:47], a[208:211], a[144:147], v[32:47]
v_exp_f32 v123, v59
v_mfma_f32_32x32x16_bf16 v[32:47], a[212:215], a[148:151], v[32:47]
v_cvt_pk_bf16_f32 v109, v122, v123
v_exp_f32 v124, v60
v_mfma_f32_32x32x16_bf16 v[32:47], a[216:219], a[152:155], v[32:47]
v_exp_f32 v125, v61
v_mfma_f32_32x32x16_bf16 v[32:47], a[220:223], a[156:159], v[32:47]
v_cvt_pk_bf16_f32 v110, v124, v125
v_exp_f32 v126, v62
s_waitcnt lgkmcnt(0)
v_mfma_f32_32x32x16_bf16 v[128:143], a[224:227], v[96:99], v[128:143]
v_exp_f32 v127, v63
v_mfma_f32_32x32x16_bf16 v[144:159], a[228:231], v[96:99], v[144:159]
v_cvt_pk_bf16_f32 v111, v126, v127
v_exp_f32 v112, v32
v_mfma_f32_32x32x16_bf16 v[160:175], a[232:235], v[96:99], v[160:175]
v_exp_f32 v113, v33
v_mfma_f32_16x16x32_bf16 v[24:27], v[28:31], v[108:111], v[24:27]
v_mfma_f32_32x32x16_bf16 v[176:191], a[236:239], v[96:99], v[176:191]
v_cvt_pk_bf16_f32 v96, v112, v113
v_exp_f32 v114, v34
v_mfma_f32_32x32x16_bf16 v[128:143], a[240:243], v[100:103], v[128:143]
v_exp_f32 v115, v35
v_cmp_ngt_f32 vcc, s71, v24
v_mfma_f32_32x32x16_bf16 v[144:159], a[244:247], v[100:103], v[144:159]
v_cvt_pk_bf16_f32 v97, v114, v115
v_exp_f32 v116, v36
s_cbranch_vccnz .Lr63
.Lr63b:
v_mfma_f32_32x32x16_bf16 v[160:175], a[248:251], v[100:103], v[160:175]
v_exp_f32 v117, v37
v_add_f32 v11, v11, v24
v_mfma_f32_32x32x16_bf16 v[176:191], a[252:255], v[100:103], v[176:191]
v_cvt_pk_bf16_f32 v98, v116, v117
v_exp_f32 v118, v38
s_waitcnt vmcnt(0)
s_barrier
s_add_u32 s24, s24, 0x2000
v_mfma_f32_32x32x16_bf16 v[48:63], a[192:195], a[160:163], v[80:95]
v_exp_f32 v119, v39
ds_read_b128 a[192:195], v2 offset:8192
v_mfma_f32_32x32x16_bf16 v[48:63], a[196:199], a[164:167], v[48:63]
v_cvt_pk_bf16_f32 v99, v118, v119
v_exp_f32 v112, v40
ds_read_b128 a[196:199], v2 offset:9216
v_mfma_f32_32x32x16_bf16 v[48:63], a[200:203], a[168:171], v[48:63]
v_exp_f32 v113, v41
v_mfma_f32_16x16x32_bf16 v[24:27], v[28:31], v[96:99], 0
ds_read_b128 a[200:203], v2 offset:10240
v_mfma_f32_32x32x16_bf16 v[48:63], a[204:207], a[172:175], v[48:63]
v_cvt_pk_bf16_f32 v100, v112, v113
v_exp_f32 v114, v42
ds_read_b128 a[204:207], v2 offset:11264
v_mfma_f32_32x32x16_bf16 v[48:63], a[208:211], a[176:179], v[48:63]
v_exp_f32 v115, v43
ds_read_b128 a[208:211], v2 offset:12288
v_mfma_f32_32x32x16_bf16 v[48:63], a[212:215], a[180:183], v[48:63]
v_cvt_pk_bf16_f32 v101, v114, v115
v_exp_f32 v116, v44
ds_read_b128 a[212:215], v2 offset:13312
v_mfma_f32_32x32x16_bf16 v[48:63], a[216:219], a[184:187], v[48:63]
v_exp_f32 v117, v45
ds_read_b128 a[216:219], v2 offset:14336
v_mfma_f32_32x32x16_bf16 v[48:63], a[220:223], a[188:191], v[48:63]
v_cvt_pk_bf16_f32 v102, v116, v117
v_exp_f32 v118, v46
ds_read_b128 a[220:223], v2 offset:15360
v_mfma_f32_32x32x16_bf16 v[192:207], a[224:227], v[104:107], v[192:207]
v_exp_f32 v119, v47
ds_read_b128 a[224:227], v1 offset:32768
v_mfma_f32_32x32x16_bf16 v[208:223], a[228:231], v[104:107], v[208:223]
v_cvt_pk_bf16_f32 v103, v118, v119
v_exp_f32 v120, v48
ds_read_b128 a[228:231], v1 offset:33792
v_mfma_f32_32x32x16_bf16 v[224:239], a[232:235], v[104:107], v[224:239]
v_mfma_f32_16x16x32_bf16 v[24:27], v[28:31], v[100:103], v[24:27]
v_exp_f32 v121, v49
ds_read_b128 a[232:235], v1 offset:34816
v_mfma_f32_32x32x16_bf16 v[240:255], a[236:239], v[104:107], v[240:255]
v_cvt_pk_bf16_f32 v104, v120, v121
v_exp_f32 v122, v50
ds_read_b128 a[236:239], v1 offset:35840
v_mfma_f32_32x32x16_bf16 v[192:207], a[240:243], v[108:111], v[192:207]
v_exp_f32 v123, v51
v_cmp_ngt_f32 vcc, s71, v24
ds_read_b128 a[240:243], v1 offset:36864
v_mfma_f32_32x32x16_bf16 v[208:223], a[244:247], v[108:111], v[208:223]
v_cvt_pk_bf16_f32 v105, v122, v123
v_exp_f32 v124, v52
s_cbranch_vccnz .Lr62
.Lr62b:
v_mfma_f32_32x32x16_bf16 v[224:239], a[248:251], v[108:111], v[224:239]
v_exp_f32 v125, v53
v_add_f32 v10, v10, v24
ds_read_b128 a[244:247], v1 offset:37888
ds_read_b128 a[248:251], v1 offset:38912
v_mfma_f32_32x32x16_bf16 v[240:255], a[252:255], v[108:111], v[240:255]
v_cvt_pk_bf16_f32 v106, v124, v125
v_exp_f32 v126, v54
ds_read_b128 a[252:255], v1 offset:39936
s_waitcnt lgkmcnt(8)
s_mov_b32 s22, s52
v_mfma_f32_32x32x16_bf16 v[32:47], a[192:195], a[128:131], v[64:79]
s_add_u32 m0, s36, 0
v_exp_f32 v127, v55
v_mfma_f32_32x32x16_bf16 v[32:47], a[196:199], a[132:135], v[32:47]
v_cvt_pk_bf16_f32 v107, v126, v127
v_exp_f32 v120, v56
buffer_load_dwordx4 v1, s[20:23], s24 offen offset:0 lds
v_mfma_f32_32x32x16_bf16 v[32:47], a[200:203], a[136:139], v[32:47]
v_exp_f32 v121, v57
v_mfma_f32_16x16x32_bf16 v[24:27], v[28:31], v[104:107], 0
v_mfma_f32_32x32x16_bf16 v[32:47], a[204:207], a[140:143], v[32:47]
v_cvt_pk_bf16_f32 v108, v120, v121
v_exp_f32 v122, v58
buffer_load_dwordx4 v1, s[20:23], s24 offen offset:1024 lds
v_mfma_f32_32x32x16_bf16 v[32:47], a[208:211], a[144:147], v[32:47]
v_exp_f32 v123, v59
buffer_load_dwordx4 v1, s[20:23], s24 offen offset:2048 lds
v_mfma_f32_32x32x16_bf16 v[32:47], a[212:215], a[148:151], v[32:47]
v_cvt_pk_bf16_f32 v109, v122, v123
v_exp_f32 v124, v60
buffer_load_dwordx4 v1, s[20:23], s24 offen offset:3072 lds
s_add_u32 m0, s36, 57344
v_mfma_f32_32x32x16_bf16 v[32:47], a[216:219], a[152:155], v[32:47]
v_exp_f32 v125, v61
v_mfma_f32_32x32x16_bf16 v[32:47], a[220:223], a[156:159], v[32:47]
v_cvt_pk_bf16_f32 v110, v124, v125
v_exp_f32 v126, v62
s_waitcnt lgkmcnt(0)
v_mfma_f32_32x32x16_bf16 v[128:143], a[224:227], v[96:99], v[128:143]
v_exp_f32 v127, v63
buffer_load_dwordx4 v3, s[20:23], s24 offen offset:0 lds
v_mfma_f32_32x32x16_bf16 v[144:159], a[228:231], v[96:99], v[144:159]
v_cvt_pk_bf16_f32 v111, v126, v127
v_exp_f32 v112, v32
buffer_load_dwordx4 v3, s[20:23], s24 offen offset:1024 lds
v_mfma_f32_32x32x16_bf16 v[160:175], a[232:235], v[96:99], v[160:175]
v_exp_f32 v113, v33
v_mfma_f32_16x16x32_bf16 v[24:27], v[28:31], v[108:111], v[24:27]
v_mfma_f32_32x32x16_bf16 v[176:191], a[236:239], v[96:99], v[176:191]
v_cvt_pk_bf16_f32 v96, v112, v113
v_exp_f32 v114, v34
buffer_load_dwordx4 v3, s[20:23], s24 offen offset:2048 lds
v_mfma_f32_32x32x16_bf16 v[128:143], a[240:243], v[100:103], v[128:143]
v_exp_f32 v115, v35
v_cmp_ngt_f32 vcc, s71, v24
v_mfma_f32_32x32x16_bf16 v[144:159], a[244:247], v[100:103], v[144:159]
v_cvt_pk_bf16_f32 v97, v114, v115
v_exp_f32 v116, v36
s_cbranch_vccnz .Lr65
.Lr65b:
v_mfma_f32_32x32x16_bf16 v[160:175], a[248:251], v[100:103], v[160:175]
v_exp_f32 v117, v37
v_add_f32 v11, v11, v24
buffer_load_dwordx4 v3, s[20:23], s24 offen offset:3072 lds
v_mfma_f32_32x32x16_bf16 v[176:191], a[252:255], v[100:103], v[176:191]
v_cvt_pk_bf16_f32 v98, v116, v117
v_exp_f32 v118, v38
s_waitcnt vmcnt(16)
s_barrier
s_add_u32 s24, s24, 0x2000
v_mfma_f32_32x32x16_bf16 v[48:63], a[192:195], a[160:163], v[80:95]
v_exp_f32 v119, v39
ds_read_b128 a[192:195], v2 offset:16384
v_mfma_f32_32x32x16_bf16 v[48:63], a[196:199], a[164:167], v[48:63]
v_cvt_pk_bf16_f32 v99, v118, v119
v_exp_f32 v112, v40
ds_read_b128 a[196:199], v2 offset:17408
v_mfma_f32_32x32x16_bf16 v[48:63], a[200:203], a[168:171], v[48:63]
v_exp_f32 v113, v41
v_mfma_f32_16x16x32_bf16 v[24:27], v[28:31], v[96:99], 0
ds_read_b128 a[200:203], v2 offset:18432
v_mfma_f32_32x32x16_bf16 v[48:63], a[204:207], a[172:175], v[48:63]
v_cvt_pk_bf16_f32 v100, v112, v113
v_exp_f32 v114, v42
ds_read_b128 a[204:207], v2 offset:19456
v_mfma_f32_32x32x16_bf16 v[48:63], a[208:211], a[176:179], v[48:63]
v_exp_f32 v115, v43
ds_read_b128 a[208:211], v2 offset:20480
v_mfma_f32_32x32x16_bf16 v[48:63], a[212:215], a[180:183], v[48:63]
v_cvt_pk_bf16_f32 v101, v114, v115
v_exp_f32 v116, v44
ds_read_b128 a[212:215], v2 offset:21504
v_mfma_f32_32x32x16_bf16 v[48:63], a[216:219], a[184:187], v[48:63]
v_exp_f32 v117, v45
ds_read_b128 a[216:219], v2 offset:22528
v_mfma_f32_32x32x16_bf16 v[48:63], a[220:223], a[188:191], v[48:63]
v_cvt_pk_bf16_f32 v102, v116, v117
v_exp_f32 v118, v46
ds_read_b128 a[220:223], v2 offset:23552
v_mfma_f32_32x32x16_bf16 v[192:207], a[224:227], v[104:107], v[192:207]
v_exp_f32 v119, v47
ds_read_b128 a[224:227], v1 offset:40960
v_mfma_f32_32x32x16_bf16 v[208:223], a[228:231], v[104:107], v[208:223]
v_cvt_pk_bf16_f32 v103, v118, v119
v_exp_f32 v120, v48
ds_read_b128 a[228:231], v1 offset:41984
v_mfma_f32_32x32x16_bf16 v[224:239], a[232:235], v[104:107], v[224:239]
v_mfma_f32_16x16x32_bf16 v[24:27], v[28:31], v[100:103], v[24:27]
v_exp_f32 v121, v49
ds_read_b128 a[232:235], v1 offset:43008
v_mfma_f32_32x32x16_bf16 v[240:255], a[236:239], v[104:107], v[240:255]
v_cvt_pk_bf16_f32 v104, v120, v121
v_exp_f32 v122, v50
ds_read_b128 a[236:239], v1 offset:44032
v_mfma_f32_32x32x16_bf16 v[192:207], a[240:243], v[108:111], v[192:207]
v_exp_f32 v123, v51
v_cmp_ngt_f32 vcc, s71, v24
ds_read_b128 a[240:243], v1 offset:45056
v_mfma_f32_32x32x16_bf16 v[208:223], a[244:247], v[108:111], v[208:223]
v_cvt_pk_bf16_f32 v105, v122, v123
v_exp_f32 v124, v52
s_cbranch_vccnz .Lr64
.Lr64b:
v_mfma_f32_32x32x16_bf16 v[224:239], a[248:251], v[108:111], v[224:239]
v_exp_f32 v125, v53
v_add_f32 v10, v10, v24
ds_read_b128 a[244:247], v1 offset:46080
ds_read_b128 a[248:251], v1 offset:47104
v_mfma_f32_32x32x16_bf16 v[240:255], a[252:255], v[108:111], v[240:255]
v_cvt_pk_bf16_f32 v106, v124, v125
v_exp_f32 v126, v54
ds_read_b128 a[252:255], v1 offset:48128
s_waitcnt lgkmcnt(8)
.Lyl1_2:
v_mfma_f32_32x32x16_bf16 v[32:47], a[192:195], a[128:131], v[64:79]
s_add_u32 m0, s36, 8192
v_exp_f32 v127, v55
v_mfma_f32_32x32x16_bf16 v[32:47], a[196:199], a[132:135], v[32:47]
v_cvt_pk_bf16_f32 v107, v126, v127
v_exp_f32 v120, v56
buffer_load_dwordx4 v1, s[20:23], s24 offen offset:0 lds
v_mfma_f32_32x32x16_bf16 v[32:47], a[200:203], a[136:139], v[32:47]
v_exp_f32 v121, v57
v_mfma_f32_16x16x32_bf16 v[24:27], v[28:31], v[104:107], 0
v_mfma_f32_32x32x16_bf16 v[32:47], a[204:207], a[140:143], v[32:47]
v_cvt_pk_bf16_f32 v108, v120, v121
v_exp_f32 v122, v58
buffer_load_dwordx4 v1, s[20:23], s24 offen offset:1024 lds
v_mfma_f32_32x32x16_bf16 v[32:47], a[208:211], a[144:147], v[32:47]
v_exp_f32 v123, v59
buffer_load_dwordx4 v1, s[20:23], s24 offen offset:2048 lds
v_mfma_f32_32x32x16_bf16 v[32:47], a[212:215], a[148:151], v[32:47]
v_cvt_pk_bf16_f32 v109, v122, v123
v_exp_f32 v124, v60
buffer_load_dwordx4 v1, s[20:23], s24 offen offset:3072 lds
s_add_u32 m0, s36, 32768
v_mfma_f32_32x32x16_bf16 v[32:47], a[216:219], a[152:155], v[32:47]
v_exp_f32 v125, v61
v_mfma_f32_32x32x16_bf16 v[32:47], a[220:223], a[156:159], v[32:47]
v_cvt_pk_bf16_f32 v110, v124, v125
v_exp_f32 v126, v62
s_waitcnt lgkmcnt(0)
v_mfma_f32_32x32x16_bf16 v[128:143], a[224:227], v[96:99], v[128:143]
v_exp_f32 v127, v63
buffer_load_dwordx4 v3, s[20:23], s24 offen offset:0 lds
v_mfma_f32_32x32x16_bf16 v[144:159], a[228:231], v[96:99], v[144:159]
v_cvt_pk_bf16_f32 v111, v126, v127
v_cndmask_b32 v32, v32, v5, s[54:55]
v_cndmask_b32 v33, v33, v5, s[56:57]
v_exp_f32 v112, v32
buffer_load_dwordx4 v3, s[20:23], s24 offen offset:1024 lds
v_mfma_f32_32x32x16_bf16 v[160:175], a[232:235], v[96:99], v[160:175]
v_cndmask_b32 v34, v34, v5, s[58:59]
v_cndmask_b32 v35, v35, v5, s[60:61]
v_exp_f32 v113, v33
v_mfma_f32_16x16x32_bf16 v[24:27], v[28:31], v[108:111], v[24:27]
v_mfma_f32_32x32x16_bf16 v[176:191], a[236:239], v[96:99], v[176:191]
v_cvt_pk_bf16_f32 v96, v112, v113
v_cndmask_b32 v36, v36, v5, s[62:63]
v_cndmask_b32 v37, v37, v5, s[64:65]
v_exp_f32 v114, v34
buffer_load_dwordx4 v3, s[20:23], s24 offen offset:2048 lds
v_mfma_f32_32x32x16_bf16 v[128:143], a[240:243], v[100:103], v[128:143]
v_cndmask_b32 v38, v38, v5, s[66:67]
v_cndmask_b32 v39, v39, v5, s[68:69]
v_exp_f32 v115, v35
v_cmp_ngt_f32 vcc, s71, v24
v_mfma_f32_32x32x16_bf16 v[144:159], a[244:247], v[100:103], v[144:159]
v_cvt_pk_bf16_f32 v97, v114, v115
v_cndmask_b32 v40, v40, v5, s[84:85]
v_cndmask_b32 v41, v41, v5, s[86:87]
v_exp_f32 v116, v36
s_cbranch_vccnz .Lr67
.Lr67b:
v_mfma_f32_32x32x16_bf16 v[160:175], a[248:251], v[100:103], v[160:175]
v_cndmask_b32 v42, v42, v5, s[88:89]
v_cndmask_b32 v43, v43, v5, s[90:91]
v_exp_f32 v117, v37
v_add_f32 v11, v11, v24
buffer_load_dwordx4 v3, s[20:23], s24 offen offset:3072 lds
v_mfma_f32_32x32x16_bf16 v[176:191], a[252:255], v[100:103], v[176:191]
v_cvt_pk_bf16_f32 v98, v116, v117
v_cndmask_b32 v44, v44, v5, s[92:93]
v_cndmask_b32 v45, v45, v5, s[94:95]
v_exp_f32 v118, v38
s_waitcnt vmcnt(16)
s_barrier
s_add_u32 s24, s24, 0x2000
v_mfma_f32_32x32x16_bf16 v[48:63], a[192:195], a[160:163], v[80:95]
v_cndmask_b32 v46, v46, v5, s[96:97]
v_cndmask_b32 v47, v47, v5, s[98:99]
v_exp_f32 v119, v39
ds_read_b128 a[192:195], v2 offset:24576
v_mfma_f32_32x32x16_bf16 v[48:63], a[196:199], a[164:167], v[48:63]
v_cvt_pk_bf16_f32 v99, v118, v119
v_exp_f32 v112, v40
ds_read_b128 a[196:199], v2 offset:25600
v_mfma_f32_32x32x16_bf16 v[48:63], a[200:203], a[168:171], v[48:63]
v_exp_f32 v113, v41
v_mfma_f32_16x16x32_bf16 v[24:27], v[28:31], v[96:99], 0
ds_read_b128 a[200:203], v2 offset:26624
v_mfma_f32_32x32x16_bf16 v[48:63], a[204:207], a[172:175], v[48:63]
v_cvt_pk_bf16_f32 v100, v112, v113
v_exp_f32 v114, v42
ds_read_b128 a[204:207], v2 offset:27648
v_mfma_f32_32x32x16_bf16 v[48:63], a[208:211], a[176:179], v[48:63]
v_exp_f32 v115, v43
ds_read_b128 a[208:211], v2 offset:28672
v_mfma_f32_32x32x16_bf16 v[48:63], a[212:215], a[180:183], v[48:63]
v_cvt_pk_bf16_f32 v101, v114, v115
v_exp_f32 v116, v44
ds_read_b128 a[212:215], v2 offset:29696
v_mfma_f32_32x32x16_bf16 v[48:63], a[216:219], a[184:187], v[48:63]
v_exp_f32 v117, v45
ds_read_b128 a[216:219], v2 offset:30720
v_mfma_f32_32x32x16_bf16 v[48:63], a[220:223], a[188:191], v[48:63]
v_cvt_pk_bf16_f32 v102, v116, v117
v_exp_f32 v118, v46
ds_read_b128 a[220:223], v2 offset:31744
v_mfma_f32_32x32x16_bf16 v[192:207], a[224:227], v[104:107], v[192:207]
v_exp_f32 v119, v47
ds_read_b128 a[224:227], v1 offset:49152
v_mfma_f32_32x32x16_bf16 v[208:223], a[228:231], v[104:107], v[208:223]
v_cvt_pk_bf16_f32 v103, v118, v119
v_exp_f32 v120, v48
ds_read_b128 a[228:231], v1 offset:50176
v_mfma_f32_32x32x16_bf16 v[224:239], a[232:235], v[104:107], v[224:239]
v_mfma_f32_16x16x32_bf16 v[24:27], v[28:31], v[100:103], v[24:27]
v_exp_f32 v121, v49
ds_read_b128 a[232:235], v1 offset:51200
v_mfma_f32_32x32x16_bf16 v[240:255], a[236:239], v[104:107], v[240:255]
v_cvt_pk_bf16_f32 v104, v120, v121
v_exp_f32 v122, v50
ds_read_b128 a[236:239], v1 offset:52224
v_mfma_f32_32x32x16_bf16 v[192:207], a[240:243], v[108:111], v[192:207]
v_exp_f32 v123, v51
v_cmp_ngt_f32 vcc, s71, v24
ds_read_b128 a[240:243], v1 offset:53248
v_mfma_f32_32x32x16_bf16 v[208:223], a[244:247], v[108:111], v[208:223]
v_cvt_pk_bf16_f32 v105, v122, v123
v_exp_f32 v124, v52
s_cbranch_vccnz .Lr66
.Lr66b:
v_mfma_f32_32x32x16_bf16 v[224:239], a[248:251], v[108:111], v[224:239]
v_exp_f32 v125, v53
v_add_f32 v10, v10, v24
ds_read_b128 a[244:247], v1 offset:54272
ds_read_b128 a[248:251], v1 offset:55296
v_mfma_f32_32x32x16_bf16 v[240:255], a[252:255], v[108:111], v[240:255]
v_cvt_pk_bf16_f32 v106, v124, v125
v_exp_f32 v126, v54
ds_read_b128 a[252:255], v1 offset:56320
s_waitcnt lgkmcnt(8)
s_add_u32 m0, s36, 16384
v_exp_f32 v127, v55
v_mov_b32 v64, 0
v_mov_b32 v65, 0
v_cvt_pk_bf16_f32 v107, v126, v127
v_exp_f32 v120, v56
s_add_u32 s25, s24, 0x6000
s_sub_u32 s41, s25, 0x2000
s_sub_i32 s44, 32, s48
s_add_i32 s45, s44, 1
v_mov_b32 v66, 0
v_mov_b32 v67, 0
buffer_load_dwordx4 v1, s[20:23], s24 offen offset:0 lds
v_exp_f32 v121, v57
v_mfma_f32_16x16x32_bf16 v[24:27], v[28:31], v[104:107], 0
s_cmp_le_i32 s44, 2
s_cselect_b32 s42, s41, -1
v_mov_b32 v68, 0
v_mov_b32 v69, 0
v_cvt_pk_bf16_f32 v108, v120, v121
v_exp_f32 v122, v58
s_cmp_le_i32 s45, 2
s_cselect_b32 s43, s41, -1
v_mov_b32 v70, 0
v_mov_b32 v71, 0
buffer_load_dwordx4 v1, s[20:23], s24 offen offset:1024 lds
v_exp_f32 v123, v59
v_mov_b32 v72, 0
v_mov_b32 v73, 0
buffer_load_dwordx4 v1, s[20:23], s24 offen offset:2048 lds
v_cvt_pk_bf16_f32 v109, v122, v123
v_exp_f32 v124, v60
v_mov_b32 v74, 0
v_mov_b32 v75, 0
buffer_load_dwordx4 v1, s[20:23], s24 offen offset:3072 lds
s_add_u32 m0, s36, 40960
v_exp_f32 v125, v61
v_mov_b32 v76, 0
v_mov_b32 v77, 0
v_cvt_pk_bf16_f32 v110, v124, v125
v_exp_f32 v126, v62
v_mov_b32 v78, 0
v_mov_b32 v79, 0
s_waitcnt lgkmcnt(0)
v_mfma_f32_32x32x16_bf16 v[128:143], a[224:227], v[96:99], v[128:143]
v_exp_f32 v127, v63
ds_read_b128 a[128:131], v6 offset:0
ds_read_b128 a[132:135], v6 offset:1024
buffer_load_dwordx4 v3, s[20:23], s24 offen offset:0 lds
v_mfma_f32_32x32x16_bf16 v[144:159], a[228:231], v[96:99], v[144:159]
v_cvt_pk_bf16_f32 v111, v126, v127
ds_read_b128 a[136:139], v6 offset:2048
ds_read_b128 a[140:143], v6 offset:3072
buffer_load_dwordx4 v3, s[20:23], s24 offen offset:1024 lds
v_mfma_f32_32x32x16_bf16 v[160:175], a[232:235], v[96:99], v[160:175]
v_mfma_f32_16x16x32_bf16 v[24:27], v[28:31], v[108:111], v[24:27]
ds_read_b128 a[144:147], v6 offset:4096
ds_read_b128 a[148:151], v6 offset:5120
v_mfma_f32_32x32x16_bf16 v[176:191], a[236:239], v[96:99], v[176:191]
ds_read_b128 a[152:155], v6 offset:6144
ds_read_b128 a[156:159], v6 offset:7168
buffer_load_dwordx4 v3, s[20:23], s24 offen offset:2048 lds
v_mfma_f32_32x32x16_bf16 v[128:143], a[240:243], v[100:103], v[128:143]
v_cmp_ngt_f32 vcc, s71, v24
v_mfma_f32_32x32x16_bf16 v[144:159], a[244:247], v[100:103], v[144:159]
s_cbranch_vccnz .Lr69
.Lr69b:
v_mfma_f32_32x32x16_bf16 v[160:175], a[248:251], v[100:103], v[160:175]
v_add_f32 v11, v11, v24
v_add_u32 v2, 0x10000, v1
buffer_load_dwordx4 v3, s[20:23], s24 offen offset:3072 lds
v_mfma_f32_32x32x16_bf16 v[176:191], a[252:255], v[100:103], v[176:191]
s_waitcnt vmcnt(16)
s_barrier
s_add_u32 s24, s24, 0x2000
v_mfma_f32_32x32x16_bf16 v[48:63], a[192:195], a[160:163], v[80:95]
ds_read_b128 a[192:195], v2 offset:0
v_mfma_f32_32x32x16_bf16 v[48:63], a[196:199], a[164:167], v[48:63]
v_mov_b32 v80, 0
v_mov_b32 v81, 0
ds_read_b128 a[196:199], v2 offset:1024
v_rcp_f32 v10, v10
s_nop 0
v_pk_mul_f32 v[128:129], v[128:129], v[10:11] op_sel:[0,0] op_sel_hi:[1,0]
v_pk_mul_f32 v[130:131], v[130:131], v[10:11] op_sel:[0,0] op_sel_hi:[1,0]
v_mfma_f32_32x32x16_bf16 v[48:63], a[200:203], a[168:171], v[48:63]
v_mov_b32 v82, 0
v_mov_b32 v83, 0
ds_read_b128 a[200:203], v2 offset:2048
v_pk_mul_f32 v[132:133], v[132:133], v[10:11] op_sel:[0,0] op_sel_hi:[1,0]
v_pk_mul_f32 v[134:135], v[134:135], v[10:11] op_sel:[0,0] op_sel_hi:[1,0]
v_pk_mul_f32 v[136:137], v[136:137], v[10:11] op_sel:[0,0] op_sel_hi:[1,0]
v_pk_mul_f32 v[138:139], v[138:139], v[10:11] op_sel:[0,0] op_sel_hi:[1,0]
v_pk_mul_f32 v[140:141], v[140:141], v[10:11] op_sel:[0,0] op_sel_hi:[1,0]
v_pk_mul_f32 v[142:143], v[142:143], v[10:11] op_sel:[0,0] op_sel_hi:[1,0]
v_mfma_f32_32x32x16_bf16 v[48:63], a[204:207], a[172:175], v[48:63]
v_mov_b32 v84, 0
v_mov_b32 v85, 0
ds_read_b128 a[204:207], v2 offset:3072
v_cvt_pk_bf16_f32 v128, v128, v129
v_cvt_pk_bf16_f32 v129, v130, v131
v_cvt_pk_bf16_f32 v130, v132, v133
v_cvt_pk_bf16_f32 v131, v134, v135
buffer_store_dwordx4 v[128:131], v1, s[16:19], s37 offen offset:0 sc1
v_cvt_pk_bf16_f32 v132, v136, v137
v_mfma_f32_32x32x16_bf16 v[48:63], a[208:211], a[176:179], v[48:63]
v_mov_b32 v86, 0
v_mov_b32 v87, 0
ds_read_b128 a[208:211], v2 offset:4096
v_cvt_pk_bf16_f32 v133, v138, v139
v_cvt_pk_bf16_f32 v134, v140, v141
v_cvt_pk_bf16_f32 v135, v142, v143
buffer_store_dwordx4 v[132:135], v1, s[16:19], s37 offen offset:1024 sc1
v_pk_mul_f32 v[144:145], v[144:145], v[10:11] op_sel:[0,0] op_sel_hi:[1,0]
v_pk_mul_f32 v[146:147], v[146:147], v[10:11] op_sel:[0,0] op_sel_hi:[1,0]
v_mfma_f32_32x32x16_bf16 v[48:63], a[212:215], a[180:183], v[48:63]
v_mov_b32 v88, 0
v_mov_b32 v89, 0
ds_read_b128 a[212:215], v2 offset:5120
v_pk_mul_f32 v[148:149], v[148:149], v[10:11] op_sel:[0,0] op_sel_hi:[1,0]
v_pk_mul_f32 v[150:151], v[150:151], v[10:11] op_sel:[0,0] op_sel_hi:[1,0]
v_pk_mul_f32 v[152:153], v[152:153], v[10:11] op_sel:[0,0] op_sel_hi:[1,0]
v_pk_mul_f32 v[154:155], v[154:155], v[10:11] op_sel:[0,0] op_sel_hi:[1,0]
v_pk_mul_f32 v[156:157], v[156:157], v[10:11] op_sel:[0,0] op_sel_hi:[1,0]
v_pk_mul_f32 v[158:159], v[158:159], v[10:11] op_sel:[0,0] op_sel_hi:[1,0]
v_mfma_f32_32x32x16_bf16 v[48:63], a[216:219], a[184:187], v[48:63]
v_mov_b32 v90, 0
v_mov_b32 v91, 0
ds_read_b128 a[216:219], v2 offset:6144
v_cvt_pk_bf16_f32 v144, v144, v145
v_cvt_pk_bf16_f32 v145, v146, v147
v_cvt_pk_bf16_f32 v146, v148, v149
v_cvt_pk_bf16_f32 v147, v150, v151
buffer_store_dwordx4 v[144:147], v1, s[16:19], s37 offen offset:2048 sc1
v_cvt_pk_bf16_f32 v148, v152, v153
v_mfma_f32_32x32x16_bf16 v[48:63], a[220:223], a[188:191], v[48:63]
v_mov_b32 v92, 0
v_mov_b32 v93, 0
ds_read_b128 a[220:223], v2 offset:7168
v_cvt_pk_bf16_f32 v149, v154, v155
v_cvt_pk_bf16_f32 v150, v156, v157
v_cvt_pk_bf16_f32 v151, v158, v159
buffer_store_dwordx4 v[148:151], v1, s[16:19], s37 offen offset:3072 sc1
v_pk_mul_f32 v[160:161], v[160:161], v[10:11] op_sel:[0,0] op_sel_hi:[1,0]
v_pk_mul_f32 v[162:163], v[162:163], v[10:11] op_sel:[0,0] op_sel_hi:[1,0]
v_mfma_f32_32x32x16_bf16 v[192:207], a[224:227], v[104:107], v[192:207]
v_mov_b32 v94, 0
v_mov_b32 v95, 0
ds_read_b128 a[224:227], v1 offset:57344
v_pk_mul_f32 v[164:165], v[164:165], v[10:11] op_sel:[0,0] op_sel_hi:[1,0]
v_pk_mul_f32 v[166:167], v[166:167], v[10:11] op_sel:[0,0] op_sel_hi:[1,0]
v_pk_mul_f32 v[168:169], v[168:169], v[10:11] op_sel:[0,0] op_sel_hi:[1,0]
v_pk_mul_f32 v[170:171], v[170:171], v[10:11] op_sel:[0,0] op_sel_hi:[1,0]
v_pk_mul_f32 v[172:173], v[172:173], v[10:11] op_sel:[0,0] op_sel_hi:[1,0]
v_mfma_f32_32x32x16_bf16 v[208:223], a[228:231], v[104:107], v[208:223]
v_cndmask_b32 v48, v48, v5, s[54:55]
v_cndmask_b32 v49, v49, v5, s[56:57]
v_cndmask_b32 v50, v50, v5, s[58:59]
v_exp_f32 v120, v48
ds_read_b128 a[228:231], v1 offset:58368
v_mfma_f32_32x32x16_bf16 v[224:239], a[232:235], v[104:107], v[224:239]
v_cndmask_b32 v51, v51, v5, s[60:61]
v_cndmask_b32 v52, v52, v5, s[62:63]
v_cndmask_b32 v53, v53, v5, s[64:65]
v_exp_f32 v121, v49
ds_read_b128 a[232:235], v1 offset:59392
v_mfma_f32_32x32x16_bf16 v[240:255], a[236:239], v[104:107], v[240:255]
v_cvt_pk_bf16_f32 v104, v120, v121
v_cndmask_b32 v54, v54, v5, s[66:67]
v_cndmask_b32 v55, v55, v5, s[68:69]
v_exp_f32 v122, v50
ds_read_b128 a[236:239], v1 offset:60416
v_mfma_f32_32x32x16_bf16 v[192:207], a[240:243], v[108:111], v[192:207]
v_cndmask_b32 v56, v56, v5, s[84:85]
v_cndmask_b32 v57, v57, v5, s[86:87]
v_exp_f32 v123, v51
ds_read_b128 a[240:243], v1 offset:61440
v_mfma_f32_32x32x16_bf16 v[208:223], a[244:247], v[108:111], v[208:223]
v_cvt_pk_bf16_f32 v105, v122, v123
v_cndmask_b32 v58, v58, v5, s[88:89]
v_cndmask_b32 v59, v59, v5, s[90:91]
v_exp_f32 v124, v52
v_mfma_f32_32x32x16_bf16 v[224:239], a[248:251], v[108:111], v[224:239]
v_cndmask_b32 v60, v60, v5, s[92:93]
v_cndmask_b32 v61, v61, v5, s[94:95]
v_exp_f32 v125, v53
s_cmp_lt_u32 s48, 32
s_cselect_b32 s22, s52, 0
s_cbranch_scc0 .Lt70
.Lt70b:
ds_read_b128 a[244:247], v1 offset:62464
ds_read_b128 a[248:251], v1 offset:63488
v_mfma_f32_32x32x16_bf16 v[240:255], a[252:255], v[108:111], v[240:255]
v_cvt_pk_bf16_f32 v106, v124, v125
v_cndmask_b32 v62, v62, v5, s[96:97]
v_cndmask_b32 v63, v63, v5, s[98:99]
v_exp_f32 v126, v54
ds_read_b128 a[252:255], v1 offset:64512
s_waitcnt lgkmcnt(8)
v_mfma_f32_32x32x16_bf16 v[32:47], a[192:195], a[128:131], v[64:79]
s_add_u32 m0, s36, 24576
v_exp_f32 v127, v55
ds_read_b128 a[160:163], v6 offset:8192
ds_read_b128 a[164:167], v6 offset:9216
v_mfma_f32_32x32x16_bf16 v[32:47], a[196:199], a[132:135], v[32:47]
v_cvt_pk_bf16_f32 v107, v126, v127
v_exp_f32 v120, v56
ds_read_b128 a[168:171], v6 offset:10240
ds_read_b128 a[172:175], v6 offset:11264
buffer_load_dwordx4 v1, s[20:23], s24 offen offset:0 lds
v_mfma_f32_32x32x16_bf16 v[32:47], a[200:203], a[136:139], v[32:47]
v_exp_f32 v121, v57
v_mfma_f32_16x16x32_bf16 v[24:27], v[28:31], v[104:107], 0
ds_read_b128 a[176:179], v6 offset:12288
ds_read_b128 a[180:183], v6 offset:13312
v_mfma_f32_32x32x16_bf16 v[32:47], a[204:207], a[140:143], v[32:47]
v_cvt_pk_bf16_f32 v108, v120, v121
v_exp_f32 v122, v58
ds_read_b128 a[184:187], v6 offset:14336
ds_read_b128 a[188:191], v6 offset:15360
buffer_load_dwordx4 v1, s[20:23], s24 offen offset:1024 lds
v_mfma_f32_32x32x16_bf16 v[32:47], a[208:211], a[144:147], v[32:47]
v_exp_f32 v123, v59
buffer_load_dwordx4 v1, s[20:23], s24 offen offset:2048 lds
v_pk_mul_f32 v[174:175], v[174:175], v[10:11] op_sel:[0,0] op_sel_hi:[1,0]
v_cvt_pk_bf16_f32 v160, v160, v161
v_mfma_f32_32x32x16_bf16 v[32:47], a[212:215], a[148:151], v[32:47]
v_cvt_pk_bf16_f32 v109, v122, v123
v_exp_f32 v124, v60
buffer_load_dwordx4 v1, s[20:23], s24 offen offset:3072 lds
s_add_u32 m0, s36, 49152
v_mfma_f32_32x32x16_bf16 v[32:47], a[216:219], a[152:155], v[32:47]
v_exp_f32 v125, v61
v_cvt_pk_bf16_f32 v161, v162, v163
v_cvt_pk_bf16_f32 v162, v164, v165
v_cvt_pk_bf16_f32 v163, v166, v167
v_mfma_f32_32x32x16_bf16 v[32:47], a[220:223], a[156:159], v[32:47]
v_cvt_pk_bf16_f32 v110, v124, v125
v_exp_f32 v126, v62
s_waitcnt lgkmcnt(0)
buffer_store_dwordx4 v[160:163], v1, s[16:19], s38 offen offset:0 sc1
v_exp_f32 v127, v63
buffer_load_dwordx4 v3, s[20:23], s24 offen offset:0 lds
v_cvt_pk_bf16_f32 v164, v168, v169
v_cvt_pk_bf16_f32 v165, v170, v171
v_cvt_pk_bf16_f32 v166, v172, v173
v_cvt_pk_bf16_f32 v111, v126, v127
s_nop 1
v_exp_f32 v112, v32
buffer_load_dwordx4 v3, s[20:23], s24 offen offset:1024 lds
v_cvt_pk_bf16_f32 v167, v174, v175
buffer_store_dwordx4 v[164:167], v1, s[16:19], s38 offen offset:1024 sc1
v_exp_f32 v113, v33
v_mfma_f32_16x16x32_bf16 v[24:27], v[28:31], v[108:111], v[24:27]
v_cvt_pk_bf16_f32 v96, v112, v113
v_exp_f32 v114, v34
buffer_load_dwordx4 v3, s[20:23], s24 offen offset:2048 lds
v_pk_mul_f32 v[176:177], v[176:177], v[10:11] op_sel:[0,0] op_sel_hi:[1,0]
v_pk_mul_f32 v[178:179], v[178:179], v[10:11] op_sel:[0,0] op_sel_hi:[1,0]
v_exp_f32 v115, v35
s_nop 1
v_cmp_ngt_f32 vcc, s71, v24
s_cmp_eq_u32 s48, 32
v_cvt_pk_bf16_f32 v97, v114, v115
v_exp_f32 v116, v36
s_nop 0
s_cbranch_vccnz .Lr72
.Lr72b:
s_cbranch_scc1 .Ls72
.Ls72b:
v_exp_f32 v117, v37
v_add_f32 v11, v11, v24
buffer_load_dwordx4 v3, s[20:23], s24 offen offset:3072 lds
v_pk_mul_f32 v[180:181], v[180:181], v[10:11] op_sel:[0,0] op_sel_hi:[1,0]
v_pk_mul_f32 v[182:183], v[182:183], v[10:11] op_sel:[0,0] op_sel_hi:[1,0]
v_cvt_pk_bf16_f32 v98, v116, v117
v_exp_f32 v118, v38
s_waitcnt vmcnt(22)
s_barrier
s_add_u32 s24, s24, 0x2000
v_mfma_f32_32x32x16_bf16 v[48:63], a[192:195], a[160:163], v[80:95]
v_exp_f32 v119, v39
ds_read_b128 a[192:195], v2 offset:8192
v_pk_mul_f32 v[184:185], v[184:185], v[10:11] op_sel:[0,0] op_sel_hi:[1,0]
v_pk_mul_f32 v[186:187], v[186:187], v[10:11] op_sel:[0,0] op_sel_hi:[1,0]
v_mfma_f32_32x32x16_bf16 v[48:63], a[196:199], a[164:167], v[48:63]
v_cvt_pk_bf16_f32 v99, v118, v119
v_exp_f32 v112, v40
ds_read_b128 a[196:199], v2 offset:9216
v_pk_mul_f32 v[188:189], v[188:189], v[10:11] op_sel:[0,0] op_sel_hi:[1,0]
v_mfma_f32_32x32x16_bf16 v[48:63], a[200:203], a[168:171], v[48:63]
v_exp_f32 v113, v41
v_mfma_f32_16x16x32_bf16 v[24:27], v[28:31], v[96:99], 0
ds_read_b128 a[200:203], v2 offset:10240
v_mfma_f32_32x32x16_bf16 v[48:63], a[204:207], a[172:175], v[48:63]
v_cvt_pk_bf16_f32 v100, v112, v113
v_exp_f32 v114, v42
ds_read_b128 a[204:207], v2 offset:11264
v_pk_mul_f32 v[190:191], v[190:191], v[10:11] op_sel:[0,0] op_sel_hi:[1,0]
v_mfma_f32_32x32x16_bf16 v[48:63], a[208:211], a[176:179], v[48:63]
v_exp_f32 v115, v43
ds_read_b128 a[208:211], v2 offset:12288
v_cvt_pk_bf16_f32 v176, v176, v177
v_cvt_pk_bf16_f32 v177, v178, v179
v_mfma_f32_32x32x16_bf16 v[48:63], a[212:215], a[180:183], v[48:63]
v_cvt_pk_bf16_f32 v101, v114, v115
v_exp_f32 v116, v44
ds_read_b128 a[212:215], v2 offset:13312
v_cvt_pk_bf16_f32 v178, v180, v181
v_mfma_f32_32x32x16_bf16 v[48:63], a[216:219], a[184:187], v[48:63]
v_exp_f32 v117, v45
ds_read_b128 a[216:219], v2 offset:14336
v_cvt_pk_bf16_f32 v179, v182, v183
buffer_store_dwordx4 v[176:179], v1, s[16:19], s38 offen offset:2048 sc1
v_mfma_f32_32x32x16_bf16 v[48:63], a[220:223], a[188:191], v[48:63]
v_cvt_pk_bf16_f32 v102, v116, v117
v_exp_f32 v118, v46
ds_read_b128 a[220:223], v2 offset:15360
v_cvt_pk_bf16_f32 v180, v184, v185
v_mfma_f32_32x32x16_bf16 v[192:207], a[224:227], v[104:107], v[192:207]
v_exp_f32 v119, v47
ds_read_b128 a[224:227], v2 offset:32768
v_cvt_pk_bf16_f32 v181, v186, v187
v_cvt_pk_bf16_f32 v182, v188, v189
v_mfma_f32_32x32x16_bf16 v[208:223], a[228:231], v[104:107], v[208:223]
v_cvt_pk_bf16_f32 v103, v118, v119
v_exp_f32 v120, v48
ds_read_b128 a[228:231], v2 offset:33792
v_cvt_pk_bf16_f32 v183, v190, v191
v_mfma_f32_32x32x16_bf16 v[224:239], a[232:235], v[104:107], v[224:239]
v_mfma_f32_16x16x32_bf16 v[24:27], v[28:31], v[100:103], v[24:27]
v_exp_f32 v121, v49
ds_read_b128 a[232:235], v2 offset:34816
v_mfma_f32_32x32x16_bf16 v[240:255], a[236:239], v[104:107], v[240:255]
v_cvt_pk_bf16_f32 v104, v120, v121
v_exp_f32 v122, v50
ds_read_b128 a[236:239], v2 offset:35840
buffer_store_dwordx4 v[180:183], v1, s[16:19], s38 offen offset:3072 sc1
v_mfma_f32_32x32x16_bf16 v[192:207], a[240:243], v[108:111], v[192:207]
v_exp_f32 v123, v51
v_add_u32 v23, 0xcf800000, v24
v_cmp_le_u32 vcc, 0x2d000000, v23
s_cmp_ge_u32 s24, s42
ds_read_b128 a[240:243], v2 offset:36864
v_mfma_f32_32x32x16_bf16 v[208:223], a[244:247], v[108:111], v[208:223]
v_cvt_pk_bf16_f32 v105, v122, v123
v_exp_f32 v124, v52
s_cbranch_vccnz .Lr71
.Lr71b:
s_cbranch_scc1 .Ls71
.Ls71b:
v_mfma_f32_32x32x16_bf16 v[224:239], a[248:251], v[108:111], v[224:239]
v_exp_f32 v125, v53
v_add_f32 v8, v8, v24
ds_read_b128 a[244:247], v2 offset:37888
ds_read_b128 a[248:251], v2 offset:38912
v_mfma_f32_32x32x16_bf16 v[240:255], a[252:255], v[108:111], v[240:255]
v_cvt_pk_bf16_f32 v106, v124, v125
v_exp_f32 v126, v54
ds_read_b128 a[252:255], v2 offset:39936
s_waitcnt lgkmcnt(8)
v_mfma_f32_32x32x16_bf16 v[32:47], a[192:195], a[128:131], v[64:79]
s_add_u32 m0, s36, 0
v_exp_f32 v127, v55
v_mfma_f32_32x32x16_bf16 v[32:47], a[196:199], a[132:135], v[32:47]
v_cvt_pk_bf16_f32 v107, v126, v127
v_exp_f32 v120, v56
buffer_load_dwordx4 v1, s[20:23], s24 offen offset:0 lds
v_rcp_f32 v11, v11
v_mfma_f32_32x32x16_bf16 v[32:47], a[200:203], a[136:139], v[32:47]
v_exp_f32 v121, v57
v_mfma_f32_16x16x32_bf16 v[24:27], v[28:31], v[104:107], 0
v_pk_mul_f32 v[192:193], v[192:193], v[10:11] op_sel:[0,1] op_sel_hi:[1,1]
v_pk_mul_f32 v[194:195], v[194:195], v[10:11] op_sel:[0,1] op_sel_hi:[1,1]
v_mfma_f32_32x32x16_bf16 v[32:47], a[204:207], a[140:143], v[32:47]
v_cvt_pk_bf16_f32 v108, v120, v121
v_exp_f32 v122, v58
buffer_load_dwordx4 v1, s[20:23], s24 offen offset:1024 lds
v_pk_mul_f32 v[196:197], v[196:197], v[10:11] op_sel:[0,1] op_sel_hi:[1,1]
v_pk_mul_f32 v[198:199], v[198:199], v[10:11] op_sel:[0,1] op_sel_hi:[1,1]
v_pk_mul_f32 v[200:201], v[200:201], v[10:11] op_sel:[0,1] op_sel_hi:[1,1]
v_pk_mul_f32 v[202:203], v[202:203], v[10:11] op_sel:[0,1] op_sel_hi:[1,1]
v_mfma_f32_32x32x16_bf16 v[32:47], a[208:211], a[144:147], v[32:47]
v_exp_f32 v123, v59
buffer_load_dwordx4 v1, s[20:23], s24 offen offset:2048 lds
v_pk_mul_f32 v[204:205], v[204:205], v[10:11] op_sel:[0,1] op_sel_hi:[1,1]
v_pk_mul_f32 v[206:207], v[206:207], v[10:11] op_sel:[0,1] op_sel_hi:[1,1]
v_cvt_pk_bf16_f32 v192, v192, v193
v_cvt_pk_bf16_f32 v193, v194, v195
v_cvt_pk_bf16_f32 v194, v196, v197
v_mfma_f32_32x32x16_bf16 v[32:47], a[212:215], a[148:151], v[32:47]
v_cvt_pk_bf16_f32 v109, v122, v123
v_exp_f32 v124, v60
buffer_load_dwordx4 v1, s[20:23], s24 offen offset:3072 lds
s_add_u32 m0, s36, 57344
v_cvt_pk_bf16_f32 v195, v198, v199
buffer_store_dwordx4 v[192:195], v1, s[16:19], s39 offen offset:0 sc1
v_cvt_pk_bf16_f32 v196, v200, v201
v_mfma_f32_32x32x16_bf16 v[32:47], a[216:219], a[152:155], v[32:47]
v_exp_f32 v125, v61
v_cvt_pk_bf16_f32 v197, v202, v203
v_cvt_pk_bf16_f32 v198, v204, v205
v_cvt_pk_bf16_f32 v199, v206, v207
buffer_store_dwordx4 v[196:199], v1, s[16:19], s39 offen offset:1024 sc1
v_pk_mul_f32 v[208:209], v[208:209], v[10:11] op_sel:[0,1] op_sel_hi:[1,1]
v_pk_mul_f32 v[210:211], v[210:211], v[10:11] op_sel:[0,1] op_sel_hi:[1,1]
v_mfma_f32_32x32x16_bf16 v[32:47], a[220:223], a[156:159], v[32:47]
v_cvt_pk_bf16_f32 v110, v124, v125
v_exp_f32 v126, v62
s_waitcnt lgkmcnt(0)
v_pk_mul_f32 v[212:213], v[212:213], v[10:11] op_sel:[0,1] op_sel_hi:[1,1]
v_pk_mul_f32 v[214:215], v[214:215], v[10:11] op_sel:[0,1] op_sel_hi:[1,1]
v_pk_mul_f32 v[216:217], v[216:217], v[10:11] op_sel:[0,1] op_sel_hi:[1,1]
v_pk_mul_f32 v[218:219], v[218:219], v[10:11] op_sel:[0,1] op_sel_hi:[1,1]
v_mfma_f32_32x32x16_bf16 a[0:15], a[224:227], v[96:99], 0
v_exp_f32 v127, v63
buffer_load_dwordx4 v3, s[20:23], s24 offen offset:0 lds
v_pk_mul_f32 v[220:221], v[220:221], v[10:11] op_sel:[0,1] op_sel_hi:[1,1]
v_pk_mul_f32 v[222:223], v[222:223], v[10:11] op_sel:[0,1] op_sel_hi:[1,1]
v_cvt_pk_bf16_f32 v208, v208, v209
v_cvt_pk_bf16_f32 v209, v210, v211
v_cvt_pk_bf16_f32 v210, v212, v213
v_mfma_f32_32x32x16_bf16 a[16:31], a[228:231], v[96:99], 0
v_cvt_pk_bf16_f32 v111, v126, v127
v_exp_f32 v112, v32
buffer_load_dwordx4 v3, s[20:23], s24 offen offset:1024 lds
v_cvt_pk_bf16_f32 v211, v214, v215
buffer_store_dwordx4 v[208:211], v1, s[16:19], s39 offen offset:2048 sc1
v_cvt_pk_bf16_f32 v212, v216, v217
v_cvt_pk_bf16_f32 v213, v218, v219
v_mfma_f32_32x32x16_bf16 a[32:47], a[232:235], v[96:99], 0
v_exp_f32 v113, v33
v_mfma_f32_16x16x32_bf16 v[24:27], v[28:31], v[108:111], v[24:27]
v_cvt_pk_bf16_f32 v214, v220, v221
v_cvt_pk_bf16_f32 v215, v222, v223
v_mfma_f32_32x32x16_bf16 a[48:63], a[236:239], v[96:99], 0
v_cvt_pk_bf16_f32 v96, v112, v113
v_exp_f32 v114, v34
buffer_load_dwordx4 v3, s[20:23], s24 offen offset:2048 lds
buffer_store_dwordx4 v[212:215], v1, s[16:19], s39 offen offset:3072 sc1
v_pk_mul_f32 v[224:225], v[224:225], v[10:11] op_sel:[0,1] op_sel_hi:[1,1]
v_pk_mul_f32 v[226:227], v[226:227], v[10:11] op_sel:[0,1] op_sel_hi:[1,1]
v_pk_mul_f32 v[228:229], v[228:229], v[10:11] op_sel:[0,1] op_sel_hi:[1,1]
v_mfma_f32_32x32x16_bf16 a[0:15], a[240:243], v[100:103], a[0:15]
v_exp_f32 v115, v35
v_add_u32 v23, 0xcf800000, v24
v_cmp_le_u32 vcc, 0x2d000000, v23
s_cmp_ge_u32 s24, s43
v_mfma_f32_32x32x16_bf16 a[16:31], a[244:247], v[100:103], a[16:31]
v_cvt_pk_bf16_f32 v97, v114, v115
v_exp_f32 v116, v36
s_cbranch_vccnz .Lr74
.Lr74b:
s_cbranch_scc1 .Ls74
.Ls74b:
v_mfma_f32_32x32x16_bf16 a[32:47], a[248:251], v[100:103], a[32:47]
v_exp_f32 v117, v37
v_add_f32 v9, v9, v24
buffer_load_dwordx4 v3, s[20:23], s24 offen offset:3072 lds
v_pk_mul_f32 v[230:231], v[230:231], v[10:11] op_sel:[0,1] op_sel_hi:[1,1]
v_pk_mul_f32 v[232:233], v[232:233], v[10:11] op_sel:[0,1] op_sel_hi:[1,1]
v_pk_mul_f32 v[234:235], v[234:235], v[10:11] op_sel:[0,1] op_sel_hi:[1,1]
v_pk_mul_f32 v[236:237], v[236:237], v[10:11] op_sel:[0,1] op_sel_hi:[1,1]
v_mfma_f32_32x32x16_bf16 a[48:63], a[252:255], v[100:103], a[48:63]
v_cvt_pk_bf16_f32 v98, v116, v117
v_exp_f32 v118, v38
s_waitcnt vmcnt(28)
s_barrier
s_add_u32 s24, s24, 0x2000
v_mfma_f32_32x32x16_bf16 v[48:63], a[192:195], a[160:163], v[80:95]
v_exp_f32 v119, v39
ds_read_b128 a[192:195], v2 offset:16384
v_pk_mul_f32 v[238:239], v[238:239], v[10:11] op_sel:[0,1] op_sel_hi:[1,1]
v_cvt_pk_bf16_f32 v224, v224, v225
v_cvt_pk_bf16_f32 v225, v226, v227
v_cvt_pk_bf16_f32 v226, v228, v229
v_cvt_pk_bf16_f32 v227, v230, v231
v_mfma_f32_32x32x16_bf16 v[48:63], a[196:199], a[164:167], v[48:63]
v_cvt_pk_bf16_f32 v99, v118, v119
v_exp_f32 v112, v40
ds_read_b128 a[196:199], v2 offset:17408
buffer_store_dwordx4 v[224:227], v1, s[16:19], s53 offen offset:0 sc1
v_cvt_pk_bf16_f32 v228, v232, v233
v_cvt_pk_bf16_f32 v229, v234, v235
v_cvt_pk_bf16_f32 v230, v236, v237
v_mfma_f32_32x32x16_bf16 v[48:63], a[200:203], a[168:171], v[48:63]
v_exp_f32 v113, v41
v_mfma_f32_16x16x32_bf16 v[24:27], v[28:31], v[96:99], 0
ds_read_b128 a[200:203], v2 offset:18432
v_cvt_pk_bf16_f32 v231, v238, v239
v_mfma_f32_32x32x16_bf16 v[48:63], a[204:207], a[172:175], v[48:63]
v_cvt_pk_bf16_f32 v100, v112, v113
v_exp_f32 v114, v42
ds_read_b128 a[204:207], v2 offset:19456
buffer_store_dwordx4 v[228:231], v1, s[16:19], s53 offen offset:1024 sc1
v_pk_mul_f32 v[240:241], v[240:241], v[10:11] op_sel:[0,1] op_sel_hi:[1,1]
v_pk_mul_f32 v[242:243], v[242:243], v[10:11] op_sel:[0,1] op_sel_hi:[1,1]
v_pk_mul_f32 v[244:245], v[244:245], v[10:11] op_sel:[0,1] op_sel_hi:[1,1]
v_mfma_f32_32x32x16_bf16 v[48:63], a[208:211], a[176:179], v[48:63]
v_exp_f32 v115, v43
ds_read_b128 a[208:211], v2 offset:20480
v_pk_mul_f32 v[246:247], v[246:247], v[10:11] op_sel:[0,1] op_sel_hi:[1,1]
v_pk_mul_f32 v[248:249], v[248:249], v[10:11] op_sel:[0,1] op_sel_hi:[1,1]
v_pk_mul_f32 v[250:251], v[250:251], v[10:11] op_sel:[0,1] op_sel_hi:[1,1]
v_pk_mul_f32 v[252:253], v[252:253], v[10:11] op_sel:[0,1] op_sel_hi:[1,1]
v_pk_mul_f32 v[254:255], v[254:255], v[10:11] op_sel:[0,1] op_sel_hi:[1,1]
v_mfma_f32_32x32x16_bf16 v[48:63], a[212:215], a[180:183], v[48:63]
v_cvt_pk_bf16_f32 v101, v114, v115
v_exp_f32 v116, v44
ds_read_b128 a[212:215], v2 offset:21504
v_cvt_pk_bf16_f32 v240, v240, v241
v_cvt_pk_bf16_f32 v241, v242, v243
v_cvt_pk_bf16_f32 v242, v244, v245
v_cvt_pk_bf16_f32 v243, v246, v247
v_mfma_f32_32x32x16_bf16 v[48:63], a[216:219], a[184:187], v[48:63]
v_exp_f32 v117, v45
ds_read_b128 a[216:219], v2 offset:22528
buffer_store_dwordx4 v[240:243], v1, s[16:19], s53 offen offset:2048 sc1
v_cvt_pk_bf16_f32 v244, v248, v249
v_cvt_pk_bf16_f32 v245, v250, v251
v_cvt_pk_bf16_f32 v246, v252, v253
v_cvt_pk_bf16_f32 v247, v254, v255
v_mfma_f32_32x32x16_bf16 v[48:63], a[220:223], a[188:191], v[48:63]
v_cvt_pk_bf16_f32 v102, v116, v117
v_exp_f32 v118, v46
ds_read_b128 a[220:223], v2 offset:23552
buffer_store_dwordx4 v[244:247], v1, s[16:19], s53 offen offset:3072 sc1
v_mfma_f32_32x32x16_bf16 a[64:79], a[224:227], v[104:107], 0
v_exp_f32 v119, v47
ds_read_b128 a[224:227], v2 offset:40960
v_mfma_f32_32x32x16_bf16 a[80:95], a[228:231], v[104:107], 0
v_cvt_pk_bf16_f32 v103, v118, v119
v_exp_f32 v120, v48
ds_read_b128 a[228:231], v2 offset:41984
v_mfma_f32_32x32x16_bf16 a[96:111], a[232:235], v[104:107], 0
v_mfma_f32_16x16x32_bf16 v[24:27], v[28:31], v[100:103], v[24:27]
v_exp_f32 v121, v49
ds_read_b128 a[232:235], v2 offset:43008
v_mfma_f32_32x32x16_bf16 a[112:127], a[236:239], v[104:107], 0
v_cvt_pk_bf16_f32 v104, v120, v121
v_exp_f32 v122, v50
ds_read_b128 a[236:239], v2 offset:44032
v_mfma_f32_32x32x16_bf16 a[64:79], a[240:243], v[108:111], a[64:79]
v_exp_f32 v123, v51
v_cmp_ngt_f32 vcc, s71, v24
s_cmp_ge_u32 s24, s42
ds_read_b128 a[240:243], v2 offset:45056
v_mfma_f32_32x32x16_bf16 a[80:95], a[244:247], v[108:111], a[80:95]
v_cvt_pk_bf16_f32 v105, v122, v123
v_exp_f32 v124, v52
s_cbranch_vccnz .Lr73
.Lr73b:
s_cbranch_scc1 .Ls73
.Ls73b:
v_mfma_f32_32x32x16_bf16 a[96:111], a[248:251], v[108:111], a[96:111]
v_exp_f32 v125, v53
v_add_f32 v8, v8, v24
ds_read_b128 a[244:247], v2 offset:46080
ds_read_b128 a[248:251], v2 offset:47104
v_mfma_f32_32x32x16_bf16 a[112:127], a[252:255], v[108:111], a[112:127]
v_cvt_pk_bf16_f32 v106, v124, v125
v_exp_f32 v126, v54
ds_read_b128 a[252:255], v2 offset:48128
s_waitcnt lgkmcnt(8)
s_cmp_eq_u32 s48, 32
s_cbranch_scc1 .LdA
v_mfma_f32_32x32x16_bf16 v[32:47], a[192:195], a[128:131], v[64:79]
s_add_u32 m0, s36, 8192
v_exp_f32 v127, v55
v_mfma_f32_32x32x16_bf16 v[32:47], a[196:199], a[132:135], v[32:47]
v_cvt_pk_bf16_f32 v107, v126, v127
v_exp_f32 v120, v56
buffer_load_dwordx4 v1, s[20:23], s24 offen offset:0 lds
v_mfma_f32_32x32x16_bf16 v[32:47], a[200:203], a[136:139], v[32:47]
v_exp_f32 v121, v57
v_mfma_f32_16x16x32_bf16 v[24:27], v[28:31], v[104:107], 0
v_mfma_f32_32x32x16_bf16 v[32:47], a[204:207], a[140:143], v[32:47]
v_cvt_pk_bf16_f32 v108, v120, v121
v_exp_f32 v122, v58
buffer_load_dwordx4 v1, s[20:23], s24 offen offset:1024 lds
v_mfma_f32_32x32x16_bf16 v[32:47], a[208:211], a[144:147], v[32:47]
v_exp_f32 v123, v59
buffer_load_dwordx4 v1, s[20:23], s24 offen offset:2048 lds
v_mfma_f32_32x32x16_bf16 v[32:47], a[212:215], a[148:151], v[32:47]
v_cvt_pk_bf16_f32 v109, v122, v123
v_exp_f32 v124, v60
buffer_load_dwordx4 v1, s[20:23], s24 offen offset:3072 lds
s_add_u32 m0, s36, 32768
v_mfma_f32_32x32x16_bf16 v[32:47], a[216:219], a[152:155], v[32:47]
v_exp_f32 v125, v61
v_mfma_f32_32x32x16_bf16 v[32:47], a[220:223], a[156:159], v[32:47]
v_cvt_pk_bf16_f32 v110, v124, v125
v_exp_f32 v126, v62
s_waitcnt lgkmcnt(0)
v_mfma_f32_32x32x16_bf16 a[0:15], a[224:227], v[96:99], a[0:15]
v_exp_f32 v127, v63
buffer_load_dwordx4 v3, s[20:23], s24 offen offset:0 lds
v_mfma_f32_32x32x16_bf16 a[16:31], a[228:231], v[96:99], a[16:31]
v_cvt_pk_bf16_f32 v111, v126, v127
v_exp_f32 v112, v32
buffer_load_dwordx4 v3, s[20:23], s24 offen offset:1024 lds
v_mfma_f32_32x32x16_bf16 a[32:47], a[232:235], v[96:99], a[32:47]
v_exp_f32 v113, v33
v_mfma_f32_16x16x32_bf16 v[24:27], v[28:31], v[108:111], v[24:27]
v_mfma_f32_32x32x16_bf16 a[48:63], a[236:239], v[96:99], a[48:63]
v_cvt_pk_bf16_f32 v96, v112, v113
v_exp_f32 v114, v34
buffer_load_dwordx4 v3, s[20:23], s24 offen offset:2048 lds
v_mfma_f32_32x32x16_bf16 a[0:15], a[240:243], v[100:103], a[0:15]
v_exp_f32 v115, v35
v_cmp_ngt_f32 vcc, s71, v24
v_mfma_f32_32x32x16_bf16 a[16:31], a[244:247], v[100:103], a[16:31]
v_cvt_pk_bf16_f32 v97, v114, v115
v_exp_f32 v116, v36
s_cbranch_vccnz .Lr76
.Lr76b:
v_mfma_f32_32x32x16_bf16 a[32:47], a[248:251], v[100:103], a[32:47]
v_exp_f32 v117, v37
v_add_f32 v9, v9, v24
buffer_load_dwordx4 v3, s[20:23], s24 offen offset:3072 lds
v_mfma_f32_32x32x16_bf16 a[48:63], a[252:255], v[100:103], a[48:63]
v_cvt_pk_bf16_f32 v98, v116, v117
v_exp_f32 v118, v38
s_waitcnt vmcnt(26)
s_barrier
s_add_u32 s24, s24, 0x2000
v_mfma_f32_32x32x16_bf16 v[48:63], a[192:195], a[160:163], v[80:95]
v_exp_f32 v119, v39
ds_read_b128 a[192:195], v2 offset:24576
v_mfma_f32_32x32x16_bf16 v[48:63], a[196:199], a[164:167], v[48:63]
v_cvt_pk_bf16_f32 v99, v118, v119
v_exp_f32 v112, v40
ds_read_b128 a[196:199], v2 offset:25600
v_mfma_f32_32x32x16_bf16 v[48:63], a[200:203], a[168:171], v[48:63]
v_exp_f32 v113, v41
v_mfma_f32_16x16x32_bf16 v[24:27], v[28:31], v[96:99], 0
ds_read_b128 a[200:203], v2 offset:26624
v_mfma_f32_32x32x16_bf16 v[48:63], a[204:207], a[172:175], v[48:63]
v_cvt_pk_bf16_f32 v100, v112, v113
v_exp_f32 v114, v42
ds_read_b128 a[204:207], v2 offset:27648
v_mfma_f32_32x32x16_bf16 v[48:63], a[208:211], a[176:179], v[48:63]
v_exp_f32 v115, v43
ds_read_b128 a[208:211], v2 offset:28672
v_mfma_f32_32x32x16_bf16 v[48:63], a[212:215], a[180:183], v[48:63]
v_cvt_pk_bf16_f32 v101, v114, v115
v_exp_f32 v116, v44
ds_read_b128 a[212:215], v2 offset:29696
v_mfma_f32_32x32x16_bf16 v[48:63], a[216:219], a[184:187], v[48:63]
v_exp_f32 v117, v45
ds_read_b128 a[216:219], v2 offset:30720
v_mfma_f32_32x32x16_bf16 v[48:63], a[220:223], a[188:191], v[48:63]
v_cvt_pk_bf16_f32 v102, v116, v117
v_exp_f32 v118, v46
ds_read_b128 a[220:223], v2 offset:31744
v_mfma_f32_32x32x16_bf16 a[64:79], a[224:227], v[104:107], a[64:79]
v_exp_f32 v119, v47
ds_read_b128 a[224:227], v2 offset:49152
v_mfma_f32_32x32x16_bf16 a[80:95], a[228:231], v[104:107], a[80:95]
v_cvt_pk_bf16_f32 v103, v118, v119
v_exp_f32 v120, v48
ds_read_b128 a[228:231], v2 offset:50176
v_mfma_f32_32x32x16_bf16 a[96:111], a[232:235], v[104:107], a[96:111]
v_mfma_f32_16x16x32_bf16 v[24:27], v[28:31], v[100:103], v[24:27]
v_exp_f32 v121, v49
ds_read_b128 a[232:235], v2 offset:51200
v_mfma_f32_32x32x16_bf16 a[112:127], a[236:239], v[104:107], a[112:127]
v_cvt_pk_bf16_f32 v104, v120, v121
v_exp_f32 v122, v50
ds_read_b128 a[236:239], v2 offset:52224
v_mfma_f32_32x32x16_bf16 a[64:79], a[240:243], v[108:111], a[64:79]
v_exp_f32 v123, v51
v_cmp_ngt_f32 vcc, s71, v24
ds_read_b128 a[240:243], v2 offset:53248
v_mfma_f32_32x32x16_bf16 a[80:95], a[244:247], v[108:111], a[80:95]
v_cvt_pk_bf16_f32 v105, v122, v123
v_exp_f32 v124, v52
s_cbranch_vccnz .Lr75
.Lr75b:
v_mfma_f32_32x32x16_bf16 a[96:111], a[248:251], v[108:111], a[96:111]
v_exp_f32 v125, v53
v_add_f32 v8, v8, v24
ds_read_b128 a[244:247], v2 offset:54272
ds_read_b128 a[248:251], v2 offset:55296
v_mfma_f32_32x32x16_bf16 a[112:127], a[252:255], v[108:111], a[112:127]
v_cvt_pk_bf16_f32 v106, v124, v125
v_exp_f32 v126, v54
ds_read_b128 a[252:255], v2 offset:56320
s_waitcnt lgkmcnt(8)
s_cmp_eq_u32 s48, 28
s_cbranch_scc1 .Lxl1_1
s_branch .Lx1_3
.Lx1_2:
v_mfma_f32_32x32x16_bf16 v[32:47], a[192:195], a[128:131], v[64:79]
s_add_u32 m0, s36, 8192
v_exp_f32 v127, v55
v_mfma_f32_32x32x16_bf16 v[32:47], a[196:199], a[132:135], v[32:47]
v_cvt_pk_bf16_f32 v107, v126, v127
v_exp_f32 v120, v56
buffer_load_dwordx4 v1, s[20:23], s24 offen offset:0 lds
v_mfma_f32_32x32x16_bf16 v[32:47], a[200:203], a[136:139], v[32:47]
v_exp_f32 v121, v57
v_mfma_f32_16x16x32_bf16 v[24:27], v[28:31], v[104:107], 0
v_mfma_f32_32x32x16_bf16 v[32:47], a[204:207], a[140:143], v[32:47]
v_cvt_pk_bf16_f32 v108, v120, v121
v_exp_f32 v122, v58
buffer_load_dwordx4 v1, s[20:23], s24 offen offset:1024 lds
v_mfma_f32_32x32x16_bf16 v[32:47], a[208:211], a[144:147], v[32:47]
v_exp_f32 v123, v59
buffer_load_dwordx4 v1, s[20:23], s24 offen offset:2048 lds
v_mfma_f32_32x32x16_bf16 v[32:47], a[212:215], a[148:151], v[32:47]
v_cvt_pk_bf16_f32 v109, v122, v123
v_exp_f32 v124, v60
buffer_load_dwordx4 v1, s[20:23], s24 offen offset:3072 lds
s_add_u32 m0, s36, 32768
v_mfma_f32_32x32x16_bf16 v[32:47], a[216:219], a[152:155], v[32:47]
v_exp_f32 v125, v61
v_mfma_f32_32x32x16_bf16 v[32:47], a[220:223], a[156:159], v[32:47]
v_cvt_pk_bf16_f32 v110, v124, v125
v_exp_f32 v126, v62
s_waitcnt lgkmcnt(0)
v_mfma_f32_32x32x16_bf16 a[0:15], a[224:227], v[96:99], a[0:15]
v_exp_f32 v127, v63
buffer_load_dwordx4 v3, s[20:23], s24 offen offset:0 lds
v_mfma_f32_32x32x16_bf16 a[16:31], a[228:231], v[96:99], a[16:31]
v_cvt_pk_bf16_f32 v111, v126, v127
v_exp_f32 v112, v32
buffer_load_dwordx4 v3, s[20:23], s24 offen offset:1024 lds
v_mfma_f32_32x32x16_bf16 a[32:47], a[232:235], v[96:99], a[32:47]
v_exp_f32 v113, v33
v_mfma_f32_16x16x32_bf16 v[24:27], v[28:31], v[108:111], v[24:27]
v_mfma_f32_32x32x16_bf16 a[48:63], a[236:239], v[96:99], a[48:63]
v_cvt_pk_bf16_f32 v96, v112, v113
v_exp_f32 v114, v34
buffer_load_dwordx4 v3, s[20:23], s24 offen offset:2048 lds
v_mfma_f32_32x32x16_bf16 a[0:15], a[240:243], v[100:103], a[0:15]
v_exp_f32 v115, v35
v_cmp_ngt_f32 vcc, s71, v24
v_mfma_f32_32x32x16_bf16 a[16:31], a[244:247], v[100:103], a[16:31]
v_cvt_pk_bf16_f32 v97, v114, v115
v_exp_f32 v116, v36
s_cbranch_vccnz .Lr47
.Lr47b:
v_mfma_f32_32x32x16_bf16 a[32:47], a[248:251], v[100:103], a[32:47]
v_exp_f32 v117, v37
v_add_f32 v9, v9, v24
buffer_load_dwordx4 v3, s[20:23], s24 offen offset:3072 lds
v_mfma_f32_32x32x16_bf16 a[48:63], a[252:255], v[100:103], a[48:63]
v_cvt_pk_bf16_f32 v98, v116, v117
v_exp_f32 v118, v38
s_waitcnt vmcnt(16)
s_barrier
s_add_u32 s24, s24, 0x2000
v_mfma_f32_32x32x16_bf16 v[48:63], a[192:195], a[160:163], v[80:95]
v_exp_f32 v119, v39
ds_read_b128 a[192:195], v2 offset:24576
v_mfma_f32_32x32x16_bf16 v[48:63], a[196:199], a[164:167], v[48:63]
v_cvt_pk_bf16_f32 v99, v118, v119
v_exp_f32 v112, v40
ds_read_b128 a[196:199], v2 offset:25600
v_mfma_f32_32x32x16_bf16 v[48:63], a[200:203], a[168:171], v[48:63]
v_exp_f32 v113, v41
v_mfma_f32_16x16x32_bf16 v[24:27], v[28:31], v[96:99], 0
ds_read_b128 a[200:203], v2 offset:26624
v_mfma_f32_32x32x16_bf16 v[48:63], a[204:207], a[172:175], v[48:63]
v_cvt_pk_bf16_f32 v100, v112, v113
v_exp_f32 v114, v42
ds_read_b128 a[204:207], v2 offset:27648
v_mfma_f32_32x32x16_bf16 v[48:63], a[208:211], a[176:179], v[48:63]
v_exp_f32 v115, v43
ds_read_b128 a[208:211], v2 offset:28672
v_mfma_f32_32x32x16_bf16 v[48:63], a[212:215], a[180:183], v[48:63]
v_cvt_pk_bf16_f32 v101, v114, v115
v_exp_f32 v116, v44
ds_read_b128 a[212:215], v2 offset:29696
v_mfma_f32_32x32x16_bf16 v[48:63], a[216:219], a[184:187], v[48:63]
v_exp_f32 v117, v45
ds_read_b128 a[216:219], v2 offset:30720
v_mfma_f32_32x32x16_bf16 v[48:63], a[220:223], a[188:191], v[48:63]
v_cvt_pk_bf16_f32 v102, v116, v117
v_exp_f32 v118, v46
ds_read_b128 a[220:223], v2 offset:31744
v_mfma_f32_32x32x16_bf16 a[64:79], a[224:227], v[104:107], a[64:79]
v_exp_f32 v119, v47
ds_read_b128 a[224:227], v2 offset:49152
v_mfma_f32_32x32x16_bf16 a[80:95], a[228:231], v[104:107], a[80:95]
v_cvt_pk_bf16_f32 v103, v118, v119
v_exp_f32 v120, v48
ds_read_b128 a[228:231], v2 offset:50176
v_mfma_f32_32x32x16_bf16 a[96:111], a[232:235], v[104:107], a[96:111]
v_mfma_f32_16x16x32_bf16 v[24:27], v[28:31], v[100:103], v[24:27]
v_exp_f32 v121, v49
ds_read_b128 a[232:235], v2 offset:51200
v_mfma_f32_32x32x16_bf16 a[112:127], a[236:239], v[104:107], a[112:127]
v_cvt_pk_bf16_f32 v104, v120, v121
v_exp_f32 v122, v50
ds_read_b128 a[236:239], v2 offset:52224
v_mfma_f32_32x32x16_bf16 a[64:79], a[240:243], v[108:111], a[64:79]
v_exp_f32 v123, v51
v_cmp_ngt_f32 vcc, s71, v24
ds_read_b128 a[240:243], v2 offset:53248
v_mfma_f32_32x32x16_bf16 a[80:95], a[244:247], v[108:111], a[80:95]
v_cvt_pk_bf16_f32 v105, v122, v123
v_exp_f32 v124, v52
s_cbranch_vccnz .Lr46
.Lr46b:
v_mfma_f32_32x32x16_bf16 a[96:111], a[248:251], v[108:111], a[96:111]
v_exp_f32 v125, v53
v_add_f32 v8, v8, v24
ds_read_b128 a[244:247], v2 offset:54272
ds_read_b128 a[248:251], v2 offset:55296
v_mfma_f32_32x32x16_bf16 a[112:127], a[252:255], v[108:111], a[112:127]
v_cvt_pk_bf16_f32 v106, v124, v125
v_exp_f32 v126, v54
ds_read_b128 a[252:255], v2 offset:56320
s_waitcnt lgkmcnt(8)
.Lx1_3:
v_mfma_f32_32x32x16_bf16 v[32:47], a[192:195], a[128:131], v[64:79]
s_add_u32 m0, s36, 16384
v_exp_f32 v127, v55
v_mfma_f32_32x32x16_bf16 v[32:47], a[196:199], a[132:135], v[32:47]
v_cvt_pk_bf16_f32 v107, v126, v127
v_exp_f32 v120, v56
buffer_load_dwordx4 v1, s[20:23], s24 offen offset:0 lds
v_mfma_f32_32x32x16_bf16 v[32:47], a[200:203], a[136:139], v[32:47]
v_exp_f32 v121, v57
v_mfma_f32_16x16x32_bf16 v[24:27], v[28:31], v[104:107], 0
v_mfma_f32_32x32x16_bf16 v[32:47], a[204:207], a[140:143], v[32:47]
v_cvt_pk_bf16_f32 v108, v120, v121
v_exp_f32 v122, v58
buffer_load_dwordx4 v1, s[20:23], s24 offen offset:1024 lds
v_mfma_f32_32x32x16_bf16 v[32:47], a[208:211], a[144:147], v[32:47]
v_exp_f32 v123, v59
buffer_load_dwordx4 v1, s[20:23], s24 offen offset:2048 lds
v_mfma_f32_32x32x16_bf16 v[32:47], a[212:215], a[148:151], v[32:47]
v_cvt_pk_bf16_f32 v109, v122, v123
v_exp_f32 v124, v60
buffer_load_dwordx4 v1, s[20:23], s24 offen offset:3072 lds
s_add_u32 m0, s36, 40960
v_mfma_f32_32x32x16_bf16 v[32:47], a[216:219], a[152:155], v[32:47]
v_exp_f32 v125, v61
v_mfma_f32_32x32x16_bf16 v[32:47], a[220:223], a[156:159], v[32:47]
v_cvt_pk_bf16_f32 v110, v124, v125
v_exp_f32 v126, v62
s_waitcnt lgkmcnt(0)
v_mfma_f32_32x32x16_bf16 a[0:15], a[224:227], v[96:99], a[0:15]
v_exp_f32 v127, v63
buffer_load_dwordx4 v3, s[20:23], s24 offen offset:0 lds
v_mfma_f32_32x32x16_bf16 a[16:31], a[228:231], v[96:99], a[16:31]
v_cvt_pk_bf16_f32 v111, v126, v127
v_exp_f32 v112, v32
buffer_load_dwordx4 v3, s[20:23], s24 offen offset:1024 lds
v_mfma_f32_32x32x16_bf16 a[32:47], a[232:235], v[96:99], a[32:47]
v_exp_f32 v113, v33
v_mfma_f32_16x16x32_bf16 v[24:27], v[28:31], v[108:111], v[24:27]
v_mfma_f32_32x32x16_bf16 a[48:63], a[236:239], v[96:99], a[48:63]
v_cvt_pk_bf16_f32 v96, v112, v113
v_exp_f32 v114, v34
buffer_load_dwordx4 v3, s[20:23], s24 offen offset:2048 lds
v_mfma_f32_32x32x16_bf16 a[0:15], a[240:243], v[100:103], a[0:15]
v_exp_f32 v115, v35
v_cmp_ngt_f32 vcc, s71, v24
v_mfma_f32_32x32x16_bf16 a[16:31], a[244:247], v[100:103], a[16:31]
v_cvt_pk_bf16_f32 v97, v114, v115
v_exp_f32 v116, v36
s_cbranch_vccnz .Lr49
.Lr49b:
v_mfma_f32_32x32x16_bf16 a[32:47], a[248:251], v[100:103], a[32:47]
v_exp_f32 v117, v37
v_add_f32 v9, v9, v24
buffer_load_dwordx4 v3, s[20:23], s24 offen offset:3072 lds
v_mfma_f32_32x32x16_bf16 a[48:63], a[252:255], v[100:103], a[48:63]
v_cvt_pk_bf16_f32 v98, v116, v117
v_exp_f32 v118, v38
s_waitcnt vmcnt(16)
s_barrier
s_add_u32 s24, s24, 0x2000
v_mfma_f32_32x32x16_bf16 v[48:63], a[192:195], a[160:163], v[80:95]
v_exp_f32 v119, v39
ds_read_b128 a[192:195], v2 offset:0
v_mfma_f32_32x32x16_bf16 v[48:63], a[196:199], a[164:167], v[48:63]
v_cvt_pk_bf16_f32 v99, v118, v119
v_exp_f32 v112, v40
ds_read_b128 a[196:199], v2 offset:1024
v_mfma_f32_32x32x16_bf16 v[48:63], a[200:203], a[168:171], v[48:63]
v_exp_f32 v113, v41
v_mfma_f32_16x16x32_bf16 v[24:27], v[28:31], v[96:99], 0
ds_read_b128 a[200:203], v2 offset:2048
v_mfma_f32_32x32x16_bf16 v[48:63], a[204:207], a[172:175], v[48:63]
v_cvt_pk_bf16_f32 v100, v112, v113
v_exp_f32 v114, v42
ds_read_b128 a[204:207], v2 offset:3072
v_mfma_f32_32x32x16_bf16 v[48:63], a[208:211], a[176:179], v[48:63]
v_exp_f32 v115, v43
ds_read_b128 a[208:211], v2 offset:4096
v_mfma_f32_32x32x16_bf16 v[48:63], a[212:215], a[180:183], v[48:63]
v_cvt_pk_bf16_f32 v101, v114, v115
v_exp_f32 v116, v44
ds_read_b128 a[212:215], v2 offset:5120
v_mfma_f32_32x32x16_bf16 v[48:63], a[216:219], a[184:187], v[48:63]
v_exp_f32 v117, v45
ds_read_b128 a[216:219], v2 offset:6144
v_mfma_f32_32x32x16_bf16 v[48:63], a[220:223], a[188:191], v[48:63]
v_cvt_pk_bf16_f32 v102, v116, v117
v_exp_f32 v118, v46
ds_read_b128 a[220:223], v2 offset:7168
v_mfma_f32_32x32x16_bf16 a[64:79], a[224:227], v[104:107], a[64:79]
v_exp_f32 v119, v47
ds_read_b128 a[224:227], v2 offset:57344
v_mfma_f32_32x32x16_bf16 a[80:95], a[228:231], v[104:107], a[80:95]
v_cvt_pk_bf16_f32 v103, v118, v119
v_exp_f32 v120, v48
ds_read_b128 a[228:231], v2 offset:58368
v_mfma_f32_32x32x16_bf16 a[96:111], a[232:235], v[104:107], a[96:111]
v_mfma_f32_16x16x32_bf16 v[24:27], v[28:31], v[100:103], v[24:27]
v_exp_f32 v121, v49
ds_read_b128 a[232:235], v2 offset:59392
v_mfma_f32_32x32x16_bf16 a[112:127], a[236:239], v[104:107], a[112:127]
v_cvt_pk_bf16_f32 v104, v120, v121
v_exp_f32 v122, v50
ds_read_b128 a[236:239], v2 offset:60416
v_mfma_f32_32x32x16_bf16 a[64:79], a[240:243], v[108:111], a[64:79]
v_exp_f32 v123, v51
v_cmp_ngt_f32 vcc, s71, v24
ds_read_b128 a[240:243], v2 offset:61440
v_mfma_f32_32x32x16_bf16 a[80:95], a[244:247], v[108:111], a[80:95]
v_cvt_pk_bf16_f32 v105, v122, v123
v_exp_f32 v124, v52
s_cbranch_vccnz .Lr48
.Lr48b:
v_mfma_f32_32x32x16_bf16 a[96:111], a[248:251], v[108:111], a[96:111]
v_exp_f32 v125, v53
v_add_f32 v8, v8, v24
ds_read_b128 a[244:247], v2 offset:62464
ds_read_b128 a[248:251], v2 offset:63488
v_mfma_f32_32x32x16_bf16 a[112:127], a[252:255], v[108:111], a[112:127]
v_cvt_pk_bf16_f32 v106, v124, v125
v_exp_f32 v126, v54
ds_read_b128 a[252:255], v2 offset:64512
s_waitcnt lgkmcnt(8)
.Lx1_0:
v_mfma_f32_32x32x16_bf16 v[32:47], a[192:195], a[128:131], v[64:79]
s_add_u32 m0, s36, 24576
v_exp_f32 v127, v55
v_mfma_f32_32x32x16_bf16 v[32:47], a[196:199], a[132:135], v[32:47]
v_cvt_pk_bf16_f32 v107, v126, v127
v_exp_f32 v120, v56
buffer_load_dwordx4 v1, s[20:23], s24 offen offset:0 lds
v_mfma_f32_32x32x16_bf16 v[32:47], a[200:203], a[136:139], v[32:47]
v_exp_f32 v121, v57
v_mfma_f32_16x16x32_bf16 v[24:27], v[28:31], v[104:107], 0
v_mfma_f32_32x32x16_bf16 v[32:47], a[204:207], a[140:143], v[32:47]
v_cvt_pk_bf16_f32 v108, v120, v121
v_exp_f32 v122, v58
buffer_load_dwordx4 v1, s[20:23], s24 offen offset:1024 lds
v_mfma_f32_32x32x16_bf16 v[32:47], a[208:211], a[144:147], v[32:47]
v_exp_f32 v123, v59
buffer_load_dwordx4 v1, s[20:23], s24 offen offset:2048 lds
v_mfma_f32_32x32x16_bf16 v[32:47], a[212:215], a[148:151], v[32:47]
v_cvt_pk_bf16_f32 v109, v122, v123
v_exp_f32 v124, v60
buffer_load_dwordx4 v1, s[20:23], s24 offen offset:3072 lds
s_add_u32 m0, s36, 49152
v_mfma_f32_32x32x16_bf16 v[32:47], a[216:219], a[152:155], v[32:47]
v_exp_f32 v125, v61
v_mfma_f32_32x32x16_bf16 v[32:47], a[220:223], a[156:159], v[32:47]
v_cvt_pk_bf16_f32 v110, v124, v125
v_exp_f32 v126, v62
s_waitcnt lgkmcnt(0)
v_mfma_f32_32x32x16_bf16 a[0:15], a[224:227], v[96:99], a[0:15]
v_exp_f32 v127, v63
buffer_load_dwordx4 v3, s[20:23], s24 offen offset:0 lds
v_mfma_f32_32x32x16_bf16 a[16:31], a[228:231], v[96:99], a[16:31]
v_cvt_pk_bf16_f32 v111, v126, v127
v_exp_f32 v112, v32
buffer_load_dwordx4 v3, s[20:23], s24 offen offset:1024 lds
v_mfma_f32_32x32x16_bf16 a[32:47], a[232:235], v[96:99], a[32:47]
v_exp_f32 v113, v33
v_mfma_f32_16x16x32_bf16 v[24:27], v[28:31], v[108:111], v[24:27]
v_mfma_f32_32x32x16_bf16 a[48:63], a[236:239], v[96:99], a[48:63]
v_cvt_pk_bf16_f32 v96, v112, v113
v_exp_f32 v114, v34
buffer_load_dwordx4 v3, s[20:23], s24 offen offset:2048 lds
v_mfma_f32_32x32x16_bf16 a[0:15], a[240:243], v[100:103], a[0:15]
v_exp_f32 v115, v35
v_cmp_ngt_f32 vcc, s71, v24
v_mfma_f32_32x32x16_bf16 a[16:31], a[244:247], v[100:103], a[16:31]
v_cvt_pk_bf16_f32 v97, v114, v115
v_exp_f32 v116, v36
s_cbranch_vccnz .Lr51
.Lr51b:
v_mfma_f32_32x32x16_bf16 a[32:47], a[248:251], v[100:103], a[32:47]
v_exp_f32 v117, v37
v_add_f32 v9, v9, v24
buffer_load_dwordx4 v3, s[20:23], s24 offen offset:3072 lds
v_mfma_f32_32x32x16_bf16 a[48:63], a[252:255], v[100:103], a[48:63]
v_cvt_pk_bf16_f32 v98, v116, v117
v_exp_f32 v118, v38
s_waitcnt vmcnt(16)
s_barrier
s_add_u32 s24, s24, 0x2000
v_mfma_f32_32x32x16_bf16 v[48:63], a[192:195], a[160:163], v[80:95]
v_exp_f32 v119, v39
ds_read_b128 a[192:195], v2 offset:8192
v_mfma_f32_32x32x16_bf16 v[48:63], a[196:199], a[164:167], v[48:63]
v_cvt_pk_bf16_f32 v99, v118, v119
v_exp_f32 v112, v40
ds_read_b128 a[196:199], v2 offset:9216
v_mfma_f32_32x32x16_bf16 v[48:63], a[200:203], a[168:171], v[48:63]
v_exp_f32 v113, v41
v_mfma_f32_16x16x32_bf16 v[24:27], v[28:31], v[96:99], 0
ds_read_b128 a[200:203], v2 offset:10240
v_mfma_f32_32x32x16_bf16 v[48:63], a[204:207], a[172:175], v[48:63]
v_cvt_pk_bf16_f32 v100, v112, v113
v_exp_f32 v114, v42
ds_read_b128 a[204:207], v2 offset:11264
v_mfma_f32_32x32x16_bf16 v[48:63], a[208:211], a[176:179], v[48:63]
v_exp_f32 v115, v43
ds_read_b128 a[208:211], v2 offset:12288
v_mfma_f32_32x32x16_bf16 v[48:63], a[212:215], a[180:183], v[48:63]
v_cvt_pk_bf16_f32 v101, v114, v115
v_exp_f32 v116, v44
ds_read_b128 a[212:215], v2 offset:13312
v_mfma_f32_32x32x16_bf16 v[48:63], a[216:219], a[184:187], v[48:63]
v_exp_f32 v117, v45
ds_read_b128 a[216:219], v2 offset:14336
v_mfma_f32_32x32x16_bf16 v[48:63], a[220:223], a[188:191], v[48:63]
v_cvt_pk_bf16_f32 v102, v116, v117
v_exp_f32 v118, v46
ds_read_b128 a[220:223], v2 offset:15360
v_mfma_f32_32x32x16_bf16 a[64:79], a[224:227], v[104:107], a[64:79]
v_exp_f32 v119, v47
ds_read_b128 a[224:227], v2 offset:32768
v_mfma_f32_32x32x16_bf16 a[80:95], a[228:231], v[104:107], a[80:95]
v_cvt_pk_bf16_f32 v103, v118, v119
v_exp_f32 v120, v48
ds_read_b128 a[228:231], v2 offset:33792
v_mfma_f32_32x32x16_bf16 a[96:111], a[232:235], v[104:107], a[96:111]
v_mfma_f32_16x16x32_bf16 v[24:27], v[28:31], v[100:103], v[24:27]
v_exp_f32 v121, v49
ds_read_b128 a[232:235], v2 offset:34816
v_mfma_f32_32x32x16_bf16 a[112:127], a[236:239], v[104:107], a[112:127]
v_cvt_pk_bf16_f32 v104, v120, v121
v_exp_f32 v122, v50
ds_read_b128 a[236:239], v2 offset:35840
v_mfma_f32_32x32x16_bf16 a[64:79], a[240:243], v[108:111], a[64:79]
v_exp_f32 v123, v51
v_cmp_ngt_f32 vcc, s71, v24
ds_read_b128 a[240:243], v2 offset:36864
v_mfma_f32_32x32x16_bf16 a[80:95], a[244:247], v[108:111], a[80:95]
v_cvt_pk_bf16_f32 v105, v122, v123
v_exp_f32 v124, v52
s_cbranch_vccnz .Lr50
.Lr50b:
v_mfma_f32_32x32x16_bf16 a[96:111], a[248:251], v[108:111], a[96:111]
v_exp_f32 v125, v53
v_add_f32 v8, v8, v24
ds_read_b128 a[244:247], v2 offset:37888
ds_read_b128 a[248:251], v2 offset:38912
v_mfma_f32_32x32x16_bf16 a[112:127], a[252:255], v[108:111], a[112:127]
v_cvt_pk_bf16_f32 v106, v124, v125
v_exp_f32 v126, v54
ds_read_b128 a[252:255], v2 offset:39936
s_waitcnt lgkmcnt(8)
.Lx1_1:
v_mfma_f32_32x32x16_bf16 v[32:47], a[192:195], a[128:131], v[64:79]
s_add_u32 m0, s36, 0
v_exp_f32 v127, v55
v_mfma_f32_32x32x16_bf16 v[32:47], a[196:199], a[132:135], v[32:47]
v_cvt_pk_bf16_f32 v107, v126, v127
v_exp_f32 v120, v56
buffer_load_dwordx4 v1, s[20:23], s24 offen offset:0 lds
v_mfma_f32_32x32x16_bf16 v[32:47], a[200:203], a[136:139], v[32:47]
v_exp_f32 v121, v57
v_mfma_f32_16x16x32_bf16 v[24:27], v[28:31], v[104:107], 0
v_mfma_f32_32x32x16_bf16 v[32:47], a[204:207], a[140:143], v[32:47]
v_cvt_pk_bf16_f32 v108, v120, v121
v_exp_f32 v122, v58
buffer_load_dwordx4 v1, s[20:23], s24 offen offset:1024 lds
v_mfma_f32_32x32x16_bf16 v[32:47], a[208:211], a[144:147], v[32:47]
v_exp_f32 v123, v59
buffer_load_dwordx4 v1, s[20:23], s24 offen offset:2048 lds
v_mfma_f32_32x32x16_bf16 v[32:47], a[212:215], a[148:151], v[32:47]
v_cvt_pk_bf16_f32 v109, v122, v123
v_exp_f32 v124, v60
buffer_load_dwordx4 v1, s[20:23], s24 offen offset:3072 lds
s_add_u32 m0, s36, 57344
v_mfma_f32_32x32x16_bf16 v[32:47], a[216:219], a[152:155], v[32:47]
v_exp_f32 v125, v61
v_mfma_f32_32x32x16_bf16 v[32:47], a[220:223], a[156:159], v[32:47]
v_cvt_pk_bf16_f32 v110, v124, v125
v_exp_f32 v126, v62
s_waitcnt lgkmcnt(0)
v_mfma_f32_32x32x16_bf16 a[0:15], a[224:227], v[96:99], a[0:15]
v_exp_f32 v127, v63
buffer_load_dwordx4 v3, s[20:23], s24 offen offset:0 lds
v_mfma_f32_32x32x16_bf16 a[16:31], a[228:231], v[96:99], a[16:31]
v_cvt_pk_bf16_f32 v111, v126, v127
v_exp_f32 v112, v32
buffer_load_dwordx4 v3, s[20:23], s24 offen offset:1024 lds
v_mfma_f32_32x32x16_bf16 a[32:47], a[232:235], v[96:99], a[32:47]
v_exp_f32 v113, v33
v_mfma_f32_16x16x32_bf16 v[24:27], v[28:31], v[108:111], v[24:27]
v_mfma_f32_32x32x16_bf16 a[48:63], a[236:239], v[96:99], a[48:63]
v_cvt_pk_bf16_f32 v96, v112, v113
v_exp_f32 v114, v34
buffer_load_dwordx4 v3, s[20:23], s24 offen offset:2048 lds
v_mfma_f32_32x32x16_bf16 a[0:15], a[240:243], v[100:103], a[0:15]
v_exp_f32 v115, v35
v_cmp_ngt_f32 vcc, s71, v24
v_mfma_f32_32x32x16_bf16 a[16:31], a[244:247], v[100:103], a[16:31]
v_cvt_pk_bf16_f32 v97, v114, v115
v_exp_f32 v116, v36
s_cbranch_vccnz .Lr53
.Lr53b:
v_mfma_f32_32x32x16_bf16 a[32:47], a[248:251], v[100:103], a[32:47]
v_exp_f32 v117, v37
v_add_f32 v9, v9, v24
buffer_load_dwordx4 v3, s[20:23], s24 offen offset:3072 lds
v_mfma_f32_32x32x16_bf16 a[48:63], a[252:255], v[100:103], a[48:63]
v_cvt_pk_bf16_f32 v98, v116, v117
v_exp_f32 v118, v38
s_waitcnt vmcnt(16)
s_barrier
s_add_u32 s24, s24, 0x2000
v_mfma_f32_32x32x16_bf16 v[48:63], a[192:195], a[160:163], v[80:95]
v_exp_f32 v119, v39
ds_read_b128 a[192:195], v2 offset:16384
v_mfma_f32_32x32x16_bf16 v[48:63], a[196:199], a[164:167], v[48:63]
v_cvt_pk_bf16_f32 v99, v118, v119
v_exp_f32 v112, v40
ds_read_b128 a[196:199], v2 offset:17408
v_mfma_f32_32x32x16_bf16 v[48:63], a[200:203], a[168:171], v[48:63]
v_exp_f32 v113, v41
v_mfma_f32_16x16x32_bf16 v[24:27], v[28:31], v[96:99], 0
ds_read_b128 a[200:203], v2 offset:18432
v_mfma_f32_32x32x16_bf16 v[48:63], a[204:207], a[172:175], v[48:63]
v_cvt_pk_bf16_f32 v100, v112, v113
v_exp_f32 v114, v42
ds_read_b128 a[204:207], v2 offset:19456
v_mfma_f32_32x32x16_bf16 v[48:63], a[208:211], a[176:179], v[48:63]
v_exp_f32 v115, v43
ds_read_b128 a[208:211], v2 offset:20480
v_mfma_f32_32x32x16_bf16 v[48:63], a[212:215], a[180:183], v[48:63]
v_cvt_pk_bf16_f32 v101, v114, v115
v_exp_f32 v116, v44
ds_read_b128 a[212:215], v2 offset:21504
v_mfma_f32_32x32x16_bf16 v[48:63], a[216:219], a[184:187], v[48:63]
v_exp_f32 v117, v45
ds_read_b128 a[216:219], v2 offset:22528
v_mfma_f32_32x32x16_bf16 v[48:63], a[220:223], a[188:191], v[48:63]
v_cvt_pk_bf16_f32 v102, v116, v117
v_exp_f32 v118, v46
ds_read_b128 a[220:223], v2 offset:23552
v_mfma_f32_32x32x16_bf16 a[64:79], a[224:227], v[104:107], a[64:79]
v_exp_f32 v119, v47
ds_read_b128 a[224:227], v2 offset:40960
v_mfma_f32_32x32x16_bf16 a[80:95], a[228:231], v[104:107], a[80:95]
v_cvt_pk_bf16_f32 v103, v118, v119
v_exp_f32 v120, v48
ds_read_b128 a[228:231], v2 offset:41984
v_mfma_f32_32x32x16_bf16 a[96:111], a[232:235], v[104:107], a[96:111]
v_mfma_f32_16x16x32_bf16 v[24:27], v[28:31], v[100:103], v[24:27]
v_exp_f32 v121, v49
ds_read_b128 a[232:235], v2 offset:43008
v_mfma_f32_32x32x16_bf16 a[112:127], a[236:239], v[104:107], a[112:127]
v_cvt_pk_bf16_f32 v104, v120, v121
v_exp_f32 v122, v50
ds_read_b128 a[236:239], v2 offset:44032
v_mfma_f32_32x32x16_bf16 a[64:79], a[240:243], v[108:111], a[64:79]
v_exp_f32 v123, v51
v_cmp_ngt_f32 vcc, s71, v24
ds_read_b128 a[240:243], v2 offset:45056
v_mfma_f32_32x32x16_bf16 a[80:95], a[244:247], v[108:111], a[80:95]
v_cvt_pk_bf16_f32 v105, v122, v123
v_exp_f32 v124, v52
s_cbranch_vccnz .Lr52
.Lr52b:
v_mfma_f32_32x32x16_bf16 a[96:111], a[248:251], v[108:111], a[96:111]
v_exp_f32 v125, v53
v_add_f32 v8, v8, v24
ds_read_b128 a[244:247], v2 offset:46080
ds_read_b128 a[248:251], v2 offset:47104
v_mfma_f32_32x32x16_bf16 a[112:127], a[252:255], v[108:111], a[112:127]
v_cvt_pk_bf16_f32 v106, v124, v125
v_exp_f32 v126, v54
ds_read_b128 a[252:255], v2 offset:48128
s_waitcnt lgkmcnt(8)
s_cmp_lg_u32 s24, s40
s_cbranch_scc1 .Lx1_2
v_mfma_f32_32x32x16_bf16 v[32:47], a[192:195], a[128:131], v[64:79]
s_add_u32 m0, s36, 8192
v_exp_f32 v127, v55
v_mfma_f32_32x32x16_bf16 v[32:47], a[196:199], a[132:135], v[32:47]
v_cvt_pk_bf16_f32 v107, v126, v127
v_exp_f32 v120, v56
buffer_load_dwordx4 v1, s[20:23], s24 offen offset:0 lds
v_mfma_f32_32x32x16_bf16 v[32:47], a[200:203], a[136:139], v[32:47]
v_exp_f32 v121, v57
v_mfma_f32_16x16x32_bf16 v[24:27], v[28:31], v[104:107], 0
v_mfma_f32_32x32x16_bf16 v[32:47], a[204:207], a[140:143], v[32:47]
v_cvt_pk_bf16_f32 v108, v120, v121
v_exp_f32 v122, v58
buffer_load_dwordx4 v1, s[20:23], s24 offen offset:1024 lds
v_mfma_f32_32x32x16_bf16 v[32:47], a[208:211], a[144:147], v[32:47]
v_exp_f32 v123, v59
buffer_load_dwordx4 v1, s[20:23], s24 offen offset:2048 lds
v_mfma_f32_32x32x16_bf16 v[32:47], a[212:215], a[148:151], v[32:47]
v_cvt_pk_bf16_f32 v109, v122, v123
v_exp_f32 v124, v60
buffer_load_dwordx4 v1, s[20:23], s24 offen offset:3072 lds
s_add_u32 m0, s36, 32768
v_mfma_f32_32x32x16_bf16 v[32:47], a[216:219], a[152:155], v[32:47]
v_exp_f32 v125, v61
v_mfma_f32_32x32x16_bf16 v[32:47], a[220:223], a[156:159], v[32:47]
v_cvt_pk_bf16_f32 v110, v124, v125
v_exp_f32 v126, v62
s_waitcnt lgkmcnt(0)
v_mfma_f32_32x32x16_bf16 a[0:15], a[224:227], v[96:99], a[0:15]
v_exp_f32 v127, v63
buffer_load_dwordx4 v3, s[20:23], s24 offen offset:0 lds
v_mfma_f32_32x32x16_bf16 a[16:31], a[228:231], v[96:99], a[16:31]
v_cvt_pk_bf16_f32 v111, v126, v127
v_exp_f32 v112, v32
buffer_load_dwordx4 v3, s[20:23], s24 offen offset:1024 lds
v_mfma_f32_32x32x16_bf16 a[32:47], a[232:235], v[96:99], a[32:47]
v_exp_f32 v113, v33
v_mfma_f32_16x16x32_bf16 v[24:27], v[28:31], v[108:111], v[24:27]
v_mfma_f32_32x32x16_bf16 a[48:63], a[236:239], v[96:99], a[48:63]
v_cvt_pk_bf16_f32 v96, v112, v113
v_exp_f32 v114, v34
buffer_load_dwordx4 v3, s[20:23], s24 offen offset:2048 lds
v_mfma_f32_32x32x16_bf16 a[0:15], a[240:243], v[100:103], a[0:15]
v_exp_f32 v115, v35
v_cmp_ngt_f32 vcc, s71, v24
v_mfma_f32_32x32x16_bf16 a[16:31], a[244:247], v[100:103], a[16:31]
v_cvt_pk_bf16_f32 v97, v114, v115
v_exp_f32 v116, v36
s_cbranch_vccnz .Lr78
.Lr78b:
v_mfma_f32_32x32x16_bf16 a[32:47], a[248:251], v[100:103], a[32:47]
v_exp_f32 v117, v37
v_add_f32 v9, v9, v24
buffer_load_dwordx4 v3, s[20:23], s24 offen offset:3072 lds
v_mfma_f32_32x32x16_bf16 a[48:63], a[252:255], v[100:103], a[48:63]
v_cvt_pk_bf16_f32 v98, v116, v117
v_exp_f32 v118, v38
s_waitcnt vmcnt(16)
s_barrier
s_add_u32 s24, s24, 0x2000
v_mfma_f32_32x32x16_bf16 v[48:63], a[192:195], a[160:163], v[80:95]
v_exp_f32 v119, v39
ds_read_b128 a[192:195], v2 offset:24576
v_mfma_f32_32x32x16_bf16 v[48:63], a[196:199], a[164:167], v[48:63]
v_cvt_pk_bf16_f32 v99, v118, v119
v_exp_f32 v112, v40
ds_read_b128 a[196:199], v2 offset:25600
v_mfma_f32_32x32x16_bf16 v[48:63], a[200:203], a[168:171], v[48:63]
v_exp_f32 v113, v41
v_mfma_f32_16x16x32_bf16 v[24:27], v[28:31], v[96:99], 0
ds_read_b128 a[200:203], v2 offset:26624
v_mfma_f32_32x32x16_bf16 v[48:63], a[204:207], a[172:175], v[48:63]
v_cvt_pk_bf16_f32 v100, v112, v113
v_exp_f32 v114, v42
ds_read_b128 a[204:207], v2 offset:27648
v_mfma_f32_32x32x16_bf16 v[48:63], a[208:211], a[176:179], v[48:63]
v_exp_f32 v115, v43
ds_read_b128 a[208:211], v2 offset:28672
v_mfma_f32_32x32x16_bf16 v[48:63], a[212:215], a[180:183], v[48:63]
v_cvt_pk_bf16_f32 v101, v114, v115
v_exp_f32 v116, v44
ds_read_b128 a[212:215], v2 offset:29696
v_mfma_f32_32x32x16_bf16 v[48:63], a[216:219], a[184:187], v[48:63]
v_exp_f32 v117, v45
ds_read_b128 a[216:219], v2 offset:30720
v_mfma_f32_32x32x16_bf16 v[48:63], a[220:223], a[188:191], v[48:63]
v_cvt_pk_bf16_f32 v102, v116, v117
v_exp_f32 v118, v46
ds_read_b128 a[220:223], v2 offset:31744
v_mfma_f32_32x32x16_bf16 a[64:79], a[224:227], v[104:107], a[64:79]
v_exp_f32 v119, v47
ds_read_b128 a[224:227], v2 offset:49152
v_mfma_f32_32x32x16_bf16 a[80:95], a[228:231], v[104:107], a[80:95]
v_cvt_pk_bf16_f32 v103, v118, v119
v_exp_f32 v120, v48
ds_read_b128 a[228:231], v2 offset:50176
v_mfma_f32_32x32x16_bf16 a[96:111], a[232:235], v[104:107], a[96:111]
v_mfma_f32_16x16x32_bf16 v[24:27], v[28:31], v[100:103], v[24:27]
v_exp_f32 v121, v49
ds_read_b128 a[232:235], v2 offset:51200
v_mfma_f32_32x32x16_bf16 a[112:127], a[236:239], v[104:107], a[112:127]
v_cvt_pk_bf16_f32 v104, v120, v121
v_exp_f32 v122, v50
ds_read_b128 a[236:239], v2 offset:52224
v_mfma_f32_32x32x16_bf16 a[64:79], a[240:243], v[108:111], a[64:79]
v_exp_f32 v123, v51
v_cmp_ngt_f32 vcc, s71, v24
ds_read_b128 a[240:243], v2 offset:53248
v_mfma_f32_32x32x16_bf16 a[80:95], a[244:247], v[108:111], a[80:95]
v_cvt_pk_bf16_f32 v105, v122, v123
v_exp_f32 v124, v52
s_cbranch_vccnz .Lr77
.Lr77b:
v_mfma_f32_32x32x16_bf16 a[96:111], a[248:251], v[108:111], a[96:111]
v_exp_f32 v125, v53
v_add_f32 v8, v8, v24
ds_read_b128 a[244:247], v2 offset:54272
ds_read_b128 a[248:251], v2 offset:55296
v_mfma_f32_32x32x16_bf16 a[112:127], a[252:255], v[108:111], a[112:127]
v_cvt_pk_bf16_f32 v106, v124, v125
v_exp_f32 v126, v54
ds_read_b128 a[252:255], v2 offset:56320
s_waitcnt lgkmcnt(8)
.Lxl1_1:
v_mfma_f32_32x32x16_bf16 v[32:47], a[192:195], a[128:131], v[64:79]
s_add_u32 m0, s36, 16384
v_exp_f32 v127, v55
v_mfma_f32_32x32x16_bf16 v[32:47], a[196:199], a[132:135], v[32:47]
v_cvt_pk_bf16_f32 v107, v126, v127
v_exp_f32 v120, v56
buffer_load_dwordx4 v1, s[20:23], s24 offen offset:0 lds
v_mfma_f32_32x32x16_bf16 v[32:47], a[200:203], a[136:139], v[32:47]
v_exp_f32 v121, v57
v_mfma_f32_16x16x32_bf16 v[24:27], v[28:31], v[104:107], 0
v_mfma_f32_32x32x16_bf16 v[32:47], a[204:207], a[140:143], v[32:47]
v_cvt_pk_bf16_f32 v108, v120, v121
v_exp_f32 v122, v58
buffer_load_dwordx4 v1, s[20:23], s24 offen offset:1024 lds
v_mfma_f32_32x32x16_bf16 v[32:47], a[208:211], a[144:147], v[32:47]
v_exp_f32 v123, v59
buffer_load_dwordx4 v1, s[20:23], s24 offen offset:2048 lds
v_mfma_f32_32x32x16_bf16 v[32:47], a[212:215], a[148:151], v[32:47]
v_cvt_pk_bf16_f32 v109, v122, v123
v_exp_f32 v124, v60
buffer_load_dwordx4 v1, s[20:23], s24 offen offset:3072 lds
s_add_u32 m0, s36, 40960
v_mfma_f32_32x32x16_bf16 v[32:47], a[216:219], a[152:155], v[32:47]
v_exp_f32 v125, v61
v_mfma_f32_32x32x16_bf16 v[32:47], a[220:223], a[156:159], v[32:47]
v_cvt_pk_bf16_f32 v110, v124, v125
v_exp_f32 v126, v62
s_waitcnt lgkmcnt(0)
v_mfma_f32_32x32x16_bf16 a[0:15], a[224:227], v[96:99], a[0:15]
v_exp_f32 v127, v63
buffer_load_dwordx4 v3, s[20:23], s24 offen offset:0 lds
v_mfma_f32_32x32x16_bf16 a[16:31], a[228:231], v[96:99], a[16:31]
v_cvt_pk_bf16_f32 v111, v126, v127
v_exp_f32 v112, v32
buffer_load_dwordx4 v3, s[20:23], s24 offen offset:1024 lds
v_mfma_f32_32x32x16_bf16 a[32:47], a[232:235], v[96:99], a[32:47]
v_exp_f32 v113, v33
v_mfma_f32_16x16x32_bf16 v[24:27], v[28:31], v[108:111], v[24:27]
v_mfma_f32_32x32x16_bf16 a[48:63], a[236:239], v[96:99], a[48:63]
v_cvt_pk_bf16_f32 v96, v112, v113
v_exp_f32 v114, v34
buffer_load_dwordx4 v3, s[20:23], s24 offen offset:2048 lds
v_mfma_f32_32x32x16_bf16 a[0:15], a[240:243], v[100:103], a[0:15]
v_exp_f32 v115, v35
v_cmp_ngt_f32 vcc, s71, v24
v_mfma_f32_32x32x16_bf16 a[16:31], a[244:247], v[100:103], a[16:31]
v_cvt_pk_bf16_f32 v97, v114, v115
v_exp_f32 v116, v36
s_cbranch_vccnz .Lr80
.Lr80b:
v_mfma_f32_32x32x16_bf16 a[32:47], a[248:251], v[100:103], a[32:47]
v_exp_f32 v117, v37
v_add_f32 v9, v9, v24
buffer_load_dwordx4 v3, s[20:23], s24 offen offset:3072 lds
v_mfma_f32_32x32x16_bf16 a[48:63], a[252:255], v[100:103], a[48:63]
v_cvt_pk_bf16_f32 v98, v116, v117
v_exp_f32 v118, v38
s_waitcnt vmcnt(16)
s_barrier
s_add_u32 s24, s24, 0x2000
v_mfma_f32_32x32x16_bf16 v[48:63], a[192:195], a[160:163], v[80:95]
v_exp_f32 v119, v39
ds_read_b128 a[192:195], v2 offset:0
v_mfma_f32_32x32x16_bf16 v[48:63], a[196:199], a[164:167], v[48:63]
v_cvt_pk_bf16_f32 v99, v118, v119
v_exp_f32 v112, v40
ds_read_b128 a[196:199], v2 offset:1024
v_mfma_f32_32x32x16_bf16 v[48:63], a[200:203], a[168:171], v[48:63]
v_exp_f32 v113, v41
v_mfma_f32_16x16x32_bf16 v[24:27], v[28:31], v[96:99], 0
ds_read_b128 a[200:203], v2 offset:2048
v_mfma_f32_32x32x16_bf16 v[48:63], a[204:207], a[172:175], v[48:63]
v_cvt_pk_bf16_f32 v100, v112, v113
v_exp_f32 v114, v42
ds_read_b128 a[204:207], v2 offset:3072
v_mfma_f32_32x32x16_bf16 v[48:63], a[208:211], a[176:179], v[48:63]
v_exp_f32 v115, v43
ds_read_b128 a[208:211], v2 offset:4096
v_mfma_f32_32x32x16_bf16 v[48:63], a[212:215], a[180:183], v[48:63]
v_cvt_pk_bf16_f32 v101, v114, v115
v_exp_f32 v116, v44
ds_read_b128 a[212:215], v2 offset:5120
v_mfma_f32_32x32x16_bf16 v[48:63], a[216:219], a[184:187], v[48:63]
v_exp_f32 v117, v45
ds_read_b128 a[216:219], v2 offset:6144
v_mfma_f32_32x32x16_bf16 v[48:63], a[220:223], a[188:191], v[48:63]
v_cvt_pk_bf16_f32 v102, v116, v117
v_exp_f32 v118, v46
ds_read_b128 a[220:223], v2 offset:7168
v_mfma_f32_32x32x16_bf16 a[64:79], a[224:227], v[104:107], a[64:79]
v_exp_f32 v119, v47
ds_read_b128 a[224:227], v2 offset:57344
v_mfma_f32_32x32x16_bf16 a[80:95], a[228:231], v[104:107], a[80:95]
v_cvt_pk_bf16_f32 v103, v118, v119
v_exp_f32 v120, v48
ds_read_b128 a[228:231], v2 offset:58368
v_mfma_f32_32x32x16_bf16 a[96:111], a[232:235], v[104:107], a[96:111]
v_mfma_f32_16x16x32_bf16 v[24:27], v[28:31], v[100:103], v[24:27]
v_exp_f32 v121, v49
ds_read_b128 a[232:235], v2 offset:59392
v_mfma_f32_32x32x16_bf16 a[112:127], a[236:239], v[104:107], a[112:127]
v_cvt_pk_bf16_f32 v104, v120, v121
v_exp_f32 v122, v50
ds_read_b128 a[236:239], v2 offset:60416
v_mfma_f32_32x32x16_bf16 a[64:79], a[240:243], v[108:111], a[64:79]
v_exp_f32 v123, v51
v_cmp_ngt_f32 vcc, s71, v24
ds_read_b128 a[240:243], v2 offset:61440
v_mfma_f32_32x32x16_bf16 a[80:95], a[244:247], v[108:111], a[80:95]
v_cvt_pk_bf16_f32 v105, v122, v123
v_exp_f32 v124, v52
s_cbranch_vccnz .Lr79
.Lr79b:
v_mfma_f32_32x32x16_bf16 a[96:111], a[248:251], v[108:111], a[96:111]
v_exp_f32 v125, v53
v_add_f32 v8, v8, v24
ds_read_b128 a[244:247], v2 offset:62464
ds_read_b128 a[248:251], v2 offset:63488
v_mfma_f32_32x32x16_bf16 a[112:127], a[252:255], v[108:111], a[112:127]
v_cvt_pk_bf16_f32 v106, v124, v125
v_exp_f32 v126, v54
ds_read_b128 a[252:255], v2 offset:64512
s_waitcnt lgkmcnt(8)
s_mov_b32 s22, 0
v_mfma_f32_32x32x16_bf16 v[32:47], a[192:195], a[128:131], v[64:79]
s_add_u32 m0, s36, 24576
v_exp_f32 v127, v55
v_mfma_f32_32x32x16_bf16 v[32:47], a[196:199], a[132:135], v[32:47]
v_cvt_pk_bf16_f32 v107, v126, v127
v_exp_f32 v120, v56
buffer_load_dwordx4 v1, s[20:23], s24 offen offset:0 lds
v_mfma_f32_32x32x16_bf16 v[32:47], a[200:203], a[136:139], v[32:47]
v_exp_f32 v121, v57
v_mfma_f32_16x16x32_bf16 v[24:27], v[28:31], v[104:107], 0
v_mfma_f32_32x32x16_bf16 v[32:47], a[204:207], a[140:143], v[32:47]
v_cvt_pk_bf16_f32 v108, v120, v121
v_exp_f32 v122, v58
buffer_load_dwordx4 v1, s[20:23], s24 offen offset:1024 lds
v_mfma_f32_32x32x16_bf16 v[32:47], a[208:211], a[144:147], v[32:47]
v_exp_f32 v123, v59
buffer_load_dwordx4 v1, s[20:23], s24 offen offset:2048 lds
v_mfma_f32_32x32x16_bf16 v[32:47], a[212:215], a[148:151], v[32:47]
v_cvt_pk_bf16_f32 v109, v122, v123
v_exp_f32 v124, v60
buffer_load_dwordx4 v1, s[20:23], s24 offen offset:3072 lds
s_add_u32 m0, s36, 49152
v_mfma_f32_32x32x16_bf16 v[32:47], a[216:219], a[152:155], v[32:47]
v_exp_f32 v125, v61
v_mfma_f32_32x32x16_bf16 v[32:47], a[220:223], a[156:159], v[32:47]
v_cvt_pk_bf16_f32 v110, v124, v125
v_exp_f32 v126, v62
s_waitcnt lgkmcnt(0)
v_mfma_f32_32x32x16_bf16 a[0:15], a[224:227], v[96:99], a[0:15]
v_exp_f32 v127, v63
buffer_load_dwordx4 v3, s[20:23], s24 offen offset:0 lds
v_mfma_f32_32x32x16_bf16 a[16:31], a[228:231], v[96:99], a[16:31]
v_cvt_pk_bf16_f32 v111, v126, v127
v_cndmask_b32 v32, v32, v5, s[54:55]
v_cndmask_b32 v33, v33, v5, s[56:57]
v_exp_f32 v112, v32
buffer_load_dwordx4 v3, s[20:23], s24 offen offset:1024 lds
v_mfma_f32_32x32x16_bf16 a[32:47], a[232:235], v[96:99], a[32:47]
v_cndmask_b32 v34, v34, v5, s[58:59]
v_cndmask_b32 v35, v35, v5, s[60:61]
v_exp_f32 v113, v33
v_mfma_f32_16x16x32_bf16 v[24:27], v[28:31], v[108:111], v[24:27]
v_mfma_f32_32x32x16_bf16 a[48:63], a[236:239], v[96:99], a[48:63]
v_cvt_pk_bf16_f32 v96, v112, v113
v_cndmask_b32 v36, v36, v5, s[62:63]
v_cndmask_b32 v37, v37, v5, s[64:65]
v_exp_f32 v114, v34
buffer_load_dwordx4 v3, s[20:23], s24 offen offset:2048 lds
v_mfma_f32_32x32x16_bf16 a[0:15], a[240:243], v[100:103], a[0:15]
v_cndmask_b32 v38, v38, v5, s[66:67]
v_cndmask_b32 v39, v39, v5, s[68:69]
v_exp_f32 v115, v35
v_cmp_ngt_f32 vcc, s71, v24
v_mfma_f32_32x32x16_bf16 a[16:31], a[244:247], v[100:103], a[16:31]
v_cvt_pk_bf16_f32 v97, v114, v115
v_cndmask_b32 v40, v40, v5, s[84:85]
v_cndmask_b32 v41, v41, v5, s[86:87]
v_exp_f32 v116, v36
s_cbranch_vccnz .Lr82
.Lr82b:
v_mfma_f32_32x32x16_bf16 a[32:47], a[248:251], v[100:103], a[32:47]
v_cndmask_b32 v42, v42, v5, s[88:89]
v_cndmask_b32 v43, v43, v5, s[90:91]
v_exp_f32 v117, v37
v_add_f32 v9, v9, v24
buffer_load_dwordx4 v3, s[20:23], s24 offen offset:3072 lds
v_mfma_f32_32x32x16_bf16 a[48:63], a[252:255], v[100:103], a[48:63]
v_cvt_pk_bf16_f32 v98, v116, v117
v_cndmask_b32 v44, v44, v5, s[92:93]
v_cndmask_b32 v45, v45, v5, s[94:95]
v_exp_f32 v118, v38
s_waitcnt vmcnt(16)
s_barrier
s_add_u32 s24, s24, 0x2000
v_mfma_f32_32x32x16_bf16 v[48:63], a[192:195], a[160:163], v[80:95]
v_cndmask_b32 v46, v46, v5, s[96:97]
v_cndmask_b32 v47, v47, v5, s[98:99]
v_exp_f32 v119, v39
ds_read_b128 a[192:195], v2 offset:8192
v_mfma_f32_32x32x16_bf16 v[48:63], a[196:199], a[164:167], v[48:63]
v_cvt_pk_bf16_f32 v99, v118, v119
v_exp_f32 v112, v40
ds_read_b128 a[196:199], v2 offset:9216
v_mfma_f32_32x32x16_bf16 v[48:63], a[200:203], a[168:171], v[48:63]
v_exp_f32 v113, v41
v_mfma_f32_16x16x32_bf16 v[24:27], v[28:31], v[96:99], 0
ds_read_b128 a[200:203], v2 offset:10240
v_mfma_f32_32x32x16_bf16 v[48:63], a[204:207], a[172:175], v[48:63]
v_cvt_pk_bf16_f32 v100, v112, v113
v_exp_f32 v114, v42
ds_read_b128 a[204:207], v2 offset:11264
v_mfma_f32_32x32x16_bf16 v[48:63], a[208:211], a[176:179], v[48:63]
v_exp_f32 v115, v43
ds_read_b128 a[208:211], v2 offset:12288
v_mfma_f32_32x32x16_bf16 v[48:63], a[212:215], a[180:183], v[48:63]
v_cvt_pk_bf16_f32 v101, v114, v115
v_exp_f32 v116, v44
ds_read_b128 a[212:215], v2 offset:13312
v_mfma_f32_32x32x16_bf16 v[48:63], a[216:219], a[184:187], v[48:63]
v_exp_f32 v117, v45
ds_read_b128 a[216:219], v2 offset:14336
v_mfma_f32_32x32x16_bf16 v[48:63], a[220:223], a[188:191], v[48:63]
v_cvt_pk_bf16_f32 v102, v116, v117
v_exp_f32 v118, v46
ds_read_b128 a[220:223], v2 offset:15360
v_mfma_f32_32x32x16_bf16 a[64:79], a[224:227], v[104:107], a[64:79]
v_exp_f32 v119, v47
ds_read_b128 a[224:227], v2 offset:32768
v_mfma_f32_32x32x16_bf16 a[80:95], a[228:231], v[104:107], a[80:95]
v_cvt_pk_bf16_f32 v103, v118, v119
v_exp_f32 v120, v48
ds_read_b128 a[228:231], v2 offset:33792
v_mfma_f32_32x32x16_bf16 a[96:111], a[232:235], v[104:107], a[96:111]
v_mfma_f32_16x16x32_bf16 v[24:27], v[28:31], v[100:103], v[24:27]
v_exp_f32 v121, v49
ds_read_b128 a[232:235], v2 offset:34816
v_mfma_f32_32x32x16_bf16 a[112:127], a[236:239], v[104:107], a[112:127]
v_cvt_pk_bf16_f32 v104, v120, v121
v_exp_f32 v122, v50
ds_read_b128 a[236:239], v2 offset:35840
v_mfma_f32_32x32x16_bf16 a[64:79], a[240:243], v[108:111], a[64:79]
v_exp_f32 v123, v51
v_cmp_ngt_f32 vcc, s71, v24
ds_read_b128 a[240:243], v2 offset:36864
v_mfma_f32_32x32x16_bf16 a[80:95], a[244:247], v[108:111], a[80:95]
v_cvt_pk_bf16_f32 v105, v122, v123
v_exp_f32 v124, v52
s_cbranch_vccnz .Lr81
.Lr81b:
v_mfma_f32_32x32x16_bf16 a[96:111], a[248:251], v[108:111], a[96:111]
v_exp_f32 v125, v53
v_add_f32 v8, v8, v24
ds_read_b128 a[244:247], v2 offset:37888
ds_read_b128 a[248:251], v2 offset:38912
v_mfma_f32_32x32x16_bf16 a[112:127], a[252:255], v[108:111], a[112:127]
v_cvt_pk_bf16_f32 v106, v124, v125
v_exp_f32 v126, v54
ds_read_b128 a[252:255], v2 offset:39936
s_waitcnt lgkmcnt(8)
s_add_u32 m0, s36, 0
v_exp_f32 v127, v55
s_nop 0
v_cvt_pk_bf16_f32 v107, v126, v127
v_exp_f32 v120, v56
buffer_load_dwordx4 v1, s[20:23], s24 offen offset:0 lds
v_exp_f32 v121, v57
v_mfma_f32_16x16x32_bf16 v[24:27], v[28:31], v[104:107], 0
v_cvt_pk_bf16_f32 v108, v120, v121
v_exp_f32 v122, v58
buffer_load_dwordx4 v1, s[20:23], s24 offen offset:1024 lds
v_exp_f32 v123, v59
buffer_load_dwordx4 v1, s[20:23], s24 offen offset:2048 lds
v_cvt_pk_bf16_f32 v109, v122, v123
v_exp_f32 v124, v60
buffer_load_dwordx4 v1, s[20:23], s24 offen offset:3072 lds
s_add_u32 m0, s36, 57344
v_exp_f32 v125, v61
s_nop 0
v_cvt_pk_bf16_f32 v110, v124, v125
v_exp_f32 v126, v62
s_waitcnt lgkmcnt(0)
v_mfma_f32_32x32x16_bf16 a[0:15], a[224:227], v[96:99], a[0:15]
v_exp_f32 v127, v63
buffer_load_dwordx4 v3, s[20:23], s24 offen offset:0 lds
v_mfma_f32_32x32x16_bf16 a[16:31], a[228:231], v[96:99], a[16:31]
v_cvt_pk_bf16_f32 v111, v126, v127
buffer_load_dwordx4 v3, s[20:23], s24 offen offset:1024 lds
v_mfma_f32_32x32x16_bf16 a[32:47], a[232:235], v[96:99], a[32:47]
v_mfma_f32_16x16x32_bf16 v[24:27], v[28:31], v[108:111], v[24:27]
v_mfma_f32_32x32x16_bf16 a[48:63], a[236:239], v[96:99], a[48:63]
buffer_load_dwordx4 v3, s[20:23], s24 offen offset:2048 lds
v_mfma_f32_32x32x16_bf16 a[0:15], a[240:243], v[100:103], a[0:15]
v_cmp_ngt_f32 vcc, s71, v24
v_mfma_f32_32x32x16_bf16 a[16:31], a[244:247], v[100:103], a[16:31]
s_cbranch_vccnz .Lr84
.Lr84b:
v_mfma_f32_32x32x16_bf16 a[32:47], a[248:251], v[100:103], a[32:47]
v_add_f32 v9, v9, v24
buffer_load_dwordx4 v3, s[20:23], s24 offen offset:3072 lds
v_mfma_f32_32x32x16_bf16 a[48:63], a[252:255], v[100:103], a[48:63]
s_waitcnt vmcnt(16)
s_barrier
s_add_u32 s24, s24, 0x2000
v_mfma_f32_32x32x16_bf16 v[48:63], a[192:195], a[160:163], v[80:95]
ds_read_b128 a[192:195], v2 offset:16384
v_mfma_f32_32x32x16_bf16 v[48:63], a[196:199], a[164:167], v[48:63]
ds_read_b128 a[196:199], v2 offset:17408
v_mfma_f32_32x32x16_bf16 v[48:63], a[200:203], a[168:171], v[48:63]
ds_read_b128 a[200:203], v2 offset:18432
v_mfma_f32_32x32x16_bf16 v[48:63], a[204:207], a[172:175], v[48:63]
ds_read_b128 a[204:207], v2 offset:19456
v_mfma_f32_32x32x16_bf16 v[48:63], a[208:211], a[176:179], v[48:63]
ds_read_b128 a[208:211], v2 offset:20480
v_mfma_f32_32x32x16_bf16 v[48:63], a[212:215], a[180:183], v[48:63]
ds_read_b128 a[212:215], v2 offset:21504
v_mfma_f32_32x32x16_bf16 v[48:63], a[216:219], a[184:187], v[48:63]
ds_read_b128 a[216:219], v2 offset:22528
v_mfma_f32_32x32x16_bf16 v[48:63], a[220:223], a[188:191], v[48:63]
ds_read_b128 a[220:223], v2 offset:23552
v_mfma_f32_32x32x16_bf16 a[64:79], a[224:227], v[104:107], a[64:79]
ds_read_b128 a[224:227], v2 offset:40960
v_mfma_f32_32x32x16_bf16 a[80:95], a[228:231], v[104:107], a[80:95]
v_cndmask_b32 v48, v48, v5, s[54:55]
v_cndmask_b32 v49, v49, v5, s[56:57]
v_cndmask_b32 v50, v50, v5, s[58:59]
v_exp_f32 v120, v48
ds_read_b128 a[228:231], v2 offset:41984
v_mfma_f32_32x32x16_bf16 a[96:111], a[232:235], v[104:107], a[96:111]
v_cndmask_b32 v51, v51, v5, s[60:61]
v_cndmask_b32 v52, v52, v5, s[62:63]
v_cndmask_b32 v53, v53, v5, s[64:65]
v_exp_f32 v121, v49
ds_read_b128 a[232:235], v2 offset:43008
v_mfma_f32_32x32x16_bf16 a[112:127], a[236:239], v[104:107], a[112:127]
v_cvt_pk_bf16_f32 v104, v120, v121
v_cndmask_b32 v54, v54, v5, s[66:67]
v_cndmask_b32 v55, v55, v5, s[68:69]
v_exp_f32 v122, v50
ds_read_b128 a[236:239], v2 offset:44032
v_mfma_f32_32x32x16_bf16 a[64:79], a[240:243], v[108:111], a[64:79]
v_cndmask_b32 v56, v56, v5, s[84:85]
v_cndmask_b32 v57, v57, v5, s[86:87]
v_exp_f32 v123, v51
ds_read_b128 a[240:243], v2 offset:45056
v_mfma_f32_32x32x16_bf16 a[80:95], a[244:247], v[108:111], a[80:95]
v_cvt_pk_bf16_f32 v105, v122, v123
v_cndmask_b32 v58, v58, v5, s[88:89]
v_cndmask_b32 v59, v59, v5, s[90:91]
v_exp_f32 v124, v52
v_mfma_f32_32x32x16_bf16 a[96:111], a[248:251], v[108:111], a[96:111]
v_cndmask_b32 v60, v60, v5, s[92:93]
v_cndmask_b32 v61, v61, v5, s[94:95]
v_exp_f32 v125, v53
ds_read_b128 a[244:247], v2 offset:46080
ds_read_b128 a[248:251], v2 offset:47104
v_mfma_f32_32x32x16_bf16 a[112:127], a[252:255], v[108:111], a[112:127]
v_cvt_pk_bf16_f32 v106, v124, v125
v_cndmask_b32 v62, v62, v5, s[96:97]
v_cndmask_b32 v63, v63, v5, s[98:99]
v_exp_f32 v126, v54
ds_read_b128 a[252:255], v2 offset:48128
s_waitcnt lgkmcnt(8)
.LdA1:
v_exp_f32 v127, v55
v_exp_f32 v120, v56
v_cvt_pk_bf16_f32 v107, v126, v127
s_and_b32 s47, s51, 1
s_lshl_b32 s72, s47, 14
s_add_u32 s72, s72, 131072
v_add_u32 v22, s72, v1
s_lshl_b32 s72, s47, 16
s_add_u32 s72, s72, 8192
s_waitcnt lgkmcnt(0)
v_exp_f32 v121, v57
v_mfma_f32_16x16x32_bf16 v[24:27], v[28:31], v[104:107], 0
v_exp_f32 v122, v58
v_cvt_pk_bf16_f32 v108, v120, v121
v_exp_f32 v123, v59
v_exp_f32 v124, v60
v_cvt_pk_bf16_f32 v109, v122, v123
v_exp_f32 v125, v61
v_exp_f32 v126, v62
v_cvt_pk_bf16_f32 v110, v124, v125
v_exp_f32 v127, v63
v_add_u32 v23, s72, v1
v_cvt_pk_bf16_f32 v111, v126, v127
s_lshl_b32 s72, s51, 10
s_nop 0
v_mfma_f32_16x16x32_bf16 v[24:27], v[28:31], v[108:111], v[24:27]
s_add_u32 s72, s72, 32768
v_lshrrev_b32 v21, 1, v1
v_add_u32 v21, s72, v21
s_lshl_b32 s47, s47, 1
s_sub_u32 s29, 64, s48
s_add_u32 s29, s29, s47
s_lshl_b32 s29, s29, 18
s_lshl_b32 s72, s49, 13
s_add_u32 s29, s29, s72
s_add_u32 s29, s29, 0x3000000
s_add_u32 s72, s29, 4096
v_cmp_ngt_f32 vcc, s71, v24
v_accvgpr_read_b32 v128, a0
v_accvgpr_read_b32 v129, a1
v_accvgpr_read_b32 v130, a2
v_accvgpr_read_b32 v131, a3
s_cbranch_vccnz .Lr6
.Lr6b:
v_add_f32 v9, v9, v24
v_accvgpr_read_b32 v132, a4
v_accvgpr_read_b32 v133, a5
v_accvgpr_read_b32 v134, a6
v_accvgpr_read_b32 v135, a7
v_mfma_f32_32x32x16_bf16 a[64:79], a[224:227], v[104:107], a[64:79]
v_mov_b32 v18, v13
v_mov_b32 v19, v9
ds_write_b64 v21, v[18:19]
v_accvgpr_read_b32 v136, a8
v_accvgpr_read_b32 v137, a9
v_accvgpr_read_b32 v138, a10
v_accvgpr_read_b32 v139, a11
v_accvgpr_read_b32 v140, a12
v_accvgpr_read_b32 v141, a13
v_accvgpr_read_b32 v142, a14
v_mfma_f32_32x32x16_bf16 a[80:95], a[228:231], v[104:107], a[80:95]
v_accvgpr_read_b32 v143, a15
v_accvgpr_read_b32 v144, a16
v_accvgpr_read_b32 v145, a17
v_accvgpr_read_b32 v146, a18
v_accvgpr_read_b32 v147, a19
v_accvgpr_read_b32 v148, a20
v_accvgpr_read_b32 v149, a21
v_mfma_f32_32x32x16_bf16 a[96:111], a[232:235], v[104:107], a[96:111]
v_accvgpr_read_b32 v150, a22
v_accvgpr_read_b32 v151, a23
v_accvgpr_read_b32 v152, a24
v_accvgpr_read_b32 v153, a25
v_accvgpr_read_b32 v154, a26
v_accvgpr_read_b32 v155, a27
v_accvgpr_read_b32 v156, a28
v_mfma_f32_32x32x16_bf16 a[112:127], a[236:239], v[104:107], a[112:127]
v_accvgpr_read_b32 v157, a29
v_accvgpr_read_b32 v158, a30
v_accvgpr_read_b32 v159, a31
v_accvgpr_read_b32 v160, a32
v_accvgpr_read_b32 v161, a33
v_accvgpr_read_b32 v162, a34
v_accvgpr_read_b32 v163, a35
v_mfma_f32_32x32x16_bf16 a[64:79], a[240:243], v[108:111], a[64:79]
v_accvgpr_read_b32 v164, a36
v_accvgpr_read_b32 v165, a37
v_accvgpr_read_b32 v166, a38
v_accvgpr_read_b32 v167, a39
v_accvgpr_read_b32 v168, a40
v_accvgpr_read_b32 v169, a41
v_accvgpr_read_b32 v170, a42
v_mfma_f32_32x32x16_bf16 a[80:95], a[244:247], v[108:111], a[80:95]
v_accvgpr_read_b32 v171, a43
v_accvgpr_read_b32 v172, a44
v_accvgpr_read_b32 v173, a45
v_accvgpr_read_b32 v174, a46
v_accvgpr_read_b32 v175, a47
v_accvgpr_read_b32 v176, a48
v_accvgpr_read_b32 v177, a49
v_mfma_f32_32x32x16_bf16 a[96:111], a[248:251], v[108:111], a[96:111]
v_accvgpr_read_b32 v178, a50
v_accvgpr_read_b32 v179, a51
v_accvgpr_read_b32 v180, a52
ds_write_b128 v23, a[64:67] offset:0
ds_write_b128 v23, a[68:71] offset:1024
ds_write_b128 v23, a[72:75] offset:2048
ds_write_b128 v23, a[76:79] offset:3072
v_mfma_f32_32x32x16_bf16 a[112:127], a[252:255], v[108:111], a[112:127]
v_accvgpr_read_b32 v181, a53
v_accvgpr_read_b32 v182, a54
v_accvgpr_read_b32 v183, a55
ds_write_b128 v23, a[80:83] offset:4096
ds_write_b128 v23, a[84:87] offset:5120
ds_write_b128 v23, a[88:91] offset:6144
ds_write_b128 v23, a[92:95] offset:7168
v_accvgpr_read_b32 v184, a56
v_accvgpr_read_b32 v185, a57
v_accvgpr_read_b32 v186, a58
v_accvgpr_read_b32 v187, a59
ds_write_b128 v23, a[96:99] offset:8192
ds_write_b128 v23, a[100:103] offset:9216
ds_write_b128 v23, a[104:107] offset:10240
ds_write_b128 v23, a[108:111] offset:11264
v_accvgpr_read_b32 v188, a60
v_accvgpr_read_b32 v189, a61
v_accvgpr_read_b32 v190, a62
v_accvgpr_read_b32 v191, a63
ds_write_b128 v23, a[112:115] offset:12288
ds_write_b128 v23, a[116:119] offset:13312
ds_write_b128 v23, a[120:123] offset:14336
ds_write_b128 v23, a[124:127] offset:15360
s_waitcnt lgkmcnt(0)
s_barrier
s_branch .LfA
.Len2:
s_cmp_ge_u32 s48, 20
s_cbranch_scc1 .Lp2
.Lx2_2:
v_mfma_f32_32x32x16_bf16 v[32:47], a[192:195], a[128:131], v[64:79]
s_add_u32 m0, s36, 8192
v_exp_f32 v127, v55
buffer_load_dwordx4 v1, s[20:23], s24 offen offset:0 lds
v_mfma_f32_32x32x16_bf16 v[32:47], a[196:199], a[132:135], v[32:47]
v_cvt_pk_bf16_f32 v107, v126, v127
v_exp_f32 v120, v56
buffer_load_dwordx4 v1, s[20:23], s24 offen offset:1024 lds
v_mfma_f32_32x32x16_bf16 v[32:47], a[200:203], a[136:139], v[32:47]
v_exp_f32 v121, v57
v_mfma_f32_16x16x32_bf16 v[24:27], v[28:31], v[104:107], 0
v_mfma_f32_32x32x16_bf16 v[32:47], a[204:207], a[140:143], v[32:47]
v_cvt_pk_bf16_f32 v108, v120, v121
v_exp_f32 v122, v58
v_mfma_f32_32x32x16_bf16 v[32:47], a[208:211], a[144:147], v[32:47]
v_exp_f32 v123, v59
buffer_load_dwordx4 v1, s[20:23], s24 offen offset:2048 lds
v_mfma_f32_32x32x16_bf16 v[32:47], a[212:215], a[148:151], v[32:47]
v_cvt_pk_bf16_f32 v109, v122, v123
v_exp_f32 v124, v60
v_mfma_f32_32x32x16_bf16 v[32:47], a[216:219], a[152:155], v[32:47]
v_exp_f32 v125, v61
buffer_load_dwordx4 v1, s[20:23], s24 offen offset:3072 lds
s_add_u32 m0, s36, 32768
v_mfma_f32_32x32x16_bf16 v[32:47], a[220:223], a[156:159], v[32:47]
v_cvt_pk_bf16_f32 v110, v124, v125
v_exp_f32 v126, v62
s_waitcnt lgkmcnt(0)
v_mfma_f32_32x32x16_bf16 v[128:143], a[224:227], v[96:99], v[128:143]
v_exp_f32 v127, v63
buffer_load_dwordx4 v3, s[20:23], s24 offen offset:0 lds
v_mfma_f32_32x32x16_bf16 v[144:159], a[228:231], v[96:99], v[144:159]
v_cvt_pk_bf16_f32 v111, v126, v127
v_exp_f32 v112, v32
buffer_load_dwordx4 v3, s[20:23], s24 offen offset:1024 lds
v_mfma_f32_32x32x16_bf16 v[160:175], a[232:235], v[96:99], v[160:175]
v_exp_f32 v113, v33
v_mfma_f32_16x16x32_bf16 v[24:27], v[28:31], v[108:111], v[24:27]
buffer_load_dwordx4 v3, s[20:23], s24 offen offset:2048 lds
v_mfma_f32_32x32x16_bf16 v[176:191], a[236:239], v[96:99], v[176:191]
v_cvt_pk_bf16_f32 v96, v112, v113
v_exp_f32 v114, v34
v_mfma_f32_32x32x16_bf16 v[128:143], a[240:243], v[100:103], v[128:143]
v_exp_f32 v115, v35
v_cmp_ngt_f32 vcc, s71, v24
s_cmp_ge_u32 s24, s43
v_mfma_f32_32x32x16_bf16 v[144:159], a[244:247], v[100:103], v[144:159]
v_cvt_pk_bf16_f32 v97, v114, v115
v_exp_f32 v116, v36
s_cbranch_vccnz .Lr86
.Lr86b:
s_cbranch_scc1 .Ls86
.Ls86b:
v_mfma_f32_32x32x16_bf16 v[160:175], a[248:251], v[100:103], v[160:175]
v_exp_f32 v117, v37
v_add_f32 v11, v11, v24
buffer_load_dwordx4 v3, s[20:23], s24 offen offset:3072 lds
v_mfma_f32_32x32x16_bf16 v[176:191], a[252:255], v[100:103], v[176:191]
v_cvt_pk_bf16_f32 v98, v116, v117
v_exp_f32 v118, v38
s_waitcnt vmcnt(16)
s_barrier
s_add_u32 s24, s24, 0x2000
v_mfma_f32_32x32x16_bf16 v[48:63], a[192:195], a[160:163], v[80:95]
v_exp_f32 v119, v39
ds_read_b128 a[192:195], v2 offset:24576
v_mfma_f32_32x32x16_bf16 v[48:63], a[196:199], a[164:167], v[48:63]
v_cvt_pk_bf16_f32 v99, v118, v119
v_exp_f32 v112, v40
ds_read_b128 a[196:199], v2 offset:25600
v_mfma_f32_32x32x16_bf16 v[48:63], a[200:203], a[168:171], v[48:63]
v_exp_f32 v113, v41
v_mfma_f32_16x16x32_bf16 v[24:27], v[28:31], v[96:99], 0
ds_read_b128 a[200:203], v2 offset:26624
v_mfma_f32_32x32x16_bf16 v[48:63], a[204:207], a[172:175], v[48:63]
v_cvt_pk_bf16_f32 v100, v112, v113
v_exp_f32 v114, v42
ds_read_b128 a[204:207], v2 offset:27648
v_mfma_f32_32x32x16_bf16 v[48:63], a[208:211], a[176:179], v[48:63]
v_exp_f32 v115, v43
ds_read_b128 a[208:211], v2 offset:28672
v_mfma_f32_32x32x16_bf16 v[48:63], a[212:215], a[180:183], v[48:63]
v_cvt_pk_bf16_f32 v101, v114, v115
v_exp_f32 v116, v44
ds_read_b128 a[212:215], v2 offset:29696
v_mfma_f32_32x32x16_bf16 v[48:63], a[216:219], a[184:187], v[48:63]
v_exp_f32 v117, v45
ds_read_b128 a[216:219], v2 offset:30720
v_mfma_f32_32x32x16_bf16 v[48:63], a[220:223], a[188:191], v[48:63]
v_cvt_pk_bf16_f32 v102, v116, v117
v_exp_f32 v118, v46
ds_read_b128 a[220:223], v2 offset:31744
v_mfma_f32_32x32x16_bf16 v[192:207], a[224:227], v[104:107], v[192:207]
v_exp_f32 v119, v47
ds_read_b128 a[224:227], v2 offset:49152
v_mfma_f32_32x32x16_bf16 v[208:223], a[228:231], v[104:107], v[208:223]
v_cvt_pk_bf16_f32 v103, v118, v119
v_exp_f32 v120, v48
ds_read_b128 a[228:231], v2 offset:50176
v_mfma_f32_32x32x16_bf16 v[224:239], a[232:235], v[104:107], v[224:239]
v_mfma_f32_16x16x32_bf16 v[24:27], v[28:31], v[100:103], v[24:27]
v_exp_f32 v121, v49
ds_read_b128 a[232:235], v2 offset:51200
v_mfma_f32_32x32x16_bf16 v[240:255], a[236:239], v[104:107], v[240:255]
v_cvt_pk_bf16_f32 v104, v120, v121
v_exp_f32 v122, v50
ds_read_b128 a[236:239], v2 offset:52224
v_mfma_f32_32x32x16_bf16 v[192:207], a[240:243], v[108:111], v[192:207]
v_exp_f32 v123, v51
v_cmp_ngt_f32 vcc, s71, v24
s_cmp_ge_u32 s24, s42
ds_read_b128 a[240:243], v2 offset:53248
v_mfma_f32_32x32x16_bf16 v[208:223], a[244:247], v[108:111], v[208:223]
v_cvt_pk_bf16_f32 v105, v122, v123
v_exp_f32 v124, v52
s_cbranch_vccnz .Lr85
.Lr85b:
s_cbranch_scc1 .Ls85
.Ls85b:
v_mfma_f32_32x32x16_bf16 v[224:239], a[248:251], v[108:111], v[224:239]
v_exp_f32 v125, v53
v_add_f32 v10, v10, v24
ds_read_b128 a[244:247], v2 offset:54272
ds_read_b128 a[248:251], v2 offset:55296
v_mfma_f32_32x32x16_bf16 v[240:255], a[252:255], v[108:111], v[240:255]
v_cvt_pk_bf16_f32 v106, v124, v125
v_exp_f32 v126, v54
ds_read_b128 a[252:255], v2 offset:56320
s_waitcnt lgkmcnt(8)
.Lx2_3:
v_mfma_f32_32x32x16_bf16 v[32:47], a[192:195], a[128:131], v[64:79]
s_add_u32 m0, s36, 16384
v_exp_f32 v127, v55
buffer_load_dwordx4 v1, s[20:23], s24 offen offset:0 lds
v_mfma_f32_32x32x16_bf16 v[32:47], a[196:199], a[132:135], v[32:47]
v_cvt_pk_bf16_f32 v107, v126, v127
v_exp_f32 v120, v56
buffer_load_dwordx4 v1, s[20:23], s24 offen offset:1024 lds
v_mfma_f32_32x32x16_bf16 v[32:47], a[200:203], a[136:139], v[32:47]
v_exp_f32 v121, v57
v_mfma_f32_16x16x32_bf16 v[24:27], v[28:31], v[104:107], 0
v_mfma_f32_32x32x16_bf16 v[32:47], a[204:207], a[140:143], v[32:47]
v_cvt_pk_bf16_f32 v108, v120, v121
v_exp_f32 v122, v58
v_mfma_f32_32x32x16_bf16 v[32:47], a[208:211], a[144:147], v[32:47]
v_exp_f32 v123, v59
buffer_load_dwordx4 v1, s[20:23], s24 offen offset:2048 lds
v_mfma_f32_32x32x16_bf16 v[32:47], a[212:215], a[148:151], v[32:47]
v_cvt_pk_bf16_f32 v109, v122, v123
v_exp_f32 v124, v60
v_mfma_f32_32x32x16_bf16 v[32:47], a[216:219], a[152:155], v[32:47]
v_exp_f32 v125, v61
buffer_load_dwordx4 v1, s[20:23], s24 offen offset:3072 lds
s_add_u32 m0, s36, 40960
v_mfma_f32_32x32x16_bf16 v[32:47], a[220:223], a[156:159], v[32:47]
v_cvt_pk_bf16_f32 v110, v124, v125
v_exp_f32 v126, v62
s_waitcnt lgkmcnt(0)
v_mfma_f32_32x32x16_bf16 v[128:143], a[224:227], v[96:99], v[128:143]
v_exp_f32 v127, v63
buffer_load_dwordx4 v3, s[20:23], s24 offen offset:0 lds
v_mfma_f32_32x32x16_bf16 v[144:159], a[228:231], v[96:99], v[144:159]
v_cvt_pk_bf16_f32 v111, v126, v127
v_exp_f32 v112, v32
buffer_load_dwordx4 v3, s[20:23], s24 offen offset:1024 lds
v_mfma_f32_32x32x16_bf16 v[160:175], a[232:235], v[96:99], v[160:175]
v_exp_f32 v113, v33
v_mfma_f32_16x16x32_bf16 v[24:27], v[28:31], v[108:111], v[24:27]
buffer_load_dwordx4 v3, s[20:23], s24 offen offset:2048 lds
v_mfma_f32_32x32x16_bf16 v[176:191], a[236:239], v[96:99], v[176:191]
v_cvt_pk_bf16_f32 v96, v112, v113
v_exp_f32 v114, v34
v_mfma_f32_32x32x16_bf16 v[128:143], a[240:243], v[100:103], v[128:143]
v_exp_f32 v115, v35
v_cmp_ngt_f32 vcc, s71, v24
s_cmp_ge_u32 s24, s43
v_mfma_f32_32x32x16_bf16 v[144:159], a[244:247], v[100:103], v[144:159]
v_cvt_pk_bf16_f32 v97, v114, v115
v_exp_f32 v116, v36
s_cbranch_vccnz .Lr88
.Lr88b:
s_cbranch_scc1 .Ls88
.Ls88b:
v_mfma_f32_32x32x16_bf16 v[160:175], a[248:251], v[100:103], v[160:175]
v_exp_f32 v117, v37
v_add_f32 v11, v11, v24
buffer_load_dwordx4 v3, s[20:23], s24 offen offset:3072 lds
v_mfma_f32_32x32x16_bf16 v[176:191], a[252:255], v[100:103], v[176:191]
v_cvt_pk_bf16_f32 v98, v116, v117
v_exp_f32 v118, v38
s_waitcnt vmcnt(16)
s_barrier
s_add_u32 s24, s24, 0x2000
v_mfma_f32_32x32x16_bf16 v[48:63], a[192:195], a[160:163], v[80:95]
v_exp_f32 v119, v39
ds_read_b128 a[192:195], v2 offset:0
v_mfma_f32_32x32x16_bf16 v[48:63], a[196:199], a[164:167], v[48:63]
v_cvt_pk_bf16_f32 v99, v118, v119
v_exp_f32 v112, v40
ds_read_b128 a[196:199], v2 offset:1024
v_mfma_f32_32x32x16_bf16 v[48:63], a[200:203], a[168:171], v[48:63]
v_exp_f32 v113, v41
v_mfma_f32_16x16x32_bf16 v[24:27], v[28:31], v[96:99], 0
ds_read_b128 a[200:203], v2 offset:2048
v_mfma_f32_32x32x16_bf16 v[48:63], a[204:207], a[172:175], v[48:63]
v_cvt_pk_bf16_f32 v100, v112, v113
v_exp_f32 v114, v42
ds_read_b128 a[204:207], v2 offset:3072
v_mfma_f32_32x32x16_bf16 v[48:63], a[208:211], a[176:179], v[48:63]
v_exp_f32 v115, v43
ds_read_b128 a[208:211], v2 offset:4096
v_mfma_f32_32x32x16_bf16 v[48:63], a[212:215], a[180:183], v[48:63]
v_cvt_pk_bf16_f32 v101, v114, v115
v_exp_f32 v116, v44
ds_read_b128 a[212:215], v2 offset:5120
v_mfma_f32_32x32x16_bf16 v[48:63], a[216:219], a[184:187], v[48:63]
v_exp_f32 v117, v45
ds_read_b128 a[216:219], v2 offset:6144
v_mfma_f32_32x32x16_bf16 v[48:63], a[220:223], a[188:191], v[48:63]
v_cvt_pk_bf16_f32 v102, v116, v117
v_exp_f32 v118, v46
ds_read_b128 a[220:223], v2 offset:7168
v_mfma_f32_32x32x16_bf16 v[192:207], a[224:227], v[104:107], v[192:207]
v_exp_f32 v119, v47
ds_read_b128 a[224:227], v2 offset:57344
v_mfma_f32_32x32x16_bf16 v[208:223], a[228:231], v[104:107], v[208:223]
v_cvt_pk_bf16_f32 v103, v118, v119
v_exp_f32 v120, v48
ds_read_b128 a[228:231], v2 offset:58368
v_mfma_f32_32x32x16_bf16 v[224:239], a[232:235], v[104:107], v[224:239]
v_mfma_f32_16x16x32_bf16 v[24:27], v[28:31], v[100:103], v[24:27]
v_exp_f32 v121, v49
ds_read_b128 a[232:235], v2 offset:59392
v_mfma_f32_32x32x16_bf16 v[240:255], a[236:239], v[104:107], v[240:255]
v_cvt_pk_bf16_f32 v104, v120, v121
v_exp_f32 v122, v50
ds_read_b128 a[236:239], v2 offset:60416
v_mfma_f32_32x32x16_bf16 v[192:207], a[240:243], v[108:111], v[192:207]
v_exp_f32 v123, v51
v_cmp_ngt_f32 vcc, s71, v24
s_cmp_ge_u32 s24, s42
ds_read_b128 a[240:243], v2 offset:61440
v_mfma_f32_32x32x16_bf16 v[208:223], a[244:247], v[108:111], v[208:223]
v_cvt_pk_bf16_f32 v105, v122, v123
v_exp_f32 v124, v52
s_cbranch_vccnz .Lr87
.Lr87b:
s_cbranch_scc1 .Ls87
.Ls87b:
v_mfma_f32_32x32x16_bf16 v[224:239], a[248:251], v[108:111], v[224:239]
v_exp_f32 v125, v53
v_add_f32 v10, v10, v24
ds_read_b128 a[244:247], v2 offset:62464
ds_read_b128 a[248:251], v2 offset:63488
v_mfma_f32_32x32x16_bf16 v[240:255], a[252:255], v[108:111], v[240:255]
v_cvt_pk_bf16_f32 v106, v124, v125
v_exp_f32 v126, v54
ds_read_b128 a[252:255], v2 offset:64512
s_waitcnt lgkmcnt(8)
s_cmp_lt_u32 s24, s27
s_cselect_b32 s22, s52, 0
.Lx2_0:
v_mfma_f32_32x32x16_bf16 v[32:47], a[192:195], a[128:131], v[64:79]
s_add_u32 m0, s36, 24576
v_exp_f32 v127, v55
buffer_load_dwordx4 v1, s[20:23], s24 offen offset:0 lds
v_mfma_f32_32x32x16_bf16 v[32:47], a[196:199], a[132:135], v[32:47]
v_cvt_pk_bf16_f32 v107, v126, v127
v_exp_f32 v120, v56
buffer_load_dwordx4 v1, s[20:23], s24 offen offset:1024 lds
v_mfma_f32_32x32x16_bf16 v[32:47], a[200:203], a[136:139], v[32:47]
v_exp_f32 v121, v57
v_mfma_f32_16x16x32_bf16 v[24:27], v[28:31], v[104:107], 0
v_mfma_f32_32x32x16_bf16 v[32:47], a[204:207], a[140:143], v[32:47]
v_cvt_pk_bf16_f32 v108, v120, v121
v_exp_f32 v122, v58
v_mfma_f32_32x32x16_bf16 v[32:47], a[208:211], a[144:147], v[32:47]
v_exp_f32 v123, v59
buffer_load_dwordx4 v1, s[20:23], s24 offen offset:2048 lds
v_mfma_f32_32x32x16_bf16 v[32:47], a[212:215], a[148:151], v[32:47]
v_cvt_pk_bf16_f32 v109, v122, v123
v_exp_f32 v124, v60
v_mfma_f32_32x32x16_bf16 v[32:47], a[216:219], a[152:155], v[32:47]
v_exp_f32 v125, v61
buffer_load_dwordx4 v1, s[20:23], s24 offen offset:3072 lds
s_add_u32 m0, s36, 49152
v_mfma_f32_32x32x16_bf16 v[32:47], a[220:223], a[156:159], v[32:47]
v_cvt_pk_bf16_f32 v110, v124, v125
v_exp_f32 v126, v62
s_waitcnt lgkmcnt(0)
v_mfma_f32_32x32x16_bf16 v[128:143], a[224:227], v[96:99], v[128:143]
v_exp_f32 v127, v63
buffer_load_dwordx4 v3, s[20:23], s24 offen offset:0 lds
v_mfma_f32_32x32x16_bf16 v[144:159], a[228:231], v[96:99], v[144:159]
v_cvt_pk_bf16_f32 v111, v126, v127
v_exp_f32 v112, v32
buffer_load_dwordx4 v3, s[20:23], s24 offen offset:1024 lds
v_mfma_f32_32x32x16_bf16 v[160:175], a[232:235], v[96:99], v[160:175]
v_exp_f32 v113, v33
v_mfma_f32_16x16x32_bf16 v[24:27], v[28:31], v[108:111], v[24:27]
buffer_load_dwordx4 v3, s[20:23], s24 offen offset:2048 lds
v_mfma_f32_32x32x16_bf16 v[176:191], a[236:239], v[96:99], v[176:191]
v_cvt_pk_bf16_f32 v96, v112, v113
v_exp_f32 v114, v34
v_mfma_f32_32x32x16_bf16 v[128:143], a[240:243], v[100:103], v[128:143]
v_exp_f32 v115, v35
v_cmp_ngt_f32 vcc, s71, v24
s_cmp_ge_u32 s24, s43
v_mfma_f32_32x32x16_bf16 v[144:159], a[244:247], v[100:103], v[144:159]
v_cvt_pk_bf16_f32 v97, v114, v115
v_exp_f32 v116, v36
s_cbranch_vccnz .Lr90
.Lr90b:
s_cbranch_scc1 .Ls90
.Ls90b:
v_mfma_f32_32x32x16_bf16 v[160:175], a[248:251], v[100:103], v[160:175]
v_exp_f32 v117, v37
v_add_f32 v11, v11, v24
buffer_load_dwordx4 v3, s[20:23], s24 offen offset:3072 lds
v_mfma_f32_32x32x16_bf16 v[176:191], a[252:255], v[100:103], v[176:191]
v_cvt_pk_bf16_f32 v98, v116, v117
v_exp_f32 v118, v38
s_waitcnt vmcnt(16)
s_barrier
s_add_u32 s24, s24, 0x2000
v_mfma_f32_32x32x16_bf16 v[48:63], a[192:195], a[160:163], v[80:95]
v_exp_f32 v119, v39
ds_read_b128 a[192:195], v2 offset:8192
v_mfma_f32_32x32x16_bf16 v[48:63], a[196:199], a[164:167], v[48:63]
v_cvt_pk_bf16_f32 v99, v118, v119
v_exp_f32 v112, v40
ds_read_b128 a[196:199], v2 offset:9216
v_mfma_f32_32x32x16_bf16 v[48:63], a[200:203], a[168:171], v[48:63]
v_exp_f32 v113, v41
v_mfma_f32_16x16x32_bf16 v[24:27], v[28:31], v[96:99], 0
ds_read_b128 a[200:203], v2 offset:10240
v_mfma_f32_32x32x16_bf16 v[48:63], a[204:207], a[172:175], v[48:63]
v_cvt_pk_bf16_f32 v100, v112, v113
v_exp_f32 v114, v42
ds_read_b128 a[204:207], v2 offset:11264
v_mfma_f32_32x32x16_bf16 v[48:63], a[208:211], a[176:179], v[48:63]
v_exp_f32 v115, v43
ds_read_b128 a[208:211], v2 offset:12288
v_mfma_f32_32x32x16_bf16 v[48:63], a[212:215], a[180:183], v[48:63]
v_cvt_pk_bf16_f32 v101, v114, v115
v_exp_f32 v116, v44
ds_read_b128 a[212:215], v2 offset:13312
v_mfma_f32_32x32x16_bf16 v[48:63], a[216:219], a[184:187], v[48:63]
v_exp_f32 v117, v45
ds_read_b128 a[216:219], v2 offset:14336
v_mfma_f32_32x32x16_bf16 v[48:63], a[220:223], a[188:191], v[48:63]
v_cvt_pk_bf16_f32 v102, v116, v117
v_exp_f32 v118, v46
ds_read_b128 a[220:223], v2 offset:15360
v_mfma_f32_32x32x16_bf16 v[192:207], a[224:227], v[104:107], v[192:207]
v_exp_f32 v119, v47
ds_read_b128 a[224:227], v2 offset:32768
v_mfma_f32_32x32x16_bf16 v[208:223], a[228:231], v[104:107], v[208:223]
v_cvt_pk_bf16_f32 v103, v118, v119
v_exp_f32 v120, v48
ds_read_b128 a[228:231], v2 offset:33792
v_mfma_f32_32x32x16_bf16 v[224:239], a[232:235], v[104:107], v[224:239]
v_mfma_f32_16x16x32_bf16 v[24:27], v[28:31], v[100:103], v[24:27]
v_exp_f32 v121, v49
ds_read_b128 a[232:235], v2 offset:34816
v_mfma_f32_32x32x16_bf16 v[240:255], a[236:239], v[104:107], v[240:255]
v_cvt_pk_bf16_f32 v104, v120, v121
v_exp_f32 v122, v50
ds_read_b128 a[236:239], v2 offset:35840
v_mfma_f32_32x32x16_bf16 v[192:207], a[240:243], v[108:111], v[192:207]
v_exp_f32 v123, v51
v_cmp_ngt_f32 vcc, s71, v24
s_cmp_ge_u32 s24, s42
ds_read_b128 a[240:243], v2 offset:36864
v_mfma_f32_32x32x16_bf16 v[208:223], a[244:247], v[108:111], v[208:223]
v_cvt_pk_bf16_f32 v105, v122, v123
v_exp_f32 v124, v52
s_cbranch_vccnz .Lr89
.Lr89b:
s_cbranch_scc1 .Ls89
.Ls89b:
v_mfma_f32_32x32x16_bf16 v[224:239], a[248:251], v[108:111], v[224:239]
v_exp_f32 v125, v53
v_add_f32 v10, v10, v24
ds_read_b128 a[244:247], v2 offset:37888
ds_read_b128 a[248:251], v2 offset:38912
v_mfma_f32_32x32x16_bf16 v[240:255], a[252:255], v[108:111], v[240:255]
v_cvt_pk_bf16_f32 v106, v124, v125
v_exp_f32 v126, v54
ds_read_b128 a[252:255], v2 offset:39936
s_waitcnt lgkmcnt(8)
.Lx2_1:
v_mfma_f32_32x32x16_bf16 v[32:47], a[192:195], a[128:131], v[64:79]
s_add_u32 m0, s36, 0
v_exp_f32 v127, v55
buffer_load_dwordx4 v1, s[20:23], s24 offen offset:0 lds
v_mfma_f32_32x32x16_bf16 v[32:47], a[196:199], a[132:135], v[32:47]
v_cvt_pk_bf16_f32 v107, v126, v127
v_exp_f32 v120, v56
buffer_load_dwordx4 v1, s[20:23], s24 offen offset:1024 lds
v_mfma_f32_32x32x16_bf16 v[32:47], a[200:203], a[136:139], v[32:47]
v_exp_f32 v121, v57
v_mfma_f32_16x16x32_bf16 v[24:27], v[28:31], v[104:107], 0
v_mfma_f32_32x32x16_bf16 v[32:47], a[204:207], a[140:143], v[32:47]
v_cvt_pk_bf16_f32 v108, v120, v121
v_exp_f32 v122, v58
v_mfma_f32_32x32x16_bf16 v[32:47], a[208:211], a[144:147], v[32:47]
v_exp_f32 v123, v59
buffer_load_dwordx4 v1, s[20:23], s24 offen offset:2048 lds
v_mfma_f32_32x32x16_bf16 v[32:47], a[212:215], a[148:151], v[32:47]
v_cvt_pk_bf16_f32 v109, v122, v123
v_exp_f32 v124, v60
v_mfma_f32_32x32x16_bf16 v[32:47], a[216:219], a[152:155], v[32:47]
v_exp_f32 v125, v61
buffer_load_dwordx4 v1, s[20:23], s24 offen offset:3072 lds
s_add_u32 m0, s36, 57344
v_mfma_f32_32x32x16_bf16 v[32:47], a[220:223], a[156:159], v[32:47]
v_cvt_pk_bf16_f32 v110, v124, v125
v_exp_f32 v126, v62
s_waitcnt lgkmcnt(0)
v_mfma_f32_32x32x16_bf16 v[128:143], a[224:227], v[96:99], v[128:143]
v_exp_f32 v127, v63
buffer_load_dwordx4 v3, s[20:23], s24 offen offset:0 lds
v_mfma_f32_32x32x16_bf16 v[144:159], a[228:231], v[96:99], v[144:159]
v_cvt_pk_bf16_f32 v111, v126, v127
v_exp_f32 v112, v32
buffer_load_dwordx4 v3, s[20:23], s24 offen offset:1024 lds
v_mfma_f32_32x32x16_bf16 v[160:175], a[232:235], v[96:99], v[160:175]
v_exp_f32 v113, v33
v_mfma_f32_16x16x32_bf16 v[24:27], v[28:31], v[108:111], v[24:27]
buffer_load_dwordx4 v3, s[20:23], s24 offen offset:2048 lds
v_mfma_f32_32x32x16_bf16 v[176:191], a[236:239], v[96:99], v[176:191]
v_cvt_pk_bf16_f32 v96, v112, v113
v_exp_f32 v114, v34
v_mfma_f32_32x32x16_bf16 v[128:143], a[240:243], v[100:103], v[128:143]
v_exp_f32 v115, v35
v_cmp_ngt_f32 vcc, s71, v24
s_cmp_ge_u32 s24, s43
v_mfma_f32_32x32x16_bf16 v[144:159], a[244:247], v[100:103], v[144:159]
v_cvt_pk_bf16_f32 v97, v114, v115
v_exp_f32 v116, v36
s_cbranch_vccnz .Lr92
.Lr92b:
s_cbranch_scc1 .Ls92
.Ls92b:
v_mfma_f32_32x32x16_bf16 v[160:175], a[248:251], v[100:103], v[160:175]
v_exp_f32 v117, v37
v_add_f32 v11, v11, v24
buffer_load_dwordx4 v3, s[20:23], s24 offen offset:3072 lds
v_mfma_f32_32x32x16_bf16 v[176:191], a[252:255], v[100:103], v[176:191]
v_cvt_pk_bf16_f32 v98, v116, v117
v_exp_f32 v118, v38
s_waitcnt vmcnt(16)
s_barrier
s_add_u32 s24, s24, 0x2000
v_mfma_f32_32x32x16_bf16 v[48:63], a[192:195], a[160:163], v[80:95]
v_exp_f32 v119, v39
ds_read_b128 a[192:195], v2 offset:16384
v_mfma_f32_32x32x16_bf16 v[48:63], a[196:199], a[164:167], v[48:63]
v_cvt_pk_bf16_f32 v99, v118, v119
v_exp_f32 v112, v40
ds_read_b128 a[196:199], v2 offset:17408
v_mfma_f32_32x32x16_bf16 v[48:63], a[200:203], a[168:171], v[48:63]
v_exp_f32 v113, v41
v_mfma_f32_16x16x32_bf16 v[24:27], v[28:31], v[96:99], 0
ds_read_b128 a[200:203], v2 offset:18432
v_mfma_f32_32x32x16_bf16 v[48:63], a[204:207], a[172:175], v[48:63]
v_cvt_pk_bf16_f32 v100, v112, v113
v_exp_f32 v114, v42
ds_read_b128 a[204:207], v2 offset:19456
v_mfma_f32_32x32x16_bf16 v[48:63], a[208:211], a[176:179], v[48:63]
v_exp_f32 v115, v43
ds_read_b128 a[208:211], v2 offset:20480
v_mfma_f32_32x32x16_bf16 v[48:63], a[212:215], a[180:183], v[48:63]
v_cvt_pk_bf16_f32 v101, v114, v115
v_exp_f32 v116, v44
ds_read_b128 a[212:215], v2 offset:21504
v_mfma_f32_32x32x16_bf16 v[48:63], a[216:219], a[184:187], v[48:63]
v_exp_f32 v117, v45
ds_read_b128 a[216:219], v2 offset:22528
v_mfma_f32_32x32x16_bf16 v[48:63], a[220:223], a[188:191], v[48:63]
v_cvt_pk_bf16_f32 v102, v116, v117
v_exp_f32 v118, v46
ds_read_b128 a[220:223], v2 offset:23552
v_mfma_f32_32x32x16_bf16 v[192:207], a[224:227], v[104:107], v[192:207]
v_exp_f32 v119, v47
ds_read_b128 a[224:227], v2 offset:40960
v_mfma_f32_32x32x16_bf16 v[208:223], a[228:231], v[104:107], v[208:223]
v_cvt_pk_bf16_f32 v103, v118, v119
v_exp_f32 v120, v48
ds_read_b128 a[228:231], v2 offset:41984
v_mfma_f32_32x32x16_bf16 v[224:239], a[232:235], v[104:107], v[224:239]
v_mfma_f32_16x16x32_bf16 v[24:27], v[28:31], v[100:103], v[24:27]
v_exp_f32 v121, v49
ds_read_b128 a[232:235], v2 offset:43008
v_mfma_f32_32x32x16_bf16 v[240:255], a[236:239], v[104:107], v[240:255]
v_cvt_pk_bf16_f32 v104, v120, v121
v_exp_f32 v122, v50
ds_read_b128 a[236:239], v2 offset:44032
v_mfma_f32_32x32x16_bf16 v[192:207], a[240:243], v[108:111], v[192:207]
v_exp_f32 v123, v51
v_cmp_ngt_f32 vcc, s71, v24
s_cmp_ge_u32 s24, s42
ds_read_b128 a[240:243], v2 offset:45056
v_mfma_f32_32x32x16_bf16 v[208:223], a[244:247], v[108:111], v[208:223]
v_cvt_pk_bf16_f32 v105, v122, v123
v_exp_f32 v124, v52
s_cbranch_vccnz .Lr91
.Lr91b:
s_cbranch_scc1 .Ls91
.Ls91b:
v_mfma_f32_32x32x16_bf16 v[224:239], a[248:251], v[108:111], v[224:239]
v_exp_f32 v125, v53
v_add_f32 v10, v10, v24
ds_read_b128 a[244:247], v2 offset:46080
ds_read_b128 a[248:251], v2 offset:47104
v_mfma_f32_32x32x16_bf16 v[240:255], a[252:255], v[108:111], v[240:255]
v_cvt_pk_bf16_f32 v106, v124, v125
v_exp_f32 v126, v54
ds_read_b128 a[252:255], v2 offset:48128
s_waitcnt lgkmcnt(8)
s_cmp_lg_u32 s24, s40
s_cbranch_scc1 .Lx2_2
s_cmp_ge_u32 s48, 20
s_cbranch_scc1 .LdV
.Lp2:
v_mfma_f32_32x32x16_bf16 v[32:47], a[192:195], a[128:131], v[64:79]
s_add_u32 m0, s36, 8192
v_exp_f32 v127, v55
buffer_load_dwordx4 v1, s[20:23], s24 offen offset:0 lds
s_mov_b32 s80, %6
s_and_b32 s81, %7, 0xffff
v_mfma_f32_32x32x16_bf16 v[32:47], a[196:199], a[132:135], v[32:47]
v_cvt_pk_bf16_f32 v107, v126, v127
v_exp_f32 v120, v56
buffer_load_dwordx4 v1, s[20:23], s24 offen offset:1024 lds
s_mov_b32 s82, 0x400000
s_mov_b32 s83, 0x20000
v_mfma_f32_32x32x16_bf16 v[32:47], a[200:203], a[136:139], v[32:47]
v_exp_f32 v121, v57
v_mfma_f32_16x16x32_bf16 v[24:27], v[28:31], v[104:107], 0
v_mfma_f32_32x32x16_bf16 v[32:47], a[204:207], a[140:143], v[32:47]
v_cvt_pk_bf16_f32 v108, v120, v121
v_exp_f32 v122, v58
s_cmp_ge_u32 s48, 20
s_cselect_b32 s70, 9, 16
s_lshl_b32 s70, s70, 13
s_lshl_b32 s26, %2, 14
v_mfma_f32_32x32x16_bf16 v[32:47], a[208:211], a[144:147], v[32:47]
v_exp_f32 v123, v59
buffer_load_dwordx4 v1, s[20:23], s24 offen offset:2048 lds
s_add_u32 s26, s26, 0
s_add_u32 s28, s26, 8192
v_bfe_u32 v20, v0, 3, 3
v_mfma_f32_32x32x16_bf16 v[32:47], a[212:215], a[148:151], v[32:47]
v_cvt_pk_bf16_f32 v109, v122, v123
v_exp_f32 v124, v60
v_lshlrev_b32 v4, 10, v20
v_bfe_u32 v20, v0, 4, 1
v_lshlrev_b32 v20, 2, v20
v_mfma_f32_32x32x16_bf16 v[32:47], a[216:219], a[152:155], v[32:47]
v_exp_f32 v125, v61
buffer_load_dwordx4 v1, s[20:23], s24 offen offset:3072 lds
s_add_u32 m0, s36, 32768
v_and_b32 v21, 7, v0
v_xor_b32 v20, v20, v21
v_mfma_f32_32x32x16_bf16 v[32:47], a[220:223], a[156:159], v[32:47]
v_cvt_pk_bf16_f32 v110, v124, v125
v_exp_f32 v126, v62
s_waitcnt lgkmcnt(0)
v_lshl_add_u32 v4, v20, 4, v4
v_mfma_f32_32x32x16_bf16 v[128:143], a[224:227], v[96:99], v[128:143]
v_exp_f32 v127, v63
buffer_load_dwordx4 v3, s[20:23], s24 offen offset:0 lds
v_mfma_f32_32x32x16_bf16 v[144:159], a[228:231], v[96:99], v[144:159]
v_cvt_pk_bf16_f32 v111, v126, v127
v_exp_f32 v112, v32
buffer_load_dwordx4 v3, s[20:23], s24 offen offset:1024 lds
v_mfma_f32_32x32x16_bf16 v[160:175], a[232:235], v[96:99], v[160:175]
v_exp_f32 v113, v33
v_mfma_f32_16x16x32_bf16 v[24:27], v[28:31], v[108:111], v[24:27]
buffer_load_dwordx4 v3, s[20:23], s24 offen offset:2048 lds
v_mfma_f32_32x32x16_bf16 v[176:191], a[236:239], v[96:99], v[176:191]
v_cvt_pk_bf16_f32 v96, v112, v113
v_exp_f32 v114, v34
v_mfma_f32_32x32x16_bf16 v[128:143], a[240:243], v[100:103], v[128:143]
v_exp_f32 v115, v35
v_cmp_ngt_f32 vcc, s71, v24
s_cmp_ge_u32 s24, s43
v_mfma_f32_32x32x16_bf16 v[144:159], a[244:247], v[100:103], v[144:159]
v_cvt_pk_bf16_f32 v97, v114, v115
v_exp_f32 v116, v36
s_cbranch_vccnz .Lr94
.Lr94b:
s_cbranch_scc1 .Ls94
.Ls94b:
v_mfma_f32_32x32x16_bf16 v[160:175], a[248:251], v[100:103], v[160:175]
v_exp_f32 v117, v37
v_add_f32 v11, v11, v24
buffer_load_dwordx4 v3, s[20:23], s24 offen offset:3072 lds
v_mfma_f32_32x32x16_bf16 v[176:191], a[252:255], v[100:103], v[176:191]
v_cvt_pk_bf16_f32 v98, v116, v117
v_exp_f32 v118, v38
s_waitcnt vmcnt(16)
s_barrier
s_add_u32 s24, s24, 0x2000
v_mfma_f32_32x32x16_bf16 v[48:63], a[192:195], a[160:163], v[80:95]
v_exp_f32 v119, v39
ds_read_b128 a[192:195], v2 offset:24576
s_add_u32 m0, s70, 0
s_nop 0
buffer_load_dwordx4 v4, s[80:83], s26 offen offset:0 lds
v_mfma_f32_32x32x16_bf16 v[48:63], a[196:199], a[164:167], v[48:63]
v_cvt_pk_bf16_f32 v99, v118, v119
v_exp_f32 v112, v40
ds_read_b128 a[196:199], v2 offset:25600
s_add_u32 m0, s70, 1024
s_nop 0
v_mfma_f32_32x32x16_bf16 v[48:63], a[200:203], a[168:171], v[48:63]
v_exp_f32 v113, v41
v_mfma_f32_16x16x32_bf16 v[24:27], v[28:31], v[96:99], 0
ds_read_b128 a[200:203], v2 offset:26624
v_mfma_f32_32x32x16_bf16 v[48:63], a[204:207], a[172:175], v[48:63]
v_cvt_pk_bf16_f32 v100, v112, v113
v_exp_f32 v114, v42
ds_read_b128 a[204:207], v2 offset:27648
buffer_load_dwordx4 v4, s[80:83], s28 offen offset:0 lds
s_add_u32 m0, s70, 1920
v_mfma_f32_32x32x16_bf16 v[48:63], a[208:211], a[176:179], v[48:63]
v_exp_f32 v115, v43
ds_read_b128 a[208:211], v2 offset:28672
s_nop 0
buffer_load_dwordx4 v4, s[80:83], s26 offen offset:128 lds
s_add_u32 m0, s70, 2944
v_mfma_f32_32x32x16_bf16 v[48:63], a[212:215], a[180:183], v[48:63]
v_cvt_pk_bf16_f32 v101, v114, v115
v_exp_f32 v116, v44
ds_read_b128 a[212:215], v2 offset:29696
s_nop 0
buffer_load_dwordx4 v4, s[80:83], s28 offen offset:128 lds
v_mfma_f32_32x32x16_bf16 v[48:63], a[216:219], a[184:187], v[48:63]
v_exp_f32 v117, v45
ds_read_b128 a[216:219], v2 offset:30720
s_add_u32 m0, s70, 3840
s_nop 0
buffer_load_dwordx4 v4, s[80:83], s26 offen offset:256 lds
v_mfma_f32_32x32x16_bf16 v[48:63], a[220:223], a[188:191], v[48:63]
v_cvt_pk_bf16_f32 v102, v116, v117
v_exp_f32 v118, v46
ds_read_b128 a[220:223], v2 offset:31744
s_add_u32 m0, s70, 4864
s_nop 0
v_mfma_f32_32x32x16_bf16 v[192:207], a[224:227], v[104:107], v[192:207]
v_exp_f32 v119, v47
ds_read_b128 a[224:227], v2 offset:49152
buffer_load_dwordx4 v4, s[80:83], s28 offen offset:256 lds
s_add_u32 m0, s70, 5760
s_nop 0
v_mfma_f32_32x32x16_bf16 v[208:223], a[228:231], v[104:107], v[208:223]
v_cvt_pk_bf16_f32 v103, v118, v119
v_exp_f32 v120, v48
ds_read_b128 a[228:231], v2 offset:50176
buffer_load_dwordx4 v4, s[80:83], s26 offen offset:384 lds
s_add_u32 m0, s70, 6784
v_mfma_f32_32x32x16_bf16 v[224:239], a[232:235], v[104:107], v[224:239]
v_mfma_f32_16x16x32_bf16 v[24:27], v[28:31], v[100:103], v[24:27]
v_exp_f32 v121, v49
ds_read_b128 a[232:235], v2 offset:51200
v_mfma_f32_32x32x16_bf16 v[240:255], a[236:239], v[104:107], v[240:255]
v_cvt_pk_bf16_f32 v104, v120, v121
v_exp_f32 v122, v50
ds_read_b128 a[236:239], v2 offset:52224
s_nop 0
buffer_load_dwordx4 v4, s[80:83], s28 offen offset:384 lds
v_mfma_f32_32x32x16_bf16 v[192:207], a[240:243], v[108:111], v[192:207]
v_exp_f32 v123, v51
v_cmp_ngt_f32 vcc, s71, v24
s_cmp_ge_u32 s24, s42
ds_read_b128 a[240:243], v2 offset:53248
v_mfma_f32_32x32x16_bf16 v[208:223], a[244:247], v[108:111], v[208:223]
v_cvt_pk_bf16_f32 v105, v122, v123
v_exp_f32 v124, v52
s_cbranch_vccnz .Lr93
.Lr93b:
s_cbranch_scc1 .Ls93
.Ls93b:
v_mfma_f32_32x32x16_bf16 v[224:239], a[248:251], v[108:111], v[224:239]
v_exp_f32 v125, v53
v_add_f32 v10, v10, v24
ds_read_b128 a[244:247], v2 offset:54272
ds_read_b128 a[248:251], v2 offset:55296
v_mfma_f32_32x32x16_bf16 v[240:255], a[252:255], v[108:111], v[240:255]
v_cvt_pk_bf16_f32 v106, v124, v125
v_exp_f32 v126, v54
ds_read_b128 a[252:255], v2 offset:56320
s_waitcnt lgkmcnt(8)
v_mfma_f32_32x32x16_bf16 v[32:47], a[192:195], a[128:131], v[64:79]
s_add_u32 m0, s36, 16384
v_exp_f32 v127, v55
buffer_load_dwordx4 v1, s[20:23], s24 offen offset:0 lds
s_mov_b32 s80, %8
v_mfma_f32_32x32x16_bf16 v[32:47], a[196:199], a[132:135], v[32:47]
v_cvt_pk_bf16_f32 v107, v126, v127
v_exp_f32 v120, v56
buffer_load_dwordx4 v1, s[20:23], s24 offen offset:1024 lds
s_and_b32 s81, %9, 0xffff
v_mfma_f32_32x32x16_bf16 v[32:47], a[200:203], a[136:139], v[32:47]
v_exp_f32 v121, v57
v_mfma_f32_16x16x32_bf16 v[24:27], v[28:31], v[104:107], 0
v_mfma_f32_32x32x16_bf16 v[32:47], a[204:207], a[140:143], v[32:47]
v_cvt_pk_bf16_f32 v108, v120, v121
v_exp_f32 v122, v58
s_mov_b32 s82, 0x1c0000
s_mov_b32 s83, 0x20000
v_mfma_f32_32x32x16_bf16 v[32:47], a[208:211], a[144:147], v[32:47]
v_exp_f32 v123, v59
buffer_load_dwordx4 v1, s[20:23], s24 offen offset:2048 lds
s_lshr_b32 s26, %2, 4
s_mul_i32 s26, s26, 114688
v_mfma_f32_32x32x16_bf16 v[32:47], a[212:215], a[148:151], v[32:47]
v_cvt_pk_bf16_f32 v109, v122, v123
v_exp_f32 v124, v60
s_bfe_u32 s28, %2, 0x30001
s_lshl_b32 s28, s28, 7
v_mfma_f32_32x32x16_bf16 v[32:47], a[216:219], a[152:155], v[32:47]
v_exp_f32 v125, v61
buffer_load_dwordx4 v1, s[20:23], s24 offen offset:3072 lds
s_add_u32 m0, s36, 40960
s_add_u32 s26, s26, s28
v_mfma_f32_32x32x16_bf16 v[32:47], a[220:223], a[156:159], v[32:47]
v_cvt_pk_bf16_f32 v110, v124, v125
v_exp_f32 v126, v62
s_waitcnt lgkmcnt(0)
s_and_b32 s28, %2, 1
v_mfma_f32_32x32x16_bf16 v[128:143], a[224:227], v[96:99], v[128:143]
v_exp_f32 v127, v63
buffer_load_dwordx4 v3, s[20:23], s24 offen offset:0 lds
s_mul_i32 s28, s28, 57344
s_add_u32 s28, s28, s26
v_mfma_f32_32x32x16_bf16 v[144:159], a[228:231], v[96:99], v[144:159]
v_cvt_pk_bf16_f32 v111, v126, v127
v_exp_f32 v112, v32
buffer_load_dwordx4 v3, s[20:23], s24 offen offset:1024 lds
v_and_b32 v4, 0x3ff, v4
v_mfma_f32_32x32x16_bf16 v[160:175], a[232:235], v[96:99], v[160:175]
v_exp_f32 v113, v33
v_mfma_f32_16x16x32_bf16 v[24:27], v[28:31], v[108:111], v[24:27]
buffer_load_dwordx4 v3, s[20:23], s24 offen offset:2048 lds
v_mfma_f32_32x32x16_bf16 v[176:191], a[236:239], v[96:99], v[176:191]
v_cvt_pk_bf16_f32 v96, v112, v113
v_exp_f32 v114, v34
v_bfe_u32 v20, v0, 3, 1
v_lshl_add_u32 v4, v20, 10, v4
v_mfma_f32_32x32x16_bf16 v[128:143], a[240:243], v[100:103], v[128:143]
v_exp_f32 v115, v35
v_cmp_ngt_f32 vcc, s71, v24
s_cmp_ge_u32 s24, s43
v_mfma_f32_32x32x16_bf16 v[144:159], a[244:247], v[100:103], v[144:159]
v_cvt_pk_bf16_f32 v97, v114, v115
v_exp_f32 v116, v36
s_cbranch_vccnz .Lr96
.Lr96b:
s_cbranch_scc1 .Ls96
.Ls96b:
v_mfma_f32_32x32x16_bf16 v[160:175], a[248:251], v[100:103], v[160:175]
v_exp_f32 v117, v37
v_add_f32 v11, v11, v24
buffer_load_dwordx4 v3, s[20:23], s24 offen offset:3072 lds
v_bfe_u32 v20, v0, 4, 1
v_mfma_f32_32x32x16_bf16 v[176:191], a[252:255], v[100:103], v[176:191]
v_cvt_pk_bf16_f32 v98, v116, v117
v_exp_f32 v118, v38
s_waitcnt vmcnt(24)
s_barrier
s_add_u32 s24, s24, 0x2000
v_mfma_f32_32x32x16_bf16 v[48:63], a[192:195], a[160:163], v[80:95]
v_exp_f32 v119, v39
ds_read_b128 a[192:195], v2 offset:0
v_bfe_u32 v21, v0, 5, 1
v_lshl_add_u32 v22, v21, 12, v4
v_mfma_f32_32x32x16_bf16 v[48:63], a[196:199], a[164:167], v[48:63]
v_cvt_pk_bf16_f32 v99, v118, v119
v_exp_f32 v112, v40
ds_read_b128 a[196:199], v2 offset:1024
v_lshl_add_u32 v4, v20, 13, v22
v_mfma_f32_32x32x16_bf16 v[48:63], a[200:203], a[168:171], v[48:63]
v_exp_f32 v113, v41
v_mfma_f32_16x16x32_bf16 v[24:27], v[28:31], v[96:99], 0
ds_read_b128 a[200:203], v2 offset:2048
v_mfma_f32_32x32x16_bf16 v[48:63], a[204:207], a[172:175], v[48:63]
v_cvt_pk_bf16_f32 v100, v112, v113
v_exp_f32 v114, v42
ds_read_b128 a[204:207], v2 offset:3072
v_accvgpr_write_b32 a92, -1
v_mfma_f32_32x32x16_bf16 v[48:63], a[208:211], a[176:179], v[48:63]
v_exp_f32 v115, v43
ds_read_b128 a[208:211], v2 offset:4096
v_accvgpr_write_b32 a93, -1
v_accvgpr_write_b32 a94, -1
v_mfma_f32_32x32x16_bf16 v[48:63], a[212:215], a[180:183], v[48:63]
v_cvt_pk_bf16_f32 v101, v114, v115
v_exp_f32 v116, v44
ds_read_b128 a[212:215], v2 offset:5120
v_accvgpr_write_b32 a95, -1
v_mfma_f32_32x32x16_bf16 v[48:63], a[216:219], a[184:187], v[48:63]
v_exp_f32 v117, v45
ds_read_b128 a[216:219], v2 offset:6144
v_mfma_f32_32x32x16_bf16 v[48:63], a[220:223], a[188:191], v[48:63]
v_cvt_pk_bf16_f32 v102, v116, v117
v_exp_f32 v118, v46
ds_read_b128 a[220:223], v2 offset:7168
v_mfma_f32_32x32x16_bf16 v[192:207], a[224:227], v[104:107], v[192:207]
v_exp_f32 v119, v47
ds_read_b128 a[224:227], v2 offset:57344
v_mfma_f32_32x32x16_bf16 v[208:223], a[228:231], v[104:107], v[208:223]
v_cvt_pk_bf16_f32 v103, v118, v119
v_exp_f32 v120, v48
ds_read_b128 a[228:231], v2 offset:58368
v_mfma_f32_32x32x16_bf16 v[224:239], a[232:235], v[104:107], v[224:239]
v_mfma_f32_16x16x32_bf16 v[24:27], v[28:31], v[100:103], v[24:27]
v_exp_f32 v121, v49
ds_read_b128 a[232:235], v2 offset:59392
v_mfma_f32_32x32x16_bf16 v[240:255], a[236:239], v[104:107], v[240:255]
v_cvt_pk_bf16_f32 v104, v120, v121
v_exp_f32 v122, v50
ds_read_b128 a[236:239], v2 offset:60416
v_mfma_f32_32x32x16_bf16 v[192:207], a[240:243], v[108:111], v[192:207]
v_exp_f32 v123, v51
v_cmp_ngt_f32 vcc, s71, v24
s_cmp_ge_u32 s24, s42
ds_read_b128 a[240:243], v2 offset:61440
v_mfma_f32_32x32x16_bf16 v[208:223], a[244:247], v[108:111], v[208:223]
v_cvt_pk_bf16_f32 v105, v122, v123
v_exp_f32 v124, v52
s_cbranch_vccnz .Lr95
.Lr95b:
s_cbranch_scc1 .Ls95
.Ls95b:
v_mfma_f32_32x32x16_bf16 v[224:239], a[248:251], v[108:111], v[224:239]
v_exp_f32 v125, v53
v_add_f32 v10, v10, v24
ds_read_b128 a[244:247], v2 offset:62464
ds_read_b128 a[248:251], v2 offset:63488
v_mfma_f32_32x32x16_bf16 v[240:255], a[252:255], v[108:111], v[240:255]
v_cvt_pk_bf16_f32 v106, v124, v125
v_exp_f32 v126, v54
ds_read_b128 a[252:255], v2 offset:64512
s_waitcnt lgkmcnt(8)
s_cmp_lt_u32 s24, s27
s_cselect_b32 s22, s52, 0
v_mfma_f32_32x32x16_bf16 v[32:47], a[192:195], a[128:131], v[64:79]
s_add_u32 m0, s36, 24576
v_exp_f32 v127, v55
buffer_load_dwordx4 v1, s[20:23], s24 offen offset:0 lds
v_bfe_u32 v20, v0, 2, 2
v_mfma_f32_32x32x16_bf16 v[32:47], a[196:199], a[132:135], v[32:47]
v_cvt_pk_bf16_f32 v107, v126, v127
v_exp_f32 v120, v56
buffer_load_dwordx4 v1, s[20:23], s24 offen offset:1024 lds
v_lshlrev_b32 v6, 7, v20
v_mfma_f32_32x32x16_bf16 v[32:47], a[200:203], a[136:139], v[32:47]
v_exp_f32 v121, v57
v_mfma_f32_16x16x32_bf16 v[24:27], v[28:31], v[104:107], 0
v_mfma_f32_32x32x16_bf16 v[32:47], a[204:207], a[140:143], v[32:47]
v_cvt_pk_bf16_f32 v108, v120, v121
v_exp_f32 v122, v58
v_bfe_u32 v20, v0, 5, 1
v_lshl_add_u32 v6, v20, 9, v6
v_mfma_f32_32x32x16_bf16 v[32:47], a[208:211], a[144:147], v[32:47]
v_exp_f32 v123, v59
buffer_load_dwordx4 v1, s[20:23], s24 offen offset:2048 lds
v_bfe_u32 v20, v0, 3, 1
v_lshl_add_u32 v6, v20, 6, v6
v_mfma_f32_32x32x16_bf16 v[32:47], a[212:215], a[148:151], v[32:47]
v_cvt_pk_bf16_f32 v109, v122, v123
v_exp_f32 v124, v60
v_bfe_u32 v20, v0, 4, 1
v_lshl_add_u32 v6, v20, 5, v6
v_mfma_f32_32x32x16_bf16 v[32:47], a[216:219], a[152:155], v[32:47]
v_exp_f32 v125, v61
buffer_load_dwordx4 v1, s[20:23], s24 offen offset:3072 lds
s_add_u32 m0, s36, 49152
v_and_b32 v20, 3, v0
v_mfma_f32_32x32x16_bf16 v[32:47], a[220:223], a[156:159], v[32:47]
v_cvt_pk_bf16_f32 v110, v124, v125
v_exp_f32 v126, v62
s_waitcnt lgkmcnt(0)
v_lshl_add_u32 v6, v20, 3, v6
v_mfma_f32_32x32x16_bf16 v[128:143], a[224:227], v[96:99], v[128:143]
v_exp_f32 v127, v63
buffer_load_dwordx4 v3, s[20:23], s24 offen offset:0 lds
v_add_u32 v6, s70, v6
v_xor_b32 v8, 64, v6
v_mfma_f32_32x32x16_bf16 v[144:159], a[228:231], v[96:99], v[144:159]
v_cvt_pk_bf16_f32 v111, v126, v127
v_exp_f32 v112, v32
buffer_load_dwordx4 v3, s[20:23], s24 offen offset:1024 lds
v_mfma_f32_32x32x16_bf16 v[160:175], a[232:235], v[96:99], v[160:175]
v_exp_f32 v113, v33
v_mfma_f32_16x16x32_bf16 v[24:27], v[28:31], v[108:111], v[24:27]
buffer_load_dwordx4 v3, s[20:23], s24 offen offset:2048 lds
v_mfma_f32_32x32x16_bf16 v[176:191], a[236:239], v[96:99], v[176:191]
v_cvt_pk_bf16_f32 v96, v112, v113
v_exp_f32 v114, v34
v_mfma_f32_32x32x16_bf16 v[128:143], a[240:243], v[100:103], v[128:143]
v_exp_f32 v115, v35
v_cmp_ngt_f32 vcc, s71, v24
s_cmp_ge_u32 s24, s43
v_mfma_f32_32x32x16_bf16 v[144:159], a[244:247], v[100:103], v[144:159]
v_cvt_pk_bf16_f32 v97, v114, v115
v_exp_f32 v116, v36
s_cbranch_vccnz .Lr98
.Lr98b:
s_cbranch_scc1 .Ls98
.Ls98b:
v_mfma_f32_32x32x16_bf16 v[160:175], a[248:251], v[100:103], v[160:175]
v_exp_f32 v117, v37
v_add_f32 v11, v11, v24
buffer_load_dwordx4 v3, s[20:23], s24 offen offset:3072 lds
v_mfma_f32_32x32x16_bf16 v[176:191], a[252:255], v[100:103], v[176:191]
v_cvt_pk_bf16_f32 v98, v116, v117
v_exp_f32 v118, v38
s_waitcnt vmcnt(24)
s_barrier
s_add_u32 s24, s24, 0x2000
v_mfma_f32_32x32x16_bf16 v[48:63], a[192:195], a[160:163], v[80:95]
v_exp_f32 v119, v39
ds_read_b128 a[192:195], v2 offset:8192
s_add_u32 m0, s70, 8192
s_add_u32 s37, s28, 0
v_mfma_f32_32x32x16_bf16 v[48:63], a[196:199], a[164:167], v[48:63]
v_cvt_pk_bf16_f32 v99, v118, v119
v_exp_f32 v112, v40
ds_read_b128 a[196:199], v2 offset:9216
buffer_load_dwordx4 v4, s[80:83], s37 offen lds
v_mfma_f32_32x32x16_bf16 v[48:63], a[200:203], a[168:171], v[48:63]
v_exp_f32 v113, v41
v_mfma_f32_16x16x32_bf16 v[24:27], v[28:31], v[96:99], 0
ds_read_b128 a[200:203], v2 offset:10240
v_mfma_f32_32x32x16_bf16 v[48:63], a[204:207], a[172:175], v[48:63]
v_cvt_pk_bf16_f32 v100, v112, v113
v_exp_f32 v114, v42
ds_read_b128 a[204:207], v2 offset:11264
s_add_u32 m0, s70, 9216
v_mfma_f32_32x32x16_bf16 v[48:63], a[208:211], a[176:179], v[48:63]
v_exp_f32 v115, v43
ds_read_b128 a[208:211], v2 offset:12288
s_add_u32 s37, s28, 16384
buffer_load_dwordx4 v4, s[80:83], s37 offen lds
v_mfma_f32_32x32x16_bf16 v[48:63], a[212:215], a[180:183], v[48:63]
v_cvt_pk_bf16_f32 v101, v114, v115
v_exp_f32 v116, v44
ds_read_b128 a[212:215], v2 offset:13312
s_add_u32 m0, s70, 10240
v_mfma_f32_32x32x16_bf16 v[48:63], a[216:219], a[184:187], v[48:63]
v_exp_f32 v117, v45
ds_read_b128 a[216:219], v2 offset:14336
s_add_u32 s37, s28, 2048
buffer_load_dwordx4 v4, s[80:83], s37 offen lds
v_mfma_f32_32x32x16_bf16 v[48:63], a[220:223], a[188:191], v[48:63]
v_cvt_pk_bf16_f32 v102, v116, v117
v_exp_f32 v118, v46
ds_read_b128 a[220:223], v2 offset:15360
s_add_u32 m0, s70, 11264
v_mfma_f32_32x32x16_bf16 v[192:207], a[224:227], v[104:107], v[192:207]
v_exp_f32 v119, v47
ds_read_b128 a[224:227], v2 offset:32768
s_add_u32 s37, s28, 18432
buffer_load_dwordx4 v4, s[80:83], s37 offen lds
v_mfma_f32_32x32x16_bf16 v[208:223], a[228:231], v[104:107], v[208:223]
v_cvt_pk_bf16_f32 v103, v118, v119
v_exp_f32 v120, v48
ds_read_b128 a[228:231], v2 offset:33792
v_mfma_f32_32x32x16_bf16 v[224:239], a[232:235], v[104:107], v[224:239]
v_mfma_f32_16x16x32_bf16 v[24:27], v[28:31], v[100:103], v[24:27]
v_exp_f32 v121, v49
ds_read_b128 a[232:235], v2 offset:34816
v_mfma_f32_32x32x16_bf16 v[240:255], a[236:239], v[104:107], v[240:255]
v_cvt_pk_bf16_f32 v104, v120, v121
v_exp_f32 v122, v50
ds_read_b128 a[236:239], v2 offset:35840
v_mfma_f32_32x32x16_bf16 v[192:207], a[240:243], v[108:111], v[192:207]
v_exp_f32 v123, v51
v_cmp_ngt_f32 vcc, s71, v24
s_cmp_ge_u32 s24, s42
ds_read_b128 a[240:243], v2 offset:36864
v_mfma_f32_32x32x16_bf16 v[208:223], a[244:247], v[108:111], v[208:223]
v_cvt_pk_bf16_f32 v105, v122, v123
v_exp_f32 v124, v52
s_cbranch_vccnz .Lr97
.Lr97b:
s_cbranch_scc1 .Ls97
.Ls97b:
v_mfma_f32_32x32x16_bf16 v[224:239], a[248:251], v[108:111], v[224:239]
v_exp_f32 v125, v53
v_add_f32 v10, v10, v24
ds_read_b128 a[244:247], v2 offset:37888
ds_read_b128 a[248:251], v2 offset:38912
v_mfma_f32_32x32x16_bf16 v[240:255], a[252:255], v[108:111], v[240:255]
v_cvt_pk_bf16_f32 v106, v124, v125
v_exp_f32 v126, v54
ds_read_b128 a[252:255], v2 offset:39936
s_waitcnt lgkmcnt(8)
v_mfma_f32_32x32x16_bf16 v[32:47], a[192:195], a[128:131], v[64:79]
s_add_u32 m0, s36, 0
v_exp_f32 v127, v55
buffer_load_dwordx4 v1, s[20:23], s24 offen offset:0 lds
v_bfe_u32 v20, v0, 3, 3
v_and_b32 v21, 7, v0
v_mfma_f32_32x32x16_bf16 v[32:47], a[196:199], a[132:135], v[32:47]
v_cvt_pk_bf16_f32 v107, v126, v127
v_exp_f32 v120, v56
buffer_load_dwordx4 v1, s[20:23], s24 offen offset:1024 lds
v_min_u32 v21, 6, v21
v_mul_u32_u24 v9, 3584, v20
v_mfma_f32_32x32x16_bf16 v[32:47], a[200:203], a[136:139], v[32:47]
v_exp_f32 v121, v57
v_mfma_f32_16x16x32_bf16 v[24:27], v[28:31], v[104:107], 0
v_mfma_f32_32x32x16_bf16 v[32:47], a[204:207], a[140:143], v[32:47]
v_cvt_pk_bf16_f32 v108, v120, v121
v_exp_f32 v122, v58
v_lshl_add_u32 v9, v21, 4, v9
v_mul_u32_u24 v12, 232, v20
v_lshl_add_u32 v12, v21, 5, v12
v_mfma_f32_32x32x16_bf16 v[32:47], a[208:211], a[144:147], v[32:47]
v_exp_f32 v123, v59
buffer_load_dwordx4 v1, s[20:23], s24 offen offset:2048 lds
v_add_u32 v12, s70, v12
s_lshl_b32 s26, %2, 1
s_add_u32 s26, s26, 0
v_mfma_f32_32x32x16_bf16 v[32:47], a[212:215], a[148:151], v[32:47]
v_cvt_pk_bf16_f32 v109, v122, v123
v_exp_f32 v124, v60
s_and_b32 s39, s26, 31
s_lshr_b32 s53, s26, 5
s_mul_i32 s26, s39, 112
v_mfma_f32_32x32x16_bf16 v[32:47], a[216:219], a[152:155], v[32:47]
v_exp_f32 v125, v61
buffer_load_dwordx4 v1, s[20:23], s24 offen offset:3072 lds
s_add_u32 m0, s36, 57344
s_mul_i32 s28, s53, 114688
s_add_u32 s26, s26, s28
v_mfma_f32_32x32x16_bf16 v[32:47], a[220:223], a[156:159], v[32:47]
v_cvt_pk_bf16_f32 v110, v124, v125
v_exp_f32 v126, v62
s_waitcnt lgkmcnt(0)
s_add_u32 s28, s26, 28672
s_add_u32 s37, s26, 57344
v_mfma_f32_32x32x16_bf16 v[128:143], a[224:227], v[96:99], v[128:143]
v_exp_f32 v127, v63
buffer_load_dwordx4 v3, s[20:23], s24 offen offset:0 lds
s_add_u32 s38, s26, 86016
s_mov_b32 s80, %10
s_and_b32 s81, %11, 0xffff
v_mfma_f32_32x32x16_bf16 v[144:159], a[228:231], v[96:99], v[144:159]
v_cvt_pk_bf16_f32 v111, v126, v127
v_exp_f32 v112, v32
buffer_load_dwordx4 v3, s[20:23], s24 offen offset:1024 lds
s_mov_b32 s82, 0x1c0000
s_mov_b32 s83, 0x20000
v_mfma_f32_32x32x16_bf16 v[160:175], a[232:235], v[96:99], v[160:175]
v_exp_f32 v113, v33
v_mfma_f32_16x16x32_bf16 v[24:27], v[28:31], v[108:111], v[24:27]
buffer_load_dwordx4 v3, s[20:23], s24 offen offset:2048 lds
v_mfma_f32_32x32x16_bf16 v[176:191], a[236:239], v[96:99], v[176:191]
v_cvt_pk_bf16_f32 v96, v112, v113
v_exp_f32 v114, v34
buffer_load_dwordx4 a[0:3], v9, s[80:83], s26 offen
buffer_load_dwordx4 a[8:11], v9, s[80:83], s28 offen
buffer_load_dwordx4 a[16:19], v9, s[80:83], s37 offen
v_mfma_f32_32x32x16_bf16 v[128:143], a[240:243], v[100:103], v[128:143]
v_exp_f32 v115, v35
v_cmp_ngt_f32 vcc, s71, v24
s_cmp_ge_u32 s24, s43
v_mfma_f32_32x32x16_bf16 v[144:159], a[244:247], v[100:103], v[144:159]
v_cvt_pk_bf16_f32 v97, v114, v115
v_exp_f32 v116, v36
s_cbranch_vccnz .Lr100
.Lr100b:
s_cbranch_scc1 .Ls100
.Ls100b:
v_mfma_f32_32x32x16_bf16 v[160:175], a[248:251], v[100:103], v[160:175]
v_exp_f32 v117, v37
v_add_f32 v11, v11, v24
buffer_load_dwordx4 v3, s[20:23], s24 offen offset:3072 lds
buffer_load_dwordx4 a[24:27], v9, s[80:83], s38 offen
s_mov_b32 s80, %12
v_mfma_f32_32x32x16_bf16 v[176:191], a[252:255], v[100:103], v[176:191]
v_cvt_pk_bf16_f32 v98, v116, v117
v_exp_f32 v118, v38
s_waitcnt vmcnt(24)
s_barrier
s_add_u32 s24, s24, 0x2000
v_mfma_f32_32x32x16_bf16 v[48:63], a[192:195], a[160:163], v[80:95]
v_exp_f32 v119, v39
ds_read_b128 a[192:195], v2 offset:16384
s_and_b32 s81, %13, 0xffff
buffer_load_dwordx4 a[4:7], v9, s[80:83], s26 offen
buffer_load_dwordx4 a[12:15], v9, s[80:83], s28 offen
v_mfma_f32_32x32x16_bf16 v[48:63], a[196:199], a[164:167], v[48:63]
v_cvt_pk_bf16_f32 v99, v118, v119
v_exp_f32 v112, v40
ds_read_b128 a[196:199], v2 offset:17408
buffer_load_dwordx4 a[20:23], v9, s[80:83], s37 offen
buffer_load_dwordx4 a[28:31], v9, s[80:83], s38 offen
v_mfma_f32_32x32x16_bf16 v[48:63], a[200:203], a[168:171], v[48:63]
v_exp_f32 v113, v41
v_mfma_f32_16x16x32_bf16 v[24:27], v[28:31], v[96:99], 0
ds_read_b128 a[200:203], v2 offset:18432
v_mfma_f32_32x32x16_bf16 v[48:63], a[204:207], a[172:175], v[48:63]
v_cvt_pk_bf16_f32 v100, v112, v113
v_exp_f32 v114, v42
ds_read_b128 a[204:207], v2 offset:19456
v_mfma_f32_32x32x16_bf16 v[48:63], a[208:211], a[176:179], v[48:63]
v_exp_f32 v115, v43
ds_read_b128 a[208:211], v2 offset:20480
v_mfma_f32_32x32x16_bf16 v[48:63], a[212:215], a[180:183], v[48:63]
v_cvt_pk_bf16_f32 v101, v114, v115
v_exp_f32 v116, v44
ds_read_b128 a[212:215], v2 offset:21504
v_mfma_f32_32x32x16_bf16 v[48:63], a[216:219], a[184:187], v[48:63]
v_exp_f32 v117, v45
ds_read_b128 a[216:219], v2 offset:22528
v_mfma_f32_32x32x16_bf16 v[48:63], a[220:223], a[188:191], v[48:63]
v_cvt_pk_bf16_f32 v102, v116, v117
v_exp_f32 v118, v46
ds_read_b128 a[220:223], v2 offset:23552
v_mfma_f32_32x32x16_bf16 v[192:207], a[224:227], v[104:107], v[192:207]
v_exp_f32 v119, v47
ds_read_b128 a[224:227], v2 offset:40960
v_mfma_f32_32x32x16_bf16 v[208:223], a[228:231], v[104:107], v[208:223]
v_cvt_pk_bf16_f32 v103, v118, v119
v_exp_f32 v120, v48
ds_read_b128 a[228:231], v2 offset:41984
v_mfma_f32_32x32x16_bf16 v[224:239], a[232:235], v[104:107], v[224:239]
v_mfma_f32_16x16x32_bf16 v[24:27], v[28:31], v[100:103], v[24:27]
v_exp_f32 v121, v49
ds_read_b128 a[232:235], v2 offset:43008
v_mfma_f32_32x32x16_bf16 v[240:255], a[236:239], v[104:107], v[240:255]
v_cvt_pk_bf16_f32 v104, v120, v121
v_exp_f32 v122, v50
ds_read_b128 a[236:239], v2 offset:44032
v_mfma_f32_32x32x16_bf16 v[192:207], a[240:243], v[108:111], v[192:207]
v_exp_f32 v123, v51
v_cmp_ngt_f32 vcc, s71, v24
s_cmp_ge_u32 s24, s42
ds_read_b128 a[240:243], v2 offset:45056
v_mfma_f32_32x32x16_bf16 v[208:223], a[244:247], v[108:111], v[208:223]
v_cvt_pk_bf16_f32 v105, v122, v123
v_exp_f32 v124, v52
s_cbranch_vccnz .Lr99
.Lr99b:
s_cbranch_scc1 .Ls99
.Ls99b:
v_mfma_f32_32x32x16_bf16 v[224:239], a[248:251], v[108:111], v[224:239]
v_exp_f32 v125, v53
v_add_f32 v10, v10, v24
ds_read_b128 a[244:247], v2 offset:46080
ds_read_b128 a[248:251], v2 offset:47104
v_mfma_f32_32x32x16_bf16 v[240:255], a[252:255], v[108:111], v[240:255]
v_cvt_pk_bf16_f32 v106, v124, v125
v_exp_f32 v126, v54
ds_read_b128 a[252:255], v2 offset:48128
s_waitcnt lgkmcnt(8)
v_mfma_f32_32x32x16_bf16 v[32:47], a[192:195], a[128:131], v[64:79]
s_add_u32 m0, s36, 8192
v_exp_f32 v127, v55
buffer_load_dwordx4 v1, s[20:23], s24 offen offset:0 lds
ds_read_b64_tr_b16 a[96:97], v6 offset:0
ds_read_b64_tr_b16 a[98:99], v6 offset:1024
v_mfma_f32_32x32x16_bf16 v[32:47], a[196:199], a[132:135], v[32:47]
v_cvt_pk_bf16_f32 v107, v126, v127
v_exp_f32 v120, v56
buffer_load_dwordx4 v1, s[20:23], s24 offen offset:1024 lds
ds_read_b64_tr_b16 a[100:101], v8 offset:0
ds_read_b64_tr_b16 a[102:103], v8 offset:1024
v_mfma_f32_32x32x16_bf16 v[32:47], a[200:203], a[136:139], v[32:47]
v_exp_f32 v121, v57
v_mfma_f32_16x16x32_bf16 v[24:27], v[28:31], v[104:107], 0
v_mfma_f32_32x32x16_bf16 v[32:47], a[204:207], a[140:143], v[32:47]
v_cvt_pk_bf16_f32 v108, v120, v121
v_exp_f32 v122, v58
ds_read_b64_tr_b16 a[104:105], v6 offset:2048
ds_read_b64_tr_b16 a[106:107], v6 offset:3072
ds_read_b64_tr_b16 a[108:109], v8 offset:2048
v_mfma_f32_32x32x16_bf16 v[32:47], a[208:211], a[144:147], v[32:47]
v_exp_f32 v123, v59
buffer_load_dwordx4 v1, s[20:23], s24 offen offset:2048 lds
ds_read_b64_tr_b16 a[110:111], v8 offset:3072
ds_read_b64_tr_b16 a[112:113], v6 offset:4096
ds_read_b64_tr_b16 a[114:115], v6 offset:5120
v_mfma_f32_32x32x16_bf16 v[32:47], a[212:215], a[148:151], v[32:47]
v_cvt_pk_bf16_f32 v109, v122, v123
v_exp_f32 v124, v60
ds_read_b64_tr_b16 a[116:117], v8 offset:4096
ds_read_b64_tr_b16 a[118:119], v8 offset:5120
ds_read_b64_tr_b16 a[120:121], v6 offset:6144
v_mfma_f32_32x32x16_bf16 v[32:47], a[216:219], a[152:155], v[32:47]
v_exp_f32 v125, v61
buffer_load_dwordx4 v1, s[20:23], s24 offen offset:3072 lds
s_add_u32 m0, s36, 32768
ds_read_b64_tr_b16 a[122:123], v6 offset:7168
ds_read_b64_tr_b16 a[124:125], v8 offset:6144
v_mfma_f32_32x32x16_bf16 v[32:47], a[220:223], a[156:159], v[32:47]
v_cvt_pk_bf16_f32 v110, v124, v125
v_exp_f32 v126, v62
s_waitcnt lgkmcnt(0)
ds_read_b64_tr_b16 a[126:127], v8 offset:7168
v_mfma_f32_32x32x16_bf16 v[128:143], a[224:227], v[96:99], v[128:143]
v_exp_f32 v127, v63
buffer_load_dwordx4 v3, s[20:23], s24 offen offset:0 lds
v_mfma_f32_32x32x16_bf16 v[144:159], a[228:231], v[96:99], v[144:159]
v_cvt_pk_bf16_f32 v111, v126, v127
v_exp_f32 v112, v32
buffer_load_dwordx4 v3, s[20:23], s24 offen offset:1024 lds
v_mfma_f32_32x32x16_bf16 v[160:175], a[232:235], v[96:99], v[160:175]
v_exp_f32 v113, v33
v_mfma_f32_16x16x32_bf16 v[24:27], v[28:31], v[108:111], v[24:27]
buffer_load_dwordx4 v3, s[20:23], s24 offen offset:2048 lds
v_mfma_f32_32x32x16_bf16 v[176:191], a[236:239], v[96:99], v[176:191]
v_cvt_pk_bf16_f32 v96, v112, v113
v_exp_f32 v114, v34
v_mfma_f32_32x32x16_bf16 v[128:143], a[240:243], v[100:103], v[128:143]
v_exp_f32 v115, v35
v_cmp_ngt_f32 vcc, s71, v24
s_cmp_ge_u32 s24, s43
v_mfma_f32_32x32x16_bf16 v[144:159], a[244:247], v[100:103], v[144:159]
v_cvt_pk_bf16_f32 v97, v114, v115
v_exp_f32 v116, v36
s_cbranch_vccnz .Lr102
.Lr102b:
s_cbranch_scc1 .Ls102
.Ls102b:
v_mfma_f32_32x32x16_bf16 v[160:175], a[248:251], v[100:103], v[160:175]
v_exp_f32 v117, v37
v_add_f32 v11, v11, v24
buffer_load_dwordx4 v3, s[20:23], s24 offen offset:3072 lds
v_mfma_f32_32x32x16_bf16 v[176:191], a[252:255], v[100:103], v[176:191]
v_cvt_pk_bf16_f32 v98, v116, v117
v_exp_f32 v118, v38
s_waitcnt vmcnt(28)
s_barrier
s_add_u32 s24, s24, 0x2000
v_mfma_f32_32x32x16_bf16 v[48:63], a[192:195], a[160:163], v[80:95]
v_exp_f32 v119, v39
ds_read_b128 a[192:195], v2 offset:24576
s_lshl_b32 s26, %2, 12
s_add_u32 s26, s26, 0x5c00000
s_add_u32 s28, s26, 1048576
v_mfma_f32_32x32x16_bf16 v[48:63], a[196:199], a[164:167], v[48:63]
v_cvt_pk_bf16_f32 v99, v118, v119
v_exp_f32 v112, v40
ds_read_b128 a[196:199], v2 offset:25600
s_bfe_u32 s37, %2, 0x20002
s_mul_i32 s37, s37, 112
v_mfma_f32_32x32x16_bf16 v[48:63], a[200:203], a[168:171], v[48:63]
v_exp_f32 v113, v41
v_mfma_f32_16x16x32_bf16 v[24:27], v[28:31], v[96:99], 0
ds_read_b128 a[200:203], v2 offset:26624
v_mfma_f32_32x32x16_bf16 v[48:63], a[204:207], a[172:175], v[48:63]
v_cvt_pk_bf16_f32 v100, v112, v113
v_exp_f32 v114, v42
ds_read_b128 a[204:207], v2 offset:27648
s_lshr_b32 s38, %2, 4
s_mul_i32 s38, s38, 7
v_mfma_f32_32x32x16_bf16 v[48:63], a[208:211], a[176:179], v[48:63]
v_exp_f32 v115, v43
ds_read_b128 a[208:211], v2 offset:28672
s_add_u32 s37, s37, s38
s_lshl_b32 s37, s37, 1
s_bfe_u32 s38, %2, 0x10001
v_mfma_f32_32x32x16_bf16 v[48:63], a[212:215], a[180:183], v[48:63]
v_cvt_pk_bf16_f32 v101, v114, v115
v_exp_f32 v116, v44
ds_read_b128 a[212:215], v2 offset:29696
s_add_u32 s37, s37, s38
s_lshl_b32 s37, s37, 11
v_mfma_f32_32x32x16_bf16 v[48:63], a[216:219], a[184:187], v[48:63]
v_exp_f32 v117, v45
ds_read_b128 a[216:219], v2 offset:30720
s_add_u32 s37, s37, 0x6000000
s_and_b32 s38, %2, 1
s_lshl_b32 s38, s38, 14
v_mfma_f32_32x32x16_bf16 v[48:63], a[220:223], a[188:191], v[48:63]
v_cvt_pk_bf16_f32 v102, v116, v117
v_exp_f32 v118, v46
ds_read_b128 a[220:223], v2 offset:31744
s_add_u32 s38, s38, s37
s_add_u32 s37, s38, 4096
v_mfma_f32_32x32x16_bf16 v[192:207], a[224:227], v[104:107], v[192:207]
v_exp_f32 v119, v47
ds_read_b128 a[224:227], v2 offset:49152
v_mfma_f32_32x32x16_bf16 v[208:223], a[228:231], v[104:107], v[208:223]
v_cvt_pk_bf16_f32 v103, v118, v119
v_exp_f32 v120, v48
ds_read_b128 a[228:231], v2 offset:50176
v_mfma_f32_32x32x16_bf16 v[224:239], a[232:235], v[104:107], v[224:239]
v_mfma_f32_16x16x32_bf16 v[24:27], v[28:31], v[100:103], v[24:27]
v_exp_f32 v121, v49
ds_read_b128 a[232:235], v2 offset:51200
v_mfma_f32_32x32x16_bf16 v[240:255], a[236:239], v[104:107], v[240:255]
v_cvt_pk_bf16_f32 v104, v120, v121
v_exp_f32 v122, v50
ds_read_b128 a[236:239], v2 offset:52224
v_mfma_f32_32x32x16_bf16 v[192:207], a[240:243], v[108:111], v[192:207]
v_exp_f32 v123, v51
v_cmp_ngt_f32 vcc, s71, v24
s_cmp_ge_u32 s24, s42
ds_read_b128 a[240:243], v2 offset:53248
v_mfma_f32_32x32x16_bf16 v[208:223], a[244:247], v[108:111], v[208:223]
v_cvt_pk_bf16_f32 v105, v122, v123
v_exp_f32 v124, v52
s_cbranch_vccnz .Lr101
.Lr101b:
s_cbranch_scc1 .Ls101
.Ls101b:
v_mfma_f32_32x32x16_bf16 v[224:239], a[248:251], v[108:111], v[224:239]
v_exp_f32 v125, v53
v_add_f32 v10, v10, v24
ds_read_b128 a[244:247], v2 offset:54272
ds_read_b128 a[248:251], v2 offset:55296
v_mfma_f32_32x32x16_bf16 v[240:255], a[252:255], v[108:111], v[240:255]
v_cvt_pk_bf16_f32 v106, v124, v125
v_exp_f32 v126, v54
ds_read_b128 a[252:255], v2 offset:56320
s_waitcnt lgkmcnt(8)
v_mfma_f32_32x32x16_bf16 v[32:47], a[192:195], a[128:131], v[64:79]
s_add_u32 m0, s36, 16384
v_exp_f32 v127, v55
buffer_load_dwordx4 v1, s[20:23], s24 offen offset:0 lds
v_mfma_f32_32x32x16_bf16 v[32:47], a[196:199], a[132:135], v[32:47]
v_cvt_pk_bf16_f32 v107, v126, v127
v_exp_f32 v120, v56
buffer_load_dwordx4 v1, s[20:23], s24 offen offset:1024 lds
v_mfma_f32_32x32x16_bf16 v[32:47], a[200:203], a[136:139], v[32:47]
v_exp_f32 v121, v57
v_mfma_f32_16x16x32_bf16 v[24:27], v[28:31], v[104:107], 0
v_mfma_f32_32x32x16_bf16 v[32:47], a[204:207], a[140:143], v[32:47]
v_cvt_pk_bf16_f32 v108, v120, v121
v_exp_f32 v122, v58
v_mfma_f32_32x32x16_bf16 v[32:47], a[208:211], a[144:147], v[32:47]
v_exp_f32 v123, v59
buffer_load_dwordx4 v1, s[20:23], s24 offen offset:2048 lds
v_mfma_f32_32x32x16_bf16 v[32:47], a[212:215], a[148:151], v[32:47]
v_cvt_pk_bf16_f32 v109, v122, v123
v_exp_f32 v124, v60
v_mfma_f32_32x32x16_bf16 v[32:47], a[216:219], a[152:155], v[32:47]
v_exp_f32 v125, v61
buffer_load_dwordx4 v1, s[20:23], s24 offen offset:3072 lds
s_add_u32 m0, s36, 40960
v_mfma_f32_32x32x16_bf16 v[32:47], a[220:223], a[156:159], v[32:47]
v_cvt_pk_bf16_f32 v110, v124, v125
v_exp_f32 v126, v62
s_waitcnt lgkmcnt(0)
v_mfma_f32_32x32x16_bf16 v[128:143], a[224:227], v[96:99], v[128:143]
v_exp_f32 v127, v63
buffer_load_dwordx4 v3, s[20:23], s24 offen offset:0 lds
buffer_store_dwordx4 a[96:99], v1, s[16:19], s26 offen offset:0 sc1
buffer_store_dwordx4 a[100:103], v1, s[16:19], s26 offen offset:1024 sc1
v_mfma_f32_32x32x16_bf16 v[144:159], a[228:231], v[96:99], v[144:159]
v_cvt_pk_bf16_f32 v111, v126, v127
v_exp_f32 v112, v32
buffer_load_dwordx4 v3, s[20:23], s24 offen offset:1024 lds
buffer_store_dwordx4 a[104:107], v1, s[16:19], s26 offen offset:2048 sc1
v_mfma_f32_32x32x16_bf16 v[160:175], a[232:235], v[96:99], v[160:175]
v_exp_f32 v113, v33
v_mfma_f32_16x16x32_bf16 v[24:27], v[28:31], v[108:111], v[24:27]
buffer_load_dwordx4 v3, s[20:23], s24 offen offset:2048 lds
v_mfma_f32_32x32x16_bf16 v[176:191], a[236:239], v[96:99], v[176:191]
v_cvt_pk_bf16_f32 v96, v112, v113
v_exp_f32 v114, v34
buffer_store_dwordx4 a[108:111], v1, s[16:19], s26 offen offset:3072 sc1
buffer_store_dwordx4 a[112:115], v1, s[16:19], s28 offen offset:0 sc1
v_mfma_f32_32x32x16_bf16 v[128:143], a[240:243], v[100:103], v[128:143]
v_exp_f32 v115, v35
v_cmp_ngt_f32 vcc, s71, v24
s_cmp_ge_u32 s24, s43
v_mfma_f32_32x32x16_bf16 v[144:159], a[244:247], v[100:103], v[144:159]
v_cvt_pk_bf16_f32 v97, v114, v115
v_exp_f32 v116, v36
s_cbranch_vccnz .Lr104
.Lr104b:
s_cbranch_scc1 .Ls104
.Ls104b:
v_mfma_f32_32x32x16_bf16 v[160:175], a[248:251], v[100:103], v[160:175]
v_exp_f32 v117, v37
v_add_f32 v11, v11, v24
buffer_load_dwordx4 v3, s[20:23], s24 offen offset:3072 lds
buffer_store_dwordx4 a[116:119], v1, s[16:19], s28 offen offset:1024 sc1
v_mfma_f32_32x32x16_bf16 v[176:191], a[252:255], v[100:103], v[176:191]
v_cvt_pk_bf16_f32 v98, v116, v117
v_exp_f32 v118, v38
s_waitcnt vmcnt(26)
s_barrier
s_add_u32 s24, s24, 0x2000
v_mfma_f32_32x32x16_bf16 v[48:63], a[192:195], a[160:163], v[80:95]
v_exp_f32 v119, v39
ds_read_b128 a[192:195], v2 offset:0
buffer_store_dwordx4 a[120:123], v1, s[16:19], s28 offen offset:2048 sc1
buffer_store_dwordx4 a[124:127], v1, s[16:19], s28 offen offset:3072 sc1
v_mfma_f32_32x32x16_bf16 v[48:63], a[196:199], a[164:167], v[48:63]
v_cvt_pk_bf16_f32 v99, v118, v119
v_exp_f32 v112, v40
ds_read_b128 a[196:199], v2 offset:1024
v_mfma_f32_32x32x16_bf16 v[48:63], a[200:203], a[168:171], v[48:63]
v_exp_f32 v113, v41
v_mfma_f32_16x16x32_bf16 v[24:27], v[28:31], v[96:99], 0
ds_read_b128 a[200:203], v2 offset:2048
v_mfma_f32_32x32x16_bf16 v[48:63], a[204:207], a[172:175], v[48:63]
v_cvt_pk_bf16_f32 v100, v112, v113
v_exp_f32 v114, v42
ds_read_b128 a[204:207], v2 offset:3072
v_mfma_f32_32x32x16_bf16 v[48:63], a[208:211], a[176:179], v[48:63]
v_exp_f32 v115, v43
ds_read_b128 a[208:211], v2 offset:4096
v_mfma_f32_32x32x16_bf16 v[48:63], a[212:215], a[180:183], v[48:63]
v_cvt_pk_bf16_f32 v101, v114, v115
v_exp_f32 v116, v44
ds_read_b128 a[212:215], v2 offset:5120
v_mfma_f32_32x32x16_bf16 v[48:63], a[216:219], a[184:187], v[48:63]
v_exp_f32 v117, v45
ds_read_b128 a[216:219], v2 offset:6144
v_mfma_f32_32x32x16_bf16 v[48:63], a[220:223], a[188:191], v[48:63]
v_cvt_pk_bf16_f32 v102, v116, v117
v_exp_f32 v118, v46
ds_read_b128 a[220:223], v2 offset:7168
v_mfma_f32_32x32x16_bf16 v[192:207], a[224:227], v[104:107], v[192:207]
v_exp_f32 v119, v47
ds_read_b128 a[224:227], v2 offset:57344
v_mfma_f32_32x32x16_bf16 v[208:223], a[228:231], v[104:107], v[208:223]
v_cvt_pk_bf16_f32 v103, v118, v119
v_exp_f32 v120, v48
ds_read_b128 a[228:231], v2 offset:58368
v_mfma_f32_32x32x16_bf16 v[224:239], a[232:235], v[104:107], v[224:239]
v_mfma_f32_16x16x32_bf16 v[24:27], v[28:31], v[100:103], v[24:27]
v_exp_f32 v121, v49
ds_read_b128 a[232:235], v2 offset:59392
v_mfma_f32_32x32x16_bf16 v[240:255], a[236:239], v[104:107], v[240:255]
v_cvt_pk_bf16_f32 v104, v120, v121
v_exp_f32 v122, v50
ds_read_b128 a[236:239], v2 offset:60416
v_mfma_f32_32x32x16_bf16 v[192:207], a[240:243], v[108:111], v[192:207]
v_exp_f32 v123, v51
v_cmp_ngt_f32 vcc, s71, v24
s_cmp_ge_u32 s24, s42
ds_read_b128 a[240:243], v2 offset:61440
v_mfma_f32_32x32x16_bf16 v[208:223], a[244:247], v[108:111], v[208:223]
v_cvt_pk_bf16_f32 v105, v122, v123
v_exp_f32 v124, v52
s_cbranch_vccnz .Lr103
.Lr103b:
s_cbranch_scc1 .Ls103
.Ls103b:
v_mfma_f32_32x32x16_bf16 v[224:239], a[248:251], v[108:111], v[224:239]
v_exp_f32 v125, v53
v_add_f32 v10, v10, v24
ds_read_b128 a[244:247], v2 offset:62464
ds_read_b128 a[248:251], v2 offset:63488
v_mfma_f32_32x32x16_bf16 v[240:255], a[252:255], v[108:111], v[240:255]
v_cvt_pk_bf16_f32 v106, v124, v125
v_exp_f32 v126, v54
ds_read_b128 a[252:255], v2 offset:64512
s_waitcnt lgkmcnt(8)
s_cmp_lt_u32 s24, s27
s_cselect_b32 s22, s52, 0
v_mfma_f32_32x32x16_bf16 v[32:47], a[192:195], a[128:131], v[64:79]
s_add_u32 m0, s36, 24576
v_exp_f32 v127, v55
buffer_load_dwordx4 v1, s[20:23], s24 offen offset:0 lds
ds_read_b64_tr_b16 a[96:97], v6 offset:8192
ds_read_b64_tr_b16 a[98:99], v6 offset:9216
v_mfma_f32_32x32x16_bf16 v[32:47], a[196:199], a[132:135], v[32:47]
v_cvt_pk_bf16_f32 v107, v126, v127
v_exp_f32 v120, v56
buffer_load_dwordx4 v1, s[20:23], s24 offen offset:1024 lds
ds_read_b64_tr_b16 a[100:101], v8 offset:8192
ds_read_b64_tr_b16 a[102:103], v8 offset:9216
v_mfma_f32_32x32x16_bf16 v[32:47], a[200:203], a[136:139], v[32:47]
v_exp_f32 v121, v57
v_mfma_f32_16x16x32_bf16 v[24:27], v[28:31], v[104:107], 0
v_mfma_f32_32x32x16_bf16 v[32:47], a[204:207], a[140:143], v[32:47]
v_cvt_pk_bf16_f32 v108, v120, v121
v_exp_f32 v122, v58
ds_read_b64_tr_b16 a[104:105], v6 offset:10240
ds_read_b64_tr_b16 a[106:107], v6 offset:11264
ds_read_b64_tr_b16 a[108:109], v8 offset:10240
v_mfma_f32_32x32x16_bf16 v[32:47], a[208:211], a[144:147], v[32:47]
v_exp_f32 v123, v59
buffer_load_dwordx4 v1, s[20:23], s24 offen offset:2048 lds
ds_read_b64_tr_b16 a[110:111], v8 offset:11264
v_mfma_f32_32x32x16_bf16 v[32:47], a[212:215], a[148:151], v[32:47]
v_cvt_pk_bf16_f32 v109, v122, v123
v_exp_f32 v124, v60
v_mfma_f32_32x32x16_bf16 v[32:47], a[216:219], a[152:155], v[32:47]
v_exp_f32 v125, v61
buffer_load_dwordx4 v1, s[20:23], s24 offen offset:3072 lds
s_add_u32 m0, s36, 49152
v_mfma_f32_32x32x16_bf16 v[32:47], a[220:223], a[156:159], v[32:47]
v_cvt_pk_bf16_f32 v110, v124, v125
v_exp_f32 v126, v62
s_waitcnt lgkmcnt(0)
v_mfma_f32_32x32x16_bf16 v[128:143], a[224:227], v[96:99], v[128:143]
v_exp_f32 v127, v63
buffer_load_dwordx4 v3, s[20:23], s24 offen offset:0 lds
buffer_store_dwordx4 a[96:99], v1, s[16:19], s38 offen offset:0 sc1
buffer_store_dwordx4 a[100:103], v1, s[16:19], s38 offen offset:1024 sc1
buffer_store_dwordx4 a[104:107], v1, s[16:19], s37 offen offset:0 sc1
v_mfma_f32_32x32x16_bf16 v[144:159], a[228:231], v[96:99], v[144:159]
v_cvt_pk_bf16_f32 v111, v126, v127
v_exp_f32 v112, v32
buffer_load_dwordx4 v3, s[20:23], s24 offen offset:1024 lds
buffer_store_dwordx4 a[108:111], v1, s[16:19], s37 offen offset:1024 sc1
v_bfe_u32 v20, v0, 5, 1
v_mfma_f32_32x32x16_bf16 v[160:175], a[232:235], v[96:99], v[160:175]
v_exp_f32 v113, v33
v_mfma_f32_16x16x32_bf16 v[24:27], v[28:31], v[108:111], v[24:27]
buffer_load_dwordx4 v3, s[20:23], s24 offen offset:2048 lds
v_mfma_f32_32x32x16_bf16 v[176:191], a[236:239], v[96:99], v[176:191]
v_cvt_pk_bf16_f32 v96, v112, v113
v_exp_f32 v114, v34
v_lshlrev_b32 v20, 4, v20
v_bfe_u32 v21, v0, 4, 1
v_lshl_add_u32 v20, v21, 2, v20
v_mfma_f32_32x32x16_bf16 v[128:143], a[240:243], v[100:103], v[128:143]
v_exp_f32 v115, v35
v_cmp_ngt_f32 vcc, s71, v24
s_cmp_ge_u32 s24, s43
v_mfma_f32_32x32x16_bf16 v[144:159], a[244:247], v[100:103], v[144:159]
v_cvt_pk_bf16_f32 v97, v114, v115
v_exp_f32 v116, v36
s_cbranch_vccnz .Lr106
.Lr106b:
s_cbranch_scc1 .Ls106
.Ls106b:
v_mfma_f32_32x32x16_bf16 v[160:175], a[248:251], v[100:103], v[160:175]
v_exp_f32 v117, v37
v_add_f32 v11, v11, v24
buffer_load_dwordx4 v3, s[20:23], s24 offen offset:3072 lds
v_bfe_u32 v21, v0, 2, 2
v_add_u32 v20, v20, v21
v_mfma_f32_32x32x16_bf16 v[176:191], a[252:255], v[100:103], v[176:191]
v_cvt_pk_bf16_f32 v98, v116, v117
v_exp_f32 v118, v38
s_waitcnt vmcnt(28)
s_barrier
s_add_u32 s24, s24, 0x2000
v_mfma_f32_32x32x16_bf16 v[48:63], a[192:195], a[160:163], v[80:95]
v_exp_f32 v119, v39
ds_read_b128 a[192:195], v2 offset:8192
v_mul_u32_u24 v20, 232, v20
v_and_b32 v21, 3, v0
v_lshl_add_u32 v20, v21, 3, v20
v_mfma_f32_32x32x16_bf16 v[48:63], a[196:199], a[164:167], v[48:63]
v_cvt_pk_bf16_f32 v99, v118, v119
v_exp_f32 v112, v40
ds_read_b128 a[196:199], v2 offset:9216
v_add_u32 v9, s70, v20
v_and_b32 v13, 15, v0
v_mfma_f32_32x32x16_bf16 v[48:63], a[200:203], a[168:171], v[48:63]
v_exp_f32 v113, v41
v_mfma_f32_16x16x32_bf16 v[24:27], v[28:31], v[96:99], 0
ds_read_b128 a[200:203], v2 offset:10240
v_mfma_f32_32x32x16_bf16 v[48:63], a[204:207], a[172:175], v[48:63]
v_cvt_pk_bf16_f32 v100, v112, v113
v_exp_f32 v114, v42
ds_read_b128 a[204:207], v2 offset:11264
v_lshlrev_b32 v13, 4, v13
v_mov_b32 v20, 0x3f803f80
v_mfma_f32_32x32x16_bf16 v[48:63], a[208:211], a[176:179], v[48:63]
v_exp_f32 v115, v43
ds_read_b128 a[208:211], v2 offset:12288
v_accvgpr_write_b32 a88, v20
v_accvgpr_write_b32 a89, v20
v_accvgpr_write_b32 a90, v20
v_mfma_f32_32x32x16_bf16 v[48:63], a[212:215], a[180:183], v[48:63]
v_cvt_pk_bf16_f32 v101, v114, v115
v_exp_f32 v116, v44
ds_read_b128 a[212:215], v2 offset:13312
v_accvgpr_write_b32 a91, v20
v_mfma_f32_32x32x16_bf16 v[48:63], a[216:219], a[184:187], v[48:63]
v_exp_f32 v117, v45
ds_read_b128 a[216:219], v2 offset:14336
v_mfma_f32_32x32x16_bf16 v[48:63], a[220:223], a[188:191], v[48:63]
v_cvt_pk_bf16_f32 v102, v116, v117
v_exp_f32 v118, v46
ds_read_b128 a[220:223], v2 offset:15360
v_mfma_f32_32x32x16_bf16 v[192:207], a[224:227], v[104:107], v[192:207]
v_exp_f32 v119, v47
ds_read_b128 a[224:227], v2 offset:32768
v_mfma_f32_32x32x16_bf16 v[208:223], a[228:231], v[104:107], v[208:223]
v_cvt_pk_bf16_f32 v103, v118, v119
v_exp_f32 v120, v48
ds_read_b128 a[228:231], v2 offset:33792
v_mfma_f32_32x32x16_bf16 v[224:239], a[232:235], v[104:107], v[224:239]
v_mfma_f32_16x16x32_bf16 v[24:27], v[28:31], v[100:103], v[24:27]
v_exp_f32 v121, v49
ds_read_b128 a[232:235], v2 offset:34816
v_mfma_f32_32x32x16_bf16 v[240:255], a[236:239], v[104:107], v[240:255]
v_cvt_pk_bf16_f32 v104, v120, v121
v_exp_f32 v122, v50
ds_read_b128 a[236:239], v2 offset:35840
v_mfma_f32_32x32x16_bf16 v[192:207], a[240:243], v[108:111], v[192:207]
v_exp_f32 v123, v51
v_cmp_ngt_f32 vcc, s71, v24
s_cmp_ge_u32 s24, s42
ds_read_b128 a[240:243], v2 offset:36864
v_mfma_f32_32x32x16_bf16 v[208:223], a[244:247], v[108:111], v[208:223]
v_cvt_pk_bf16_f32 v105, v122, v123
v_exp_f32 v124, v52
s_cbranch_vccnz .Lr105
.Lr105b:
s_cbranch_scc1 .Ls105
.Ls105b:
v_mfma_f32_32x32x16_bf16 v[224:239], a[248:251], v[108:111], v[224:239]
v_exp_f32 v125, v53
v_add_f32 v10, v10, v24
ds_read_b128 a[244:247], v2 offset:37888
ds_read_b128 a[248:251], v2 offset:38912
v_mfma_f32_32x32x16_bf16 v[240:255], a[252:255], v[108:111], v[240:255]
v_cvt_pk_bf16_f32 v106, v124, v125
v_exp_f32 v126, v54
ds_read_b128 a[252:255], v2 offset:39936
s_waitcnt lgkmcnt(8)
v_mfma_f32_32x32x16_bf16 v[32:47], a[192:195], a[128:131], v[64:79]
s_add_u32 m0, s36, 0
v_exp_f32 v127, v55
buffer_load_dwordx4 v1, s[20:23], s24 offen offset:0 lds
ds_write2_b32 v12, a0, a4 offset0:0 offset1:1
ds_write2_b32 v12, a2, a6 offset0:2 offset1:3
v_mfma_f32_32x32x16_bf16 v[32:47], a[196:199], a[132:135], v[32:47]
v_cvt_pk_bf16_f32 v107, v126, v127
v_exp_f32 v120, v56
buffer_load_dwordx4 v1, s[20:23], s24 offen offset:1024 lds
ds_write2_b32 v12, a1, a5 offset0:4 offset1:5
ds_write2_b32 v12, a3, a7 offset0:6 offset1:7
v_mfma_f32_32x32x16_bf16 v[32:47], a[200:203], a[136:139], v[32:47]
v_exp_f32 v121, v57
v_mfma_f32_16x16x32_bf16 v[24:27], v[28:31], v[104:107], 0
v_mfma_f32_32x32x16_bf16 v[32:47], a[204:207], a[140:143], v[32:47]
v_cvt_pk_bf16_f32 v108, v120, v121
v_exp_f32 v122, v58
v_add_u32 v12, 1856, v12
ds_write2_b32 v12, a8, a12 offset0:0 offset1:1
ds_write2_b32 v12, a10, a14 offset0:2 offset1:3
v_mfma_f32_32x32x16_bf16 v[32:47], a[208:211], a[144:147], v[32:47]
v_exp_f32 v123, v59
buffer_load_dwordx4 v1, s[20:23], s24 offen offset:2048 lds
ds_write2_b32 v12, a9, a13 offset0:4 offset1:5
ds_write2_b32 v12, a11, a15 offset0:6 offset1:7
s_nop 1
v_add_u32 v12, 1856, v12
v_mfma_f32_32x32x16_bf16 v[32:47], a[212:215], a[148:151], v[32:47]
v_cvt_pk_bf16_f32 v109, v122, v123
v_exp_f32 v124, v60
ds_write2_b32 v12, a16, a20 offset0:0 offset1:1
ds_write2_b32 v12, a18, a22 offset0:2 offset1:3
ds_write2_b32 v12, a17, a21 offset0:4 offset1:5
v_mfma_f32_32x32x16_bf16 v[32:47], a[216:219], a[152:155], v[32:47]
v_exp_f32 v125, v61
buffer_load_dwordx4 v1, s[20:23], s24 offen offset:3072 lds
s_add_u32 m0, s36, 57344
ds_write2_b32 v12, a19, a23 offset0:6 offset1:7
s_nop 1
v_add_u32 v12, 1856, v12
v_mfma_f32_32x32x16_bf16 v[32:47], a[220:223], a[156:159], v[32:47]
v_cvt_pk_bf16_f32 v110, v124, v125
v_exp_f32 v126, v62
s_waitcnt lgkmcnt(0)
ds_write2_b32 v12, a24, a28 offset0:0 offset1:1
ds_write2_b32 v12, a26, a30 offset0:2 offset1:3
v_mfma_f32_32x32x16_bf16 v[128:143], a[224:227], v[96:99], v[128:143]
v_exp_f32 v127, v63
buffer_load_dwordx4 v3, s[20:23], s24 offen offset:0 lds
ds_write2_b32 v12, a25, a29 offset0:4 offset1:5
ds_write2_b32 v12, a27, a31 offset0:6 offset1:7
v_mfma_f32_32x32x16_bf16 v[144:159], a[228:231], v[96:99], v[144:159]
v_cvt_pk_bf16_f32 v111, v126, v127
v_exp_f32 v112, v32
buffer_load_dwordx4 v3, s[20:23], s24 offen offset:1024 lds
v_mfma_f32_32x32x16_bf16 v[160:175], a[232:235], v[96:99], v[160:175]
v_exp_f32 v113, v33
v_mfma_f32_16x16x32_bf16 v[24:27], v[28:31], v[108:111], v[24:27]
buffer_load_dwordx4 v3, s[20:23], s24 offen offset:2048 lds
v_mfma_f32_32x32x16_bf16 v[176:191], a[236:239], v[96:99], v[176:191]
v_cvt_pk_bf16_f32 v96, v112, v113
v_exp_f32 v114, v34
v_mfma_f32_32x32x16_bf16 v[128:143], a[240:243], v[100:103], v[128:143]
v_exp_f32 v115, v35
v_cmp_ngt_f32 vcc, s71, v24
s_cmp_ge_u32 s24, s43
v_mfma_f32_32x32x16_bf16 v[144:159], a[244:247], v[100:103], v[144:159]
v_cvt_pk_bf16_f32 v97, v114, v115
v_exp_f32 v116, v36
s_cbranch_vccnz .Lr108
.Lr108b:
s_cbranch_scc1 .Ls108
.Ls108b:
v_mfma_f32_32x32x16_bf16 v[160:175], a[248:251], v[100:103], v[160:175]
v_exp_f32 v117, v37
v_add_f32 v11, v11, v24
buffer_load_dwordx4 v3, s[20:23], s24 offen offset:3072 lds
v_mfma_f32_32x32x16_bf16 v[176:191], a[252:255], v[100:103], v[176:191]
v_cvt_pk_bf16_f32 v98, v116, v117
v_exp_f32 v118, v38
s_waitcnt vmcnt(22)
s_barrier
s_add_u32 s24, s24, 0x2000
v_mfma_f32_32x32x16_bf16 v[48:63], a[192:195], a[160:163], v[80:95]
v_exp_f32 v119, v39
ds_read_b128 a[192:195], v2 offset:16384
s_mul_i32 s26, s39, 122880
s_add_u32 s26, s26, 0x5800000
s_mul_i32 s28, s53, 7168
v_mfma_f32_32x32x16_bf16 v[48:63], a[196:199], a[164:167], v[48:63]
v_cvt_pk_bf16_f32 v99, v118, v119
v_exp_f32 v112, v40
ds_read_b128 a[196:199], v2 offset:17408
s_add_u32 s28, s28, s26
s_add_u32 s37, s28, 4096
v_mfma_f32_32x32x16_bf16 v[48:63], a[200:203], a[168:171], v[48:63]
v_exp_f32 v113, v41
v_mfma_f32_16x16x32_bf16 v[24:27], v[28:31], v[96:99], 0
ds_read_b128 a[200:203], v2 offset:18432
v_mfma_f32_32x32x16_bf16 v[48:63], a[204:207], a[172:175], v[48:63]
v_cvt_pk_bf16_f32 v100, v112, v113
v_exp_f32 v114, v42
ds_read_b128 a[204:207], v2 offset:19456
s_lshr_b32 s38, s53, 2
s_lshl_b32 s38, s38, 8
v_mfma_f32_32x32x16_bf16 v[48:63], a[208:211], a[176:179], v[48:63]
v_exp_f32 v115, v43
ds_read_b128 a[208:211], v2 offset:20480
s_add_u32 s26, s26, s38
s_and_b32 s38, s53, 3
s_lshl_b32 s38, s38, 2
v_mfma_f32_32x32x16_bf16 v[48:63], a[212:215], a[180:183], v[48:63]
v_cvt_pk_bf16_f32 v101, v114, v115
v_exp_f32 v116, v44
ds_read_b128 a[212:215], v2 offset:21504
s_add_u32 s26, s26, s38
s_add_u32 s26, s26, 114688
v_mfma_f32_32x32x16_bf16 v[48:63], a[216:219], a[184:187], v[48:63]
v_exp_f32 v117, v45
ds_read_b128 a[216:219], v2 offset:22528
s_add_u32 s38, s26, 4096
v_mfma_f32_32x32x16_bf16 v[48:63], a[220:223], a[188:191], v[48:63]
v_cvt_pk_bf16_f32 v102, v116, v117
v_exp_f32 v118, v46
ds_read_b128 a[220:223], v2 offset:23552
v_mfma_f32_32x32x16_bf16 v[192:207], a[224:227], v[104:107], v[192:207]
v_exp_f32 v119, v47
ds_read_b128 a[224:227], v2 offset:40960
v_mfma_f32_32x32x16_bf16 v[208:223], a[228:231], v[104:107], v[208:223]
v_cvt_pk_bf16_f32 v103, v118, v119
v_exp_f32 v120, v48
ds_read_b128 a[228:231], v2 offset:41984
v_mfma_f32_32x32x16_bf16 v[224:239], a[232:235], v[104:107], v[224:239]
v_mfma_f32_16x16x32_bf16 v[24:27], v[28:31], v[100:103], v[24:27]
v_exp_f32 v121, v49
ds_read_b128 a[232:235], v2 offset:43008
v_mfma_f32_32x32x16_bf16 v[240:255], a[236:239], v[104:107], v[240:255]
v_cvt_pk_bf16_f32 v104, v120, v121
v_exp_f32 v122, v50
ds_read_b128 a[236:239], v2 offset:44032
v_mfma_f32_32x32x16_bf16 v[192:207], a[240:243], v[108:111], v[192:207]
v_exp_f32 v123, v51
v_cmp_ngt_f32 vcc, s71, v24
s_cmp_ge_u32 s24, s42
ds_read_b128 a[240:243], v2 offset:45056
v_mfma_f32_32x32x16_bf16 v[208:223], a[244:247], v[108:111], v[208:223]
v_cvt_pk_bf16_f32 v105, v122, v123
v_exp_f32 v124, v52
s_cbranch_vccnz .Lr107
.Lr107b:
s_cbranch_scc1 .Ls107
.Ls107b:
v_mfma_f32_32x32x16_bf16 v[224:239], a[248:251], v[108:111], v[224:239]
v_exp_f32 v125, v53
v_add_f32 v10, v10, v24
ds_read_b128 a[244:247], v2 offset:46080
ds_read_b128 a[248:251], v2 offset:47104
v_mfma_f32_32x32x16_bf16 v[240:255], a[252:255], v[108:111], v[240:255]
v_cvt_pk_bf16_f32 v106, v124, v125
v_exp_f32 v126, v54
ds_read_b128 a[252:255], v2 offset:48128
s_waitcnt lgkmcnt(8)
v_mfma_f32_32x32x16_bf16 v[32:47], a[192:195], a[128:131], v[64:79]
s_add_u32 m0, s36, 8192
v_exp_f32 v127, v55
buffer_load_dwordx4 v1, s[20:23], s24 offen offset:0 lds
ds_read_b64_tr_b16 a[32:33], v9 offset:0
v_mfma_f32_32x32x16_bf16 v[32:47], a[196:199], a[132:135], v[32:47]
v_cvt_pk_bf16_f32 v107, v126, v127
v_exp_f32 v120, v56
buffer_load_dwordx4 v1, s[20:23], s24 offen offset:1024 lds
ds_read_b64_tr_b16 a[34:35], v9 offset:1856
v_mfma_f32_32x32x16_bf16 v[32:47], a[200:203], a[136:139], v[32:47]
v_exp_f32 v121, v57
v_mfma_f32_16x16x32_bf16 v[24:27], v[28:31], v[104:107], 0
v_mfma_f32_32x32x16_bf16 v[32:47], a[204:207], a[140:143], v[32:47]
v_cvt_pk_bf16_f32 v108, v120, v121
v_exp_f32 v122, v58
ds_read_b64_tr_b16 a[36:37], v9 offset:32
ds_read_b64_tr_b16 a[38:39], v9 offset:1888
v_mfma_f32_32x32x16_bf16 v[32:47], a[208:211], a[144:147], v[32:47]
v_exp_f32 v123, v59
buffer_load_dwordx4 v1, s[20:23], s24 offen offset:2048 lds
ds_read_b64_tr_b16 a[40:41], v9 offset:64
ds_read_b64_tr_b16 a[42:43], v9 offset:1920
v_mfma_f32_32x32x16_bf16 v[32:47], a[212:215], a[148:151], v[32:47]
v_cvt_pk_bf16_f32 v109, v122, v123
v_exp_f32 v124, v60
ds_read_b64_tr_b16 a[44:45], v9 offset:96
ds_read_b64_tr_b16 a[46:47], v9 offset:1952
v_mfma_f32_32x32x16_bf16 v[32:47], a[216:219], a[152:155], v[32:47]
v_exp_f32 v125, v61
buffer_load_dwordx4 v1, s[20:23], s24 offen offset:3072 lds
s_add_u32 m0, s36, 32768
ds_read_b64_tr_b16 a[48:49], v9 offset:128
v_mfma_f32_32x32x16_bf16 v[32:47], a[220:223], a[156:159], v[32:47]
v_cvt_pk_bf16_f32 v110, v124, v125
v_exp_f32 v126, v62
s_waitcnt lgkmcnt(0)
ds_read_b64_tr_b16 a[50:51], v9 offset:1984
v_mfma_f32_32x32x16_bf16 v[128:143], a[224:227], v[96:99], v[128:143]
v_exp_f32 v127, v63
buffer_load_dwordx4 v3, s[20:23], s24 offen offset:0 lds
ds_read_b64_tr_b16 a[52:53], v9 offset:160
ds_read_b64_tr_b16 a[54:55], v9 offset:2016
v_mfma_f32_32x32x16_bf16 v[144:159], a[228:231], v[96:99], v[144:159]
v_cvt_pk_bf16_f32 v111, v126, v127
v_exp_f32 v112, v32
buffer_load_dwordx4 v3, s[20:23], s24 offen offset:1024 lds
ds_read_b64_tr_b16 a[56:57], v9 offset:192
v_mfma_f32_32x32x16_bf16 v[160:175], a[232:235], v[96:99], v[160:175]
v_exp_f32 v113, v33
v_mfma_f32_16x16x32_bf16 v[24:27], v[28:31], v[108:111], v[24:27]
buffer_load_dwordx4 v3, s[20:23], s24 offen offset:2048 lds
v_mfma_f32_32x32x16_bf16 v[176:191], a[236:239], v[96:99], v[176:191]
v_cvt_pk_bf16_f32 v96, v112, v113
v_exp_f32 v114, v34
ds_read_b64_tr_b16 a[58:59], v9 offset:2048
v_mfma_f32_32x32x16_bf16 v[128:143], a[240:243], v[100:103], v[128:143]
v_exp_f32 v115, v35
v_cmp_ngt_f32 vcc, s71, v24
s_cmp_ge_u32 s24, s43
v_mfma_f32_32x32x16_bf16 v[144:159], a[244:247], v[100:103], v[144:159]
v_cvt_pk_bf16_f32 v97, v114, v115
v_exp_f32 v116, v36
s_cbranch_vccnz .Lr110
.Lr110b:
s_cbranch_scc1 .Ls110
.Ls110b:
v_mfma_f32_32x32x16_bf16 v[160:175], a[248:251], v[100:103], v[160:175]
v_exp_f32 v117, v37
v_add_f32 v11, v11, v24
buffer_load_dwordx4 v3, s[20:23], s24 offen offset:3072 lds
v_mfma_f32_32x32x16_bf16 v[176:191], a[252:255], v[100:103], v[176:191]
v_cvt_pk_bf16_f32 v98, v116, v117
v_exp_f32 v118, v38
s_waitcnt vmcnt(16)
s_barrier
s_add_u32 s24, s24, 0x2000
v_mfma_f32_32x32x16_bf16 v[48:63], a[192:195], a[160:163], v[80:95]
v_exp_f32 v119, v39
ds_read_b128 a[192:195], v2 offset:24576
v_and_b32 v20, 7, v0
v_bfe_u32 v12, v0, 3, 3
v_mfma_f32_32x32x16_bf16 v[48:63], a[196:199], a[164:167], v[48:63]
v_cvt_pk_bf16_f32 v99, v118, v119
v_exp_f32 v112, v40
ds_read_b128 a[196:199], v2 offset:25600
v_lshlrev_b32 v12, 27, v12
v_mfma_f32_32x32x16_bf16 v[48:63], a[200:203], a[168:171], v[48:63]
v_exp_f32 v113, v41
v_mfma_f32_16x16x32_bf16 v[24:27], v[28:31], v[96:99], 0
ds_read_b128 a[200:203], v2 offset:26624
v_mfma_f32_32x32x16_bf16 v[48:63], a[204:207], a[172:175], v[48:63]
v_cvt_pk_bf16_f32 v100, v112, v113
v_exp_f32 v114, v42
ds_read_b128 a[204:207], v2 offset:27648
v_lshl_add_u32 v12, v20, 10, v12
v_mfma_f32_32x32x16_bf16 v[48:63], a[208:211], a[176:179], v[48:63]
v_exp_f32 v115, v43
ds_read_b128 a[208:211], v2 offset:28672
s_lshl_b32 s39, %2, 2
s_add_u32 s39, s39, 0x6300000
v_mfma_f32_32x32x16_bf16 v[48:63], a[212:215], a[180:183], v[48:63]
v_cvt_pk_bf16_f32 v101, v114, v115
v_exp_f32 v116, v44
ds_read_b128 a[212:215], v2 offset:29696
v_mov_b32 v20, 1
v_mfma_f32_32x32x16_bf16 v[48:63], a[216:219], a[184:187], v[48:63]
v_exp_f32 v117, v45
ds_read_b128 a[216:219], v2 offset:30720
buffer_store_dword v20, v12, s[16:19], s39 offen sc1
v_mfma_f32_32x32x16_bf16 v[48:63], a[220:223], a[188:191], v[48:63]
v_cvt_pk_bf16_f32 v102, v116, v117
v_exp_f32 v118, v46
ds_read_b128 a[220:223], v2 offset:31744
v_mfma_f32_32x32x16_bf16 v[192:207], a[224:227], v[104:107], v[192:207]
v_exp_f32 v119, v47
ds_read_b128 a[224:227], v2 offset:49152
v_mfma_f32_32x32x16_bf16 v[208:223], a[228:231], v[104:107], v[208:223]
v_cvt_pk_bf16_f32 v103, v118, v119
v_exp_f32 v120, v48
ds_read_b128 a[228:231], v2 offset:50176
v_mfma_f32_32x32x16_bf16 v[224:239], a[232:235], v[104:107], v[224:239]
v_mfma_f32_16x16x32_bf16 v[24:27], v[28:31], v[100:103], v[24:27]
v_exp_f32 v121, v49
ds_read_b128 a[232:235], v2 offset:51200
v_mfma_f32_32x32x16_bf16 v[240:255], a[236:239], v[104:107], v[240:255]
v_cvt_pk_bf16_f32 v104, v120, v121
v_exp_f32 v122, v50
ds_read_b128 a[236:239], v2 offset:52224
v_mfma_f32_32x32x16_bf16 v[192:207], a[240:243], v[108:111], v[192:207]
v_exp_f32 v123, v51
v_cmp_ngt_f32 vcc, s71, v24
s_cmp_ge_u32 s24, s42
ds_read_b128 a[240:243], v2 offset:53248
v_mfma_f32_32x32x16_bf16 v[208:223], a[244:247], v[108:111], v[208:223]
v_cvt_pk_bf16_f32 v105, v122, v123
v_exp_f32 v124, v52
s_cbranch_vccnz .Lr109
.Lr109b:
s_cbranch_scc1 .Ls109
.Ls109b:
v_mfma_f32_32x32x16_bf16 v[224:239], a[248:251], v[108:111], v[224:239]
v_exp_f32 v125, v53
v_add_f32 v10, v10, v24
ds_read_b128 a[244:247], v2 offset:54272
ds_read_b128 a[248:251], v2 offset:55296
v_mfma_f32_32x32x16_bf16 v[240:255], a[252:255], v[108:111], v[240:255]
v_cvt_pk_bf16_f32 v106, v124, v125
v_exp_f32 v126, v54
ds_read_b128 a[252:255], v2 offset:56320
s_waitcnt lgkmcnt(8)
v_mfma_f32_32x32x16_bf16 v[32:47], a[192:195], a[128:131], v[64:79]
s_add_u32 m0, s36, 16384
v_exp_f32 v127, v55
buffer_load_dwordx4 v1, s[20:23], s24 offen offset:0 lds
v_mfma_f32_32x32x16_bf16 v[32:47], a[196:199], a[132:135], v[32:47]
v_cvt_pk_bf16_f32 v107, v126, v127
v_exp_f32 v120, v56
buffer_load_dwordx4 v1, s[20:23], s24 offen offset:1024 lds
v_mfma_f32_32x32x16_bf16 v[32:47], a[200:203], a[136:139], v[32:47]
v_exp_f32 v121, v57
v_mfma_f32_16x16x32_bf16 v[24:27], v[28:31], v[104:107], 0
v_mfma_f32_32x32x16_bf16 v[32:47], a[204:207], a[140:143], v[32:47]
v_cvt_pk_bf16_f32 v108, v120, v121
v_exp_f32 v122, v58
v_mfma_f32_32x32x16_bf16 v[32:47], a[208:211], a[144:147], v[32:47]
v_exp_f32 v123, v59
buffer_load_dwordx4 v1, s[20:23], s24 offen offset:2048 lds
v_mfma_f32_32x32x16_bf16 v[32:47], a[212:215], a[148:151], v[32:47]
v_cvt_pk_bf16_f32 v109, v122, v123
v_exp_f32 v124, v60
v_mfma_f32_32x32x16_bf16 v[32:47], a[216:219], a[152:155], v[32:47]
v_exp_f32 v125, v61
buffer_load_dwordx4 v1, s[20:23], s24 offen offset:3072 lds
s_add_u32 m0, s36, 40960
v_mfma_f32_32x32x16_bf16 v[32:47], a[220:223], a[156:159], v[32:47]
v_cvt_pk_bf16_f32 v110, v124, v125
v_exp_f32 v126, v62
s_waitcnt lgkmcnt(0)
v_mfma_f32_32x32x16_bf16 v[128:143], a[224:227], v[96:99], v[128:143]
v_exp_f32 v127, v63
buffer_load_dwordx4 v3, s[20:23], s24 offen offset:0 lds
buffer_store_dwordx4 a[32:35], v1, s[16:19], s28 offen offset:0 sc1
v_mfma_f32_16x16x32_bf16 a[60:63], a[88:91], a[32:35], 0
v_mfma_f32_32x32x16_bf16 v[144:159], a[228:231], v[96:99], v[144:159]
v_cvt_pk_bf16_f32 v111, v126, v127
v_exp_f32 v112, v32
buffer_load_dwordx4 v3, s[20:23], s24 offen offset:1024 lds
buffer_store_dwordx4 a[36:39], v1, s[16:19], s28 offen offset:1024 sc1
v_mfma_f32_32x32x16_bf16 v[160:175], a[232:235], v[96:99], v[160:175]
v_exp_f32 v113, v33
v_mfma_f32_16x16x32_bf16 v[24:27], v[28:31], v[108:111], v[24:27]
buffer_load_dwordx4 v3, s[20:23], s24 offen offset:2048 lds
v_mfma_f32_32x32x16_bf16 v[176:191], a[236:239], v[96:99], v[176:191]
v_cvt_pk_bf16_f32 v96, v112, v113
v_exp_f32 v114, v34
v_mfma_f32_16x16x32_bf16 a[64:67], a[88:91], a[36:39], 0
buffer_store_dwordx4 a[40:43], v1, s[16:19], s28 offen offset:2048 sc1
v_mfma_f32_32x32x16_bf16 v[128:143], a[240:243], v[100:103], v[128:143]
v_exp_f32 v115, v35
v_cmp_ngt_f32 vcc, s71, v24
s_cmp_ge_u32 s24, s43
v_mfma_f32_32x32x16_bf16 v[144:159], a[244:247], v[100:103], v[144:159]
v_cvt_pk_bf16_f32 v97, v114, v115
v_exp_f32 v116, v36
s_cbranch_vccnz .Lr112
.Lr112b:
s_cbranch_scc1 .Ls112
.Ls112b:
v_mfma_f32_32x32x16_bf16 v[160:175], a[248:251], v[100:103], v[160:175]
v_exp_f32 v117, v37
v_add_f32 v11, v11, v24
buffer_load_dwordx4 v3, s[20:23], s24 offen offset:3072 lds
v_mfma_f32_16x16x32_bf16 a[68:71], a[88:91], a[40:43], 0
v_mfma_f32_32x32x16_bf16 v[176:191], a[252:255], v[100:103], v[176:191]
v_cvt_pk_bf16_f32 v98, v116, v117
v_exp_f32 v118, v38
s_waitcnt vmcnt(20)
s_barrier
s_add_u32 s24, s24, 0x2000
v_mfma_f32_32x32x16_bf16 v[48:63], a[192:195], a[160:163], v[80:95]
v_exp_f32 v119, v39
ds_read_b128 a[192:195], v2 offset:0
buffer_store_dwordx4 a[44:47], v1, s[16:19], s28 offen offset:3072 sc1
v_mfma_f32_16x16x32_bf16 a[72:75], a[88:91], a[44:47], 0
v_mfma_f32_32x32x16_bf16 v[48:63], a[196:199], a[164:167], v[48:63]
v_cvt_pk_bf16_f32 v99, v118, v119
v_exp_f32 v112, v40
ds_read_b128 a[196:199], v2 offset:1024
buffer_store_dwordx4 a[48:51], v1, s[16:19], s37 offen offset:0 sc1
v_mfma_f32_32x32x16_bf16 v[48:63], a[200:203], a[168:171], v[48:63]
v_exp_f32 v113, v41
v_mfma_f32_16x16x32_bf16 v[24:27], v[28:31], v[96:99], 0
ds_read_b128 a[200:203], v2 offset:2048
v_mfma_f32_32x32x16_bf16 v[48:63], a[204:207], a[172:175], v[48:63]
v_cvt_pk_bf16_f32 v100, v112, v113
v_exp_f32 v114, v42
ds_read_b128 a[204:207], v2 offset:3072
v_mfma_f32_16x16x32_bf16 a[76:79], a[88:91], a[48:51], 0
v_mfma_f32_32x32x16_bf16 v[48:63], a[208:211], a[176:179], v[48:63]
v_exp_f32 v115, v43
ds_read_b128 a[208:211], v2 offset:4096
buffer_store_dwordx4 a[52:55], v1, s[16:19], s37 offen offset:1024 sc1
v_mfma_f32_16x16x32_bf16 a[80:83], a[88:91], a[52:55], 0
v_mfma_f32_32x32x16_bf16 v[48:63], a[212:215], a[180:183], v[48:63]
v_cvt_pk_bf16_f32 v101, v114, v115
v_exp_f32 v116, v44
ds_read_b128 a[212:215], v2 offset:5120
buffer_store_dwordx4 a[56:59], v1, s[16:19], s37 offen offset:2048 sc1
v_mfma_f32_32x32x16_bf16 v[48:63], a[216:219], a[184:187], v[48:63]
v_exp_f32 v117, v45
ds_read_b128 a[216:219], v2 offset:6144
v_mfma_f32_16x16x32_bf16 a[84:87], a[88:91], a[56:59], 0
v_mfma_f32_32x32x16_bf16 v[48:63], a[220:223], a[188:191], v[48:63]
v_cvt_pk_bf16_f32 v102, v116, v117
v_exp_f32 v118, v46
ds_read_b128 a[220:223], v2 offset:7168
v_mfma_f32_32x32x16_bf16 v[192:207], a[224:227], v[104:107], v[192:207]
v_exp_f32 v119, v47
ds_read_b128 a[224:227], v2 offset:57344
v_mfma_f32_32x32x16_bf16 v[208:223], a[228:231], v[104:107], v[208:223]
v_cvt_pk_bf16_f32 v103, v118, v119
v_exp_f32 v120, v48
ds_read_b128 a[228:231], v2 offset:58368
v_mfma_f32_32x32x16_bf16 v[224:239], a[232:235], v[104:107], v[224:239]
v_mfma_f32_16x16x32_bf16 v[24:27], v[28:31], v[100:103], v[24:27]
v_exp_f32 v121, v49
ds_read_b128 a[232:235], v2 offset:59392
v_mfma_f32_32x32x16_bf16 v[240:255], a[236:239], v[104:107], v[240:255]
v_cvt_pk_bf16_f32 v104, v120, v121
v_exp_f32 v122, v50
ds_read_b128 a[236:239], v2 offset:60416
v_mfma_f32_32x32x16_bf16 v[192:207], a[240:243], v[108:111], v[192:207]
v_exp_f32 v123, v51
v_cmp_ngt_f32 vcc, s71, v24
s_cmp_ge_u32 s24, s42
ds_read_b128 a[240:243], v2 offset:61440
v_mfma_f32_32x32x16_bf16 v[208:223], a[244:247], v[108:111], v[208:223]
v_cvt_pk_bf16_f32 v105, v122, v123
v_exp_f32 v124, v52
s_cbranch_vccnz .Lr111
.Lr111b:
s_cbranch_scc1 .Ls111
.Ls111b:
v_mfma_f32_32x32x16_bf16 v[224:239], a[248:251], v[108:111], v[224:239]
v_exp_f32 v125, v53
v_add_f32 v10, v10, v24
ds_read_b128 a[244:247], v2 offset:62464
ds_read_b128 a[248:251], v2 offset:63488
v_mfma_f32_32x32x16_bf16 v[240:255], a[252:255], v[108:111], v[240:255]
v_cvt_pk_bf16_f32 v106, v124, v125
v_exp_f32 v126, v54
ds_read_b128 a[252:255], v2 offset:64512
s_waitcnt lgkmcnt(8)
s_cmp_lt_u32 s24, s27
s_cselect_b32 s22, s52, 0
v_mfma_f32_32x32x16_bf16 v[32:47], a[192:195], a[128:131], v[64:79]
s_add_u32 m0, s36, 24576
v_exp_f32 v127, v55
buffer_load_dwordx4 v1, s[20:23], s24 offen offset:0 lds
buffer_store_dword a60, v13, s[16:19], s26 offen offset:0 sc1
v_mfma_f32_32x32x16_bf16 v[32:47], a[196:199], a[132:135], v[32:47]
v_cvt_pk_bf16_f32 v107, v126, v127
v_exp_f32 v120, v56
buffer_load_dwordx4 v1, s[20:23], s24 offen offset:1024 lds
buffer_store_dword a64, v13, s[16:19], s26 offen offset:1024 sc1
v_mfma_f32_32x32x16_bf16 v[32:47], a[200:203], a[136:139], v[32:47]
v_exp_f32 v121, v57
v_mfma_f32_16x16x32_bf16 v[24:27], v[28:31], v[104:107], 0
v_mfma_f32_32x32x16_bf16 v[32:47], a[204:207], a[140:143], v[32:47]
v_cvt_pk_bf16_f32 v108, v120, v121
v_exp_f32 v122, v58
buffer_store_dword a68, v13, s[16:19], s26 offen offset:2048 sc1
buffer_store_dword a72, v13, s[16:19], s26 offen offset:3072 sc1
v_mfma_f32_32x32x16_bf16 v[32:47], a[208:211], a[144:147], v[32:47]
v_exp_f32 v123, v59
buffer_load_dwordx4 v1, s[20:23], s24 offen offset:2048 lds
buffer_store_dword a76, v13, s[16:19], s38 offen offset:0 sc1
buffer_store_dword a80, v13, s[16:19], s38 offen offset:1024 sc1
v_mfma_f32_32x32x16_bf16 v[32:47], a[212:215], a[148:151], v[32:47]
v_cvt_pk_bf16_f32 v109, v122, v123
v_exp_f32 v124, v60
buffer_store_dword a84, v13, s[16:19], s38 offen offset:2048 sc1
v_mfma_f32_32x32x16_bf16 v[32:47], a[216:219], a[152:155], v[32:47]
v_exp_f32 v125, v61
buffer_load_dwordx4 v1, s[20:23], s24 offen offset:3072 lds
s_add_u32 m0, s36, 49152
v_mfma_f32_32x32x16_bf16 v[32:47], a[220:223], a[156:159], v[32:47]
v_cvt_pk_bf16_f32 v110, v124, v125
v_exp_f32 v126, v62
s_waitcnt lgkmcnt(0)
v_mfma_f32_32x32x16_bf16 v[128:143], a[224:227], v[96:99], v[128:143]
v_exp_f32 v127, v63
buffer_load_dwordx4 v3, s[20:23], s24 offen offset:0 lds
v_mfma_f32_32x32x16_bf16 v[144:159], a[228:231], v[96:99], v[144:159]
v_cvt_pk_bf16_f32 v111, v126, v127
v_exp_f32 v112, v32
buffer_load_dwordx4 v3, s[20:23], s24 offen offset:1024 lds
v_mfma_f32_32x32x16_bf16 v[160:175], a[232:235], v[96:99], v[160:175]
v_exp_f32 v113, v33
v_mfma_f32_16x16x32_bf16 v[24:27], v[28:31], v[108:111], v[24:27]
buffer_load_dwordx4 v3, s[20:23], s24 offen offset:2048 lds
v_mfma_f32_32x32x16_bf16 v[176:191], a[236:239], v[96:99], v[176:191]
v_cvt_pk_bf16_f32 v96, v112, v113
v_exp_f32 v114, v34
v_mfma_f32_32x32x16_bf16 v[128:143], a[240:243], v[100:103], v[128:143]
v_exp_f32 v115, v35
v_cmp_ngt_f32 vcc, s71, v24
s_cmp_ge_u32 s24, s43
v_mfma_f32_32x32x16_bf16 v[144:159], a[244:247], v[100:103], v[144:159]
v_cvt_pk_bf16_f32 v97, v114, v115
v_exp_f32 v116, v36
s_cbranch_vccnz .Lr114
.Lr114b:
s_cbranch_scc1 .Ls114
.Ls114b:
v_mfma_f32_32x32x16_bf16 v[160:175], a[248:251], v[100:103], v[160:175]
v_exp_f32 v117, v37
v_add_f32 v11, v11, v24
buffer_load_dwordx4 v3, s[20:23], s24 offen offset:3072 lds
v_mfma_f32_32x32x16_bf16 v[176:191], a[252:255], v[100:103], v[176:191]
v_cvt_pk_bf16_f32 v98, v116, v117
v_exp_f32 v118, v38
s_waitcnt vmcnt(31)
s_barrier
s_add_u32 s24, s24, 0x2000
v_mfma_f32_32x32x16_bf16 v[48:63], a[192:195], a[160:163], v[80:95]
v_exp_f32 v119, v39
ds_read_b128 a[192:195], v2 offset:8192
v_mfma_f32_32x32x16_bf16 v[48:63], a[196:199], a[164:167], v[48:63]
v_cvt_pk_bf16_f32 v99, v118, v119
v_exp_f32 v112, v40
ds_read_b128 a[196:199], v2 offset:9216
v_mfma_f32_32x32x16_bf16 v[48:63], a[200:203], a[168:171], v[48:63]
v_exp_f32 v113, v41
v_mfma_f32_16x16x32_bf16 v[24:27], v[28:31], v[96:99], 0
ds_read_b128 a[200:203], v2 offset:10240
v_mfma_f32_32x32x16_bf16 v[48:63], a[204:207], a[172:175], v[48:63]
v_cvt_pk_bf16_f32 v100, v112, v113
v_exp_f32 v114, v42
ds_read_b128 a[204:207], v2 offset:11264
v_mfma_f32_32x32x16_bf16 v[48:63], a[208:211], a[176:179], v[48:63]
v_exp_f32 v115, v43
ds_read_b128 a[208:211], v2 offset:12288
v_mfma_f32_32x32x16_bf16 v[48:63], a[212:215], a[180:183], v[48:63]
v_cvt_pk_bf16_f32 v101, v114, v115
v_exp_f32 v116, v44
ds_read_b128 a[212:215], v2 offset:13312
v_mfma_f32_32x32x16_bf16 v[48:63], a[216:219], a[184:187], v[48:63]
v_exp_f32 v117, v45
ds_read_b128 a[216:219], v2 offset:14336
v_mfma_f32_32x32x16_bf16 v[48:63], a[220:223], a[188:191], v[48:63]
v_cvt_pk_bf16_f32 v102, v116, v117
v_exp_f32 v118, v46
ds_read_b128 a[220:223], v2 offset:15360
v_mfma_f32_32x32x16_bf16 v[192:207], a[224:227], v[104:107], v[192:207]
v_exp_f32 v119, v47
ds_read_b128 a[224:227], v2 offset:32768
v_mfma_f32_32x32x16_bf16 v[208:223], a[228:231], v[104:107], v[208:223]
v_cvt_pk_bf16_f32 v103, v118, v119
v_exp_f32 v120, v48
ds_read_b128 a[228:231], v2 offset:33792
v_mfma_f32_32x32x16_bf16 v[224:239], a[232:235], v[104:107], v[224:239]
v_mfma_f32_16x16x32_bf16 v[24:27], v[28:31], v[100:103], v[24:27]
v_exp_f32 v121, v49
ds_read_b128 a[232:235], v2 offset:34816
v_mfma_f32_32x32x16_bf16 v[240:255], a[236:239], v[104:107], v[240:255]
v_cvt_pk_bf16_f32 v104, v120, v121
v_exp_f32 v122, v50
ds_read_b128 a[236:239], v2 offset:35840
v_mfma_f32_32x32x16_bf16 v[192:207], a[240:243], v[108:111], v[192:207]
v_exp_f32 v123, v51
v_cmp_ngt_f32 vcc, s71, v24
s_cmp_ge_u32 s24, s42
ds_read_b128 a[240:243], v2 offset:36864
v_mfma_f32_32x32x16_bf16 v[208:223], a[244:247], v[108:111], v[208:223]
v_cvt_pk_bf16_f32 v105, v122, v123
v_exp_f32 v124, v52
s_cbranch_vccnz .Lr113
.Lr113b:
s_cbranch_scc1 .Ls113
.Ls113b:
v_mfma_f32_32x32x16_bf16 v[224:239], a[248:251], v[108:111], v[224:239]
v_exp_f32 v125, v53
v_add_f32 v10, v10, v24
ds_read_b128 a[244:247], v2 offset:37888
ds_read_b128 a[248:251], v2 offset:38912
v_mfma_f32_32x32x16_bf16 v[240:255], a[252:255], v[108:111], v[240:255]
v_cvt_pk_bf16_f32 v106, v124, v125
v_exp_f32 v126, v54
ds_read_b128 a[252:255], v2 offset:39936
s_waitcnt lgkmcnt(8)
v_mfma_f32_32x32x16_bf16 v[32:47], a[192:195], a[128:131], v[64:79]
s_add_u32 m0, s36, 0
v_exp_f32 v127, v55
buffer_load_dwordx4 v1, s[20:23], s24 offen offset:0 lds
s_and_b32 s74, %2, 7
v_mfma_f32_32x32x16_bf16 v[32:47], a[196:199], a[132:135], v[32:47]
v_cvt_pk_bf16_f32 v107, v126, v127
v_exp_f32 v120, v56
buffer_load_dwordx4 v1, s[20:23], s24 offen offset:1024 lds
s_lshl_b32 s74, s74, 2
v_mfma_f32_32x32x16_bf16 v[32:47], a[200:203], a[136:139], v[32:47]
v_exp_f32 v121, v57
v_mfma_f32_16x16x32_bf16 v[24:27], v[28:31], v[104:107], 0
v_mfma_f32_32x32x16_bf16 v[32:47], a[204:207], a[140:143], v[32:47]
v_cvt_pk_bf16_f32 v108, v120, v121
v_exp_f32 v122, v58
s_bfe_u32 s75, %2, 0x20003
s_add_u32 s74, s74, s75
v_mfma_f32_32x32x16_bf16 v[32:47], a[208:211], a[144:147], v[32:47]
v_exp_f32 v123, v59
buffer_load_dwordx4 v1, s[20:23], s24 offen offset:2048 lds
s_lshl_b32 s74, s74, 1
s_add_u32 s74, s74, 0
v_mfma_f32_32x32x16_bf16 v[32:47], a[212:215], a[148:151], v[32:47]
v_cvt_pk_bf16_f32 v109, v122, v123
v_exp_f32 v124, v60
s_lshr_b32 s75, %2, 5
s_lshl_b32 s76, s74, 3
v_mfma_f32_32x32x16_bf16 v[32:47], a[216:219], a[152:155], v[32:47]
v_exp_f32 v125, v61
buffer_load_dwordx4 v1, s[20:23], s24 offen offset:3072 lds
s_add_u32 m0, s36, 57344
s_add_u32 s76, s76, s75
v_mfma_f32_32x32x16_bf16 v[32:47], a[220:223], a[156:159], v[32:47]
v_cvt_pk_bf16_f32 v110, v124, v125
v_exp_f32 v126, v62
s_waitcnt lgkmcnt(0)
s_lshl_b32 s76, s76, 12
v_mfma_f32_32x32x16_bf16 v[128:143], a[224:227], v[96:99], v[128:143]
v_exp_f32 v127, v63
buffer_load_dwordx4 v3, s[20:23], s24 offen offset:0 lds
s_add_u32 s76, s76, 0x4000000
buffer_store_dwordx4 a[92:95], v1, s[16:19], s76 offen offset:0 sc1
v_mfma_f32_32x32x16_bf16 v[144:159], a[228:231], v[96:99], v[144:159]
v_cvt_pk_bf16_f32 v111, v126, v127
v_exp_f32 v112, v32
buffer_load_dwordx4 v3, s[20:23], s24 offen offset:1024 lds
buffer_store_dwordx4 a[92:95], v1, s[16:19], s76 offen offset:1024 sc1
v_mfma_f32_32x32x16_bf16 v[160:175], a[232:235], v[96:99], v[160:175]
v_exp_f32 v113, v33
v_mfma_f32_16x16x32_bf16 v[24:27], v[28:31], v[108:111], v[24:27]
buffer_load_dwordx4 v3, s[20:23], s24 offen offset:2048 lds
v_mfma_f32_32x32x16_bf16 v[176:191], a[236:239], v[96:99], v[176:191]
v_cvt_pk_bf16_f32 v96, v112, v113
v_exp_f32 v114, v34
buffer_store_dwordx4 a[92:95], v1, s[16:19], s76 offen offset:2048 sc1
buffer_store_dwordx4 a[92:95], v1, s[16:19], s76 offen offset:3072 sc1
v_mfma_f32_32x32x16_bf16 v[128:143], a[240:243], v[100:103], v[128:143]
v_exp_f32 v115, v35
v_cmp_ngt_f32 vcc, s71, v24
s_cmp_ge_u32 s24, s43
v_mfma_f32_32x32x16_bf16 v[144:159], a[244:247], v[100:103], v[144:159]
v_cvt_pk_bf16_f32 v97, v114, v115
v_exp_f32 v116, v36
s_cbranch_vccnz .Lr116
.Lr116b:
s_cbranch_scc1 .Ls116
.Ls116b:
v_mfma_f32_32x32x16_bf16 v[160:175], a[248:251], v[100:103], v[160:175]
v_exp_f32 v117, v37
v_add_f32 v11, v11, v24
buffer_load_dwordx4 v3, s[20:23], s24 offen offset:3072 lds
s_lshl_b32 s74, s74, 4
v_mfma_f32_32x32x16_bf16 v[176:191], a[252:255], v[100:103], v[176:191]
v_cvt_pk_bf16_f32 v98, v116, v117
v_exp_f32 v118, v38
s_waitcnt vmcnt(31)
s_barrier
s_add_u32 s24, s24, 0x2000
v_mfma_f32_32x32x16_bf16 v[48:63], a[192:195], a[160:163], v[80:95]
v_exp_f32 v119, v39
ds_read_b128 a[192:195], v2 offset:16384
s_add_u32 s74, s74, s75
s_lshl_b32 s74, s74, 8
v_mfma_f32_32x32x16_bf16 v[48:63], a[196:199], a[164:167], v[48:63]
v_cvt_pk_bf16_f32 v99, v118, v119
v_exp_f32 v112, v40
ds_read_b128 a[196:199], v2 offset:17408
s_add_u32 s74, s74, 0x5500000
v_mfma_f32_32x32x16_bf16 v[48:63], a[200:203], a[168:171], v[48:63]
v_exp_f32 v113, v41
v_mfma_f32_16x16x32_bf16 v[24:27], v[28:31], v[96:99], 0
ds_read_b128 a[200:203], v2 offset:18432
v_mfma_f32_32x32x16_bf16 v[48:63], a[204:207], a[172:175], v[48:63]
v_cvt_pk_bf16_f32 v100, v112, v113
v_exp_f32 v114, v42
ds_read_b128 a[204:207], v2 offset:19456
v_lshrrev_b32 v7, 2, v1
v_mfma_f32_32x32x16_bf16 v[48:63], a[208:211], a[176:179], v[48:63]
v_exp_f32 v115, v43
ds_read_b128 a[208:211], v2 offset:20480
s_nop 0
buffer_store_dword a92, v7, s[16:19], s74 offen offset:0 sc1
v_mfma_f32_32x32x16_bf16 v[48:63], a[212:215], a[180:183], v[48:63]
v_cvt_pk_bf16_f32 v101, v114, v115
v_exp_f32 v116, v44
ds_read_b128 a[212:215], v2 offset:21504
buffer_store_dword a92, v7, s[16:19], s74 offen offset:2048 sc1
v_mfma_f32_32x32x16_bf16 v[48:63], a[216:219], a[184:187], v[48:63]
v_exp_f32 v117, v45
ds_read_b128 a[216:219], v2 offset:22528
v_mfma_f32_32x32x16_bf16 v[48:63], a[220:223], a[188:191], v[48:63]
v_cvt_pk_bf16_f32 v102, v116, v117
v_exp_f32 v118, v46
ds_read_b128 a[220:223], v2 offset:23552
v_mfma_f32_32x32x16_bf16 v[192:207], a[224:227], v[104:107], v[192:207]
v_exp_f32 v119, v47
ds_read_b128 a[224:227], v2 offset:40960
v_mfma_f32_32x32x16_bf16 v[208:223], a[228:231], v[104:107], v[208:223]
v_cvt_pk_bf16_f32 v103, v118, v119
v_exp_f32 v120, v48
ds_read_b128 a[228:231], v2 offset:41984
v_mfma_f32_32x32x16_bf16 v[224:239], a[232:235], v[104:107], v[224:239]
v_mfma_f32_16x16x32_bf16 v[24:27], v[28:31], v[100:103], v[24:27]
v_exp_f32 v121, v49
ds_read_b128 a[232:235], v2 offset:43008
v_mfma_f32_32x32x16_bf16 v[240:255], a[236:239], v[104:107], v[240:255]
v_cvt_pk_bf16_f32 v104, v120, v121
v_exp_f32 v122, v50
ds_read_b128 a[236:239], v2 offset:44032
v_mfma_f32_32x32x16_bf16 v[192:207], a[240:243], v[108:111], v[192:207]
v_exp_f32 v123, v51
v_cmp_ngt_f32 vcc, s71, v24
s_cmp_ge_u32 s24, s42
ds_read_b128 a[240:243], v2 offset:45056
v_mfma_f32_32x32x16_bf16 v[208:223], a[244:247], v[108:111], v[208:223]
v_cvt_pk_bf16_f32 v105, v122, v123
v_exp_f32 v124, v52
s_cbranch_vccnz .Lr115
.Lr115b:
s_cbranch_scc1 .Ls115
.Ls115b:
v_mfma_f32_32x32x16_bf16 v[224:239], a[248:251], v[108:111], v[224:239]
v_exp_f32 v125, v53
v_add_f32 v10, v10, v24
ds_read_b128 a[244:247], v2 offset:46080
ds_read_b128 a[248:251], v2 offset:47104
v_mfma_f32_32x32x16_bf16 v[240:255], a[252:255], v[108:111], v[240:255]
v_cvt_pk_bf16_f32 v106, v124, v125
v_exp_f32 v126, v54
ds_read_b128 a[252:255], v2 offset:48128
s_waitcnt lgkmcnt(8)
s_cmp_ge_u32 s48, 20
s_cbranch_scc1 .Lx2_2
s_branch .LdV
.Len3:
s_cmp_ge_u32 s48, 20
s_cbranch_scc1 .Lp3
.Lx3_2:
v_mfma_f32_32x32x16_bf16 v[32:47], a[192:195], a[128:131], v[64:79]
s_add_u32 m0, s36, 8192
v_exp_f32 v127, v55
buffer_load_dwordx4 v1, s[20:23], s24 offen offset:0 lds
v_mfma_f32_32x32x16_bf16 v[32:47], a[196:199], a[132:135], v[32:47]
v_cvt_pk_bf16_f32 v107, v126, v127
v_exp_f32 v120, v56
v_mfma_f32_32x32x16_bf16 v[32:47], a[200:203], a[136:139], v[32:47]
v_exp_f32 v121, v57
v_mfma_f32_16x16x32_bf16 v[24:27], v[28:31], v[104:107], 0
v_mfma_f32_32x32x16_bf16 v[32:47], a[204:207], a[140:143], v[32:47]
v_cvt_pk_bf16_f32 v108, v120, v121
v_exp_f32 v122, v58
buffer_load_dwordx4 v1, s[20:23], s24 offen offset:1024 lds
v_mfma_f32_32x32x16_bf16 v[32:47], a[208:211], a[144:147], v[32:47]
v_exp_f32 v123, v59
v_mfma_f32_32x32x16_bf16 v[32:47], a[212:215], a[148:151], v[32:47]
v_cvt_pk_bf16_f32 v109, v122, v123
v_exp_f32 v124, v60
buffer_load_dwordx4 v1, s[20:23], s24 offen offset:2048 lds
v_mfma_f32_32x32x16_bf16 v[32:47], a[216:219], a[152:155], v[32:47]
v_exp_f32 v125, v61
buffer_load_dwordx4 v1, s[20:23], s24 offen offset:3072 lds
s_add_u32 m0, s36, 32768
v_mfma_f32_32x32x16_bf16 v[32:47], a[220:223], a[156:159], v[32:47]
v_cvt_pk_bf16_f32 v110, v124, v125
v_exp_f32 v126, v62
s_waitcnt lgkmcnt(0)
v_mfma_f32_32x32x16_bf16 v[128:143], a[224:227], v[96:99], v[128:143]
v_exp_f32 v127, v63
buffer_load_dwordx4 v3, s[20:23], s24 offen offset:0 lds
v_mfma_f32_32x32x16_bf16 v[144:159], a[228:231], v[96:99], v[144:159]
v_cvt_pk_bf16_f32 v111, v126, v127
v_exp_f32 v112, v32
v_mfma_f32_32x32x16_bf16 v[160:175], a[232:235], v[96:99], v[160:175]
v_exp_f32 v113, v33
v_mfma_f32_16x16x32_bf16 v[24:27], v[28:31], v[108:111], v[24:27]
buffer_load_dwordx4 v3, s[20:23], s24 offen offset:1024 lds
v_mfma_f32_32x32x16_bf16 v[176:191], a[236:239], v[96:99], v[176:191]
v_cvt_pk_bf16_f32 v96, v112, v113
v_exp_f32 v114, v34
buffer_load_dwordx4 v3, s[20:23], s24 offen offset:2048 lds
v_mfma_f32_32x32x16_bf16 v[128:143], a[240:243], v[100:103], v[128:143]
v_exp_f32 v115, v35
v_cmp_ngt_f32 vcc, s71, v24
s_cmp_ge_u32 s24, s43
v_mfma_f32_32x32x16_bf16 v[144:159], a[244:247], v[100:103], v[144:159]
v_cvt_pk_bf16_f32 v97, v114, v115
v_exp_f32 v116, v36
s_cbranch_vccnz .Lr118
.Lr118b:
s_cbranch_scc1 .Ls118
.Ls118b:
v_mfma_f32_32x32x16_bf16 v[160:175], a[248:251], v[100:103], v[160:175]
v_exp_f32 v117, v37
v_add_f32 v11, v11, v24
buffer_load_dwordx4 v3, s[20:23], s24 offen offset:3072 lds
v_mfma_f32_32x32x16_bf16 v[176:191], a[252:255], v[100:103], v[176:191]
v_cvt_pk_bf16_f32 v98, v116, v117
v_exp_f32 v118, v38
s_waitcnt vmcnt(16)
s_barrier
s_add_u32 s24, s24, 0x2000
v_mfma_f32_32x32x16_bf16 v[48:63], a[192:195], a[160:163], v[80:95]
v_exp_f32 v119, v39
ds_read_b128 a[192:195], v2 offset:24576
v_mfma_f32_32x32x16_bf16 v[48:63], a[196:199], a[164:167], v[48:63]
v_cvt_pk_bf16_f32 v99, v118, v119
v_exp_f32 v112, v40
ds_read_b128 a[196:199], v2 offset:25600
v_mfma_f32_32x32x16_bf16 v[48:63], a[200:203], a[168:171], v[48:63]
v_exp_f32 v113, v41
v_mfma_f32_16x16x32_bf16 v[24:27], v[28:31], v[96:99], 0
ds_read_b128 a[200:203], v2 offset:26624
v_mfma_f32_32x32x16_bf16 v[48:63], a[204:207], a[172:175], v[48:63]
v_cvt_pk_bf16_f32 v100, v112, v113
v_exp_f32 v114, v42
ds_read_b128 a[204:207], v2 offset:27648
v_mfma_f32_32x32x16_bf16 v[48:63], a[208:211], a[176:179], v[48:63]
v_exp_f32 v115, v43
ds_read_b128 a[208:211], v2 offset:28672
v_mfma_f32_32x32x16_bf16 v[48:63], a[212:215], a[180:183], v[48:63]
v_cvt_pk_bf16_f32 v101, v114, v115
v_exp_f32 v116, v44
ds_read_b128 a[212:215], v2 offset:29696
v_mfma_f32_32x32x16_bf16 v[48:63], a[216:219], a[184:187], v[48:63]
v_exp_f32 v117, v45
ds_read_b128 a[216:219], v2 offset:30720
v_mfma_f32_32x32x16_bf16 v[48:63], a[220:223], a[188:191], v[48:63]
v_cvt_pk_bf16_f32 v102, v116, v117
v_exp_f32 v118, v46
ds_read_b128 a[220:223], v2 offset:31744
v_mfma_f32_32x32x16_bf16 v[192:207], a[224:227], v[104:107], v[192:207]
v_exp_f32 v119, v47
ds_read_b128 a[224:227], v2 offset:49152
v_mfma_f32_32x32x16_bf16 v[208:223], a[228:231], v[104:107], v[208:223]
v_cvt_pk_bf16_f32 v103, v118, v119
v_exp_f32 v120, v48
ds_read_b128 a[228:231], v2 offset:50176
v_mfma_f32_32x32x16_bf16 v[224:239], a[232:235], v[104:107], v[224:239]
v_mfma_f32_16x16x32_bf16 v[24:27], v[28:31], v[100:103], v[24:27]
v_exp_f32 v121, v49
ds_read_b128 a[232:235], v2 offset:51200
v_mfma_f32_32x32x16_bf16 v[240:255], a[236:239], v[104:107], v[240:255]
v_cvt_pk_bf16_f32 v104, v120, v121
v_exp_f32 v122, v50
ds_read_b128 a[236:239], v2 offset:52224
v_mfma_f32_32x32x16_bf16 v[192:207], a[240:243], v[108:111], v[192:207]
v_exp_f32 v123, v51
v_cmp_ngt_f32 vcc, s71, v24
s_cmp_ge_u32 s24, s42
ds_read_b128 a[240:243], v2 offset:53248
v_mfma_f32_32x32x16_bf16 v[208:223], a[244:247], v[108:111], v[208:223]
v_cvt_pk_bf16_f32 v105, v122, v123
v_exp_f32 v124, v52
s_cbranch_vccnz .Lr117
.Lr117b:
s_cbranch_scc1 .Ls117
.Ls117b:
v_mfma_f32_32x32x16_bf16 v[224:239], a[248:251], v[108:111], v[224:239]
v_exp_f32 v125, v53
v_add_f32 v10, v10, v24
ds_read_b128 a[244:247], v2 offset:54272
ds_read_b128 a[248:251], v2 offset:55296
v_mfma_f32_32x32x16_bf16 v[240:255], a[252:255], v[108:111], v[240:255]
v_cvt_pk_bf16_f32 v106, v124, v125
v_exp_f32 v126, v54
ds_read_b128 a[252:255], v2 offset:56320
s_waitcnt lgkmcnt(8)
.Lx3_3:
v_mfma_f32_32x32x16_bf16 v[32:47], a[192:195], a[128:131], v[64:79]
s_add_u32 m0, s36, 16384
v_exp_f32 v127, v55
buffer_load_dwordx4 v1, s[20:23], s24 offen offset:0 lds
v_mfma_f32_32x32x16_bf16 v[32:47], a[196:199], a[132:135], v[32:47]
v_cvt_pk_bf16_f32 v107, v126, v127
v_exp_f32 v120, v56
v_mfma_f32_32x32x16_bf16 v[32:47], a[200:203], a[136:139], v[32:47]
v_exp_f32 v121, v57
v_mfma_f32_16x16x32_bf16 v[24:27], v[28:31], v[104:107], 0
v_mfma_f32_32x32x16_bf16 v[32:47], a[204:207], a[140:143], v[32:47]
v_cvt_pk_bf16_f32 v108, v120, v121
v_exp_f32 v122, v58
buffer_load_dwordx4 v1, s[20:23], s24 offen offset:1024 lds
v_mfma_f32_32x32x16_bf16 v[32:47], a[208:211], a[144:147], v[32:47]
v_exp_f32 v123, v59
v_mfma_f32_32x32x16_bf16 v[32:47], a[212:215], a[148:151], v[32:47]
v_cvt_pk_bf16_f32 v109, v122, v123
v_exp_f32 v124, v60
buffer_load_dwordx4 v1, s[20:23], s24 offen offset:2048 lds
v_mfma_f32_32x32x16_bf16 v[32:47], a[216:219], a[152:155], v[32:47]
v_exp_f32 v125, v61
buffer_load_dwordx4 v1, s[20:23], s24 offen offset:3072 lds
s_add_u32 m0, s36, 40960
v_mfma_f32_32x32x16_bf16 v[32:47], a[220:223], a[156:159], v[32:47]
v_cvt_pk_bf16_f32 v110, v124, v125
v_exp_f32 v126, v62
s_waitcnt lgkmcnt(0)
v_mfma_f32_32x32x16_bf16 v[128:143], a[224:227], v[96:99], v[128:143]
v_exp_f32 v127, v63
buffer_load_dwordx4 v3, s[20:23], s24 offen offset:0 lds
v_mfma_f32_32x32x16_bf16 v[144:159], a[228:231], v[96:99], v[144:159]
v_cvt_pk_bf16_f32 v111, v126, v127
v_exp_f32 v112, v32
v_mfma_f32_32x32x16_bf16 v[160:175], a[232:235], v[96:99], v[160:175]
v_exp_f32 v113, v33
v_mfma_f32_16x16x32_bf16 v[24:27], v[28:31], v[108:111], v[24:27]
buffer_load_dwordx4 v3, s[20:23], s24 offen offset:1024 lds
v_mfma_f32_32x32x16_bf16 v[176:191], a[236:239], v[96:99], v[176:191]
v_cvt_pk_bf16_f32 v96, v112, v113
v_exp_f32 v114, v34
buffer_load_dwordx4 v3, s[20:23], s24 offen offset:2048 lds
v_mfma_f32_32x32x16_bf16 v[128:143], a[240:243], v[100:103], v[128:143]
v_exp_f32 v115, v35
v_cmp_ngt_f32 vcc, s71, v24
s_cmp_ge_u32 s24, s43
v_mfma_f32_32x32x16_bf16 v[144:159], a[244:247], v[100:103], v[144:159]
v_cvt_pk_bf16_f32 v97, v114, v115
v_exp_f32 v116, v36
s_cbranch_vccnz .Lr120
.Lr120b:
s_cbranch_scc1 .Ls120
.Ls120b:
v_mfma_f32_32x32x16_bf16 v[160:175], a[248:251], v[100:103], v[160:175]
v_exp_f32 v117, v37
v_add_f32 v11, v11, v24
buffer_load_dwordx4 v3, s[20:23], s24 offen offset:3072 lds
v_mfma_f32_32x32x16_bf16 v[176:191], a[252:255], v[100:103], v[176:191]
v_cvt_pk_bf16_f32 v98, v116, v117
v_exp_f32 v118, v38
s_waitcnt vmcnt(16)
s_barrier
s_add_u32 s24, s24, 0x2000
v_mfma_f32_32x32x16_bf16 v[48:63], a[192:195], a[160:163], v[80:95]
v_exp_f32 v119, v39
ds_read_b128 a[192:195], v2 offset:0
v_mfma_f32_32x32x16_bf16 v[48:63], a[196:199], a[164:167], v[48:63]
v_cvt_pk_bf16_f32 v99, v118, v119
v_exp_f32 v112, v40
ds_read_b128 a[196:199], v2 offset:1024
v_mfma_f32_32x32x16_bf16 v[48:63], a[200:203], a[168:171], v[48:63]
v_exp_f32 v113, v41
v_mfma_f32_16x16x32_bf16 v[24:27], v[28:31], v[96:99], 0
ds_read_b128 a[200:203], v2 offset:2048
v_mfma_f32_32x32x16_bf16 v[48:63], a[204:207], a[172:175], v[48:63]
v_cvt_pk_bf16_f32 v100, v112, v113
v_exp_f32 v114, v42
ds_read_b128 a[204:207], v2 offset:3072
v_mfma_f32_32x32x16_bf16 v[48:63], a[208:211], a[176:179], v[48:63]
v_exp_f32 v115, v43
ds_read_b128 a[208:211], v2 offset:4096
v_mfma_f32_32x32x16_bf16 v[48:63], a[212:215], a[180:183], v[48:63]
v_cvt_pk_bf16_f32 v101, v114, v115
v_exp_f32 v116, v44
ds_read_b128 a[212:215], v2 offset:5120
v_mfma_f32_32x32x16_bf16 v[48:63], a[216:219], a[184:187], v[48:63]
v_exp_f32 v117, v45
ds_read_b128 a[216:219], v2 offset:6144
v_mfma_f32_32x32x16_bf16 v[48:63], a[220:223], a[188:191], v[48:63]
v_cvt_pk_bf16_f32 v102, v116, v117
v_exp_f32 v118, v46
ds_read_b128 a[220:223], v2 offset:7168
v_mfma_f32_32x32x16_bf16 v[192:207], a[224:227], v[104:107], v[192:207]
v_exp_f32 v119, v47
ds_read_b128 a[224:227], v2 offset:57344
v_mfma_f32_32x32x16_bf16 v[208:223], a[228:231], v[104:107], v[208:223]
v_cvt_pk_bf16_f32 v103, v118, v119
v_exp_f32 v120, v48
ds_read_b128 a[228:231], v2 offset:58368
v_mfma_f32_32x32x16_bf16 v[224:239], a[232:235], v[104:107], v[224:239]
v_mfma_f32_16x16x32_bf16 v[24:27], v[28:31], v[100:103], v[24:27]
v_exp_f32 v121, v49
ds_read_b128 a[232:235], v2 offset:59392
v_mfma_f32_32x32x16_bf16 v[240:255], a[236:239], v[104:107], v[240:255]
v_cvt_pk_bf16_f32 v104, v120, v121
v_exp_f32 v122, v50
ds_read_b128 a[236:239], v2 offset:60416
v_mfma_f32_32x32x16_bf16 v[192:207], a[240:243], v[108:111], v[192:207]
v_exp_f32 v123, v51
v_cmp_ngt_f32 vcc, s71, v24
s_cmp_ge_u32 s24, s42
ds_read_b128 a[240:243], v2 offset:61440
v_mfma_f32_32x32x16_bf16 v[208:223], a[244:247], v[108:111], v[208:223]
v_cvt_pk_bf16_f32 v105, v122, v123
v_exp_f32 v124, v52
s_cbranch_vccnz .Lr119
.Lr119b:
s_cbranch_scc1 .Ls119
.Ls119b:
v_mfma_f32_32x32x16_bf16 v[224:239], a[248:251], v[108:111], v[224:239]
v_exp_f32 v125, v53
v_add_f32 v10, v10, v24
ds_read_b128 a[244:247], v2 offset:62464
ds_read_b128 a[248:251], v2 offset:63488
v_mfma_f32_32x32x16_bf16 v[240:255], a[252:255], v[108:111], v[240:255]
v_cvt_pk_bf16_f32 v106, v124, v125
v_exp_f32 v126, v54
ds_read_b128 a[252:255], v2 offset:64512
s_waitcnt lgkmcnt(8)
s_cmp_lt_u32 s24, s27
s_cselect_b32 s22, s52, 0
.Lx3_0:
v_mfma_f32_32x32x16_bf16 v[32:47], a[192:195], a[128:131], v[64:79]
s_add_u32 m0, s36, 24576
v_exp_f32 v127, v55
buffer_load_dwordx4 v1, s[20:23], s24 offen offset:0 lds
v_mfma_f32_32x32x16_bf16 v[32:47], a[196:199], a[132:135], v[32:47]
v_cvt_pk_bf16_f32 v107, v126, v127
v_exp_f32 v120, v56
v_mfma_f32_32x32x16_bf16 v[32:47], a[200:203], a[136:139], v[32:47]
v_exp_f32 v121, v57
v_mfma_f32_16x16x32_bf16 v[24:27], v[28:31], v[104:107], 0
v_mfma_f32_32x32x16_bf16 v[32:47], a[204:207], a[140:143], v[32:47]
v_cvt_pk_bf16_f32 v108, v120, v121
v_exp_f32 v122, v58
buffer_load_dwordx4 v1, s[20:23], s24 offen offset:1024 lds
v_mfma_f32_32x32x16_bf16 v[32:47], a[208:211], a[144:147], v[32:47]
v_exp_f32 v123, v59
v_mfma_f32_32x32x16_bf16 v[32:47], a[212:215], a[148:151], v[32:47]
v_cvt_pk_bf16_f32 v109, v122, v123
v_exp_f32 v124, v60
buffer_load_dwordx4 v1, s[20:23], s24 offen offset:2048 lds
v_mfma_f32_32x32x16_bf16 v[32:47], a[216:219], a[152:155], v[32:47]
v_exp_f32 v125, v61
buffer_load_dwordx4 v1, s[20:23], s24 offen offset:3072 lds
s_add_u32 m0, s36, 49152
v_mfma_f32_32x32x16_bf16 v[32:47], a[220:223], a[156:159], v[32:47]
v_cvt_pk_bf16_f32 v110, v124, v125
v_exp_f32 v126, v62
s_waitcnt lgkmcnt(0)
v_mfma_f32_32x32x16_bf16 v[128:143], a[224:227], v[96:99], v[128:143]
v_exp_f32 v127, v63
buffer_load_dwordx4 v3, s[20:23], s24 offen offset:0 lds
v_mfma_f32_32x32x16_bf16 v[144:159], a[228:231], v[96:99], v[144:159]
v_cvt_pk_bf16_f32 v111, v126, v127
v_exp_f32 v112, v32
v_mfma_f32_32x32x16_bf16 v[160:175], a[232:235], v[96:99], v[160:175]
v_exp_f32 v113, v33
v_mfma_f32_16x16x32_bf16 v[24:27], v[28:31], v[108:111], v[24:27]
buffer_load_dwordx4 v3, s[20:23], s24 offen offset:1024 lds
v_mfma_f32_32x32x16_bf16 v[176:191], a[236:239], v[96:99], v[176:191]
v_cvt_pk_bf16_f32 v96, v112, v113
v_exp_f32 v114, v34
buffer_load_dwordx4 v3, s[20:23], s24 offen offset:2048 lds
v_mfma_f32_32x32x16_bf16 v[128:143], a[240:243], v[100:103], v[128:143]
v_exp_f32 v115, v35
v_cmp_ngt_f32 vcc, s71, v24
s_cmp_ge_u32 s24, s43
v_mfma_f32_32x32x16_bf16 v[144:159], a[244:247], v[100:103], v[144:159]
v_cvt_pk_bf16_f32 v97, v114, v115
v_exp_f32 v116, v36
s_cbranch_vccnz .Lr122
.Lr122b:
s_cbranch_scc1 .Ls122
.Ls122b:
v_mfma_f32_32x32x16_bf16 v[160:175], a[248:251], v[100:103], v[160:175]
v_exp_f32 v117, v37
v_add_f32 v11, v11, v24
buffer_load_dwordx4 v3, s[20:23], s24 offen offset:3072 lds
v_mfma_f32_32x32x16_bf16 v[176:191], a[252:255], v[100:103], v[176:191]
v_cvt_pk_bf16_f32 v98, v116, v117
v_exp_f32 v118, v38
s_waitcnt vmcnt(16)
s_barrier
s_add_u32 s24, s24, 0x2000
v_mfma_f32_32x32x16_bf16 v[48:63], a[192:195], a[160:163], v[80:95]
v_exp_f32 v119, v39
ds_read_b128 a[192:195], v2 offset:8192
v_mfma_f32_32x32x16_bf16 v[48:63], a[196:199], a[164:167], v[48:63]
v_cvt_pk_bf16_f32 v99, v118, v119
v_exp_f32 v112, v40
ds_read_b128 a[196:199], v2 offset:9216
v_mfma_f32_32x32x16_bf16 v[48:63], a[200:203], a[168:171], v[48:63]
v_exp_f32 v113, v41
v_mfma_f32_16x16x32_bf16 v[24:27], v[28:31], v[96:99], 0
ds_read_b128 a[200:203], v2 offset:10240
v_mfma_f32_32x32x16_bf16 v[48:63], a[204:207], a[172:175], v[48:63]
v_cvt_pk_bf16_f32 v100, v112, v113
v_exp_f32 v114, v42
ds_read_b128 a[204:207], v2 offset:11264
v_mfma_f32_32x32x16_bf16 v[48:63], a[208:211], a[176:179], v[48:63]
v_exp_f32 v115, v43
ds_read_b128 a[208:211], v2 offset:12288
v_mfma_f32_32x32x16_bf16 v[48:63], a[212:215], a[180:183], v[48:63]
v_cvt_pk_bf16_f32 v101, v114, v115
v_exp_f32 v116, v44
ds_read_b128 a[212:215], v2 offset:13312
v_mfma_f32_32x32x16_bf16 v[48:63], a[216:219], a[184:187], v[48:63]
v_exp_f32 v117, v45
ds_read_b128 a[216:219], v2 offset:14336
v_mfma_f32_32x32x16_bf16 v[48:63], a[220:223], a[188:191], v[48:63]
v_cvt_pk_bf16_f32 v102, v116, v117
v_exp_f32 v118, v46
ds_read_b128 a[220:223], v2 offset:15360
v_mfma_f32_32x32x16_bf16 v[192:207], a[224:227], v[104:107], v[192:207]
v_exp_f32 v119, v47
ds_read_b128 a[224:227], v2 offset:32768
v_mfma_f32_32x32x16_bf16 v[208:223], a[228:231], v[104:107], v[208:223]
v_cvt_pk_bf16_f32 v103, v118, v119
v_exp_f32 v120, v48
ds_read_b128 a[228:231], v2 offset:33792
v_mfma_f32_32x32x16_bf16 v[224:239], a[232:235], v[104:107], v[224:239]
v_mfma_f32_16x16x32_bf16 v[24:27], v[28:31], v[100:103], v[24:27]
v_exp_f32 v121, v49
ds_read_b128 a[232:235], v2 offset:34816
v_mfma_f32_32x32x16_bf16 v[240:255], a[236:239], v[104:107], v[240:255]
v_cvt_pk_bf16_f32 v104, v120, v121
v_exp_f32 v122, v50
ds_read_b128 a[236:239], v2 offset:35840
v_mfma_f32_32x32x16_bf16 v[192:207], a[240:243], v[108:111], v[192:207]
v_exp_f32 v123, v51
v_cmp_ngt_f32 vcc, s71, v24
s_cmp_ge_u32 s24, s42
ds_read_b128 a[240:243], v2 offset:36864
v_mfma_f32_32x32x16_bf16 v[208:223], a[244:247], v[108:111], v[208:223]
v_cvt_pk_bf16_f32 v105, v122, v123
v_exp_f32 v124, v52
s_cbranch_vccnz .Lr121
.Lr121b:
s_cbranch_scc1 .Ls121
.Ls121b:
v_mfma_f32_32x32x16_bf16 v[224:239], a[248:251], v[108:111], v[224:239]
v_exp_f32 v125, v53
v_add_f32 v10, v10, v24
ds_read_b128 a[244:247], v2 offset:37888
ds_read_b128 a[248:251], v2 offset:38912
v_mfma_f32_32x32x16_bf16 v[240:255], a[252:255], v[108:111], v[240:255]
v_cvt_pk_bf16_f32 v106, v124, v125
v_exp_f32 v126, v54
ds_read_b128 a[252:255], v2 offset:39936
s_waitcnt lgkmcnt(8)
.Lx3_1:
v_mfma_f32_32x32x16_bf16 v[32:47], a[192:195], a[128:131], v[64:79]
s_add_u32 m0, s36, 0
v_exp_f32 v127, v55
buffer_load_dwordx4 v1, s[20:23], s24 offen offset:0 lds
v_mfma_f32_32x32x16_bf16 v[32:47], a[196:199], a[132:135], v[32:47]
v_cvt_pk_bf16_f32 v107, v126, v127
v_exp_f32 v120, v56
v_mfma_f32_32x32x16_bf16 v[32:47], a[200:203], a[136:139], v[32:47]
v_exp_f32 v121, v57
v_mfma_f32_16x16x32_bf16 v[24:27], v[28:31], v[104:107], 0
v_mfma_f32_32x32x16_bf16 v[32:47], a[204:207], a[140:143], v[32:47]
v_cvt_pk_bf16_f32 v108, v120, v121
v_exp_f32 v122, v58
buffer_load_dwordx4 v1, s[20:23], s24 offen offset:1024 lds
v_mfma_f32_32x32x16_bf16 v[32:47], a[208:211], a[144:147], v[32:47]
v_exp_f32 v123, v59
v_mfma_f32_32x32x16_bf16 v[32:47], a[212:215], a[148:151], v[32:47]
v_cvt_pk_bf16_f32 v109, v122, v123
v_exp_f32 v124, v60
buffer_load_dwordx4 v1, s[20:23], s24 offen offset:2048 lds
v_mfma_f32_32x32x16_bf16 v[32:47], a[216:219], a[152:155], v[32:47]
v_exp_f32 v125, v61
buffer_load_dwordx4 v1, s[20:23], s24 offen offset:3072 lds
s_add_u32 m0, s36, 57344
v_mfma_f32_32x32x16_bf16 v[32:47], a[220:223], a[156:159], v[32:47]
v_cvt_pk_bf16_f32 v110, v124, v125
v_exp_f32 v126, v62
s_waitcnt lgkmcnt(0)
v_mfma_f32_32x32x16_bf16 v[128:143], a[224:227], v[96:99], v[128:143]
v_exp_f32 v127, v63
buffer_load_dwordx4 v3, s[20:23], s24 offen offset:0 lds
v_mfma_f32_32x32x16_bf16 v[144:159], a[228:231], v[96:99], v[144:159]
v_cvt_pk_bf16_f32 v111, v126, v127
v_exp_f32 v112, v32
v_mfma_f32_32x32x16_bf16 v[160:175], a[232:235], v[96:99], v[160:175]
v_exp_f32 v113, v33
v_mfma_f32_16x16x32_bf16 v[24:27], v[28:31], v[108:111], v[24:27]
buffer_load_dwordx4 v3, s[20:23], s24 offen offset:1024 lds
v_mfma_f32_32x32x16_bf16 v[176:191], a[236:239], v[96:99], v[176:191]
v_cvt_pk_bf16_f32 v96, v112, v113
v_exp_f32 v114, v34
buffer_load_dwordx4 v3, s[20:23], s24 offen offset:2048 lds
v_mfma_f32_32x32x16_bf16 v[128:143], a[240:243], v[100:103], v[128:143]
v_exp_f32 v115, v35
v_cmp_ngt_f32 vcc, s71, v24
s_cmp_ge_u32 s24, s43
v_mfma_f32_32x32x16_bf16 v[144:159], a[244:247], v[100:103], v[144:159]
v_cvt_pk_bf16_f32 v97, v114, v115
v_exp_f32 v116, v36
s_cbranch_vccnz .Lr124
.Lr124b:
s_cbranch_scc1 .Ls124
.Ls124b:
v_mfma_f32_32x32x16_bf16 v[160:175], a[248:251], v[100:103], v[160:175]
v_exp_f32 v117, v37
v_add_f32 v11, v11, v24
buffer_load_dwordx4 v3, s[20:23], s24 offen offset:3072 lds
v_mfma_f32_32x32x16_bf16 v[176:191], a[252:255], v[100:103], v[176:191]
v_cvt_pk_bf16_f32 v98, v116, v117
v_exp_f32 v118, v38
s_waitcnt vmcnt(16)
s_barrier
s_add_u32 s24, s24, 0x2000
v_mfma_f32_32x32x16_bf16 v[48:63], a[192:195], a[160:163], v[80:95]
v_exp_f32 v119, v39
ds_read_b128 a[192:195], v2 offset:16384
v_mfma_f32_32x32x16_bf16 v[48:63], a[196:199], a[164:167], v[48:63]
v_cvt_pk_bf16_f32 v99, v118, v119
v_exp_f32 v112, v40
ds_read_b128 a[196:199], v2 offset:17408
v_mfma_f32_32x32x16_bf16 v[48:63], a[200:203], a[168:171], v[48:63]
v_exp_f32 v113, v41
v_mfma_f32_16x16x32_bf16 v[24:27], v[28:31], v[96:99], 0
ds_read_b128 a[200:203], v2 offset:18432
v_mfma_f32_32x32x16_bf16 v[48:63], a[204:207], a[172:175], v[48:63]
v_cvt_pk_bf16_f32 v100, v112, v113
v_exp_f32 v114, v42
ds_read_b128 a[204:207], v2 offset:19456
v_mfma_f32_32x32x16_bf16 v[48:63], a[208:211], a[176:179], v[48:63]
v_exp_f32 v115, v43
ds_read_b128 a[208:211], v2 offset:20480
v_mfma_f32_32x32x16_bf16 v[48:63], a[212:215], a[180:183], v[48:63]
v_cvt_pk_bf16_f32 v101, v114, v115
v_exp_f32 v116, v44
ds_read_b128 a[212:215], v2 offset:21504
v_mfma_f32_32x32x16_bf16 v[48:63], a[216:219], a[184:187], v[48:63]
v_exp_f32 v117, v45
ds_read_b128 a[216:219], v2 offset:22528
v_mfma_f32_32x32x16_bf16 v[48:63], a[220:223], a[188:191], v[48:63]
v_cvt_pk_bf16_f32 v102, v116, v117
v_exp_f32 v118, v46
ds_read_b128 a[220:223], v2 offset:23552
v_mfma_f32_32x32x16_bf16 v[192:207], a[224:227], v[104:107], v[192:207]
v_exp_f32 v119, v47
ds_read_b128 a[224:227], v2 offset:40960
v_mfma_f32_32x32x16_bf16 v[208:223], a[228:231], v[104:107], v[208:223]
v_cvt_pk_bf16_f32 v103, v118, v119
v_exp_f32 v120, v48
ds_read_b128 a[228:231], v2 offset:41984
v_mfma_f32_32x32x16_bf16 v[224:239], a[232:235], v[104:107], v[224:239]
v_mfma_f32_16x16x32_bf16 v[24:27], v[28:31], v[100:103], v[24:27]
v_exp_f32 v121, v49
ds_read_b128 a[232:235], v2 offset:43008
v_mfma_f32_32x32x16_bf16 v[240:255], a[236:239], v[104:107], v[240:255]
v_cvt_pk_bf16_f32 v104, v120, v121
v_exp_f32 v122, v50
ds_read_b128 a[236:239], v2 offset:44032
v_mfma_f32_32x32x16_bf16 v[192:207], a[240:243], v[108:111], v[192:207]
v_exp_f32 v123, v51
v_cmp_ngt_f32 vcc, s71, v24
s_cmp_ge_u32 s24, s42
ds_read_b128 a[240:243], v2 offset:45056
v_mfma_f32_32x32x16_bf16 v[208:223], a[244:247], v[108:111], v[208:223]
v_cvt_pk_bf16_f32 v105, v122, v123
v_exp_f32 v124, v52
s_cbranch_vccnz .Lr123
.Lr123b:
s_cbranch_scc1 .Ls123
.Ls123b:
v_mfma_f32_32x32x16_bf16 v[224:239], a[248:251], v[108:111], v[224:239]
v_exp_f32 v125, v53
v_add_f32 v10, v10, v24
ds_read_b128 a[244:247], v2 offset:46080
ds_read_b128 a[248:251], v2 offset:47104
v_mfma_f32_32x32x16_bf16 v[240:255], a[252:255], v[108:111], v[240:255]
v_cvt_pk_bf16_f32 v106, v124, v125
v_exp_f32 v126, v54
ds_read_b128 a[252:255], v2 offset:48128
s_waitcnt lgkmcnt(8)
s_cmp_lg_u32 s24, s40
s_cbranch_scc1 .Lx3_2
s_cmp_ge_u32 s48, 20
s_cbranch_scc1 .LdV
.Lp3:
v_mfma_f32_32x32x16_bf16 v[32:47], a[192:195], a[128:131], v[64:79]
s_add_u32 m0, s36, 8192
v_exp_f32 v127, v55
buffer_load_dwordx4 v1, s[20:23], s24 offen offset:0 lds
s_mov_b32 s80, %6
s_and_b32 s81, %7, 0xffff
v_mfma_f32_32x32x16_bf16 v[32:47], a[196:199], a[132:135], v[32:47]
v_cvt_pk_bf16_f32 v107, v126, v127
v_exp_f32 v120, v56
s_mov_b32 s82, 0x400000
s_mov_b32 s83, 0x20000
s_cmp_ge_u32 s48, 20
s_cselect_b32 s70, 11, 18
v_mfma_f32_32x32x16_bf16 v[32:47], a[200:203], a[136:139], v[32:47]
v_exp_f32 v121, v57
v_mfma_f32_16x16x32_bf16 v[24:27], v[28:31], v[104:107], 0
v_mfma_f32_32x32x16_bf16 v[32:47], a[204:207], a[140:143], v[32:47]
v_cvt_pk_bf16_f32 v108, v120, v121
v_exp_f32 v122, v58
buffer_load_dwordx4 v1, s[20:23], s24 offen offset:1024 lds
s_lshl_b32 s70, s70, 13
s_lshl_b32 s26, %2, 14
v_mfma_f32_32x32x16_bf16 v[32:47], a[208:211], a[144:147], v[32:47]
v_exp_f32 v123, v59
s_add_u32 s26, s26, 512
s_add_u32 s28, s26, 8192
v_bfe_u32 v20, v0, 3, 3
v_lshlrev_b32 v4, 10, v20
v_mfma_f32_32x32x16_bf16 v[32:47], a[212:215], a[148:151], v[32:47]
v_cvt_pk_bf16_f32 v109, v122, v123
v_exp_f32 v124, v60
buffer_load_dwordx4 v1, s[20:23], s24 offen offset:2048 lds
v_bfe_u32 v20, v0, 4, 1
v_lshlrev_b32 v20, 2, v20
v_mfma_f32_32x32x16_bf16 v[32:47], a[216:219], a[152:155], v[32:47]
v_exp_f32 v125, v61
buffer_load_dwordx4 v1, s[20:23], s24 offen offset:3072 lds
s_add_u32 m0, s36, 32768
v_and_b32 v21, 7, v0
v_xor_b32 v20, v20, v21
v_mfma_f32_32x32x16_bf16 v[32:47], a[220:223], a[156:159], v[32:47]
v_cvt_pk_bf16_f32 v110, v124, v125
v_exp_f32 v126, v62
s_waitcnt lgkmcnt(0)
v_lshl_add_u32 v4, v20, 4, v4
v_mfma_f32_32x32x16_bf16 v[128:143], a[224:227], v[96:99], v[128:143]
v_exp_f32 v127, v63
buffer_load_dwordx4 v3, s[20:23], s24 offen offset:0 lds
v_mfma_f32_32x32x16_bf16 v[144:159], a[228:231], v[96:99], v[144:159]
v_cvt_pk_bf16_f32 v111, v126, v127
v_exp_f32 v112, v32
v_mfma_f32_32x32x16_bf16 v[160:175], a[232:235], v[96:99], v[160:175]
v_exp_f32 v113, v33
v_mfma_f32_16x16x32_bf16 v[24:27], v[28:31], v[108:111], v[24:27]
buffer_load_dwordx4 v3, s[20:23], s24 offen offset:1024 lds
v_mfma_f32_32x32x16_bf16 v[176:191], a[236:239], v[96:99], v[176:191]
v_cvt_pk_bf16_f32 v96, v112, v113
v_exp_f32 v114, v34
buffer_load_dwordx4 v3, s[20:23], s24 offen offset:2048 lds
v_mfma_f32_32x32x16_bf16 v[128:143], a[240:243], v[100:103], v[128:143]
v_exp_f32 v115, v35
v_cmp_ngt_f32 vcc, s71, v24
s_cmp_ge_u32 s24, s43
v_mfma_f32_32x32x16_bf16 v[144:159], a[244:247], v[100:103], v[144:159]
v_cvt_pk_bf16_f32 v97, v114, v115
v_exp_f32 v116, v36
s_cbranch_vccnz .Lr126
.Lr126b:
s_cbranch_scc1 .Ls126
.Ls126b:
v_mfma_f32_32x32x16_bf16 v[160:175], a[248:251], v[100:103], v[160:175]
v_exp_f32 v117, v37
v_add_f32 v11, v11, v24
buffer_load_dwordx4 v3, s[20:23], s24 offen offset:3072 lds
v_mfma_f32_32x32x16_bf16 v[176:191], a[252:255], v[100:103], v[176:191]
v_cvt_pk_bf16_f32 v98, v116, v117
v_exp_f32 v118, v38
s_waitcnt vmcnt(16)
s_barrier
s_add_u32 s24, s24, 0x2000
v_mfma_f32_32x32x16_bf16 v[48:63], a[192:195], a[160:163], v[80:95]
v_exp_f32 v119, v39
ds_read_b128 a[192:195], v2 offset:24576
s_add_u32 m0, s70, 0
s_nop 0
buffer_load_dwordx4 v4, s[80:83], s26 offen offset:0 lds
v_mfma_f32_32x32x16_bf16 v[48:63], a[196:199], a[164:167], v[48:63]
v_cvt_pk_bf16_f32 v99, v118, v119
v_exp_f32 v112, v40
ds_read_b128 a[196:199], v2 offset:25600
s_add_u32 m0, s70, 1024
s_nop 0
v_mfma_f32_32x32x16_bf16 v[48:63], a[200:203], a[168:171], v[48:63]
v_exp_f32 v113, v41
v_mfma_f32_16x16x32_bf16 v[24:27], v[28:31], v[96:99], 0
ds_read_b128 a[200:203], v2 offset:26624
v_mfma_f32_32x32x16_bf16 v[48:63], a[204:207], a[172:175], v[48:63]
v_cvt_pk_bf16_f32 v100, v112, v113
v_exp_f32 v114, v42
ds_read_b128 a[204:207], v2 offset:27648
buffer_load_dwordx4 v4, s[80:83], s28 offen offset:0 lds
s_add_u32 m0, s70, 1920
v_mfma_f32_32x32x16_bf16 v[48:63], a[208:211], a[176:179], v[48:63]
v_exp_f32 v115, v43
ds_read_b128 a[208:211], v2 offset:28672
s_nop 0
buffer_load_dwordx4 v4, s[80:83], s26 offen offset:128 lds
s_add_u32 m0, s70, 2944
v_mfma_f32_32x32x16_bf16 v[48:63], a[212:215], a[180:183], v[48:63]
v_cvt_pk_bf16_f32 v101, v114, v115
v_exp_f32 v116, v44
ds_read_b128 a[212:215], v2 offset:29696
s_nop 0
buffer_load_dwordx4 v4, s[80:83], s28 offen offset:128 lds
v_mfma_f32_32x32x16_bf16 v[48:63], a[216:219], a[184:187], v[48:63]
v_exp_f32 v117, v45
ds_read_b128 a[216:219], v2 offset:30720
s_add_u32 m0, s70, 3840
s_nop 0
buffer_load_dwordx4 v4, s[80:83], s26 offen offset:256 lds
v_mfma_f32_32x32x16_bf16 v[48:63], a[220:223], a[188:191], v[48:63]
v_cvt_pk_bf16_f32 v102, v116, v117
v_exp_f32 v118, v46
ds_read_b128 a[220:223], v2 offset:31744
s_add_u32 m0, s70, 4864
s_nop 0
v_mfma_f32_32x32x16_bf16 v[192:207], a[224:227], v[104:107], v[192:207]
v_exp_f32 v119, v47
ds_read_b128 a[224:227], v2 offset:49152
buffer_load_dwordx4 v4, s[80:83], s28 offen offset:256 lds
s_add_u32 m0, s70, 5760
s_nop 0
v_mfma_f32_32x32x16_bf16 v[208:223], a[228:231], v[104:107], v[208:223]
v_cvt_pk_bf16_f32 v103, v118, v119
v_exp_f32 v120, v48
ds_read_b128 a[228:231], v2 offset:50176
buffer_load_dwordx4 v4, s[80:83], s26 offen offset:384 lds
s_add_u32 m0, s70, 6784
v_mfma_f32_32x32x16_bf16 v[224:239], a[232:235], v[104:107], v[224:239]
v_mfma_f32_16x16x32_bf16 v[24:27], v[28:31], v[100:103], v[24:27]
v_exp_f32 v121, v49
ds_read_b128 a[232:235], v2 offset:51200
v_mfma_f32_32x32x16_bf16 v[240:255], a[236:239], v[104:107], v[240:255]
v_cvt_pk_bf16_f32 v104, v120, v121
v_exp_f32 v122, v50
ds_read_b128 a[236:239], v2 offset:52224
s_nop 0
buffer_load_dwordx4 v4, s[80:83], s28 offen offset:384 lds
v_mfma_f32_32x32x16_bf16 v[192:207], a[240:243], v[108:111], v[192:207]
v_exp_f32 v123, v51
v_cmp_ngt_f32 vcc, s71, v24
s_cmp_ge_u32 s24, s42
ds_read_b128 a[240:243], v2 offset:53248
v_mfma_f32_32x32x16_bf16 v[208:223], a[244:247], v[108:111], v[208:223]
v_cvt_pk_bf16_f32 v105, v122, v123
v_exp_f32 v124, v52
s_cbranch_vccnz .Lr125
.Lr125b:
s_cbranch_scc1 .Ls125
.Ls125b:
v_mfma_f32_32x32x16_bf16 v[224:239], a[248:251], v[108:111], v[224:239]
v_exp_f32 v125, v53
v_add_f32 v10, v10, v24
ds_read_b128 a[244:247], v2 offset:54272
ds_read_b128 a[248:251], v2 offset:55296
v_mfma_f32_32x32x16_bf16 v[240:255], a[252:255], v[108:111], v[240:255]
v_cvt_pk_bf16_f32 v106, v124, v125
v_exp_f32 v126, v54
ds_read_b128 a[252:255], v2 offset:56320
s_waitcnt lgkmcnt(8)
v_mfma_f32_32x32x16_bf16 v[32:47], a[192:195], a[128:131], v[64:79]
s_add_u32 m0, s36, 16384
v_exp_f32 v127, v55
buffer_load_dwordx4 v1, s[20:23], s24 offen offset:0 lds
s_mov_b32 s80, %8
s_and_b32 s81, %9, 0xffff
v_mfma_f32_32x32x16_bf16 v[32:47], a[196:199], a[132:135], v[32:47]
v_cvt_pk_bf16_f32 v107, v126, v127
v_exp_f32 v120, v56
s_mov_b32 s82, 0x1c0000
s_mov_b32 s83, 0x20000
s_lshr_b32 s26, %2, 4
v_mfma_f32_32x32x16_bf16 v[32:47], a[200:203], a[136:139], v[32:47]
v_exp_f32 v121, v57
v_mfma_f32_16x16x32_bf16 v[24:27], v[28:31], v[104:107], 0
v_mfma_f32_32x32x16_bf16 v[32:47], a[204:207], a[140:143], v[32:47]
v_cvt_pk_bf16_f32 v108, v120, v121
v_exp_f32 v122, v58
buffer_load_dwordx4 v1, s[20:23], s24 offen offset:1024 lds
s_mul_i32 s26, s26, 114688
s_bfe_u32 s28, %2, 0x30001
v_mfma_f32_32x32x16_bf16 v[32:47], a[208:211], a[144:147], v[32:47]
v_exp_f32 v123, v59
s_lshl_b32 s28, s28, 7
s_add_u32 s26, s26, s28
s_and_b32 s28, %2, 1
s_mul_i32 s28, s28, 57344
v_mfma_f32_32x32x16_bf16 v[32:47], a[212:215], a[148:151], v[32:47]
v_cvt_pk_bf16_f32 v109, v122, v123
v_exp_f32 v124, v60
buffer_load_dwordx4 v1, s[20:23], s24 offen offset:2048 lds
s_add_u32 s28, s28, s26
v_and_b32 v4, 0x3ff, v4
v_mfma_f32_32x32x16_bf16 v[32:47], a[216:219], a[152:155], v[32:47]
v_exp_f32 v125, v61
buffer_load_dwordx4 v1, s[20:23], s24 offen offset:3072 lds
s_add_u32 m0, s36, 40960
v_bfe_u32 v20, v0, 3, 1
v_lshl_add_u32 v4, v20, 10, v4
v_mfma_f32_32x32x16_bf16 v[32:47], a[220:223], a[156:159], v[32:47]
v_cvt_pk_bf16_f32 v110, v124, v125
v_exp_f32 v126, v62
s_waitcnt lgkmcnt(0)
v_bfe_u32 v20, v0, 4, 1
v_bfe_u32 v21, v0, 5, 1
v_mfma_f32_32x32x16_bf16 v[128:143], a[224:227], v[96:99], v[128:143]
v_exp_f32 v127, v63
buffer_load_dwordx4 v3, s[20:23], s24 offen offset:0 lds
v_mul_u32_u24 v22, 57344, v21
v_add_u32 v22, v22, v4
v_lshl_add_u32 v12, v20, 13, v22
v_mfma_f32_32x32x16_bf16 v[144:159], a[228:231], v[96:99], v[144:159]
v_cvt_pk_bf16_f32 v111, v126, v127
v_exp_f32 v112, v32
v_add_u32 v12, 34816, v12
v_lshl_add_u32 v13, v20, 12, v22
v_add_u32 v13, 51200, v13
v_mfma_f32_32x32x16_bf16 v[160:175], a[232:235], v[96:99], v[160:175]
v_exp_f32 v113, v33
v_mfma_f32_16x16x32_bf16 v[24:27], v[28:31], v[108:111], v[24:27]
buffer_load_dwordx4 v3, s[20:23], s24 offen offset:1024 lds
v_mfma_f32_32x32x16_bf16 v[176:191], a[236:239], v[96:99], v[176:191]
v_cvt_pk_bf16_f32 v96, v112, v113
v_exp_f32 v114, v34
buffer_load_dwordx4 v3, s[20:23], s24 offen offset:2048 lds
v_lshl_add_u32 v22, v21, 12, v4
v_lshlrev_b32 v21, 12, v21
v_mfma_f32_32x32x16_bf16 v[128:143], a[240:243], v[100:103], v[128:143]
v_exp_f32 v115, v35
v_cmp_ngt_f32 vcc, s71, v24
s_cmp_ge_u32 s24, s43
v_mfma_f32_32x32x16_bf16 v[144:159], a[244:247], v[100:103], v[144:159]
v_cvt_pk_bf16_f32 v97, v114, v115
v_exp_f32 v116, v36
s_cbranch_vccnz .Lr128
.Lr128b:
s_cbranch_scc1 .Ls128
.Ls128b:
v_mfma_f32_32x32x16_bf16 v[160:175], a[248:251], v[100:103], v[160:175]
v_exp_f32 v117, v37
v_add_f32 v11, v11, v24
buffer_load_dwordx4 v3, s[20:23], s24 offen offset:3072 lds
v_subrev_u32 v21, 10240, v21
v_mul_lo_u32 v21, v21, v20
v_mfma_f32_32x32x16_bf16 v[176:191], a[252:255], v[100:103], v[176:191]
v_cvt_pk_bf16_f32 v98, v116, v117
v_exp_f32 v118, v38
s_waitcnt vmcnt(24)
s_barrier
s_add_u32 s24, s24, 0x2000
v_mfma_f32_32x32x16_bf16 v[48:63], a[192:195], a[160:163], v[80:95]
v_exp_f32 v119, v39
ds_read_b128 a[192:195], v2 offset:0
v_add_u32 v9, v21, v22
v_add_u32 v9, 49152, v9
v_lshl_add_u32 v4, v20, 13, v22
v_mfma_f32_32x32x16_bf16 v[48:63], a[196:199], a[164:167], v[48:63]
v_cvt_pk_bf16_f32 v99, v118, v119
v_exp_f32 v112, v40
ds_read_b128 a[196:199], v2 offset:1024
v_accvgpr_write_b32 a92, -1
v_accvgpr_write_b32 a93, -1
v_mfma_f32_32x32x16_bf16 v[48:63], a[200:203], a[168:171], v[48:63]
v_exp_f32 v113, v41
v_mfma_f32_16x16x32_bf16 v[24:27], v[28:31], v[96:99], 0
ds_read_b128 a[200:203], v2 offset:2048
v_mfma_f32_32x32x16_bf16 v[48:63], a[204:207], a[172:175], v[48:63]
v_cvt_pk_bf16_f32 v100, v112, v113
v_exp_f32 v114, v42
ds_read_b128 a[204:207], v2 offset:3072
v_accvgpr_write_b32 a94, -1
v_accvgpr_write_b32 a95, -1
v_mfma_f32_32x32x16_bf16 v[48:63], a[208:211], a[176:179], v[48:63]
v_exp_f32 v115, v43
ds_read_b128 a[208:211], v2 offset:4096
v_mfma_f32_32x32x16_bf16 v[48:63], a[212:215], a[180:183], v[48:63]
v_cvt_pk_bf16_f32 v101, v114, v115
v_exp_f32 v116, v44
ds_read_b128 a[212:215], v2 offset:5120
v_mfma_f32_32x32x16_bf16 v[48:63], a[216:219], a[184:187], v[48:63]
v_exp_f32 v117, v45
ds_read_b128 a[216:219], v2 offset:6144
v_mfma_f32_32x32x16_bf16 v[48:63], a[220:223], a[188:191], v[48:63]
v_cvt_pk_bf16_f32 v102, v116, v117
v_exp_f32 v118, v46
ds_read_b128 a[220:223], v2 offset:7168
v_mfma_f32_32x32x16_bf16 v[192:207], a[224:227], v[104:107], v[192:207]
v_exp_f32 v119, v47
ds_read_b128 a[224:227], v2 offset:57344
v_mfma_f32_32x32x16_bf16 v[208:223], a[228:231], v[104:107], v[208:223]
v_cvt_pk_bf16_f32 v103, v118, v119
v_exp_f32 v120, v48
ds_read_b128 a[228:231], v2 offset:58368
v_mfma_f32_32x32x16_bf16 v[224:239], a[232:235], v[104:107], v[224:239]
v_mfma_f32_16x16x32_bf16 v[24:27], v[28:31], v[100:103], v[24:27]
v_exp_f32 v121, v49
ds_read_b128 a[232:235], v2 offset:59392
v_mfma_f32_32x32x16_bf16 v[240:255], a[236:239], v[104:107], v[240:255]
v_cvt_pk_bf16_f32 v104, v120, v121
v_exp_f32 v122, v50
ds_read_b128 a[236:239], v2 offset:60416
v_mfma_f32_32x32x16_bf16 v[192:207], a[240:243], v[108:111], v[192:207]
v_exp_f32 v123, v51
v_cmp_ngt_f32 vcc, s71, v24
s_cmp_ge_u32 s24, s42
ds_read_b128 a[240:243], v2 offset:61440
v_mfma_f32_32x32x16_bf16 v[208:223], a[244:247], v[108:111], v[208:223]
v_cvt_pk_bf16_f32 v105, v122, v123
v_exp_f32 v124, v52
s_cbranch_vccnz .Lr127
.Lr127b:
s_cbranch_scc1 .Ls127
.Ls127b:
v_mfma_f32_32x32x16_bf16 v[224:239], a[248:251], v[108:111], v[224:239]
v_exp_f32 v125, v53
v_add_f32 v10, v10, v24
ds_read_b128 a[244:247], v2 offset:62464
ds_read_b128 a[248:251], v2 offset:63488
v_mfma_f32_32x32x16_bf16 v[240:255], a[252:255], v[108:111], v[240:255]
v_cvt_pk_bf16_f32 v106, v124, v125
v_exp_f32 v126, v54
ds_read_b128 a[252:255], v2 offset:64512
s_waitcnt lgkmcnt(8)
s_cmp_lt_u32 s24, s27
s_cselect_b32 s22, s52, 0
v_mfma_f32_32x32x16_bf16 v[32:47], a[192:195], a[128:131], v[64:79]
s_add_u32 m0, s36, 24576
v_exp_f32 v127, v55
buffer_load_dwordx4 v1, s[20:23], s24 offen offset:0 lds
v_bfe_u32 v20, v0, 2, 2
v_mfma_f32_32x32x16_bf16 v[32:47], a[196:199], a[132:135], v[32:47]
v_cvt_pk_bf16_f32 v107, v126, v127
v_exp_f32 v120, v56
v_lshlrev_b32 v6, 7, v20
v_bfe_u32 v20, v0, 5, 1
v_mfma_f32_32x32x16_bf16 v[32:47], a[200:203], a[136:139], v[32:47]
v_exp_f32 v121, v57
v_mfma_f32_16x16x32_bf16 v[24:27], v[28:31], v[104:107], 0
v_mfma_f32_32x32x16_bf16 v[32:47], a[204:207], a[140:143], v[32:47]
v_cvt_pk_bf16_f32 v108, v120, v121
v_exp_f32 v122, v58
buffer_load_dwordx4 v1, s[20:23], s24 offen offset:1024 lds
v_lshl_add_u32 v6, v20, 9, v6
v_mfma_f32_32x32x16_bf16 v[32:47], a[208:211], a[144:147], v[32:47]
v_exp_f32 v123, v59
v_bfe_u32 v20, v0, 3, 1
v_lshl_add_u32 v6, v20, 6, v6
v_bfe_u32 v20, v0, 4, 1
v_mfma_f32_32x32x16_bf16 v[32:47], a[212:215], a[148:151], v[32:47]
v_cvt_pk_bf16_f32 v109, v122, v123
v_exp_f32 v124, v60
buffer_load_dwordx4 v1, s[20:23], s24 offen offset:2048 lds
v_lshl_add_u32 v6, v20, 5, v6
v_mfma_f32_32x32x16_bf16 v[32:47], a[216:219], a[152:155], v[32:47]
v_exp_f32 v125, v61
buffer_load_dwordx4 v1, s[20:23], s24 offen offset:3072 lds
s_add_u32 m0, s36, 49152
v_and_b32 v20, 3, v0
v_mfma_f32_32x32x16_bf16 v[32:47], a[220:223], a[156:159], v[32:47]
v_cvt_pk_bf16_f32 v110, v124, v125
v_exp_f32 v126, v62
s_waitcnt lgkmcnt(0)
v_lshl_add_u32 v6, v20, 3, v6
v_mfma_f32_32x32x16_bf16 v[128:143], a[224:227], v[96:99], v[128:143]
v_exp_f32 v127, v63
buffer_load_dwordx4 v3, s[20:23], s24 offen offset:0 lds
v_add_u32 v6, s70, v6
v_xor_b32 v8, 64, v6
v_mfma_f32_32x32x16_bf16 v[144:159], a[228:231], v[96:99], v[144:159]
v_cvt_pk_bf16_f32 v111, v126, v127
v_exp_f32 v112, v32
v_mfma_f32_32x32x16_bf16 v[160:175], a[232:235], v[96:99], v[160:175]
v_exp_f32 v113, v33
v_mfma_f32_16x16x32_bf16 v[24:27], v[28:31], v[108:111], v[24:27]
buffer_load_dwordx4 v3, s[20:23], s24 offen offset:1024 lds
v_mfma_f32_32x32x16_bf16 v[176:191], a[236:239], v[96:99], v[176:191]
v_cvt_pk_bf16_f32 v96, v112, v113
v_exp_f32 v114, v34
buffer_load_dwordx4 v3, s[20:23], s24 offen offset:2048 lds
v_mfma_f32_32x32x16_bf16 v[128:143], a[240:243], v[100:103], v[128:143]
v_exp_f32 v115, v35
v_cmp_ngt_f32 vcc, s71, v24
s_cmp_ge_u32 s24, s43
v_mfma_f32_32x32x16_bf16 v[144:159], a[244:247], v[100:103], v[144:159]
v_cvt_pk_bf16_f32 v97, v114, v115
v_exp_f32 v116, v36
s_cbranch_vccnz .Lr130
.Lr130b:
s_cbranch_scc1 .Ls130
.Ls130b:
v_mfma_f32_32x32x16_bf16 v[160:175], a[248:251], v[100:103], v[160:175]
v_exp_f32 v117, v37
v_add_f32 v11, v11, v24
buffer_load_dwordx4 v3, s[20:23], s24 offen offset:3072 lds
v_mfma_f32_32x32x16_bf16 v[176:191], a[252:255], v[100:103], v[176:191]
v_cvt_pk_bf16_f32 v98, v116, v117
v_exp_f32 v118, v38
s_waitcnt vmcnt(24)
s_barrier
s_add_u32 s24, s24, 0x2000
v_mfma_f32_32x32x16_bf16 v[48:63], a[192:195], a[160:163], v[80:95]
v_exp_f32 v119, v39
ds_read_b128 a[192:195], v2 offset:8192
s_add_u32 m0, s70, 8192
s_add_u32 s37, s28, 32768
v_mfma_f32_32x32x16_bf16 v[48:63], a[196:199], a[164:167], v[48:63]
v_cvt_pk_bf16_f32 v99, v118, v119
v_exp_f32 v112, v40
ds_read_b128 a[196:199], v2 offset:9216
buffer_load_dwordx4 v4, s[80:83], s37 offen lds
v_mfma_f32_32x32x16_bf16 v[48:63], a[200:203], a[168:171], v[48:63]
v_exp_f32 v113, v41
v_mfma_f32_16x16x32_bf16 v[24:27], v[28:31], v[96:99], 0
ds_read_b128 a[200:203], v2 offset:10240
v_mfma_f32_32x32x16_bf16 v[48:63], a[204:207], a[172:175], v[48:63]
v_cvt_pk_bf16_f32 v100, v112, v113
v_exp_f32 v114, v42
ds_read_b128 a[204:207], v2 offset:11264
s_add_u32 m0, s70, 9216
v_mfma_f32_32x32x16_bf16 v[48:63], a[208:211], a[176:179], v[48:63]
v_exp_f32 v115, v43
ds_read_b128 a[208:211], v2 offset:12288
s_add_u32 s37, s28, 0
buffer_load_dwordx4 v9, s[80:83], s37 offen lds
v_mfma_f32_32x32x16_bf16 v[48:63], a[212:215], a[180:183], v[48:63]
v_cvt_pk_bf16_f32 v101, v114, v115
v_exp_f32 v116, v44
ds_read_b128 a[212:215], v2 offset:13312
s_add_u32 m0, s70, 10240
v_mfma_f32_32x32x16_bf16 v[48:63], a[216:219], a[184:187], v[48:63]
v_exp_f32 v117, v45
ds_read_b128 a[216:219], v2 offset:14336
s_add_u32 s37, s26, 0
buffer_load_dwordx4 v12, s[80:83], s37 offen lds
v_mfma_f32_32x32x16_bf16 v[48:63], a[220:223], a[188:191], v[48:63]
v_cvt_pk_bf16_f32 v102, v116, v117
v_exp_f32 v118, v46
ds_read_b128 a[220:223], v2 offset:15360
s_add_u32 m0, s70, 11264
v_mfma_f32_32x32x16_bf16 v[192:207], a[224:227], v[104:107], v[192:207]
v_exp_f32 v119, v47
ds_read_b128 a[224:227], v2 offset:32768
s_add_u32 s37, s26, 0
buffer_load_dwordx4 v13, s[80:83], s37 offen lds
v_mfma_f32_32x32x16_bf16 v[208:223], a[228:231], v[104:107], v[208:223]
v_cvt_pk_bf16_f32 v103, v118, v119
v_exp_f32 v120, v48
ds_read_b128 a[228:231], v2 offset:33792
v_mfma_f32_32x32x16_bf16 v[224:239], a[232:235], v[104:107], v[224:239]
v_mfma_f32_16x16x32_bf16 v[24:27], v[28:31], v[100:103], v[24:27]
v_exp_f32 v121, v49
ds_read_b128 a[232:235], v2 offset:34816
v_mfma_f32_32x32x16_bf16 v[240:255], a[236:239], v[104:107], v[240:255]
v_cvt_pk_bf16_f32 v104, v120, v121
v_exp_f32 v122, v50
ds_read_b128 a[236:239], v2 offset:35840
v_mfma_f32_32x32x16_bf16 v[192:207], a[240:243], v[108:111], v[192:207]
v_exp_f32 v123, v51
v_cmp_ngt_f32 vcc, s71, v24
s_cmp_ge_u32 s24, s42
ds_read_b128 a[240:243], v2 offset:36864
v_mfma_f32_32x32x16_bf16 v[208:223], a[244:247], v[108:111], v[208:223]
v_cvt_pk_bf16_f32 v105, v122, v123
v_exp_f32 v124, v52
s_cbranch_vccnz .Lr129
.Lr129b:
s_cbranch_scc1 .Ls129
.Ls129b:
v_mfma_f32_32x32x16_bf16 v[224:239], a[248:251], v[108:111], v[224:239]
v_exp_f32 v125, v53
v_add_f32 v10, v10, v24
ds_read_b128 a[244:247], v2 offset:37888
ds_read_b128 a[248:251], v2 offset:38912
v_mfma_f32_32x32x16_bf16 v[240:255], a[252:255], v[108:111], v[240:255]
v_cvt_pk_bf16_f32 v106, v124, v125
v_exp_f32 v126, v54
ds_read_b128 a[252:255], v2 offset:39936
s_waitcnt lgkmcnt(8)
v_mfma_f32_32x32x16_bf16 v[32:47], a[192:195], a[128:131], v[64:79]
s_add_u32 m0, s36, 0
v_exp_f32 v127, v55
buffer_load_dwordx4 v1, s[20:23], s24 offen offset:0 lds
v_bfe_u32 v20, v0, 3, 3
v_and_b32 v21, 7, v0
v_mfma_f32_32x32x16_bf16 v[32:47], a[196:199], a[132:135], v[32:47]
v_cvt_pk_bf16_f32 v107, v126, v127
v_exp_f32 v120, v56
v_min_u32 v21, 6, v21
v_mul_u32_u24 v9, 3584, v20
v_lshl_add_u32 v9, v21, 4, v9
v_mfma_f32_32x32x16_bf16 v[32:47], a[200:203], a[136:139], v[32:47]
v_exp_f32 v121, v57
v_mfma_f32_16x16x32_bf16 v[24:27], v[28:31], v[104:107], 0
v_mfma_f32_32x32x16_bf16 v[32:47], a[204:207], a[140:143], v[32:47]
v_cvt_pk_bf16_f32 v108, v120, v121
v_exp_f32 v122, v58
buffer_load_dwordx4 v1, s[20:23], s24 offen offset:1024 lds
v_mul_u32_u24 v12, 232, v20
v_lshl_add_u32 v12, v21, 5, v12
v_mfma_f32_32x32x16_bf16 v[32:47], a[208:211], a[144:147], v[32:47]
v_exp_f32 v123, v59
v_add_u32 v12, s70, v12
s_lshl_b32 s26, %2, 1
s_add_u32 s26, s26, 1
s_and_b32 s39, s26, 31
v_mfma_f32_32x32x16_bf16 v[32:47], a[212:215], a[148:151], v[32:47]
v_cvt_pk_bf16_f32 v109, v122, v123
v_exp_f32 v124, v60
buffer_load_dwordx4 v1, s[20:23], s24 offen offset:2048 lds
s_lshr_b32 s53, s26, 5
s_mul_i32 s26, s39, 112
v_mfma_f32_32x32x16_bf16 v[32:47], a[216:219], a[152:155], v[32:47]
v_exp_f32 v125, v61
buffer_load_dwordx4 v1, s[20:23], s24 offen offset:3072 lds
s_add_u32 m0, s36, 57344
s_mul_i32 s28, s53, 114688
s_add_u32 s26, s26, s28
v_mfma_f32_32x32x16_bf16 v[32:47], a[220:223], a[156:159], v[32:47]
v_cvt_pk_bf16_f32 v110, v124, v125
v_exp_f32 v126, v62
s_waitcnt lgkmcnt(0)
s_add_u32 s28, s26, 28672
s_add_u32 s37, s26, 57344
v_mfma_f32_32x32x16_bf16 v[128:143], a[224:227], v[96:99], v[128:143]
v_exp_f32 v127, v63
buffer_load_dwordx4 v3, s[20:23], s24 offen offset:0 lds
s_add_u32 s38, s26, 86016
s_mov_b32 s80, %10
s_and_b32 s81, %11, 0xffff
v_mfma_f32_32x32x16_bf16 v[144:159], a[228:231], v[96:99], v[144:159]
v_cvt_pk_bf16_f32 v111, v126, v127
v_exp_f32 v112, v32
s_mov_b32 s82, 0x1c0000
s_mov_b32 s83, 0x20000
buffer_load_dwordx4 a[0:3], v9, s[80:83], s26 offen
v_mfma_f32_32x32x16_bf16 v[160:175], a[232:235], v[96:99], v[160:175]
v_exp_f32 v113, v33
v_mfma_f32_16x16x32_bf16 v[24:27], v[28:31], v[108:111], v[24:27]
buffer_load_dwordx4 v3, s[20:23], s24 offen offset:1024 lds
v_mfma_f32_32x32x16_bf16 v[176:191], a[236:239], v[96:99], v[176:191]
v_cvt_pk_bf16_f32 v96, v112, v113
v_exp_f32 v114, v34
buffer_load_dwordx4 v3, s[20:23], s24 offen offset:2048 lds
buffer_load_dwordx4 a[8:11], v9, s[80:83], s28 offen
buffer_load_dwordx4 a[16:19], v9, s[80:83], s37 offen
v_mfma_f32_32x32x16_bf16 v[128:143], a[240:243], v[100:103], v[128:143]
v_exp_f32 v115, v35
v_cmp_ngt_f32 vcc, s71, v24
s_cmp_ge_u32 s24, s43
v_mfma_f32_32x32x16_bf16 v[144:159], a[244:247], v[100:103], v[144:159]
v_cvt_pk_bf16_f32 v97, v114, v115
v_exp_f32 v116, v36
s_cbranch_vccnz .Lr132
.Lr132b:
s_cbranch_scc1 .Ls132
.Ls132b:
v_mfma_f32_32x32x16_bf16 v[160:175], a[248:251], v[100:103], v[160:175]
v_exp_f32 v117, v37
v_add_f32 v11, v11, v24
buffer_load_dwordx4 v3, s[20:23], s24 offen offset:3072 lds
buffer_load_dwordx4 a[24:27], v9, s[80:83], s38 offen
s_mov_b32 s80, %12
v_mfma_f32_32x32x16_bf16 v[176:191], a[252:255], v[100:103], v[176:191]
v_cvt_pk_bf16_f32 v98, v116, v117
v_exp_f32 v118, v38
s_waitcnt vmcnt(24)
s_barrier
s_add_u32 s24, s24, 0x2000
v_mfma_f32_32x32x16_bf16 v[48:63], a[192:195], a[160:163], v[80:95]
v_exp_f32 v119, v39
ds_read_b128 a[192:195], v2 offset:16384
s_and_b32 s81, %13, 0xffff
buffer_load_dwordx4 a[4:7], v9, s[80:83], s26 offen
buffer_load_dwordx4 a[12:15], v9, s[80:83], s28 offen
v_mfma_f32_32x32x16_bf16 v[48:63], a[196:199], a[164:167], v[48:63]
v_cvt_pk_bf16_f32 v99, v118, v119
v_exp_f32 v112, v40
ds_read_b128 a[196:199], v2 offset:17408
buffer_load_dwordx4 a[20:23], v9, s[80:83], s37 offen
buffer_load_dwordx4 a[28:31], v9, s[80:83], s38 offen
v_mfma_f32_32x32x16_bf16 v[48:63], a[200:203], a[168:171], v[48:63]
v_exp_f32 v113, v41
v_mfma_f32_16x16x32_bf16 v[24:27], v[28:31], v[96:99], 0
ds_read_b128 a[200:203], v2 offset:18432
v_mfma_f32_32x32x16_bf16 v[48:63], a[204:207], a[172:175], v[48:63]
v_cvt_pk_bf16_f32 v100, v112, v113
v_exp_f32 v114, v42
ds_read_b128 a[204:207], v2 offset:19456
v_mfma_f32_32x32x16_bf16 v[48:63], a[208:211], a[176:179], v[48:63]
v_exp_f32 v115, v43
ds_read_b128 a[208:211], v2 offset:20480
v_mfma_f32_32x32x16_bf16 v[48:63], a[212:215], a[180:183], v[48:63]
v_cvt_pk_bf16_f32 v101, v114, v115
v_exp_f32 v116, v44
ds_read_b128 a[212:215], v2 offset:21504
v_mfma_f32_32x32x16_bf16 v[48:63], a[216:219], a[184:187], v[48:63]
v_exp_f32 v117, v45
ds_read_b128 a[216:219], v2 offset:22528
v_mfma_f32_32x32x16_bf16 v[48:63], a[220:223], a[188:191], v[48:63]
v_cvt_pk_bf16_f32 v102, v116, v117
v_exp_f32 v118, v46
ds_read_b128 a[220:223], v2 offset:23552
v_mfma_f32_32x32x16_bf16 v[192:207], a[224:227], v[104:107], v[192:207]
v_exp_f32 v119, v47
ds_read_b128 a[224:227], v2 offset:40960
v_mfma_f32_32x32x16_bf16 v[208:223], a[228:231], v[104:107], v[208:223]
v_cvt_pk_bf16_f32 v103, v118, v119
v_exp_f32 v120, v48
ds_read_b128 a[228:231], v2 offset:41984
v_mfma_f32_32x32x16_bf16 v[224:239], a[232:235], v[104:107], v[224:239]
v_mfma_f32_16x16x32_bf16 v[24:27], v[28:31], v[100:103], v[24:27]
v_exp_f32 v121, v49
ds_read_b128 a[232:235], v2 offset:43008
v_mfma_f32_32x32x16_bf16 v[240:255], a[236:239], v[104:107], v[240:255]
v_cvt_pk_bf16_f32 v104, v120, v121
v_exp_f32 v122, v50
ds_read_b128 a[236:239], v2 offset:44032
v_mfma_f32_32x32x16_bf16 v[192:207], a[240:243], v[108:111], v[192:207]
v_exp_f32 v123, v51
v_cmp_ngt_f32 vcc, s71, v24
s_cmp_ge_u32 s24, s42
ds_read_b128 a[240:243], v2 offset:45056
v_mfma_f32_32x32x16_bf16 v[208:223], a[244:247], v[108:111], v[208:223]
v_cvt_pk_bf16_f32 v105, v122, v123
v_exp_f32 v124, v52
s_cbranch_vccnz .Lr131
.Lr131b:
s_cbranch_scc1 .Ls131
.Ls131b:
v_mfma_f32_32x32x16_bf16 v[224:239], a[248:251], v[108:111], v[224:239]
v_exp_f32 v125, v53
v_add_f32 v10, v10, v24
ds_read_b128 a[244:247], v2 offset:46080
ds_read_b128 a[248:251], v2 offset:47104
v_mfma_f32_32x32x16_bf16 v[240:255], a[252:255], v[108:111], v[240:255]
v_cvt_pk_bf16_f32 v106, v124, v125
v_exp_f32 v126, v54
ds_read_b128 a[252:255], v2 offset:48128
s_waitcnt lgkmcnt(8)
v_mfma_f32_32x32x16_bf16 v[32:47], a[192:195], a[128:131], v[64:79]
s_add_u32 m0, s36, 8192
v_exp_f32 v127, v55
buffer_load_dwordx4 v1, s[20:23], s24 offen offset:0 lds
ds_read_b64_tr_b16 a[96:97], v6 offset:0
ds_read_b64_tr_b16 a[98:99], v6 offset:1024
v_mfma_f32_32x32x16_bf16 v[32:47], a[196:199], a[132:135], v[32:47]
v_cvt_pk_bf16_f32 v107, v126, v127
v_exp_f32 v120, v56
ds_read_b64_tr_b16 a[100:101], v8 offset:0
ds_read_b64_tr_b16 a[102:103], v8 offset:1024
ds_read_b64_tr_b16 a[104:105], v6 offset:2048
v_mfma_f32_32x32x16_bf16 v[32:47], a[200:203], a[136:139], v[32:47]
v_exp_f32 v121, v57
v_mfma_f32_16x16x32_bf16 v[24:27], v[28:31], v[104:107], 0
v_mfma_f32_32x32x16_bf16 v[32:47], a[204:207], a[140:143], v[32:47]
v_cvt_pk_bf16_f32 v108, v120, v121
v_exp_f32 v122, v58
buffer_load_dwordx4 v1, s[20:23], s24 offen offset:1024 lds
ds_read_b64_tr_b16 a[106:107], v6 offset:3072
ds_read_b64_tr_b16 a[108:109], v8 offset:2048
v_mfma_f32_32x32x16_bf16 v[32:47], a[208:211], a[144:147], v[32:47]
v_exp_f32 v123, v59
ds_read_b64_tr_b16 a[110:111], v8 offset:3072
ds_read_b64_tr_b16 a[112:113], v6 offset:4096
ds_read_b64_tr_b16 a[114:115], v6 offset:5120
ds_read_b64_tr_b16 a[116:117], v8 offset:4096
v_mfma_f32_32x32x16_bf16 v[32:47], a[212:215], a[148:151], v[32:47]
v_cvt_pk_bf16_f32 v109, v122, v123
v_exp_f32 v124, v60
buffer_load_dwordx4 v1, s[20:23], s24 offen offset:2048 lds
ds_read_b64_tr_b16 a[118:119], v8 offset:5120
ds_read_b64_tr_b16 a[120:121], v6 offset:6144
v_mfma_f32_32x32x16_bf16 v[32:47], a[216:219], a[152:155], v[32:47]
v_exp_f32 v125, v61
buffer_load_dwordx4 v1, s[20:23], s24 offen offset:3072 lds
s_add_u32 m0, s36, 32768
ds_read_b64_tr_b16 a[122:123], v6 offset:7168
ds_read_b64_tr_b16 a[124:125], v8 offset:6144
v_mfma_f32_32x32x16_bf16 v[32:47], a[220:223], a[156:159], v[32:47]
v_cvt_pk_bf16_f32 v110, v124, v125
v_exp_f32 v126, v62
s_waitcnt lgkmcnt(0)
ds_read_b64_tr_b16 a[126:127], v8 offset:7168
v_mfma_f32_32x32x16_bf16 v[128:143], a[224:227], v[96:99], v[128:143]
v_exp_f32 v127, v63
buffer_load_dwordx4 v3, s[20:23], s24 offen offset:0 lds
v_mfma_f32_32x32x16_bf16 v[144:159], a[228:231], v[96:99], v[144:159]
v_cvt_pk_bf16_f32 v111, v126, v127
v_exp_f32 v112, v32
v_mfma_f32_32x32x16_bf16 v[160:175], a[232:235], v[96:99], v[160:175]
v_exp_f32 v113, v33
v_mfma_f32_16x16x32_bf16 v[24:27], v[28:31], v[108:111], v[24:27]
buffer_load_dwordx4 v3, s[20:23], s24 offen offset:1024 lds
v_mfma_f32_32x32x16_bf16 v[176:191], a[236:239], v[96:99], v[176:191]
v_cvt_pk_bf16_f32 v96, v112, v113
v_exp_f32 v114, v34
buffer_load_dwordx4 v3, s[20:23], s24 offen offset:2048 lds
v_mfma_f32_32x32x16_bf16 v[128:143], a[240:243], v[100:103], v[128:143]
v_exp_f32 v115, v35
v_cmp_ngt_f32 vcc, s71, v24
s_cmp_ge_u32 s24, s43
v_mfma_f32_32x32x16_bf16 v[144:159], a[244:247], v[100:103], v[144:159]
v_cvt_pk_bf16_f32 v97, v114, v115
v_exp_f32 v116, v36
s_cbranch_vccnz .Lr134
.Lr134b:
s_cbranch_scc1 .Ls134
.Ls134b:
v_mfma_f32_32x32x16_bf16 v[160:175], a[248:251], v[100:103], v[160:175]
v_exp_f32 v117, v37
v_add_f32 v11, v11, v24
buffer_load_dwordx4 v3, s[20:23], s24 offen offset:3072 lds
v_mfma_f32_32x32x16_bf16 v[176:191], a[252:255], v[100:103], v[176:191]
v_cvt_pk_bf16_f32 v98, v116, v117
v_exp_f32 v118, v38
s_waitcnt vmcnt(28)
s_barrier
s_add_u32 s24, s24, 0x2000
v_mfma_f32_32x32x16_bf16 v[48:63], a[192:195], a[160:163], v[80:95]
v_exp_f32 v119, v39
ds_read_b128 a[192:195], v2 offset:24576
s_lshl_b32 s26, %2, 12
s_add_u32 s26, s26, 0x5e00000
s_add_u32 s28, s26, 1048576
v_mfma_f32_32x32x16_bf16 v[48:63], a[196:199], a[164:167], v[48:63]
v_cvt_pk_bf16_f32 v99, v118, v119
v_exp_f32 v112, v40
ds_read_b128 a[196:199], v2 offset:25600
s_bfe_u32 s37, %2, 0x20002
s_mul_i32 s37, s37, 112
v_mfma_f32_32x32x16_bf16 v[48:63], a[200:203], a[168:171], v[48:63]
v_exp_f32 v113, v41
v_mfma_f32_16x16x32_bf16 v[24:27], v[28:31], v[96:99], 0
ds_read_b128 a[200:203], v2 offset:26624
v_mfma_f32_32x32x16_bf16 v[48:63], a[204:207], a[172:175], v[48:63]
v_cvt_pk_bf16_f32 v100, v112, v113
v_exp_f32 v114, v42
ds_read_b128 a[204:207], v2 offset:27648
s_lshr_b32 s38, %2, 4
s_mul_i32 s38, s38, 7
v_mfma_f32_32x32x16_bf16 v[48:63], a[208:211], a[176:179], v[48:63]
v_exp_f32 v115, v43
ds_read_b128 a[208:211], v2 offset:28672
s_add_u32 s37, s37, s38
s_lshl_b32 s37, s37, 1
s_bfe_u32 s38, %2, 0x10001
v_mfma_f32_32x32x16_bf16 v[48:63], a[212:215], a[180:183], v[48:63]
v_cvt_pk_bf16_f32 v101, v114, v115
v_exp_f32 v116, v44
ds_read_b128 a[212:215], v2 offset:29696
s_add_u32 s37, s37, s38
s_lshl_b32 s37, s37, 11
v_mfma_f32_32x32x16_bf16 v[48:63], a[216:219], a[184:187], v[48:63]
v_exp_f32 v117, v45
ds_read_b128 a[216:219], v2 offset:30720
s_add_u32 s37, s37, 0x6003000
s_and_b32 s38, %2, 1
s_lshl_b32 s38, s38, 14
v_mfma_f32_32x32x16_bf16 v[48:63], a[220:223], a[188:191], v[48:63]
v_cvt_pk_bf16_f32 v102, v116, v117
v_exp_f32 v118, v46
ds_read_b128 a[220:223], v2 offset:31744
s_add_u32 s38, s38, s37
s_sub_u32 s38, s38, 4096
v_mfma_f32_32x32x16_bf16 v[192:207], a[224:227], v[104:107], v[192:207]
v_exp_f32 v119, v47
ds_read_b128 a[224:227], v2 offset:49152
v_mfma_f32_32x32x16_bf16 v[208:223], a[228:231], v[104:107], v[208:223]
v_cvt_pk_bf16_f32 v103, v118, v119
v_exp_f32 v120, v48
ds_read_b128 a[228:231], v2 offset:50176
v_mfma_f32_32x32x16_bf16 v[224:239], a[232:235], v[104:107], v[224:239]
v_mfma_f32_16x16x32_bf16 v[24:27], v[28:31], v[100:103], v[24:27]
v_exp_f32 v121, v49
ds_read_b128 a[232:235], v2 offset:51200
v_mfma_f32_32x32x16_bf16 v[240:255], a[236:239], v[104:107], v[240:255]
v_cvt_pk_bf16_f32 v104, v120, v121
v_exp_f32 v122, v50
ds_read_b128 a[236:239], v2 offset:52224
v_mfma_f32_32x32x16_bf16 v[192:207], a[240:243], v[108:111], v[192:207]
v_exp_f32 v123, v51
v_cmp_ngt_f32 vcc, s71, v24
s_cmp_ge_u32 s24, s42
ds_read_b128 a[240:243], v2 offset:53248
v_mfma_f32_32x32x16_bf16 v[208:223], a[244:247], v[108:111], v[208:223]
v_cvt_pk_bf16_f32 v105, v122, v123
v_exp_f32 v124, v52
s_cbranch_vccnz .Lr133
.Lr133b:
s_cbranch_scc1 .Ls133
.Ls133b:
v_mfma_f32_32x32x16_bf16 v[224:239], a[248:251], v[108:111], v[224:239]
v_exp_f32 v125, v53
v_add_f32 v10, v10, v24
ds_read_b128 a[244:247], v2 offset:54272
ds_read_b128 a[248:251], v2 offset:55296
v_mfma_f32_32x32x16_bf16 v[240:255], a[252:255], v[108:111], v[240:255]
v_cvt_pk_bf16_f32 v106, v124, v125
v_exp_f32 v126, v54
ds_read_b128 a[252:255], v2 offset:56320
s_waitcnt lgkmcnt(8)
v_mfma_f32_32x32x16_bf16 v[32:47], a[192:195], a[128:131], v[64:79]
s_add_u32 m0, s36, 16384
v_exp_f32 v127, v55
buffer_load_dwordx4 v1, s[20:23], s24 offen offset:0 lds
v_mfma_f32_32x32x16_bf16 v[32:47], a[196:199], a[132:135], v[32:47]
v_cvt_pk_bf16_f32 v107, v126, v127
v_exp_f32 v120, v56
v_mfma_f32_32x32x16_bf16 v[32:47], a[200:203], a[136:139], v[32:47]
v_exp_f32 v121, v57
v_mfma_f32_16x16x32_bf16 v[24:27], v[28:31], v[104:107], 0
v_mfma_f32_32x32x16_bf16 v[32:47], a[204:207], a[140:143], v[32:47]
v_cvt_pk_bf16_f32 v108, v120, v121
v_exp_f32 v122, v58
buffer_load_dwordx4 v1, s[20:23], s24 offen offset:1024 lds
v_mfma_f32_32x32x16_bf16 v[32:47], a[208:211], a[144:147], v[32:47]
v_exp_f32 v123, v59
v_mfma_f32_32x32x16_bf16 v[32:47], a[212:215], a[148:151], v[32:47]
v_cvt_pk_bf16_f32 v109, v122, v123
v_exp_f32 v124, v60
buffer_load_dwordx4 v1, s[20:23], s24 offen offset:2048 lds
v_mfma_f32_32x32x16_bf16 v[32:47], a[216:219], a[152:155], v[32:47]
v_exp_f32 v125, v61
buffer_load_dwordx4 v1, s[20:23], s24 offen offset:3072 lds
s_add_u32 m0, s36, 40960
v_mfma_f32_32x32x16_bf16 v[32:47], a[220:223], a[156:159], v[32:47]
v_cvt_pk_bf16_f32 v110, v124, v125
v_exp_f32 v126, v62
s_waitcnt lgkmcnt(0)
v_mfma_f32_32x32x16_bf16 v[128:143], a[224:227], v[96:99], v[128:143]
v_exp_f32 v127, v63
buffer_load_dwordx4 v3, s[20:23], s24 offen offset:0 lds
buffer_store_dwordx4 a[96:99], v1, s[16:19], s26 offen offset:0 sc1
buffer_store_dwordx4 a[100:103], v1, s[16:19], s26 offen offset:1024 sc1
v_mfma_f32_32x32x16_bf16 v[144:159], a[228:231], v[96:99], v[144:159]
v_cvt_pk_bf16_f32 v111, v126, v127
v_exp_f32 v112, v32
buffer_store_dwordx4 a[104:107], v1, s[16:19], s26 offen offset:2048 sc1
buffer_store_dwordx4 a[108:111], v1, s[16:19], s26 offen offset:3072 sc1
v_mfma_f32_32x32x16_bf16 v[160:175], a[232:235], v[96:99], v[160:175]
v_exp_f32 v113, v33
v_mfma_f32_16x16x32_bf16 v[24:27], v[28:31], v[108:111], v[24:27]
buffer_load_dwordx4 v3, s[20:23], s24 offen offset:1024 lds
v_mfma_f32_32x32x16_bf16 v[176:191], a[236:239], v[96:99], v[176:191]
v_cvt_pk_bf16_f32 v96, v112, v113
v_exp_f32 v114, v34
buffer_load_dwordx4 v3, s[20:23], s24 offen offset:2048 lds
buffer_store_dwordx4 a[112:115], v1, s[16:19], s28 offen offset:0 sc1
v_mfma_f32_32x32x16_bf16 v[128:143], a[240:243], v[100:103], v[128:143]
v_exp_f32 v115, v35
v_cmp_ngt_f32 vcc, s71, v24
s_cmp_ge_u32 s24, s43
v_mfma_f32_32x32x16_bf16 v[144:159], a[244:247], v[100:103], v[144:159]
v_cvt_pk_bf16_f32 v97, v114, v115
v_exp_f32 v116, v36
s_cbranch_vccnz .Lr136
.Lr136b:
s_cbranch_scc1 .Ls136
.Ls136b:
v_mfma_f32_32x32x16_bf16 v[160:175], a[248:251], v[100:103], v[160:175]
v_exp_f32 v117, v37
v_add_f32 v11, v11, v24
buffer_load_dwordx4 v3, s[20:23], s24 offen offset:3072 lds
buffer_store_dwordx4 a[116:119], v1, s[16:19], s28 offen offset:1024 sc1
v_mfma_f32_32x32x16_bf16 v[176:191], a[252:255], v[100:103], v[176:191]
v_cvt_pk_bf16_f32 v98, v116, v117
v_exp_f32 v118, v38
s_waitcnt vmcnt(26)
s_barrier
s_add_u32 s24, s24, 0x2000
v_mfma_f32_32x32x16_bf16 v[48:63], a[192:195], a[160:163], v[80:95]
v_exp_f32 v119, v39
ds_read_b128 a[192:195], v2 offset:0
buffer_store_dwordx4 a[120:123], v1, s[16:19], s28 offen offset:2048 sc1
buffer_store_dwordx4 a[124:127], v1, s[16:19], s28 offen offset:3072 sc1
v_mfma_f32_32x32x16_bf16 v[48:63], a[196:199], a[164:167], v[48:63]
v_cvt_pk_bf16_f32 v99, v118, v119
v_exp_f32 v112, v40
ds_read_b128 a[196:199], v2 offset:1024
v_mfma_f32_32x32x16_bf16 v[48:63], a[200:203], a[168:171], v[48:63]
v_exp_f32 v113, v41
v_mfma_f32_16x16x32_bf16 v[24:27], v[28:31], v[96:99], 0
ds_read_b128 a[200:203], v2 offset:2048
v_mfma_f32_32x32x16_bf16 v[48:63], a[204:207], a[172:175], v[48:63]
v_cvt_pk_bf16_f32 v100, v112, v113
v_exp_f32 v114, v42
ds_read_b128 a[204:207], v2 offset:3072
v_mfma_f32_32x32x16_bf16 v[48:63], a[208:211], a[176:179], v[48:63]
v_exp_f32 v115, v43
ds_read_b128 a[208:211], v2 offset:4096
v_mfma_f32_32x32x16_bf16 v[48:63], a[212:215], a[180:183], v[48:63]
v_cvt_pk_bf16_f32 v101, v114, v115
v_exp_f32 v116, v44
ds_read_b128 a[212:215], v2 offset:5120
v_mfma_f32_32x32x16_bf16 v[48:63], a[216:219], a[184:187], v[48:63]
v_exp_f32 v117, v45
ds_read_b128 a[216:219], v2 offset:6144
v_mfma_f32_32x32x16_bf16 v[48:63], a[220:223], a[188:191], v[48:63]
v_cvt_pk_bf16_f32 v102, v116, v117
v_exp_f32 v118, v46
ds_read_b128 a[220:223], v2 offset:7168
v_mfma_f32_32x32x16_bf16 v[192:207], a[224:227], v[104:107], v[192:207]
v_exp_f32 v119, v47
ds_read_b128 a[224:227], v2 offset:57344
v_mfma_f32_32x32x16_bf16 v[208:223], a[228:231], v[104:107], v[208:223]
v_cvt_pk_bf16_f32 v103, v118, v119
v_exp_f32 v120, v48
ds_read_b128 a[228:231], v2 offset:58368
v_mfma_f32_32x32x16_bf16 v[224:239], a[232:235], v[104:107], v[224:239]
v_mfma_f32_16x16x32_bf16 v[24:27], v[28:31], v[100:103], v[24:27]
v_exp_f32 v121, v49
ds_read_b128 a[232:235], v2 offset:59392
v_mfma_f32_32x32x16_bf16 v[240:255], a[236:239], v[104:107], v[240:255]
v_cvt_pk_bf16_f32 v104, v120, v121
v_exp_f32 v122, v50
ds_read_b128 a[236:239], v2 offset:60416
v_mfma_f32_32x32x16_bf16 v[192:207], a[240:243], v[108:111], v[192:207]
v_exp_f32 v123, v51
v_cmp_ngt_f32 vcc, s71, v24
s_cmp_ge_u32 s24, s42
ds_read_b128 a[240:243], v2 offset:61440
v_mfma_f32_32x32x16_bf16 v[208:223], a[244:247], v[108:111], v[208:223]
v_cvt_pk_bf16_f32 v105, v122, v123
v_exp_f32 v124, v52
s_cbranch_vccnz .Lr135
.Lr135b:
s_cbranch_scc1 .Ls135
.Ls135b:
v_mfma_f32_32x32x16_bf16 v[224:239], a[248:251], v[108:111], v[224:239]
v_exp_f32 v125, v53
v_add_f32 v10, v10, v24
ds_read_b128 a[244:247], v2 offset:62464
ds_read_b128 a[248:251], v2 offset:63488
v_mfma_f32_32x32x16_bf16 v[240:255], a[252:255], v[108:111], v[240:255]
v_cvt_pk_bf16_f32 v106, v124, v125
v_exp_f32 v126, v54
ds_read_b128 a[252:255], v2 offset:64512
s_waitcnt lgkmcnt(8)
s_cmp_lt_u32 s24, s27
s_cselect_b32 s22, s52, 0
v_mfma_f32_32x32x16_bf16 v[32:47], a[192:195], a[128:131], v[64:79]
s_add_u32 m0, s36, 24576
v_exp_f32 v127, v55
buffer_load_dwordx4 v1, s[20:23], s24 offen offset:0 lds
ds_read_b64_tr_b16 a[96:97], v6 offset:8192
ds_read_b64_tr_b16 a[98:99], v6 offset:9216
v_mfma_f32_32x32x16_bf16 v[32:47], a[196:199], a[132:135], v[32:47]
v_cvt_pk_bf16_f32 v107, v126, v127
v_exp_f32 v120, v56
ds_read_b64_tr_b16 a[100:101], v8 offset:8192
ds_read_b64_tr_b16 a[102:103], v8 offset:9216
ds_read_b64_tr_b16 a[104:105], v6 offset:10240
v_mfma_f32_32x32x16_bf16 v[32:47], a[200:203], a[136:139], v[32:47]
v_exp_f32 v121, v57
v_mfma_f32_16x16x32_bf16 v[24:27], v[28:31], v[104:107], 0
v_mfma_f32_32x32x16_bf16 v[32:47], a[204:207], a[140:143], v[32:47]
v_cvt_pk_bf16_f32 v108, v120, v121
v_exp_f32 v122, v58
buffer_load_dwordx4 v1, s[20:23], s24 offen offset:1024 lds
ds_read_b64_tr_b16 a[106:107], v6 offset:11264
ds_read_b64_tr_b16 a[108:109], v8 offset:10240
v_mfma_f32_32x32x16_bf16 v[32:47], a[208:211], a[144:147], v[32:47]
v_exp_f32 v123, v59
ds_read_b64_tr_b16 a[110:111], v8 offset:11264
v_mfma_f32_32x32x16_bf16 v[32:47], a[212:215], a[148:151], v[32:47]
v_cvt_pk_bf16_f32 v109, v122, v123
v_exp_f32 v124, v60
buffer_load_dwordx4 v1, s[20:23], s24 offen offset:2048 lds
v_mfma_f32_32x32x16_bf16 v[32:47], a[216:219], a[152:155], v[32:47]
v_exp_f32 v125, v61
buffer_load_dwordx4 v1, s[20:23], s24 offen offset:3072 lds
s_add_u32 m0, s36, 49152
v_mfma_f32_32x32x16_bf16 v[32:47], a[220:223], a[156:159], v[32:47]
v_cvt_pk_bf16_f32 v110, v124, v125
v_exp_f32 v126, v62
s_waitcnt lgkmcnt(0)
v_mfma_f32_32x32x16_bf16 v[128:143], a[224:227], v[96:99], v[128:143]
v_exp_f32 v127, v63
buffer_load_dwordx4 v3, s[20:23], s24 offen offset:0 lds
buffer_store_dwordx4 a[96:99], v1, s[16:19], s38 offen offset:0 sc1
buffer_store_dwordx4 a[100:103], v1, s[16:19], s38 offen offset:1024 sc1
buffer_store_dwordx4 a[104:107], v1, s[16:19], s37 offen offset:0 sc1
v_mfma_f32_32x32x16_bf16 v[144:159], a[228:231], v[96:99], v[144:159]
v_cvt_pk_bf16_f32 v111, v126, v127
v_exp_f32 v112, v32
buffer_store_dwordx4 a[108:111], v1, s[16:19], s37 offen offset:1024 sc1
v_bfe_u32 v20, v0, 5, 1
v_lshlrev_b32 v20, 4, v20
v_mfma_f32_32x32x16_bf16 v[160:175], a[232:235], v[96:99], v[160:175]
v_exp_f32 v113, v33
v_mfma_f32_16x16x32_bf16 v[24:27], v[28:31], v[108:111], v[24:27]
buffer_load_dwordx4 v3, s[20:23], s24 offen offset:1024 lds
v_mfma_f32_32x32x16_bf16 v[176:191], a[236:239], v[96:99], v[176:191]
v_cvt_pk_bf16_f32 v96, v112, v113
v_exp_f32 v114, v34
buffer_load_dwordx4 v3, s[20:23], s24 offen offset:2048 lds
v_bfe_u32 v21, v0, 4, 1
v_lshl_add_u32 v20, v21, 2, v20
v_mfma_f32_32x32x16_bf16 v[128:143], a[240:243], v[100:103], v[128:143]
v_exp_f32 v115, v35
v_cmp_ngt_f32 vcc, s71, v24
s_cmp_ge_u32 s24, s43
v_mfma_f32_32x32x16_bf16 v[144:159], a[244:247], v[100:103], v[144:159]
v_cvt_pk_bf16_f32 v97, v114, v115
v_exp_f32 v116, v36
s_cbranch_vccnz .Lr138
.Lr138b:
s_cbranch_scc1 .Ls138
.Ls138b:
v_mfma_f32_32x32x16_bf16 v[160:175], a[248:251], v[100:103], v[160:175]
v_exp_f32 v117, v37
v_add_f32 v11, v11, v24
buffer_load_dwordx4 v3, s[20:23], s24 offen offset:3072 lds
v_bfe_u32 v21, v0, 2, 2
v_add_u32 v20, v20, v21
v_mfma_f32_32x32x16_bf16 v[176:191], a[252:255], v[100:103], v[176:191]
v_cvt_pk_bf16_f32 v98, v116, v117
v_exp_f32 v118, v38
s_waitcnt vmcnt(28)
s_barrier
s_add_u32 s24, s24, 0x2000
v_mfma_f32_32x32x16_bf16 v[48:63], a[192:195], a[160:163], v[80:95]
v_exp_f32 v119, v39
ds_read_b128 a[192:195], v2 offset:8192
v_mul_u32_u24 v20, 232, v20
v_and_b32 v21, 3, v0
v_lshl_add_u32 v20, v21, 3, v20
v_mfma_f32_32x32x16_bf16 v[48:63], a[196:199], a[164:167], v[48:63]
v_cvt_pk_bf16_f32 v99, v118, v119
v_exp_f32 v112, v40
ds_read_b128 a[196:199], v2 offset:9216
v_add_u32 v9, s70, v20
v_and_b32 v13, 15, v0
v_mfma_f32_32x32x16_bf16 v[48:63], a[200:203], a[168:171], v[48:63]
v_exp_f32 v113, v41
v_mfma_f32_16x16x32_bf16 v[24:27], v[28:31], v[96:99], 0
ds_read_b128 a[200:203], v2 offset:10240
v_mfma_f32_32x32x16_bf16 v[48:63], a[204:207], a[172:175], v[48:63]
v_cvt_pk_bf16_f32 v100, v112, v113
v_exp_f32 v114, v42
ds_read_b128 a[204:207], v2 offset:11264
v_lshlrev_b32 v13, 4, v13
v_mov_b32 v20, 0x3f803f80
v_mfma_f32_32x32x16_bf16 v[48:63], a[208:211], a[176:179], v[48:63]
v_exp_f32 v115, v43
ds_read_b128 a[208:211], v2 offset:12288
v_accvgpr_write_b32 a88, v20
v_accvgpr_write_b32 a89, v20
v_accvgpr_write_b32 a90, v20
v_mfma_f32_32x32x16_bf16 v[48:63], a[212:215], a[180:183], v[48:63]
v_cvt_pk_bf16_f32 v101, v114, v115
v_exp_f32 v116, v44
ds_read_b128 a[212:215], v2 offset:13312
v_accvgpr_write_b32 a91, v20
v_mfma_f32_32x32x16_bf16 v[48:63], a[216:219], a[184:187], v[48:63]
v_exp_f32 v117, v45
ds_read_b128 a[216:219], v2 offset:14336
v_mfma_f32_32x32x16_bf16 v[48:63], a[220:223], a[188:191], v[48:63]
v_cvt_pk_bf16_f32 v102, v116, v117
v_exp_f32 v118, v46
ds_read_b128 a[220:223], v2 offset:15360
v_mfma_f32_32x32x16_bf16 v[192:207], a[224:227], v[104:107], v[192:207]
v_exp_f32 v119, v47
ds_read_b128 a[224:227], v2 offset:32768
v_mfma_f32_32x32x16_bf16 v[208:223], a[228:231], v[104:107], v[208:223]
v_cvt_pk_bf16_f32 v103, v118, v119
v_exp_f32 v120, v48
ds_read_b128 a[228:231], v2 offset:33792
v_mfma_f32_32x32x16_bf16 v[224:239], a[232:235], v[104:107], v[224:239]
v_mfma_f32_16x16x32_bf16 v[24:27], v[28:31], v[100:103], v[24:27]
v_exp_f32 v121, v49
ds_read_b128 a[232:235], v2 offset:34816
v_mfma_f32_32x32x16_bf16 v[240:255], a[236:239], v[104:107], v[240:255]
v_cvt_pk_bf16_f32 v104, v120, v121
v_exp_f32 v122, v50
ds_read_b128 a[236:239], v2 offset:35840
v_mfma_f32_32x32x16_bf16 v[192:207], a[240:243], v[108:111], v[192:207]
v_exp_f32 v123, v51
v_cmp_ngt_f32 vcc, s71, v24
s_cmp_ge_u32 s24, s42
ds_read_b128 a[240:243], v2 offset:36864
v_mfma_f32_32x32x16_bf16 v[208:223], a[244:247], v[108:111], v[208:223]
v_cvt_pk_bf16_f32 v105, v122, v123
v_exp_f32 v124, v52
s_cbranch_vccnz .Lr137
.Lr137b:
s_cbranch_scc1 .Ls137
.Ls137b:
v_mfma_f32_32x32x16_bf16 v[224:239], a[248:251], v[108:111], v[224:239]
v_exp_f32 v125, v53
v_add_f32 v10, v10, v24
ds_read_b128 a[244:247], v2 offset:37888
ds_read_b128 a[248:251], v2 offset:38912
v_mfma_f32_32x32x16_bf16 v[240:255], a[252:255], v[108:111], v[240:255]
v_cvt_pk_bf16_f32 v106, v124, v125
v_exp_f32 v126, v54
ds_read_b128 a[252:255], v2 offset:39936
s_waitcnt lgkmcnt(8)
v_mfma_f32_32x32x16_bf16 v[32:47], a[192:195], a[128:131], v[64:79]
s_add_u32 m0, s36, 0
v_exp_f32 v127, v55
buffer_load_dwordx4 v1, s[20:23], s24 offen offset:0 lds
ds_write2_b32 v12, a0, a4 offset0:0 offset1:1
ds_write2_b32 v12, a2, a6 offset0:2 offset1:3
v_mfma_f32_32x32x16_bf16 v[32:47], a[196:199], a[132:135], v[32:47]
v_cvt_pk_bf16_f32 v107, v126, v127
v_exp_f32 v120, v56
ds_write2_b32 v12, a1, a5 offset0:4 offset1:5
ds_write2_b32 v12, a3, a7 offset0:6 offset1:7
s_nop 1
v_add_u32 v12, 1856, v12
v_mfma_f32_32x32x16_bf16 v[32:47], a[200:203], a[136:139], v[32:47]
v_exp_f32 v121, v57
v_mfma_f32_16x16x32_bf16 v[24:27], v[28:31], v[104:107], 0
v_mfma_f32_32x32x16_bf16 v[32:47], a[204:207], a[140:143], v[32:47]
v_cvt_pk_bf16_f32 v108, v120, v121
v_exp_f32 v122, v58
buffer_load_dwordx4 v1, s[20:23], s24 offen offset:1024 lds
ds_write2_b32 v12, a8, a12 offset0:0 offset1:1
ds_write2_b32 v12, a10, a14 offset0:2 offset1:3
v_mfma_f32_32x32x16_bf16 v[32:47], a[208:211], a[144:147], v[32:47]
v_exp_f32 v123, v59
ds_write2_b32 v12, a9, a13 offset0:4 offset1:5
ds_write2_b32 v12, a11, a15 offset0:6 offset1:7
s_nop 1
v_add_u32 v12, 1856, v12
ds_write2_b32 v12, a16, a20 offset0:0 offset1:1
v_mfma_f32_32x32x16_bf16 v[32:47], a[212:215], a[148:151], v[32:47]
v_cvt_pk_bf16_f32 v109, v122, v123
v_exp_f32 v124, v60
buffer_load_dwordx4 v1, s[20:23], s24 offen offset:2048 lds
ds_write2_b32 v12, a18, a22 offset0:2 offset1:3
ds_write2_b32 v12, a17, a21 offset0:4 offset1:5
v_mfma_f32_32x32x16_bf16 v[32:47], a[216:219], a[152:155], v[32:47]
v_exp_f32 v125, v61
buffer_load_dwordx4 v1, s[20:23], s24 offen offset:3072 lds
s_add_u32 m0, s36, 57344
ds_write2_b32 v12, a19, a23 offset0:6 offset1:7
s_nop 1
v_add_u32 v12, 1856, v12
v_mfma_f32_32x32x16_bf16 v[32:47], a[220:223], a[156:159], v[32:47]
v_cvt_pk_bf16_f32 v110, v124, v125
v_exp_f32 v126, v62
s_waitcnt lgkmcnt(0)
ds_write2_b32 v12, a24, a28 offset0:0 offset1:1
ds_write2_b32 v12, a26, a30 offset0:2 offset1:3
v_mfma_f32_32x32x16_bf16 v[128:143], a[224:227], v[96:99], v[128:143]
v_exp_f32 v127, v63
buffer_load_dwordx4 v3, s[20:23], s24 offen offset:0 lds
ds_write2_b32 v12, a25, a29 offset0:4 offset1:5
ds_write2_b32 v12, a27, a31 offset0:6 offset1:7
v_mfma_f32_32x32x16_bf16 v[144:159], a[228:231], v[96:99], v[144:159]
v_cvt_pk_bf16_f32 v111, v126, v127
v_exp_f32 v112, v32
v_mfma_f32_32x32x16_bf16 v[160:175], a[232:235], v[96:99], v[160:175]
v_exp_f32 v113, v33
v_mfma_f32_16x16x32_bf16 v[24:27], v[28:31], v[108:111], v[24:27]
buffer_load_dwordx4 v3, s[20:23], s24 offen offset:1024 lds
v_mfma_f32_32x32x16_bf16 v[176:191], a[236:239], v[96:99], v[176:191]
v_cvt_pk_bf16_f32 v96, v112, v113
v_exp_f32 v114, v34
buffer_load_dwordx4 v3, s[20:23], s24 offen offset:2048 lds
v_mfma_f32_32x32x16_bf16 v[128:143], a[240:243], v[100:103], v[128:143]
v_exp_f32 v115, v35
v_cmp_ngt_f32 vcc, s71, v24
s_cmp_ge_u32 s24, s43
v_mfma_f32_32x32x16_bf16 v[144:159], a[244:247], v[100:103], v[144:159]
v_cvt_pk_bf16_f32 v97, v114, v115
v_exp_f32 v116, v36
s_cbranch_vccnz .Lr140
.Lr140b:
s_cbranch_scc1 .Ls140
.Ls140b:
v_mfma_f32_32x32x16_bf16 v[160:175], a[248:251], v[100:103], v[160:175]
v_exp_f32 v117, v37
v_add_f32 v11, v11, v24
buffer_load_dwordx4 v3, s[20:23], s24 offen offset:3072 lds
v_mfma_f32_32x32x16_bf16 v[176:191], a[252:255], v[100:103], v[176:191]
v_cvt_pk_bf16_f32 v98, v116, v117
v_exp_f32 v118, v38
s_waitcnt vmcnt(22)
s_barrier
s_add_u32 s24, s24, 0x2000
v_mfma_f32_32x32x16_bf16 v[48:63], a[192:195], a[160:163], v[80:95]
v_exp_f32 v119, v39
ds_read_b128 a[192:195], v2 offset:16384
s_mul_i32 s26, s39, 122880
s_add_u32 s26, s26, 0x5800000
s_mul_i32 s28, s53, 7168
v_mfma_f32_32x32x16_bf16 v[48:63], a[196:199], a[164:167], v[48:63]
v_cvt_pk_bf16_f32 v99, v118, v119
v_exp_f32 v112, v40
ds_read_b128 a[196:199], v2 offset:17408
s_add_u32 s28, s28, s26
s_add_u32 s37, s28, 4096
v_mfma_f32_32x32x16_bf16 v[48:63], a[200:203], a[168:171], v[48:63]
v_exp_f32 v113, v41
v_mfma_f32_16x16x32_bf16 v[24:27], v[28:31], v[96:99], 0
ds_read_b128 a[200:203], v2 offset:18432
v_mfma_f32_32x32x16_bf16 v[48:63], a[204:207], a[172:175], v[48:63]
v_cvt_pk_bf16_f32 v100, v112, v113
v_exp_f32 v114, v42
ds_read_b128 a[204:207], v2 offset:19456
s_lshr_b32 s38, s53, 2
s_lshl_b32 s38, s38, 8
v_mfma_f32_32x32x16_bf16 v[48:63], a[208:211], a[176:179], v[48:63]
v_exp_f32 v115, v43
ds_read_b128 a[208:211], v2 offset:20480
s_add_u32 s26, s26, s38
s_and_b32 s38, s53, 3
s_lshl_b32 s38, s38, 2
v_mfma_f32_32x32x16_bf16 v[48:63], a[212:215], a[180:183], v[48:63]
v_cvt_pk_bf16_f32 v101, v114, v115
v_exp_f32 v116, v44
ds_read_b128 a[212:215], v2 offset:21504
s_add_u32 s26, s26, s38
s_add_u32 s26, s26, 114688
v_mfma_f32_32x32x16_bf16 v[48:63], a[216:219], a[184:187], v[48:63]
v_exp_f32 v117, v45
ds_read_b128 a[216:219], v2 offset:22528
s_add_u32 s38, s26, 4096
v_mfma_f32_32x32x16_bf16 v[48:63], a[220:223], a[188:191], v[48:63]
v_cvt_pk_bf16_f32 v102, v116, v117
v_exp_f32 v118, v46
ds_read_b128 a[220:223], v2 offset:23552
v_mfma_f32_32x32x16_bf16 v[192:207], a[224:227], v[104:107], v[192:207]
v_exp_f32 v119, v47
ds_read_b128 a[224:227], v2 offset:40960
v_mfma_f32_32x32x16_bf16 v[208:223], a[228:231], v[104:107], v[208:223]
v_cvt_pk_bf16_f32 v103, v118, v119
v_exp_f32 v120, v48
ds_read_b128 a[228:231], v2 offset:41984
v_mfma_f32_32x32x16_bf16 v[224:239], a[232:235], v[104:107], v[224:239]
v_mfma_f32_16x16x32_bf16 v[24:27], v[28:31], v[100:103], v[24:27]
v_exp_f32 v121, v49
ds_read_b128 a[232:235], v2 offset:43008
v_mfma_f32_32x32x16_bf16 v[240:255], a[236:239], v[104:107], v[240:255]
v_cvt_pk_bf16_f32 v104, v120, v121
v_exp_f32 v122, v50
ds_read_b128 a[236:239], v2 offset:44032
v_mfma_f32_32x32x16_bf16 v[192:207], a[240:243], v[108:111], v[192:207]
v_exp_f32 v123, v51
v_cmp_ngt_f32 vcc, s71, v24
s_cmp_ge_u32 s24, s42
ds_read_b128 a[240:243], v2 offset:45056
v_mfma_f32_32x32x16_bf16 v[208:223], a[244:247], v[108:111], v[208:223]
v_cvt_pk_bf16_f32 v105, v122, v123
v_exp_f32 v124, v52
s_cbranch_vccnz .Lr139
.Lr139b:
s_cbranch_scc1 .Ls139
.Ls139b:
v_mfma_f32_32x32x16_bf16 v[224:239], a[248:251], v[108:111], v[224:239]
v_exp_f32 v125, v53
v_add_f32 v10, v10, v24
ds_read_b128 a[244:247], v2 offset:46080
ds_read_b128 a[248:251], v2 offset:47104
v_mfma_f32_32x32x16_bf16 v[240:255], a[252:255], v[108:111], v[240:255]
v_cvt_pk_bf16_f32 v106, v124, v125
v_exp_f32 v126, v54
ds_read_b128 a[252:255], v2 offset:48128
s_waitcnt lgkmcnt(8)
v_mfma_f32_32x32x16_bf16 v[32:47], a[192:195], a[128:131], v[64:79]
s_add_u32 m0, s36, 8192
v_exp_f32 v127, v55
buffer_load_dwordx4 v1, s[20:23], s24 offen offset:0 lds
ds_read_b64_tr_b16 a[32:33], v9 offset:0
v_mfma_f32_32x32x16_bf16 v[32:47], a[196:199], a[132:135], v[32:47]
v_cvt_pk_bf16_f32 v107, v126, v127
v_exp_f32 v120, v56
ds_read_b64_tr_b16 a[34:35], v9 offset:1856
ds_read_b64_tr_b16 a[36:37], v9 offset:32
v_mfma_f32_32x32x16_bf16 v[32:47], a[200:203], a[136:139], v[32:47]
v_exp_f32 v121, v57
v_mfma_f32_16x16x32_bf16 v[24:27], v[28:31], v[104:107], 0
v_mfma_f32_32x32x16_bf16 v[32:47], a[204:207], a[140:143], v[32:47]
v_cvt_pk_bf16_f32 v108, v120, v121
v_exp_f32 v122, v58
buffer_load_dwordx4 v1, s[20:23], s24 offen offset:1024 lds
ds_read_b64_tr_b16 a[38:39], v9 offset:1888
v_mfma_f32_32x32x16_bf16 v[32:47], a[208:211], a[144:147], v[32:47]
v_exp_f32 v123, v59
ds_read_b64_tr_b16 a[40:41], v9 offset:64
ds_read_b64_tr_b16 a[42:43], v9 offset:1920
ds_read_b64_tr_b16 a[44:45], v9 offset:96
v_mfma_f32_32x32x16_bf16 v[32:47], a[212:215], a[148:151], v[32:47]
v_cvt_pk_bf16_f32 v109, v122, v123
v_exp_f32 v124, v60
buffer_load_dwordx4 v1, s[20:23], s24 offen offset:2048 lds
ds_read_b64_tr_b16 a[46:47], v9 offset:1952
v_mfma_f32_32x32x16_bf16 v[32:47], a[216:219], a[152:155], v[32:47]
v_exp_f32 v125, v61
buffer_load_dwordx4 v1, s[20:23], s24 offen offset:3072 lds
s_add_u32 m0, s36, 32768
ds_read_b64_tr_b16 a[48:49], v9 offset:128
v_mfma_f32_32x32x16_bf16 v[32:47], a[220:223], a[156:159], v[32:47]
v_cvt_pk_bf16_f32 v110, v124, v125
v_exp_f32 v126, v62
s_waitcnt lgkmcnt(0)
ds_read_b64_tr_b16 a[50:51], v9 offset:1984
v_mfma_f32_32x32x16_bf16 v[128:143], a[224:227], v[96:99], v[128:143]
v_exp_f32 v127, v63
buffer_load_dwordx4 v3, s[20:23], s24 offen offset:0 lds
ds_read_b64_tr_b16 a[52:53], v9 offset:160
ds_read_b64_tr_b16 a[54:55], v9 offset:2016
v_mfma_f32_32x32x16_bf16 v[144:159], a[228:231], v[96:99], v[144:159]
v_cvt_pk_bf16_f32 v111, v126, v127
v_exp_f32 v112, v32
ds_read_b64_tr_b16 a[56:57], v9 offset:192
ds_read_b64_tr_b16 a[58:59], v9 offset:2048
v_mfma_f32_32x32x16_bf16 v[160:175], a[232:235], v[96:99], v[160:175]
v_exp_f32 v113, v33
v_mfma_f32_16x16x32_bf16 v[24:27], v[28:31], v[108:111], v[24:27]
buffer_load_dwordx4 v3, s[20:23], s24 offen offset:1024 lds
v_mfma_f32_32x32x16_bf16 v[176:191], a[236:239], v[96:99], v[176:191]
v_cvt_pk_bf16_f32 v96, v112, v113
v_exp_f32 v114, v34
buffer_load_dwordx4 v3, s[20:23], s24 offen offset:2048 lds
v_mfma_f32_32x32x16_bf16 v[128:143], a[240:243], v[100:103], v[128:143]
v_exp_f32 v115, v35
v_cmp_ngt_f32 vcc, s71, v24
s_cmp_ge_u32 s24, s43
v_mfma_f32_32x32x16_bf16 v[144:159], a[244:247], v[100:103], v[144:159]
v_cvt_pk_bf16_f32 v97, v114, v115
v_exp_f32 v116, v36
s_cbranch_vccnz .Lr142
.Lr142b:
s_cbranch_scc1 .Ls142
.Ls142b:
v_mfma_f32_32x32x16_bf16 v[160:175], a[248:251], v[100:103], v[160:175]
v_exp_f32 v117, v37
v_add_f32 v11, v11, v24
buffer_load_dwordx4 v3, s[20:23], s24 offen offset:3072 lds
v_mfma_f32_32x32x16_bf16 v[176:191], a[252:255], v[100:103], v[176:191]
v_cvt_pk_bf16_f32 v98, v116, v117
v_exp_f32 v118, v38
s_waitcnt vmcnt(16)
s_barrier
s_add_u32 s24, s24, 0x2000
v_mfma_f32_32x32x16_bf16 v[48:63], a[192:195], a[160:163], v[80:95]
v_exp_f32 v119, v39
ds_read_b128 a[192:195], v2 offset:24576
v_mfma_f32_32x32x16_bf16 v[48:63], a[196:199], a[164:167], v[48:63]
v_cvt_pk_bf16_f32 v99, v118, v119
v_exp_f32 v112, v40
ds_read_b128 a[196:199], v2 offset:25600
v_mfma_f32_32x32x16_bf16 v[48:63], a[200:203], a[168:171], v[48:63]
v_exp_f32 v113, v41
v_mfma_f32_16x16x32_bf16 v[24:27], v[28:31], v[96:99], 0
ds_read_b128 a[200:203], v2 offset:26624
v_mfma_f32_32x32x16_bf16 v[48:63], a[204:207], a[172:175], v[48:63]
v_cvt_pk_bf16_f32 v100, v112, v113
v_exp_f32 v114, v42
ds_read_b128 a[204:207], v2 offset:27648
v_mfma_f32_32x32x16_bf16 v[48:63], a[208:211], a[176:179], v[48:63]
v_exp_f32 v115, v43
ds_read_b128 a[208:211], v2 offset:28672
v_mfma_f32_32x32x16_bf16 v[48:63], a[212:215], a[180:183], v[48:63]
v_cvt_pk_bf16_f32 v101, v114, v115
v_exp_f32 v116, v44
ds_read_b128 a[212:215], v2 offset:29696
v_mfma_f32_32x32x16_bf16 v[48:63], a[216:219], a[184:187], v[48:63]
v_exp_f32 v117, v45
ds_read_b128 a[216:219], v2 offset:30720
v_mfma_f32_32x32x16_bf16 v[48:63], a[220:223], a[188:191], v[48:63]
v_cvt_pk_bf16_f32 v102, v116, v117
v_exp_f32 v118, v46
ds_read_b128 a[220:223], v2 offset:31744
v_mfma_f32_32x32x16_bf16 v[192:207], a[224:227], v[104:107], v[192:207]
v_exp_f32 v119, v47
ds_read_b128 a[224:227], v2 offset:49152
v_mfma_f32_32x32x16_bf16 v[208:223], a[228:231], v[104:107], v[208:223]
v_cvt_pk_bf16_f32 v103, v118, v119
v_exp_f32 v120, v48
ds_read_b128 a[228:231], v2 offset:50176
v_mfma_f32_32x32x16_bf16 v[224:239], a[232:235], v[104:107], v[224:239]
v_mfma_f32_16x16x32_bf16 v[24:27], v[28:31], v[100:103], v[24:27]
v_exp_f32 v121, v49
ds_read_b128 a[232:235], v2 offset:51200
v_mfma_f32_32x32x16_bf16 v[240:255], a[236:239], v[104:107], v[240:255]
v_cvt_pk_bf16_f32 v104, v120, v121
v_exp_f32 v122, v50
ds_read_b128 a[236:239], v2 offset:52224
v_mfma_f32_32x32x16_bf16 v[192:207], a[240:243], v[108:111], v[192:207]
v_exp_f32 v123, v51
v_cmp_ngt_f32 vcc, s71, v24
s_cmp_ge_u32 s24, s42
ds_read_b128 a[240:243], v2 offset:53248
v_mfma_f32_32x32x16_bf16 v[208:223], a[244:247], v[108:111], v[208:223]
v_cvt_pk_bf16_f32 v105, v122, v123
v_exp_f32 v124, v52
s_cbranch_vccnz .Lr141
.Lr141b:
s_cbranch_scc1 .Ls141
.Ls141b:
v_mfma_f32_32x32x16_bf16 v[224:239], a[248:251], v[108:111], v[224:239]
v_exp_f32 v125, v53
v_add_f32 v10, v10, v24
ds_read_b128 a[244:247], v2 offset:54272
ds_read_b128 a[248:251], v2 offset:55296
v_mfma_f32_32x32x16_bf16 v[240:255], a[252:255], v[108:111], v[240:255]
v_cvt_pk_bf16_f32 v106, v124, v125
v_exp_f32 v126, v54
ds_read_b128 a[252:255], v2 offset:56320
s_waitcnt lgkmcnt(8)
v_mfma_f32_32x32x16_bf16 v[32:47], a[192:195], a[128:131], v[64:79]
s_add_u32 m0, s36, 16384
v_exp_f32 v127, v55
buffer_load_dwordx4 v1, s[20:23], s24 offen offset:0 lds
v_mfma_f32_32x32x16_bf16 v[32:47], a[196:199], a[132:135], v[32:47]
v_cvt_pk_bf16_f32 v107, v126, v127
v_exp_f32 v120, v56
v_mfma_f32_32x32x16_bf16 v[32:47], a[200:203], a[136:139], v[32:47]
v_exp_f32 v121, v57
v_mfma_f32_16x16x32_bf16 v[24:27], v[28:31], v[104:107], 0
v_mfma_f32_32x32x16_bf16 v[32:47], a[204:207], a[140:143], v[32:47]
v_cvt_pk_bf16_f32 v108, v120, v121
v_exp_f32 v122, v58
buffer_load_dwordx4 v1, s[20:23], s24 offen offset:1024 lds
v_mfma_f32_32x32x16_bf16 v[32:47], a[208:211], a[144:147], v[32:47]
v_exp_f32 v123, v59
v_mfma_f32_32x32x16_bf16 v[32:47], a[212:215], a[148:151], v[32:47]
v_cvt_pk_bf16_f32 v109, v122, v123
v_exp_f32 v124, v60
buffer_load_dwordx4 v1, s[20:23], s24 offen offset:2048 lds
v_mfma_f32_32x32x16_bf16 v[32:47], a[216:219], a[152:155], v[32:47]
v_exp_f32 v125, v61
buffer_load_dwordx4 v1, s[20:23], s24 offen offset:3072 lds
s_add_u32 m0, s36, 40960
v_mfma_f32_32x32x16_bf16 v[32:47], a[220:223], a[156:159], v[32:47]
v_cvt_pk_bf16_f32 v110, v124, v125
v_exp_f32 v126, v62
s_waitcnt lgkmcnt(0)
v_mfma_f32_32x32x16_bf16 v[128:143], a[224:227], v[96:99], v[128:143]
v_exp_f32 v127, v63
buffer_load_dwordx4 v3, s[20:23], s24 offen offset:0 lds
buffer_store_dwordx4 a[32:35], v1, s[16:19], s28 offen offset:0 sc1
v_mfma_f32_16x16x32_bf16 a[60:63], a[88:91], a[32:35], 0
v_mfma_f32_32x32x16_bf16 v[144:159], a[228:231], v[96:99], v[144:159]
v_cvt_pk_bf16_f32 v111, v126, v127
v_exp_f32 v112, v32
buffer_store_dwordx4 a[36:39], v1, s[16:19], s28 offen offset:1024 sc1
v_mfma_f32_16x16x32_bf16 a[64:67], a[88:91], a[36:39], 0
v_mfma_f32_32x32x16_bf16 v[160:175], a[232:235], v[96:99], v[160:175]
v_exp_f32 v113, v33
v_mfma_f32_16x16x32_bf16 v[24:27], v[28:31], v[108:111], v[24:27]
buffer_load_dwordx4 v3, s[20:23], s24 offen offset:1024 lds
buffer_store_dwordx4 a[40:43], v1, s[16:19], s28 offen offset:2048 sc1
v_mfma_f32_32x32x16_bf16 v[176:191], a[236:239], v[96:99], v[176:191]
v_cvt_pk_bf16_f32 v96, v112, v113
v_exp_f32 v114, v34
buffer_load_dwordx4 v3, s[20:23], s24 offen offset:2048 lds
v_mfma_f32_16x16x32_bf16 a[68:71], a[88:91], a[40:43], 0
v_mfma_f32_32x32x16_bf16 v[128:143], a[240:243], v[100:103], v[128:143]
v_exp_f32 v115, v35
v_cmp_ngt_f32 vcc, s71, v24
s_cmp_ge_u32 s24, s43
v_mfma_f32_32x32x16_bf16 v[144:159], a[244:247], v[100:103], v[144:159]
v_cvt_pk_bf16_f32 v97, v114, v115
v_exp_f32 v116, v36
s_cbranch_vccnz .Lr144
.Lr144b:
s_cbranch_scc1 .Ls144
.Ls144b:
v_mfma_f32_32x32x16_bf16 v[160:175], a[248:251], v[100:103], v[160:175]
v_exp_f32 v117, v37
v_add_f32 v11, v11, v24
buffer_load_dwordx4 v3, s[20:23], s24 offen offset:3072 lds
buffer_store_dwordx4 a[44:47], v1, s[16:19], s28 offen offset:3072 sc1
v_mfma_f32_32x32x16_bf16 v[176:191], a[252:255], v[100:103], v[176:191]
v_cvt_pk_bf16_f32 v98, v116, v117
v_exp_f32 v118, v38
s_waitcnt vmcnt(20)
s_barrier
s_add_u32 s24, s24, 0x2000
v_mfma_f32_32x32x16_bf16 v[48:63], a[192:195], a[160:163], v[80:95]
v_exp_f32 v119, v39
ds_read_b128 a[192:195], v2 offset:0
v_mfma_f32_16x16x32_bf16 a[72:75], a[88:91], a[44:47], 0
buffer_store_dwordx4 a[48:51], v1, s[16:19], s37 offen offset:0 sc1
v_mfma_f32_32x32x16_bf16 v[48:63], a[196:199], a[164:167], v[48:63]
v_cvt_pk_bf16_f32 v99, v118, v119
v_exp_f32 v112, v40
ds_read_b128 a[196:199], v2 offset:1024
v_mfma_f32_16x16x32_bf16 a[76:79], a[88:91], a[48:51], 0
v_mfma_f32_32x32x16_bf16 v[48:63], a[200:203], a[168:171], v[48:63]
v_exp_f32 v113, v41
v_mfma_f32_16x16x32_bf16 v[24:27], v[28:31], v[96:99], 0
ds_read_b128 a[200:203], v2 offset:2048
buffer_store_dwordx4 a[52:55], v1, s[16:19], s37 offen offset:1024 sc1
v_mfma_f32_32x32x16_bf16 v[48:63], a[204:207], a[172:175], v[48:63]
v_cvt_pk_bf16_f32 v100, v112, v113
v_exp_f32 v114, v42
ds_read_b128 a[204:207], v2 offset:3072
v_mfma_f32_16x16x32_bf16 a[80:83], a[88:91], a[52:55], 0
v_mfma_f32_32x32x16_bf16 v[48:63], a[208:211], a[176:179], v[48:63]
v_exp_f32 v115, v43
ds_read_b128 a[208:211], v2 offset:4096
buffer_store_dwordx4 a[56:59], v1, s[16:19], s37 offen offset:2048 sc1
v_mfma_f32_16x16x32_bf16 a[84:87], a[88:91], a[56:59], 0
v_mfma_f32_32x32x16_bf16 v[48:63], a[212:215], a[180:183], v[48:63]
v_cvt_pk_bf16_f32 v101, v114, v115
v_exp_f32 v116, v44
ds_read_b128 a[212:215], v2 offset:5120
v_mfma_f32_32x32x16_bf16 v[48:63], a[216:219], a[184:187], v[48:63]
v_exp_f32 v117, v45
ds_read_b128 a[216:219], v2 offset:6144
v_mfma_f32_32x32x16_bf16 v[48:63], a[220:223], a[188:191], v[48:63]
v_cvt_pk_bf16_f32 v102, v116, v117
v_exp_f32 v118, v46
ds_read_b128 a[220:223], v2 offset:7168
v_mfma_f32_32x32x16_bf16 v[192:207], a[224:227], v[104:107], v[192:207]
v_exp_f32 v119, v47
ds_read_b128 a[224:227], v2 offset:57344
v_mfma_f32_32x32x16_bf16 v[208:223], a[228:231], v[104:107], v[208:223]
v_cvt_pk_bf16_f32 v103, v118, v119
v_exp_f32 v120, v48
ds_read_b128 a[228:231], v2 offset:58368
v_mfma_f32_32x32x16_bf16 v[224:239], a[232:235], v[104:107], v[224:239]
v_mfma_f32_16x16x32_bf16 v[24:27], v[28:31], v[100:103], v[24:27]
v_exp_f32 v121, v49
ds_read_b128 a[232:235], v2 offset:59392
v_mfma_f32_32x32x16_bf16 v[240:255], a[236:239], v[104:107], v[240:255]
v_cvt_pk_bf16_f32 v104, v120, v121
v_exp_f32 v122, v50
ds_read_b128 a[236:239], v2 offset:60416
v_mfma_f32_32x32x16_bf16 v[192:207], a[240:243], v[108:111], v[192:207]
v_exp_f32 v123, v51
v_cmp_ngt_f32 vcc, s71, v24
s_cmp_ge_u32 s24, s42
ds_read_b128 a[240:243], v2 offset:61440
v_mfma_f32_32x32x16_bf16 v[208:223], a[244:247], v[108:111], v[208:223]
v_cvt_pk_bf16_f32 v105, v122, v123
v_exp_f32 v124, v52
s_cbranch_vccnz .Lr143
.Lr143b:
s_cbranch_scc1 .Ls143
.Ls143b:
v_mfma_f32_32x32x16_bf16 v[224:239], a[248:251], v[108:111], v[224:239]
v_exp_f32 v125, v53
v_add_f32 v10, v10, v24
ds_read_b128 a[244:247], v2 offset:62464
ds_read_b128 a[248:251], v2 offset:63488
v_mfma_f32_32x32x16_bf16 v[240:255], a[252:255], v[108:111], v[240:255]
v_cvt_pk_bf16_f32 v106, v124, v125
v_exp_f32 v126, v54
ds_read_b128 a[252:255], v2 offset:64512
s_waitcnt lgkmcnt(8)
s_cmp_lt_u32 s24, s27
s_cselect_b32 s22, s52, 0
v_mfma_f32_32x32x16_bf16 v[32:47], a[192:195], a[128:131], v[64:79]
s_add_u32 m0, s36, 24576
v_exp_f32 v127, v55
buffer_load_dwordx4 v1, s[20:23], s24 offen offset:0 lds
buffer_store_dword a60, v13, s[16:19], s26 offen offset:0 sc1
v_mfma_f32_32x32x16_bf16 v[32:47], a[196:199], a[132:135], v[32:47]
v_cvt_pk_bf16_f32 v107, v126, v127
v_exp_f32 v120, v56
buffer_store_dword a64, v13, s[16:19], s26 offen offset:1024 sc1
buffer_store_dword a68, v13, s[16:19], s26 offen offset:2048 sc1
v_mfma_f32_32x32x16_bf16 v[32:47], a[200:203], a[136:139], v[32:47]
v_exp_f32 v121, v57
v_mfma_f32_16x16x32_bf16 v[24:27], v[28:31], v[104:107], 0
v_mfma_f32_32x32x16_bf16 v[32:47], a[204:207], a[140:143], v[32:47]
v_cvt_pk_bf16_f32 v108, v120, v121
v_exp_f32 v122, v58
buffer_load_dwordx4 v1, s[20:23], s24 offen offset:1024 lds
buffer_store_dword a72, v13, s[16:19], s26 offen offset:3072 sc1
v_mfma_f32_32x32x16_bf16 v[32:47], a[208:211], a[144:147], v[32:47]
v_exp_f32 v123, v59
buffer_store_dword a76, v13, s[16:19], s38 offen offset:0 sc1
buffer_store_dword a80, v13, s[16:19], s38 offen offset:1024 sc1
buffer_store_dword a84, v13, s[16:19], s38 offen offset:2048 sc1
v_mfma_f32_32x32x16_bf16 v[32:47], a[212:215], a[148:151], v[32:47]
v_cvt_pk_bf16_f32 v109, v122, v123
v_exp_f32 v124, v60
buffer_load_dwordx4 v1, s[20:23], s24 offen offset:2048 lds
v_mfma_f32_32x32x16_bf16 v[32:47], a[216:219], a[152:155], v[32:47]
v_exp_f32 v125, v61
buffer_load_dwordx4 v1, s[20:23], s24 offen offset:3072 lds
s_add_u32 m0, s36, 49152
v_mfma_f32_32x32x16_bf16 v[32:47], a[220:223], a[156:159], v[32:47]
v_cvt_pk_bf16_f32 v110, v124, v125
v_exp_f32 v126, v62
s_waitcnt lgkmcnt(0)
v_mfma_f32_32x32x16_bf16 v[128:143], a[224:227], v[96:99], v[128:143]
v_exp_f32 v127, v63
buffer_load_dwordx4 v3, s[20:23], s24 offen offset:0 lds
v_mfma_f32_32x32x16_bf16 v[144:159], a[228:231], v[96:99], v[144:159]
v_cvt_pk_bf16_f32 v111, v126, v127
v_exp_f32 v112, v32
v_mfma_f32_32x32x16_bf16 v[160:175], a[232:235], v[96:99], v[160:175]
v_exp_f32 v113, v33
v_mfma_f32_16x16x32_bf16 v[24:27], v[28:31], v[108:111], v[24:27]
buffer_load_dwordx4 v3, s[20:23], s24 offen offset:1024 lds
v_mfma_f32_32x32x16_bf16 v[176:191], a[236:239], v[96:99], v[176:191]
v_cvt_pk_bf16_f32 v96, v112, v113
v_exp_f32 v114, v34
buffer_load_dwordx4 v3, s[20:23], s24 offen offset:2048 lds
v_mfma_f32_32x32x16_bf16 v[128:143], a[240:243], v[100:103], v[128:143]
v_exp_f32 v115, v35
v_cmp_ngt_f32 vcc, s71, v24
s_cmp_ge_u32 s24, s43
v_mfma_f32_32x32x16_bf16 v[144:159], a[244:247], v[100:103], v[144:159]
v_cvt_pk_bf16_f32 v97, v114, v115
v_exp_f32 v116, v36
s_cbranch_vccnz .Lr146
.Lr146b:
s_cbranch_scc1 .Ls146
.Ls146b:
v_mfma_f32_32x32x16_bf16 v[160:175], a[248:251], v[100:103], v[160:175]
v_exp_f32 v117, v37
v_add_f32 v11, v11, v24
buffer_load_dwordx4 v3, s[20:23], s24 offen offset:3072 lds
v_mfma_f32_32x32x16_bf16 v[176:191], a[252:255], v[100:103], v[176:191]
v_cvt_pk_bf16_f32 v98, v116, v117
v_exp_f32 v118, v38
s_waitcnt vmcnt(30)
s_barrier
s_add_u32 s24, s24, 0x2000
v_mfma_f32_32x32x16_bf16 v[48:63], a[192:195], a[160:163], v[80:95]
v_exp_f32 v119, v39
ds_read_b128 a[192:195], v2 offset:8192
v_mfma_f32_32x32x16_bf16 v[48:63], a[196:199], a[164:167], v[48:63]
v_cvt_pk_bf16_f32 v99, v118, v119
v_exp_f32 v112, v40
ds_read_b128 a[196:199], v2 offset:9216
v_mfma_f32_32x32x16_bf16 v[48:63], a[200:203], a[168:171], v[48:63]
v_exp_f32 v113, v41
v_mfma_f32_16x16x32_bf16 v[24:27], v[28:31], v[96:99], 0
ds_read_b128 a[200:203], v2 offset:10240
v_mfma_f32_32x32x16_bf16 v[48:63], a[204:207], a[172:175], v[48:63]
v_cvt_pk_bf16_f32 v100, v112, v113
v_exp_f32 v114, v42
ds_read_b128 a[204:207], v2 offset:11264
v_mfma_f32_32x32x16_bf16 v[48:63], a[208:211], a[176:179], v[48:63]
v_exp_f32 v115, v43
ds_read_b128 a[208:211], v2 offset:12288
v_mfma_f32_32x32x16_bf16 v[48:63], a[212:215], a[180:183], v[48:63]
v_cvt_pk_bf16_f32 v101, v114, v115
v_exp_f32 v116, v44
ds_read_b128 a[212:215], v2 offset:13312
v_mfma_f32_32x32x16_bf16 v[48:63], a[216:219], a[184:187], v[48:63]
v_exp_f32 v117, v45
ds_read_b128 a[216:219], v2 offset:14336
v_mfma_f32_32x32x16_bf16 v[48:63], a[220:223], a[188:191], v[48:63]
v_cvt_pk_bf16_f32 v102, v116, v117
v_exp_f32 v118, v46
ds_read_b128 a[220:223], v2 offset:15360
v_mfma_f32_32x32x16_bf16 v[192:207], a[224:227], v[104:107], v[192:207]
v_exp_f32 v119, v47
ds_read_b128 a[224:227], v2 offset:32768
v_mfma_f32_32x32x16_bf16 v[208:223], a[228:231], v[104:107], v[208:223]
v_cvt_pk_bf16_f32 v103, v118, v119
v_exp_f32 v120, v48
ds_read_b128 a[228:231], v2 offset:33792
v_mfma_f32_32x32x16_bf16 v[224:239], a[232:235], v[104:107], v[224:239]
v_mfma_f32_16x16x32_bf16 v[24:27], v[28:31], v[100:103], v[24:27]
v_exp_f32 v121, v49
ds_read_b128 a[232:235], v2 offset:34816
v_mfma_f32_32x32x16_bf16 v[240:255], a[236:239], v[104:107], v[240:255]
v_cvt_pk_bf16_f32 v104, v120, v121
v_exp_f32 v122, v50
ds_read_b128 a[236:239], v2 offset:35840
v_mfma_f32_32x32x16_bf16 v[192:207], a[240:243], v[108:111], v[192:207]
v_exp_f32 v123, v51
v_cmp_ngt_f32 vcc, s71, v24
s_cmp_ge_u32 s24, s42
ds_read_b128 a[240:243], v2 offset:36864
v_mfma_f32_32x32x16_bf16 v[208:223], a[244:247], v[108:111], v[208:223]
v_cvt_pk_bf16_f32 v105, v122, v123
v_exp_f32 v124, v52
s_cbranch_vccnz .Lr145
.Lr145b:
s_cbranch_scc1 .Ls145
.Ls145b:
v_mfma_f32_32x32x16_bf16 v[224:239], a[248:251], v[108:111], v[224:239]
v_exp_f32 v125, v53
v_add_f32 v10, v10, v24
ds_read_b128 a[244:247], v2 offset:37888
ds_read_b128 a[248:251], v2 offset:38912
v_mfma_f32_32x32x16_bf16 v[240:255], a[252:255], v[108:111], v[240:255]
v_cvt_pk_bf16_f32 v106, v124, v125
v_exp_f32 v126, v54
ds_read_b128 a[252:255], v2 offset:39936
s_waitcnt lgkmcnt(8)
v_mfma_f32_32x32x16_bf16 v[32:47], a[192:195], a[128:131], v[64:79]
s_add_u32 m0, s36, 0
v_exp_f32 v127, v55
buffer_load_dwordx4 v1, s[20:23], s24 offen offset:0 lds
s_and_b32 s74, %2, 7
v_mfma_f32_32x32x16_bf16 v[32:47], a[196:199], a[132:135], v[32:47]
v_cvt_pk_bf16_f32 v107, v126, v127
v_exp_f32 v120, v56
s_lshl_b32 s74, s74, 2
s_bfe_u32 s75, %2, 0x20003
v_mfma_f32_32x32x16_bf16 v[32:47], a[200:203], a[136:139], v[32:47]
v_exp_f32 v121, v57
v_mfma_f32_16x16x32_bf16 v[24:27], v[28:31], v[104:107], 0
v_mfma_f32_32x32x16_bf16 v[32:47], a[204:207], a[140:143], v[32:47]
v_cvt_pk_bf16_f32 v108, v120, v121
v_exp_f32 v122, v58
buffer_load_dwordx4 v1, s[20:23], s24 offen offset:1024 lds
s_add_u32 s74, s74, s75
v_mfma_f32_32x32x16_bf16 v[32:47], a[208:211], a[144:147], v[32:47]
v_exp_f32 v123, v59
s_lshl_b32 s74, s74, 1
s_add_u32 s74, s74, 1
s_lshr_b32 s75, %2, 5
v_mfma_f32_32x32x16_bf16 v[32:47], a[212:215], a[148:151], v[32:47]
v_cvt_pk_bf16_f32 v109, v122, v123
v_exp_f32 v124, v60
buffer_load_dwordx4 v1, s[20:23], s24 offen offset:2048 lds
s_lshl_b32 s76, s74, 3
v_mfma_f32_32x32x16_bf16 v[32:47], a[216:219], a[152:155], v[32:47]
v_exp_f32 v125, v61
buffer_load_dwordx4 v1, s[20:23], s24 offen offset:3072 lds
s_add_u32 m0, s36, 57344
s_add_u32 s76, s76, s75
v_mfma_f32_32x32x16_bf16 v[32:47], a[220:223], a[156:159], v[32:47]
v_cvt_pk_bf16_f32 v110, v124, v125
v_exp_f32 v126, v62
s_waitcnt lgkmcnt(0)
s_lshl_b32 s76, s76, 12
v_mfma_f32_32x32x16_bf16 v[128:143], a[224:227], v[96:99], v[128:143]
v_exp_f32 v127, v63
buffer_load_dwordx4 v3, s[20:23], s24 offen offset:0 lds
s_add_u32 s76, s76, 0x4000000
buffer_store_dwordx4 a[92:95], v1, s[16:19], s76 offen offset:0 sc1
v_mfma_f32_32x32x16_bf16 v[144:159], a[228:231], v[96:99], v[144:159]
v_cvt_pk_bf16_f32 v111, v126, v127
v_exp_f32 v112, v32
buffer_store_dwordx4 a[92:95], v1, s[16:19], s76 offen offset:1024 sc1
buffer_store_dwordx4 a[92:95], v1, s[16:19], s76 offen offset:2048 sc1
v_mfma_f32_32x32x16_bf16 v[160:175], a[232:235], v[96:99], v[160:175]
v_exp_f32 v113, v33
v_mfma_f32_16x16x32_bf16 v[24:27], v[28:31], v[108:111], v[24:27]
buffer_load_dwordx4 v3, s[20:23], s24 offen offset:1024 lds
v_mfma_f32_32x32x16_bf16 v[176:191], a[236:239], v[96:99], v[176:191]
v_cvt_pk_bf16_f32 v96, v112, v113
v_exp_f32 v114, v34
buffer_load_dwordx4 v3, s[20:23], s24 offen offset:2048 lds
buffer_store_dwordx4 a[92:95], v1, s[16:19], s76 offen offset:3072 sc1
v_mfma_f32_32x32x16_bf16 v[128:143], a[240:243], v[100:103], v[128:143]
v_exp_f32 v115, v35
v_cmp_ngt_f32 vcc, s71, v24
s_cmp_ge_u32 s24, s43
v_mfma_f32_32x32x16_bf16 v[144:159], a[244:247], v[100:103], v[144:159]
v_cvt_pk_bf16_f32 v97, v114, v115
v_exp_f32 v116, v36
s_cbranch_vccnz .Lr148
.Lr148b:
s_cbranch_scc1 .Ls148
.Ls148b:
v_mfma_f32_32x32x16_bf16 v[160:175], a[248:251], v[100:103], v[160:175]
v_exp_f32 v117, v37
v_add_f32 v11, v11, v24
buffer_load_dwordx4 v3, s[20:23], s24 offen offset:3072 lds
s_lshl_b32 s74, s74, 4
v_mfma_f32_32x32x16_bf16 v[176:191], a[252:255], v[100:103], v[176:191]
v_cvt_pk_bf16_f32 v98, v116, v117
v_exp_f32 v118, v38
s_waitcnt vmcnt(30)
s_barrier
s_add_u32 s24, s24, 0x2000
v_mfma_f32_32x32x16_bf16 v[48:63], a[192:195], a[160:163], v[80:95]
v_exp_f32 v119, v39
ds_read_b128 a[192:195], v2 offset:16384
s_add_u32 s74, s74, s75
s_lshl_b32 s74, s74, 8
v_mfma_f32_32x32x16_bf16 v[48:63], a[196:199], a[164:167], v[48:63]
v_cvt_pk_bf16_f32 v99, v118, v119
v_exp_f32 v112, v40
ds_read_b128 a[196:199], v2 offset:17408
s_add_u32 s74, s74, 0x5500000
v_mfma_f32_32x32x16_bf16 v[48:63], a[200:203], a[168:171], v[48:63]
v_exp_f32 v113, v41
v_mfma_f32_16x16x32_bf16 v[24:27], v[28:31], v[96:99], 0
ds_read_b128 a[200:203], v2 offset:18432
v_mfma_f32_32x32x16_bf16 v[48:63], a[204:207], a[172:175], v[48:63]
v_cvt_pk_bf16_f32 v100, v112, v113
v_exp_f32 v114, v42
ds_read_b128 a[204:207], v2 offset:19456
v_lshrrev_b32 v7, 2, v1
v_mfma_f32_32x32x16_bf16 v[48:63], a[208:211], a[176:179], v[48:63]
v_exp_f32 v115, v43
ds_read_b128 a[208:211], v2 offset:20480
s_nop 0
buffer_store_dword a92, v7, s[16:19], s74 offen offset:0 sc1
v_mfma_f32_32x32x16_bf16 v[48:63], a[212:215], a[180:183], v[48:63]
v_cvt_pk_bf16_f32 v101, v114, v115
v_exp_f32 v116, v44
ds_read_b128 a[212:215], v2 offset:21504
buffer_store_dword a92, v7, s[16:19], s74 offen offset:2048 sc1
v_mfma_f32_32x32x16_bf16 v[48:63], a[216:219], a[184:187], v[48:63]
v_exp_f32 v117, v45
ds_read_b128 a[216:219], v2 offset:22528
v_mfma_f32_32x32x16_bf16 v[48:63], a[220:223], a[188:191], v[48:63]
v_cvt_pk_bf16_f32 v102, v116, v117
v_exp_f32 v118, v46
ds_read_b128 a[220:223], v2 offset:23552
v_mfma_f32_32x32x16_bf16 v[192:207], a[224:227], v[104:107], v[192:207]
v_exp_f32 v119, v47
ds_read_b128 a[224:227], v2 offset:40960
v_mfma_f32_32x32x16_bf16 v[208:223], a[228:231], v[104:107], v[208:223]
v_cvt_pk_bf16_f32 v103, v118, v119
v_exp_f32 v120, v48
ds_read_b128 a[228:231], v2 offset:41984
v_mfma_f32_32x32x16_bf16 v[224:239], a[232:235], v[104:107], v[224:239]
v_mfma_f32_16x16x32_bf16 v[24:27], v[28:31], v[100:103], v[24:27]
v_exp_f32 v121, v49
ds_read_b128 a[232:235], v2 offset:43008
v_mfma_f32_32x32x16_bf16 v[240:255], a[236:239], v[104:107], v[240:255]
v_cvt_pk_bf16_f32 v104, v120, v121
v_exp_f32 v122, v50
ds_read_b128 a[236:239], v2 offset:44032
v_mfma_f32_32x32x16_bf16 v[192:207], a[240:243], v[108:111], v[192:207]
v_exp_f32 v123, v51
v_cmp_ngt_f32 vcc, s71, v24
s_cmp_ge_u32 s24, s42
ds_read_b128 a[240:243], v2 offset:45056
v_mfma_f32_32x32x16_bf16 v[208:223], a[244:247], v[108:111], v[208:223]
v_cvt_pk_bf16_f32 v105, v122, v123
v_exp_f32 v124, v52
s_cbranch_vccnz .Lr147
.Lr147b:
s_cbranch_scc1 .Ls147
.Ls147b:
v_mfma_f32_32x32x16_bf16 v[224:239], a[248:251], v[108:111], v[224:239]
v_exp_f32 v125, v53
v_add_f32 v10, v10, v24
ds_read_b128 a[244:247], v2 offset:46080
ds_read_b128 a[248:251], v2 offset:47104
v_mfma_f32_32x32x16_bf16 v[240:255], a[252:255], v[108:111], v[240:255]
v_cvt_pk_bf16_f32 v106, v124, v125
v_exp_f32 v126, v54
ds_read_b128 a[252:255], v2 offset:48128
s_waitcnt lgkmcnt(8)
s_cmp_ge_u32 s48, 20
s_cbranch_scc1 .Lx3_2
s_branch .LdV
.LdA:
v_exp_f32 v127, v55
v_exp_f32 v120, v56
v_cvt_pk_bf16_f32 v107, v126, v127
s_and_b32 s47, s51, 1
s_lshl_b32 s72, s47, 14
s_add_u32 s72, s72, 131072
v_add_u32 v22, s72, v1
s_lshl_b32 s72, s47, 16
s_add_u32 s72, s72, 8192
s_waitcnt lgkmcnt(0)
v_mfma_f32_32x32x16_bf16 a[0:15], a[224:227], v[96:99], a[0:15]
v_exp_f32 v121, v57
v_mfma_f32_16x16x32_bf16 v[24:27], v[28:31], v[104:107], 0
v_exp_f32 v122, v58
v_cvt_pk_bf16_f32 v108, v120, v121
v_mfma_f32_32x32x16_bf16 a[16:31], a[228:231], v[96:99], a[16:31]
v_exp_f32 v123, v59
v_exp_f32 v124, v60
v_cvt_pk_bf16_f32 v109, v122, v123
v_mfma_f32_32x32x16_bf16 a[32:47], a[232:235], v[96:99], a[32:47]
v_exp_f32 v125, v61
v_exp_f32 v126, v62
v_cvt_pk_bf16_f32 v110, v124, v125
v_mfma_f32_32x32x16_bf16 a[48:63], a[236:239], v[96:99], a[48:63]
v_exp_f32 v127, v63
v_add_u32 v23, s72, v1
v_cvt_pk_bf16_f32 v111, v126, v127
v_mfma_f32_32x32x16_bf16 a[0:15], a[240:243], v[100:103], a[0:15]
s_lshl_b32 s72, s51, 10
v_mfma_f32_16x16x32_bf16 v[24:27], v[28:31], v[108:111], v[24:27]
s_add_u32 s72, s72, 32768
v_lshrrev_b32 v21, 1, v1
v_add_u32 v21, s72, v21
v_mfma_f32_32x32x16_bf16 a[16:31], a[244:247], v[100:103], a[16:31]
s_lshl_b32 s47, s47, 1
s_sub_u32 s29, 64, s48
s_add_u32 s29, s29, s47
s_lshl_b32 s29, s29, 18
s_lshl_b32 s72, s49, 13
s_add_u32 s29, s29, s72
s_add_u32 s29, s29, 0x3000000
s_add_u32 s72, s29, 4096
v_mfma_f32_32x32x16_bf16 a[32:47], a[248:251], v[100:103], a[32:47]
v_cmp_ngt_f32 vcc, s71, v24
v_mfma_f32_32x32x16_bf16 a[48:63], a[252:255], v[100:103], a[48:63]
s_cbranch_vccnz .Lr4
.Lr4b:
v_add_f32 v9, v9, v24
v_accvgpr_read_b32 v128, a0
v_accvgpr_read_b32 v129, a1
v_accvgpr_read_b32 v130, a2
v_accvgpr_read_b32 v131, a3
v_mfma_f32_32x32x16_bf16 a[64:79], a[224:227], v[104:107], a[64:79]
v_mov_b32 v18, v13
v_mov_b32 v19, v9
ds_write_b64 v21, v[18:19]
v_accvgpr_read_b32 v132, a4
v_accvgpr_read_b32 v133, a5
v_accvgpr_read_b32 v134, a6
v_accvgpr_read_b32 v135, a7
v_accvgpr_read_b32 v136, a8
v_accvgpr_read_b32 v137, a9
v_accvgpr_read_b32 v138, a10
v_mfma_f32_32x32x16_bf16 a[80:95], a[228:231], v[104:107], a[80:95]
v_accvgpr_read_b32 v139, a11
v_accvgpr_read_b32 v140, a12
v_accvgpr_read_b32 v141, a13
v_accvgpr_read_b32 v142, a14
v_accvgpr_read_b32 v143, a15
v_accvgpr_read_b32 v144, a16
v_accvgpr_read_b32 v145, a17
v_mfma_f32_32x32x16_bf16 a[96:111], a[232:235], v[104:107], a[96:111]
v_accvgpr_read_b32 v146, a18
v_accvgpr_read_b32 v147, a19
v_accvgpr_read_b32 v148, a20
v_accvgpr_read_b32 v149, a21
v_accvgpr_read_b32 v150, a22
v_accvgpr_read_b32 v151, a23
v_accvgpr_read_b32 v152, a24
v_mfma_f32_32x32x16_bf16 a[112:127], a[236:239], v[104:107], a[112:127]
v_accvgpr_read_b32 v153, a25
v_accvgpr_read_b32 v154, a26
v_accvgpr_read_b32 v155, a27
v_accvgpr_read_b32 v156, a28
v_accvgpr_read_b32 v157, a29
v_accvgpr_read_b32 v158, a30
v_accvgpr_read_b32 v159, a31
v_mfma_f32_32x32x16_bf16 a[64:79], a[240:243], v[108:111], a[64:79]
v_accvgpr_read_b32 v160, a32
v_accvgpr_read_b32 v161, a33
v_accvgpr_read_b32 v162, a34
v_accvgpr_read_b32 v163, a35
v_accvgpr_read_b32 v164, a36
v_accvgpr_read_b32 v165, a37
v_accvgpr_read_b32 v166, a38
v_mfma_f32_32x32x16_bf16 a[80:95], a[244:247], v[108:111], a[80:95]
v_accvgpr_read_b32 v167, a39
v_accvgpr_read_b32 v168, a40
v_accvgpr_read_b32 v169, a41
v_accvgpr_read_b32 v170, a42
v_accvgpr_read_b32 v171, a43
v_accvgpr_read_b32 v172, a44
v_accvgpr_read_b32 v173, a45
v_mfma_f32_32x32x16_bf16 a[96:111], a[248:251], v[108:111], a[96:111]
v_accvgpr_read_b32 v174, a46
v_accvgpr_read_b32 v175, a47
v_accvgpr_read_b32 v176, a48
ds_write_b128 v23, a[64:67] offset:0
ds_write_b128 v23, a[68:71] offset:1024
ds_write_b128 v23, a[72:75] offset:2048
ds_write_b128 v23, a[76:79] offset:3072
v_mfma_f32_32x32x16_bf16 a[112:127], a[252:255], v[108:111], a[112:127]
v_accvgpr_read_b32 v177, a49
v_accvgpr_read_b32 v178, a50
v_accvgpr_read_b32 v179, a51
ds_write_b128 v23, a[80:83] offset:4096
ds_write_b128 v23, a[84:87] offset:5120
ds_write_b128 v23, a[88:91] offset:6144
ds_write_b128 v23, a[92:95] offset:7168
v_accvgpr_read_b32 v180, a52
v_accvgpr_read_b32 v181, a53
v_accvgpr_read_b32 v182, a54
v_accvgpr_read_b32 v183, a55
ds_write_b128 v23, a[96:99] offset:8192
ds_write_b128 v23, a[100:103] offset:9216
ds_write_b128 v23, a[104:107] offset:10240
ds_write_b128 v23, a[108:111] offset:11264
v_accvgpr_read_b32 v184, a56
v_accvgpr_read_b32 v185, a57
v_accvgpr_read_b32 v186, a58
v_accvgpr_read_b32 v187, a59
ds_write_b128 v23, a[112:115] offset:12288
ds_write_b128 v23, a[116:119] offset:13312
ds_write_b128 v23, a[120:123] offset:14336
ds_write_b128 v23, a[124:127] offset:15360
v_accvgpr_read_b32 v188, a60
v_accvgpr_read_b32 v189, a61
v_accvgpr_read_b32 v190, a62
v_accvgpr_read_b32 v191, a63
s_waitcnt lgkmcnt(0)
s_barrier
.LfA:
v_xor_b32 v21, 2048, v21
ds_read_b64 v[18:19], v21
ds_read_b128 v[192:195], v22 offset:0
ds_read_b128 v[196:199], v22 offset:1024
ds_read_b128 v[200:203], v22 offset:2048
ds_read_b128 v[204:207], v22 offset:3072
ds_read_b128 v[208:211], v22 offset:4096
ds_read_b128 v[212:215], v22 offset:5120
ds_read_b128 v[216:219], v22 offset:6144
ds_read_b128 v[220:223], v22 offset:7168
ds_read_b128 v[224:227], v22 offset:8192
ds_read_b128 v[228:231], v22 offset:9216
ds_read_b128 v[232:235], v22 offset:10240
ds_read_b128 v[236:239], v22 offset:11264
ds_read_b128 v[240:243], v22 offset:12288
ds_read_b128 v[244:247], v22 offset:13312
ds_read_b128 v[248:251], v22 offset:14336
ds_read_b128 v[252:255], v22 offset:15360
s_mov_b32 s28, s51
s_mov_b32 s47, 32768
s_lshr_b32 s37, %2, 5
s_and_b32 s30, s37, 7
s_lshl_b32 s38, s30, 10
s_add_u32 s38, s38, 0x6300000
s_lshl_b32 s30, s28, 8
s_add_u32 s39, s30, s38
v_lshrrev_b32 v6, 2, v1
buffer_load_dword v24, v6, s[16:19], s39 offen sc1
v_and_b32 v4, 124, v6
v_lshrrev_b32 v7, 3, v0
v_lshlrev_b32 v7, 27, v7
v_and_b32 v5, 7, v0
v_lshl_add_u32 v5, v5, 10, v7
s_lshl_b32 s46, %2, 2
s_add_u32 s46, s46, 0x6300000
v_mov_b32 v7, 3
s_and_b32 s30, %2, 7
s_lshl_b32 s30, s30, 2
s_bfe_u32 s35, %2, 0x20003
s_add_u32 s36, s30, s35
s_lshr_b32 s30, %2, 6
s_lshl_b32 s30, s30, 2
s_add_u32 s30, s30, s28
s_mul_i32 s34, s30, 262144
s_mul_i32 s45, s30, 114688
s_bfe_u32 s30, %2, 0x10005
s_lshl_b32 s30, s30, 11
s_add_u32 s34, s34, s30
s_add_u32 s34, s34, 0x5c00000
s_add_u32 s45, s45, s30
s_add_u32 s45, s45, 0x6000000
s_lshr_b32 s30, s28, 1
s_lshl_b32 s30, s30, 10
v_add_u32 v2, s30, v1
v_xor_b32 v3, 1024, v2
s_and_b32 s32, s36, 31
s_mul_i32 s32, s32, 524288
s_mul_i32 s30, s28, 65536
s_add_u32 s32, s32, s30
s_add_u32 s32, s32, 0x3000000
s_and_b32 s30, s28, 1
s_mul_i32 s30, s30, 262144
s_add_u32 s33, s32, 262144
s_sub_u32 s33, s33, s30
s_add_u32 s32, s32, s30
s_lshr_b32 s30, s36, 1
s_sub_u32 s35, 15, s30
s_min_u32 s30, s30, s35
s_lshl_b32 s30, s30, 7
s_add_u32 s31, s30, s38
s_bfe_u32 s30, %2, 0x20001
s_lshl_b32 s30, s30, 3
s_add_u32 s30, s30, s37
s_mul_i32 s30, s30, 122880
s_add_u32 s30, s30, 0x5800000
s_add_u32 s40, %0, s30
s_addc_u32 s41, %1, 0
s_and_b32 s41, s41, 0xffff
s_mov_b32 s42, 122880
s_mov_b32 s43, 0x20000
s_lshl_b32 s44, s28, 10
s_mov_b32 s35, s39
s_waitcnt lgkmcnt(15)
v_max_f32 v20, v12, v18
v_sub_f32 v12, v12, v20
v_sub_f32 v18, v18, v20
v_exp_f32 v16, v12
v_exp_f32 v17, v18
s_nop 0
v_mul_f32 v8, v8, v16
v_fmac_f32 v8, v19, v17
v_rcp_f32 v8, v8
s_nop 0
v_mul_f32 v16, v16, v8
v_mul_f32 v17, v17, v8
v_pk_mul_f32 v[128:129], v[128:129], v[16:17] op_sel:[0,0] op_sel_hi:[1,0]
v_pk_mul_f32 v[130:131], v[130:131], v[16:17] op_sel:[0,0] op_sel_hi:[1,0]
v_pk_mul_f32 v[132:133], v[132:133], v[16:17] op_sel:[0,0] op_sel_hi:[1,0]
v_pk_mul_f32 v[134:135], v[134:135], v[16:17] op_sel:[0,0] op_sel_hi:[1,0]
v_pk_mul_f32 v[136:137], v[136:137], v[16:17] op_sel:[0,0] op_sel_hi:[1,0]
v_pk_mul_f32 v[138:139], v[138:139], v[16:17] op_sel:[0,0] op_sel_hi:[1,0]
v_pk_mul_f32 v[140:141], v[140:141], v[16:17] op_sel:[0,0] op_sel_hi:[1,0]
v_pk_mul_f32 v[142:143], v[142:143], v[16:17] op_sel:[0,0] op_sel_hi:[1,0]
v_pk_fma_f32 v[128:129], v[192:193], v[16:17], v[128:129] op_sel:[0,1,0] op_sel_hi:[1,1,1]
v_pk_fma_f32 v[130:131], v[194:195], v[16:17], v[130:131] op_sel:[0,1,0] op_sel_hi:[1,1,1]
s_waitcnt lgkmcnt(14)
v_pk_fma_f32 v[132:133], v[196:197], v[16:17], v[132:133] op_sel:[0,1,0] op_sel_hi:[1,1,1]
v_pk_fma_f32 v[134:135], v[198:199], v[16:17], v[134:135] op_sel:[0,1,0] op_sel_hi:[1,1,1]
v_cvt_pk_bf16_f32 v192, v128, v129
v_cvt_pk_bf16_f32 v193, v130, v131
v_cvt_pk_bf16_f32 v194, v132, v133
v_cvt_pk_bf16_f32 v195, v134, v135
buffer_store_dwordx4 v[192:195], v1, s[16:19], s29 offen offset:0 sc1
s_waitcnt lgkmcnt(13)
v_pk_fma_f32 v[136:137], v[200:201], v[16:17], v[136:137] op_sel:[0,1,0] op_sel_hi:[1,1,1]
v_pk_fma_f32 v[138:139], v[202:203], v[16:17], v[138:139] op_sel:[0,1,0] op_sel_hi:[1,1,1]
s_waitcnt lgkmcnt(12)
v_pk_fma_f32 v[140:141], v[204:205], v[16:17], v[140:141] op_sel:[0,1,0] op_sel_hi:[1,1,1]
v_pk_fma_f32 v[142:143], v[206:207], v[16:17], v[142:143] op_sel:[0,1,0] op_sel_hi:[1,1,1]
v_cvt_pk_bf16_f32 v196, v136, v137
v_cvt_pk_bf16_f32 v197, v138, v139
v_cvt_pk_bf16_f32 v198, v140, v141
v_cvt_pk_bf16_f32 v199, v142, v143
buffer_store_dwordx4 v[196:199], v1, s[16:19], s29 offen offset:1024 sc1
v_pk_mul_f32 v[144:145], v[144:145], v[16:17] op_sel:[0,0] op_sel_hi:[1,0]
v_pk_mul_f32 v[146:147], v[146:147], v[16:17] op_sel:[0,0] op_sel_hi:[1,0]
v_pk_mul_f32 v[148:149], v[148:149], v[16:17] op_sel:[0,0] op_sel_hi:[1,0]
v_pk_mul_f32 v[150:151], v[150:151], v[16:17] op_sel:[0,0] op_sel_hi:[1,0]
v_pk_mul_f32 v[152:153], v[152:153], v[16:17] op_sel:[0,0] op_sel_hi:[1,0]
v_pk_mul_f32 v[154:155], v[154:155], v[16:17] op_sel:[0,0] op_sel_hi:[1,0]
v_pk_mul_f32 v[156:157], v[156:157], v[16:17] op_sel:[0,0] op_sel_hi:[1,0]
v_pk_mul_f32 v[158:159], v[158:159], v[16:17] op_sel:[0,0] op_sel_hi:[1,0]
s_waitcnt lgkmcnt(11)
v_pk_fma_f32 v[144:145], v[208:209], v[16:17], v[144:145] op_sel:[0,1,0] op_sel_hi:[1,1,1]
v_pk_fma_f32 v[146:147], v[210:211], v[16:17], v[146:147] op_sel:[0,1,0] op_sel_hi:[1,1,1]
s_waitcnt lgkmcnt(10)
v_pk_fma_f32 v[148:149], v[212:213], v[16:17], v[148:149] op_sel:[0,1,0] op_sel_hi:[1,1,1]
v_pk_fma_f32 v[150:151], v[214:215], v[16:17], v[150:151] op_sel:[0,1,0] op_sel_hi:[1,1,1]
v_cvt_pk_bf16_f32 v208, v144, v145
v_cvt_pk_bf16_f32 v209, v146, v147
v_cvt_pk_bf16_f32 v210, v148, v149
v_cvt_pk_bf16_f32 v211, v150, v151
buffer_store_dwordx4 v[208:211], v1, s[16:19], s29 offen offset:2048 sc1
s_waitcnt lgkmcnt(9)
v_pk_fma_f32 v[152:153], v[216:217], v[16:17], v[152:153] op_sel:[0,1,0] op_sel_hi:[1,1,1]
v_pk_fma_f32 v[154:155], v[218:219], v[16:17], v[154:155] op_sel:[0,1,0] op_sel_hi:[1,1,1]
s_waitcnt lgkmcnt(8)
v_pk_fma_f32 v[156:157], v[220:221], v[16:17], v[156:157] op_sel:[0,1,0] op_sel_hi:[1,1,1]
v_pk_fma_f32 v[158:159], v[222:223], v[16:17], v[158:159] op_sel:[0,1,0] op_sel_hi:[1,1,1]
v_cvt_pk_bf16_f32 v212, v152, v153
v_cvt_pk_bf16_f32 v213, v154, v155
v_cvt_pk_bf16_f32 v214, v156, v157
v_cvt_pk_bf16_f32 v215, v158, v159
buffer_store_dwordx4 v[212:215], v1, s[16:19], s29 offen offset:3072 sc1
v_pk_mul_f32 v[160:161], v[160:161], v[16:17] op_sel:[0,0] op_sel_hi:[1,0]
v_pk_mul_f32 v[162:163], v[162:163], v[16:17] op_sel:[0,0] op_sel_hi:[1,0]
v_pk_mul_f32 v[164:165], v[164:165], v[16:17] op_sel:[0,0] op_sel_hi:[1,0]
v_pk_mul_f32 v[166:167], v[166:167], v[16:17] op_sel:[0,0] op_sel_hi:[1,0]
v_pk_mul_f32 v[168:169], v[168:169], v[16:17] op_sel:[0,0] op_sel_hi:[1,0]
v_pk_mul_f32 v[170:171], v[170:171], v[16:17] op_sel:[0,0] op_sel_hi:[1,0]
v_pk_mul_f32 v[172:173], v[172:173], v[16:17] op_sel:[0,0] op_sel_hi:[1,0]
v_pk_mul_f32 v[174:175], v[174:175], v[16:17] op_sel:[0,0] op_sel_hi:[1,0]
s_waitcnt lgkmcnt(7)
v_pk_fma_f32 v[160:161], v[224:225], v[16:17], v[160:161] op_sel:[0,1,0] op_sel_hi:[1,1,1]
v_pk_fma_f32 v[162:163], v[226:227], v[16:17], v[162:163] op_sel:[0,1,0] op_sel_hi:[1,1,1]
s_waitcnt lgkmcnt(6)
v_pk_fma_f32 v[164:165], v[228:229], v[16:17], v[164:165] op_sel:[0,1,0] op_sel_hi:[1,1,1]
v_pk_fma_f32 v[166:167], v[230:231], v[16:17], v[166:167] op_sel:[0,1,0] op_sel_hi:[1,1,1]
v_cvt_pk_bf16_f32 v224, v160, v161
v_cvt_pk_bf16_f32 v225, v162, v163
v_cvt_pk_bf16_f32 v226, v164, v165
v_cvt_pk_bf16_f32 v227, v166, v167
buffer_store_dwordx4 v[224:227], v1, s[16:19], s72 offen offset:0 sc1
s_waitcnt lgkmcnt(5)
v_pk_fma_f32 v[168:169], v[232:233], v[16:17], v[168:169] op_sel:[0,1,0] op_sel_hi:[1,1,1]
v_pk_fma_f32 v[170:171], v[234:235], v[16:17], v[170:171] op_sel:[0,1,0] op_sel_hi:[1,1,1]
s_waitcnt lgkmcnt(4)
v_pk_fma_f32 v[172:173], v[236:237], v[16:17], v[172:173] op_sel:[0,1,0] op_sel_hi:[1,1,1]
v_pk_fma_f32 v[174:175], v[238:239], v[16:17], v[174:175] op_sel:[0,1,0] op_sel_hi:[1,1,1]
v_cvt_pk_bf16_f32 v228, v168, v169
v_cvt_pk_bf16_f32 v229, v170, v171
v_cvt_pk_bf16_f32 v230, v172, v173
v_cvt_pk_bf16_f32 v231, v174, v175
buffer_store_dwordx4 v[228:231], v1, s[16:19], s72 offen offset:1024 sc1
v_pk_mul_f32 v[176:177], v[176:177], v[16:17] op_sel:[0,0] op_sel_hi:[1,0]
v_pk_mul_f32 v[178:179], v[178:179], v[16:17] op_sel:[0,0] op_sel_hi:[1,0]
v_pk_mul_f32 v[180:181], v[180:181], v[16:17] op_sel:[0,0] op_sel_hi:[1,0]
v_pk_mul_f32 v[182:183], v[182:183], v[16:17] op_sel:[0,0] op_sel_hi:[1,0]
v_pk_mul_f32 v[184:185], v[184:185], v[16:17] op_sel:[0,0] op_sel_hi:[1,0]
v_pk_mul_f32 v[186:187], v[186:187], v[16:17] op_sel:[0,0] op_sel_hi:[1,0]
v_pk_mul_f32 v[188:189], v[188:189], v[16:17] op_sel:[0,0] op_sel_hi:[1,0]
v_pk_mul_f32 v[190:191], v[190:191], v[16:17] op_sel:[0,0] op_sel_hi:[1,0]
s_waitcnt lgkmcnt(3)
v_pk_fma_f32 v[176:177], v[240:241], v[16:17], v[176:177] op_sel:[0,1,0] op_sel_hi:[1,1,1]
v_pk_fma_f32 v[178:179], v[242:243], v[16:17], v[178:179] op_sel:[0,1,0] op_sel_hi:[1,1,1]
s_waitcnt lgkmcnt(2)
v_pk_fma_f32 v[180:181], v[244:245], v[16:17], v[180:181] op_sel:[0,1,0] op_sel_hi:[1,1,1]
v_pk_fma_f32 v[182:183], v[246:247], v[16:17], v[182:183] op_sel:[0,1,0] op_sel_hi:[1,1,1]
v_cvt_pk_bf16_f32 v240, v176, v177
v_cvt_pk_bf16_f32 v241, v178, v179
v_cvt_pk_bf16_f32 v242, v180, v181
v_cvt_pk_bf16_f32 v243, v182, v183
buffer_store_dwordx4 v[240:243], v1, s[16:19], s72 offen offset:2048 sc1
s_waitcnt lgkmcnt(1)
v_pk_fma_f32 v[184:185], v[248:249], v[16:17], v[184:185] op_sel:[0,1,0] op_sel_hi:[1,1,1]
v_pk_fma_f32 v[186:187], v[250:251], v[16:17], v[186:187] op_sel:[0,1,0] op_sel_hi:[1,1,1]
s_waitcnt lgkmcnt(0)
v_pk_fma_f32 v[188:189], v[252:253], v[16:17], v[188:189] op_sel:[0,1,0] op_sel_hi:[1,1,1]
v_pk_fma_f32 v[190:191], v[254:255], v[16:17], v[190:191] op_sel:[0,1,0] op_sel_hi:[1,1,1]
v_cvt_pk_bf16_f32 v244, v184, v185
v_cvt_pk_bf16_f32 v245, v186, v187
v_cvt_pk_bf16_f32 v246, v188, v189
v_cvt_pk_bf16_f32 v247, v190, v191
buffer_store_dwordx4 v[244:247], v1, s[16:19], s72 offen offset:3072 sc1
s_branch .Lend
.LdV:
v_exp_f32 v127, v55
v_exp_f32 v120, v56
v_cvt_pk_bf16_f32 v107, v126, v127
s_and_b32 s47, s51, 1
s_lshl_b32 s72, s47, 14
s_add_u32 s72, s72, 131072
v_add_u32 v22, s72, v1
s_lshl_b32 s72, s47, 16
s_add_u32 s72, s72, 8192
s_waitcnt lgkmcnt(0)
v_mfma_f32_32x32x16_bf16 v[128:143], a[224:227], v[96:99], v[128:143]
v_exp_f32 v121, v57
v_mfma_f32_16x16x32_bf16 v[24:27], v[28:31], v[104:107], 0
v_exp_f32 v122, v58
v_cvt_pk_bf16_f32 v108, v120, v121
v_mfma_f32_32x32x16_bf16 v[144:159], a[228:231], v[96:99], v[144:159]
v_exp_f32 v123, v59
v_exp_f32 v124, v60
v_cvt_pk_bf16_f32 v109, v122, v123
v_mfma_f32_32x32x16_bf16 v[160:175], a[232:235], v[96:99], v[160:175]
v_exp_f32 v125, v61
v_exp_f32 v126, v62
v_cvt_pk_bf16_f32 v110, v124, v125
v_mfma_f32_32x32x16_bf16 v[176:191], a[236:239], v[96:99], v[176:191]
v_exp_f32 v127, v63
v_add_u32 v23, s72, v1
v_cvt_pk_bf16_f32 v111, v126, v127
v_mfma_f32_32x32x16_bf16 v[128:143], a[240:243], v[100:103], v[128:143]
s_lshl_b32 s72, s51, 10
v_mfma_f32_16x16x32_bf16 v[24:27], v[28:31], v[108:111], v[24:27]
s_add_u32 s72, s72, 32768
v_lshrrev_b32 v21, 1, v1
v_add_u32 v21, s72, v21
v_mfma_f32_32x32x16_bf16 v[144:159], a[244:247], v[100:103], v[144:159]
s_lshl_b32 s47, s47, 1
s_sub_u32 s29, 65, s48
s_add_u32 s29, s29, s47
s_lshl_b32 s29, s29, 18
s_lshl_b32 s72, s49, 13
s_add_u32 s29, s29, s72
s_add_u32 s29, s29, 0x3000000
s_add_u32 s72, s29, 4096
v_mfma_f32_32x32x16_bf16 v[160:175], a[248:251], v[100:103], v[160:175]
v_cmp_ngt_f32 vcc, s71, v24
v_mfma_f32_32x32x16_bf16 v[176:191], a[252:255], v[100:103], v[176:191]
s_cbranch_vccnz .Lr5
.Lr5b:
v_add_f32 v11, v11, v24
v_mfma_f32_32x32x16_bf16 v[192:207], a[224:227], v[104:107], v[192:207]
ds_write_b128 v22, v[128:131] offset:0
ds_write_b128 v22, v[132:135] offset:1024
ds_write_b128 v22, v[136:139] offset:2048
v_mfma_f32_32x32x16_bf16 v[208:223], a[228:231], v[104:107], v[208:223]
ds_write_b128 v22, v[140:143] offset:3072
ds_write_b128 v22, v[144:147] offset:4096
ds_write_b128 v22, v[148:151] offset:5120
v_mfma_f32_32x32x16_bf16 v[224:239], a[232:235], v[104:107], v[224:239]
ds_write_b128 v22, v[152:155] offset:6144
ds_write_b128 v22, v[156:159] offset:7168
ds_write_b128 v22, v[160:163] offset:8192
v_mfma_f32_32x32x16_bf16 v[240:255], a[236:239], v[104:107], v[240:255]
ds_write_b128 v22, v[164:167] offset:9216
ds_write_b128 v22, v[168:171] offset:10240
ds_write_b128 v22, v[172:175] offset:11264
v_mfma_f32_32x32x16_bf16 v[192:207], a[240:243], v[108:111], v[192:207]
ds_write_b128 v22, v[176:179] offset:12288
ds_write_b128 v22, v[180:183] offset:13312
ds_write_b128 v22, v[184:187] offset:14336
v_mfma_f32_32x32x16_bf16 v[208:223], a[244:247], v[108:111], v[208:223]
ds_write_b128 v22, v[188:191] offset:15360
v_mov_b32 v18, v14
v_mov_b32 v19, v10
v_mfma_f32_32x32x16_bf16 v[224:239], a[248:251], v[108:111], v[224:239]
ds_write_b64 v21, v[18:19]
v_mfma_f32_32x32x16_bf16 v[240:255], a[252:255], v[108:111], v[240:255]
s_waitcnt lgkmcnt(0)
s_barrier
.LfV:
v_xor_b32 v21, 2048, v21
ds_read_b64 v[18:19], v21
ds_read_b128 v[128:131], v23 offset:0
ds_read_b128 v[132:135], v23 offset:1024
ds_read_b128 v[136:139], v23 offset:2048
ds_read_b128 v[140:143], v23 offset:3072
ds_read_b128 v[144:147], v23 offset:4096
ds_read_b128 v[148:151], v23 offset:5120
ds_read_b128 v[152:155], v23 offset:6144
ds_read_b128 v[156:159], v23 offset:7168
ds_read_b128 v[160:163], v23 offset:8192
ds_read_b128 v[164:167], v23 offset:9216
ds_read_b128 v[168:171], v23 offset:10240
ds_read_b128 v[172:175], v23 offset:11264
ds_read_b128 v[176:179], v23 offset:12288
ds_read_b128 v[180:183], v23 offset:13312
ds_read_b128 v[184:187], v23 offset:14336
ds_read_b128 v[188:191], v23 offset:15360
s_mov_b32 s28, s51
s_mov_b32 s47, 32768
s_lshr_b32 s37, %2, 5
s_and_b32 s30, s37, 7
s_lshl_b32 s38, s30, 10
s_add_u32 s38, s38, 0x6300000
s_lshl_b32 s30, s28, 8
s_add_u32 s39, s30, s38
v_lshrrev_b32 v6, 2, v1
buffer_load_dword v24, v6, s[16:19], s39 offen sc1
v_and_b32 v4, 124, v6
v_lshrrev_b32 v7, 3, v0
v_lshlrev_b32 v7, 27, v7
v_and_b32 v5, 7, v0
v_lshl_add_u32 v5, v5, 10, v7
s_lshl_b32 s46, %2, 2
s_add_u32 s46, s46, 0x6300000
v_mov_b32 v7, 3
s_and_b32 s30, %2, 7
s_lshl_b32 s30, s30, 2
s_bfe_u32 s35, %2, 0x20003
s_add_u32 s36, s30, s35
s_lshr_b32 s30, %2, 6
s_lshl_b32 s30, s30, 2
s_add_u32 s30, s30, s28
s_mul_i32 s34, s30, 262144
s_mul_i32 s45, s30, 114688
s_bfe_u32 s30, %2, 0x10005
s_lshl_b32 s30, s30, 11
s_add_u32 s34, s34, s30
s_add_u32 s34, s34, 0x5c00000
s_add_u32 s45, s45, s30
s_add_u32 s45, s45, 0x6000000
s_lshr_b32 s30, s28, 1
s_lshl_b32 s30, s30, 10
v_add_u32 v2, s30, v1
v_xor_b32 v3, 1024, v2
s_and_b32 s32, s36, 31
s_mul_i32 s32, s32, 524288
s_mul_i32 s30, s28, 65536
s_add_u32 s32, s32, s30
s_add_u32 s32, s32, 0x3000000
s_and_b32 s30, s28, 1
s_mul_i32 s30, s30, 262144
s_add_u32 s33, s32, 262144
s_sub_u32 s33, s33, s30
s_add_u32 s32, s32, s30
s_lshr_b32 s30, s36, 1
s_sub_u32 s35, 15, s30
s_min_u32 s30, s30, s35
s_lshl_b32 s30, s30, 7
s_add_u32 s31, s30, s38
s_bfe_u32 s30, %2, 0x20001
s_lshl_b32 s30, s30, 3
s_add_u32 s30, s30, s37
s_mul_i32 s30, s30, 122880
s_add_u32 s30, s30, 0x5800000
s_add_u32 s40, %0, s30
s_addc_u32 s41, %1, 0
s_and_b32 s41, s41, 0xffff
s_mov_b32 s42, 122880
s_mov_b32 s43, 0x20000
s_lshl_b32 s44, s28, 10
s_mov_b32 s35, s39
s_waitcnt lgkmcnt(15)
v_max_f32 v20, v15, v18
v_sub_f32 v15, v15, v20
v_sub_f32 v18, v18, v20
v_exp_f32 v16, v15
v_exp_f32 v17, v18
s_nop 0
v_mul_f32 v11, v11, v16
v_fmac_f32 v11, v19, v17
v_rcp_f32 v11, v11
s_nop 0
v_mul_f32 v16, v16, v11
v_mul_f32 v17, v17, v11
v_pk_mul_f32 v[192:193], v[192:193], v[16:17] op_sel:[0,0] op_sel_hi:[1,0]
v_pk_mul_f32 v[194:195], v[194:195], v[16:17] op_sel:[0,0] op_sel_hi:[1,0]
v_pk_mul_f32 v[196:197], v[196:197], v[16:17] op_sel:[0,0] op_sel_hi:[1,0]
v_pk_mul_f32 v[198:199], v[198:199], v[16:17] op_sel:[0,0] op_sel_hi:[1,0]
v_pk_mul_f32 v[200:201], v[200:201], v[16:17] op_sel:[0,0] op_sel_hi:[1,0]
v_pk_mul_f32 v[202:203], v[202:203], v[16:17] op_sel:[0,0] op_sel_hi:[1,0]
v_pk_mul_f32 v[204:205], v[204:205], v[16:17] op_sel:[0,0] op_sel_hi:[1,0]
v_pk_mul_f32 v[206:207], v[206:207], v[16:17] op_sel:[0,0] op_sel_hi:[1,0]
v_pk_fma_f32 v[192:193], v[128:129], v[16:17], v[192:193] op_sel:[0,1,0] op_sel_hi:[1,1,1]
v_pk_fma_f32 v[194:195], v[130:131], v[16:17], v[194:195] op_sel:[0,1,0] op_sel_hi:[1,1,1]
s_waitcnt lgkmcnt(14)
v_pk_fma_f32 v[196:197], v[132:133], v[16:17], v[196:197] op_sel:[0,1,0] op_sel_hi:[1,1,1]
v_pk_fma_f32 v[198:199], v[134:135], v[16:17], v[198:199] op_sel:[0,1,0] op_sel_hi:[1,1,1]
v_cvt_pk_bf16_f32 v128, v192, v193
v_cvt_pk_bf16_f32 v129, v194, v195
v_cvt_pk_bf16_f32 v130, v196, v197
v_cvt_pk_bf16_f32 v131, v198, v199
buffer_store_dwordx4 v[128:131], v1, s[16:19], s29 offen offset:0 sc1
s_waitcnt lgkmcnt(13)
v_pk_fma_f32 v[200:201], v[136:137], v[16:17], v[200:201] op_sel:[0,1,0] op_sel_hi:[1,1,1]
v_pk_fma_f32 v[202:203], v[138:139], v[16:17], v[202:203] op_sel:[0,1,0] op_sel_hi:[1,1,1]
s_waitcnt lgkmcnt(12)
v_pk_fma_f32 v[204:205], v[140:141], v[16:17], v[204:205] op_sel:[0,1,0] op_sel_hi:[1,1,1]
v_pk_fma_f32 v[206:207], v[142:143], v[16:17], v[206:207] op_sel:[0,1,0] op_sel_hi:[1,1,1]
v_cvt_pk_bf16_f32 v132, v200, v201
v_cvt_pk_bf16_f32 v133, v202, v203
v_cvt_pk_bf16_f32 v134, v204, v205
v_cvt_pk_bf16_f32 v135, v206, v207
buffer_store_dwordx4 v[132:135], v1, s[16:19], s29 offen offset:1024 sc1
v_pk_mul_f32 v[208:209], v[208:209], v[16:17] op_sel:[0,0] op_sel_hi:[1,0]
v_pk_mul_f32 v[210:211], v[210:211], v[16:17] op_sel:[0,0] op_sel_hi:[1,0]
v_pk_mul_f32 v[212:213], v[212:213], v[16:17] op_sel:[0,0] op_sel_hi:[1,0]
v_pk_mul_f32 v[214:215], v[214:215], v[16:17] op_sel:[0,0] op_sel_hi:[1,0]
v_pk_mul_f32 v[216:217], v[216:217], v[16:17] op_sel:[0,0] op_sel_hi:[1,0]
v_pk_mul_f32 v[218:219], v[218:219], v[16:17] op_sel:[0,0] op_sel_hi:[1,0]
v_pk_mul_f32 v[220:221], v[220:221], v[16:17] op_sel:[0,0] op_sel_hi:[1,0]
v_pk_mul_f32 v[222:223], v[222:223], v[16:17] op_sel:[0,0] op_sel_hi:[1,0]
s_waitcnt lgkmcnt(11)
v_pk_fma_f32 v[208:209], v[144:145], v[16:17], v[208:209] op_sel:[0,1,0] op_sel_hi:[1,1,1]
v_pk_fma_f32 v[210:211], v[146:147], v[16:17], v[210:211] op_sel:[0,1,0] op_sel_hi:[1,1,1]
s_waitcnt lgkmcnt(10)
v_pk_fma_f32 v[212:213], v[148:149], v[16:17], v[212:213] op_sel:[0,1,0] op_sel_hi:[1,1,1]
v_pk_fma_f32 v[214:215], v[150:151], v[16:17], v[214:215] op_sel:[0,1,0] op_sel_hi:[1,1,1]
v_cvt_pk_bf16_f32 v144, v208, v209
v_cvt_pk_bf16_f32 v145, v210, v211
v_cvt_pk_bf16_f32 v146, v212, v213
v_cvt_pk_bf16_f32 v147, v214, v215
buffer_store_dwordx4 v[144:147], v1, s[16:19], s29 offen offset:2048 sc1
s_waitcnt lgkmcnt(9)
v_pk_fma_f32 v[216:217], v[152:153], v[16:17], v[216:217] op_sel:[0,1,0] op_sel_hi:[1,1,1]
v_pk_fma_f32 v[218:219], v[154:155], v[16:17], v[218:219] op_sel:[0,1,0] op_sel_hi:[1,1,1]
s_waitcnt lgkmcnt(8)
v_pk_fma_f32 v[220:221], v[156:157], v[16:17], v[220:221] op_sel:[0,1,0] op_sel_hi:[1,1,1]
v_pk_fma_f32 v[222:223], v[158:159], v[16:17], v[222:223] op_sel:[0,1,0] op_sel_hi:[1,1,1]
v_cvt_pk_bf16_f32 v148, v216, v217
v_cvt_pk_bf16_f32 v149, v218, v219
v_cvt_pk_bf16_f32 v150, v220, v221
v_cvt_pk_bf16_f32 v151, v222, v223
buffer_store_dwordx4 v[148:151], v1, s[16:19], s29 offen offset:3072 sc1
v_pk_mul_f32 v[224:225], v[224:225], v[16:17] op_sel:[0,0] op_sel_hi:[1,0]
v_pk_mul_f32 v[226:227], v[226:227], v[16:17] op_sel:[0,0] op_sel_hi:[1,0]
v_pk_mul_f32 v[228:229], v[228:229], v[16:17] op_sel:[0,0] op_sel_hi:[1,0]
v_pk_mul_f32 v[230:231], v[230:231], v[16:17] op_sel:[0,0] op_sel_hi:[1,0]
v_pk_mul_f32 v[232:233], v[232:233], v[16:17] op_sel:[0,0] op_sel_hi:[1,0]
v_pk_mul_f32 v[234:235], v[234:235], v[16:17] op_sel:[0,0] op_sel_hi:[1,0]
v_pk_mul_f32 v[236:237], v[236:237], v[16:17] op_sel:[0,0] op_sel_hi:[1,0]
v_pk_mul_f32 v[238:239], v[238:239], v[16:17] op_sel:[0,0] op_sel_hi:[1,0]
s_waitcnt lgkmcnt(7)
v_pk_fma_f32 v[224:225], v[160:161], v[16:17], v[224:225] op_sel:[0,1,0] op_sel_hi:[1,1,1]
v_pk_fma_f32 v[226:227], v[162:163], v[16:17], v[226:227] op_sel:[0,1,0] op_sel_hi:[1,1,1]
s_waitcnt lgkmcnt(6)
v_pk_fma_f32 v[228:229], v[164:165], v[16:17], v[228:229] op_sel:[0,1,0] op_sel_hi:[1,1,1]
v_pk_fma_f32 v[230:231], v[166:167], v[16:17], v[230:231] op_sel:[0,1,0] op_sel_hi:[1,1,1]
v_cvt_pk_bf16_f32 v160, v224, v225
v_cvt_pk_bf16_f32 v161, v226, v227
v_cvt_pk_bf16_f32 v162, v228, v229
v_cvt_pk_bf16_f32 v163, v230, v231
buffer_store_dwordx4 v[160:163], v1, s[16:19], s72 offen offset:0 sc1
s_waitcnt lgkmcnt(5)
v_pk_fma_f32 v[232:233], v[168:169], v[16:17], v[232:233] op_sel:[0,1,0] op_sel_hi:[1,1,1]
v_pk_fma_f32 v[234:235], v[170:171], v[16:17], v[234:235] op_sel:[0,1,0] op_sel_hi:[1,1,1]
s_waitcnt lgkmcnt(4)
v_pk_fma_f32 v[236:237], v[172:173], v[16:17], v[236:237] op_sel:[0,1,0] op_sel_hi:[1,1,1]
v_pk_fma_f32 v[238:239], v[174:175], v[16:17], v[238:239] op_sel:[0,1,0] op_sel_hi:[1,1,1]
v_cvt_pk_bf16_f32 v164, v232, v233
v_cvt_pk_bf16_f32 v165, v234, v235
v_cvt_pk_bf16_f32 v166, v236, v237
v_cvt_pk_bf16_f32 v167, v238, v239
buffer_store_dwordx4 v[164:167], v1, s[16:19], s72 offen offset:1024 sc1
v_pk_mul_f32 v[240:241], v[240:241], v[16:17] op_sel:[0,0] op_sel_hi:[1,0]
v_pk_mul_f32 v[242:243], v[242:243], v[16:17] op_sel:[0,0] op_sel_hi:[1,0]
v_pk_mul_f32 v[244:245], v[244:245], v[16:17] op_sel:[0,0] op_sel_hi:[1,0]
v_pk_mul_f32 v[246:247], v[246:247], v[16:17] op_sel:[0,0] op_sel_hi:[1,0]
v_pk_mul_f32 v[248:249], v[248:249], v[16:17] op_sel:[0,0] op_sel_hi:[1,0]
v_pk_mul_f32 v[250:251], v[250:251], v[16:17] op_sel:[0,0] op_sel_hi:[1,0]
v_pk_mul_f32 v[252:253], v[252:253], v[16:17] op_sel:[0,0] op_sel_hi:[1,0]
v_pk_mul_f32 v[254:255], v[254:255], v[16:17] op_sel:[0,0] op_sel_hi:[1,0]
s_waitcnt lgkmcnt(3)
v_pk_fma_f32 v[240:241], v[176:177], v[16:17], v[240:241] op_sel:[0,1,0] op_sel_hi:[1,1,1]
v_pk_fma_f32 v[242:243], v[178:179], v[16:17], v[242:243] op_sel:[0,1,0] op_sel_hi:[1,1,1]
s_waitcnt lgkmcnt(2)
v_pk_fma_f32 v[244:245], v[180:181], v[16:17], v[244:245] op_sel:[0,1,0] op_sel_hi:[1,1,1]
v_pk_fma_f32 v[246:247], v[182:183], v[16:17], v[246:247] op_sel:[0,1,0] op_sel_hi:[1,1,1]
v_cvt_pk_bf16_f32 v176, v240, v241
v_cvt_pk_bf16_f32 v177, v242, v243
v_cvt_pk_bf16_f32 v178, v244, v245
v_cvt_pk_bf16_f32 v179, v246, v247
buffer_store_dwordx4 v[176:179], v1, s[16:19], s72 offen offset:2048 sc1
s_waitcnt lgkmcnt(1)
v_pk_fma_f32 v[248:249], v[184:185], v[16:17], v[248:249] op_sel:[0,1,0] op_sel_hi:[1,1,1]
v_pk_fma_f32 v[250:251], v[186:187], v[16:17], v[250:251] op_sel:[0,1,0] op_sel_hi:[1,1,1]
s_waitcnt lgkmcnt(0)
v_pk_fma_f32 v[252:253], v[188:189], v[16:17], v[252:253] op_sel:[0,1,0] op_sel_hi:[1,1,1]
v_pk_fma_f32 v[254:255], v[190:191], v[16:17], v[254:255] op_sel:[0,1,0] op_sel_hi:[1,1,1]
v_cvt_pk_bf16_f32 v180, v248, v249
v_cvt_pk_bf16_f32 v181, v250, v251
v_cvt_pk_bf16_f32 v182, v252, v253
v_cvt_pk_bf16_f32 v183, v254, v255
buffer_store_dwordx4 v[180:183], v1, s[16:19], s72 offen offset:3072 sc1
s_branch .Lend
.Lm0:
v_cndmask_b32 v64, v64, v5, s[54:55]
v_cndmask_b32 v65, v65, v5, s[56:57]
v_cndmask_b32 v66, v66, v5, s[58:59]
v_cndmask_b32 v67, v67, v5, s[60:61]
v_cndmask_b32 v68, v68, v5, s[62:63]
v_cndmask_b32 v69, v69, v5, s[64:65]
v_cndmask_b32 v70, v70, v5, s[66:67]
v_cndmask_b32 v71, v71, v5, s[68:69]
v_cndmask_b32 v72, v72, v5, s[84:85]
v_cndmask_b32 v73, v73, v5, s[86:87]
v_cndmask_b32 v74, v74, v5, s[88:89]
v_cndmask_b32 v75, v75, v5, s[90:91]
v_cndmask_b32 v76, v76, v5, s[92:93]
v_cndmask_b32 v77, v77, v5, s[94:95]
v_cndmask_b32 v78, v78, v5, s[96:97]
v_cndmask_b32 v79, v79, v5, s[98:99]
s_branch .Lm0b
.Lr1:
s_cselect_b32 s47, 1, 0
s_mov_b32 s46, 0xf149f2ca
s_getpc_b64 s[32:33]
s_branch .LrareV0
.Lr1d:
s_cmp_lg_u32 s47, 0
s_branch .Lr1b
.Ls1:
v_mov_b32 v64, v5
v_mov_b32 v65, v5
v_mov_b32 v66, v5
v_mov_b32 v67, v5
v_mov_b32 v68, v5
v_mov_b32 v69, v5
v_mov_b32 v70, v5
v_mov_b32 v71, v5
v_mov_b32 v72, v5
v_mov_b32 v73, v5
v_mov_b32 v74, v5
v_mov_b32 v75, v5
v_mov_b32 v76, v5
v_mov_b32 v77, v5
v_mov_b32 v78, v5
v_mov_b32 v79, v5
s_branch .Ls1b
.Lr2:
s_cselect_b32 s47, 1, 0
s_mov_b32 s46, 0
s_getpc_b64 s[32:33]
s_branch .LrareV0
.Lr2d:
s_cmp_lg_u32 s47, 0
s_branch .Lr2b
.Lr3:
s_cselect_b32 s47, 1, 0
s_mov_b32 s46, 0xf149f2ca
s_getpc_b64 s[32:33]
s_branch .LrareV1
.Lr3d:
s_cmp_lg_u32 s47, 0
s_branch .Lr3b
.Ls3:
v_cndmask_b32 v80, v80, v5, s[54:55]
v_cndmask_b32 v81, v81, v5, s[56:57]
v_cndmask_b32 v82, v82, v5, s[58:59]
v_cndmask_b32 v83, v83, v5, s[60:61]
v_cndmask_b32 v84, v84, v5, s[62:63]
v_cndmask_b32 v85, v85, v5, s[64:65]
v_cndmask_b32 v86, v86, v5, s[66:67]
v_cndmask_b32 v87, v87, v5, s[68:69]
v_cndmask_b32 v88, v88, v5, s[84:85]
v_cndmask_b32 v89, v89, v5, s[86:87]
v_cndmask_b32 v90, v90, v5, s[88:89]
v_cndmask_b32 v91, v91, v5, s[90:91]
v_cndmask_b32 v92, v92, v5, s[92:93]
v_cndmask_b32 v93, v93, v5, s[94:95]
v_cndmask_b32 v94, v94, v5, s[96:97]
v_cndmask_b32 v95, v95, v5, s[98:99]
s_branch .Ls3b
.Lr4:
s_cselect_b32 s47, 1, 0
s_mov_b32 s46, 0
s_getpc_b64 s[32:33]
s_branch .LrareA1
.Lr4d:
s_cmp_lg_u32 s47, 0
s_branch .Lr4b
.Lr5:
s_cselect_b32 s47, 1, 0
s_mov_b32 s46, 0
s_getpc_b64 s[32:33]
s_branch .LrareV1
.Lr5d:
s_cmp_lg_u32 s47, 0
s_branch .Lr5b
.Lr6:
s_cselect_b32 s47, 1, 0
s_mov_b32 s46, 0
s_getpc_b64 s[32:33]
s_branch .LrareA1
.Lr6d:
s_cmp_lg_u32 s47, 0
s_branch .Lr6b
.Lr7:
s_cselect_b32 s47, 1, 0
s_mov_b32 s46, 0
s_getpc_b64 s[32:33]
s_branch .LrareA0
.Lr7d:
s_cmp_lg_u32 s47, 0
s_branch .Lr7b
.Lr8:
s_cselect_b32 s47, 1, 0
s_mov_b32 s46, 0
s_getpc_b64 s[32:33]
s_branch .LrareA1
.Lr8d:
s_cmp_lg_u32 s47, 0
s_branch .Lr8b
.Lr9:
s_cselect_b32 s47, 1, 0
s_mov_b32 s46, 0
s_getpc_b64 s[32:33]
s_branch .LrareA0
.Lr9d:
s_cmp_lg_u32 s47, 0
s_branch .Lr9b
.Lr10:
s_cselect_b32 s47, 1, 0
s_mov_b32 s46, 0
s_getpc_b64 s[32:33]
s_branch .LrareA1
.Lr10d:
s_cmp_lg_u32 s47, 0
s_branch .Lr10b
.Lr11:
s_cselect_b32 s47, 1, 0
s_mov_b32 s46, 0
s_getpc_b64 s[32:33]
s_branch .LrareA0
.Lr11d:
s_cmp_lg_u32 s47, 0
s_branch .Lr11b
.Lr12:
s_cselect_b32 s47, 1, 0
s_mov_b32 s46, 0
s_getpc_b64 s[32:33]
s_branch .LrareA1
.Lr12d:
s_cmp_lg_u32 s47, 0
s_branch .Lr12b
.Lr13:
s_cselect_b32 s47, 1, 0
s_mov_b32 s46, 0
s_getpc_b64 s[32:33]
s_branch .LrareA0
.Lr13d:
s_cmp_lg_u32 s47, 0
s_branch .Lr13b
.Lr14:
s_cselect_b32 s47, 1, 0
s_mov_b32 s46, 0
s_getpc_b64 s[32:33]
s_branch .LrareA1
.Lr14d:
s_cmp_lg_u32 s47, 0
s_branch .Lr14b
.Lr15:
s_cselect_b32 s47, 1, 0
s_mov_b32 s46, 0
s_getpc_b64 s[32:33]
s_branch .LrareV0
.Lr15d:
s_cmp_lg_u32 s47, 0
s_branch .Lr15b
.Lr16:
s_cselect_b32 s47, 1, 0
s_mov_b32 s46, 0
s_getpc_b64 s[32:33]
s_branch .LrareV1
.Lr16d:
s_cmp_lg_u32 s47, 0
s_branch .Lr16b
.Lr17:
s_cselect_b32 s47, 1, 0
s_mov_b32 s46, 0
s_getpc_b64 s[32:33]
s_branch .LrareV0
.Lr17d:
s_cmp_lg_u32 s47, 0
s_branch .Lr17b
.Lr18:
s_cselect_b32 s47, 1, 0
s_mov_b32 s46, 0
s_getpc_b64 s[32:33]
s_branch .LrareV1
.Lr18d:
s_cmp_lg_u32 s47, 0
s_branch .Lr18b
.Lr19:
s_cselect_b32 s47, 1, 0
s_mov_b32 s46, 0
s_getpc_b64 s[32:33]
s_branch .LrareV0
.Lr19d:
s_cmp_lg_u32 s47, 0
s_branch .Lr19b
.Lr20:
s_cselect_b32 s47, 1, 0
s_mov_b32 s46, 0
s_getpc_b64 s[32:33]
s_branch .LrareV1
.Lr20d:
s_cmp_lg_u32 s47, 0
s_branch .Lr20b
.Lr21:
s_cselect_b32 s47, 1, 0
s_mov_b32 s46, 0
s_getpc_b64 s[32:33]
s_branch .LrareV0
.Lr21d:
s_cmp_lg_u32 s47, 0
s_branch .Lr21b
.Lr22:
s_cselect_b32 s47, 1, 0
s_mov_b32 s46, 0
s_getpc_b64 s[32:33]
s_branch .LrareV1
.Lr22d:
s_cmp_lg_u32 s47, 0
s_branch .Lr22b
.Lr23:
s_cselect_b32 s47, 1, 0
s_mov_b32 s46, 0
s_getpc_b64 s[32:33]
s_branch .LrareV0
.Lr23d:
s_cmp_lg_u32 s47, 0
s_branch .Lr23b
.Lr24:
s_cselect_b32 s47, 1, 0
s_mov_b32 s46, 0
s_getpc_b64 s[32:33]
s_branch .LrareV1
.Lr24d:
s_cmp_lg_u32 s47, 0
s_branch .Lr24b
.Lr26:
s_cselect_b32 s47, 1, 0
s_mov_b32 s46, 0
s_getpc_b64 s[32:33]
s_branch .LrareV1
.Lr26d:
s_cmp_lg_u32 s47, 0
s_branch .Lr26b
.Lr28:
s_cselect_b32 s47, 1, 0
s_mov_b32 s46, 0
s_getpc_b64 s[32:33]
s_branch .LrareV1
.Lr28d:
s_cmp_lg_u32 s47, 0
s_branch .Lr28b
.Lt31:
s_getpc_b64 s[30:31]
s_branch .Lsp0
s_branch .Lt31b
.Lr32:
s_cselect_b32 s47, 1, 0
s_cmp_lt_i32 s44, 0
s_cbranch_scc0 .Lr32c
v_mov_b32 v12, 0xf149f2ca
s_branch .Lr32d
.Lr32c:
s_mov_b32 s46, 0xf149f2ca
s_getpc_b64 s[32:33]
s_branch .LrareA0
.Lr32d:
s_cmp_lg_u32 s47, 0
s_branch .Lr32b
.Ls32:
s_getpc_b64 s[30:31]
s_branch .Lsp0
s_branch .Ls32b
.Ls33:
s_getpc_b64 s[30:31]
s_branch .Lsp1
s_branch .Ls33b
.Lr34:
s_cselect_b32 s47, 1, 0
s_mov_b32 s46, 0
s_getpc_b64 s[32:33]
s_branch .LrareA0
.Lr34d:
s_cmp_lg_u32 s47, 0
s_branch .Lr34b
.Ls34:
s_getpc_b64 s[30:31]
s_branch .Lsp0
s_branch .Ls34b
.Lr35:
s_cselect_b32 s47, 1, 0
s_cmp_lt_i32 s45, 0
s_cbranch_scc0 .Lr35c
v_mov_b32 v13, 0xf149f2ca
s_branch .Lr35d
.Lr35c:
s_mov_b32 s46, 0xf149f2ca
s_getpc_b64 s[32:33]
s_branch .LrareA1
.Lr35d:
s_cmp_lg_u32 s47, 0
s_branch .Lr35b
.Ls35:
s_getpc_b64 s[30:31]
s_branch .Lsp1
s_branch .Ls35b
.Lr36:
s_cselect_b32 s47, 1, 0
s_mov_b32 s46, 0
s_getpc_b64 s[32:33]
s_branch .LrareA0
.Lr36d:
s_cmp_lg_u32 s47, 0
s_branch .Lr36b
.Lr37:
s_cselect_b32 s47, 1, 0
s_mov_b32 s46, 0
s_getpc_b64 s[32:33]
s_branch .LrareA1
.Lr37d:
s_cmp_lg_u32 s47, 0
s_branch .Lr37b
.Lr38:
s_cselect_b32 s47, 1, 0
s_mov_b32 s46, 0
s_getpc_b64 s[32:33]
s_branch .LrareA0
.Lr38d:
s_cmp_lg_u32 s47, 0
s_branch .Lr38b
.Lr39:
s_cselect_b32 s47, 1, 0
s_mov_b32 s46, 0
s_getpc_b64 s[32:33]
s_branch .LrareA1
.Lr39d:
s_cmp_lg_u32 s47, 0
s_branch .Lr39b
.Lr41:
s_cselect_b32 s47, 1, 0
s_mov_b32 s46, 0
s_getpc_b64 s[32:33]
s_branch .LrareA1
.Lr41d:
s_cmp_lg_u32 s47, 0
s_branch .Lr41b
.Lr43:
s_cselect_b32 s47, 1, 0
s_mov_b32 s46, 0
s_getpc_b64 s[32:33]
s_branch .LrareA1
.Lr43d:
s_cmp_lg_u32 s47, 0
s_branch .Lr43b
.Lr46:
s_cselect_b32 s47, 1, 0
s_mov_b32 s46, 0
s_getpc_b64 s[32:33]
s_branch .LrareA0
.Lr46d:
s_cmp_lg_u32 s47, 0
s_branch .Lr46b
.Lr47:
s_cselect_b32 s47, 1, 0
s_mov_b32 s46, 0
s_getpc_b64 s[32:33]
s_branch .LrareA1
.Lr47d:
s_cmp_lg_u32 s47, 0
s_branch .Lr47b
.Lr48:
s_cselect_b32 s47, 1, 0
s_mov_b32 s46, 0
s_getpc_b64 s[32:33]
s_branch .LrareA0
.Lr48d:
s_cmp_lg_u32 s47, 0
s_branch .Lr48b
.Lr49:
s_cselect_b32 s47, 1, 0
s_mov_b32 s46, 0
s_getpc_b64 s[32:33]
s_branch .LrareA1
.Lr49d:
s_cmp_lg_u32 s47, 0
s_branch .Lr49b
.Lr50:
s_cselect_b32 s47, 1, 0
s_mov_b32 s46, 0
s_getpc_b64 s[32:33]
s_branch .LrareA0
.Lr50d:
s_cmp_lg_u32 s47, 0
s_branch .Lr50b
.Lr51:
s_cselect_b32 s47, 1, 0
s_mov_b32 s46, 0
s_getpc_b64 s[32:33]
s_branch .LrareA1
.Lr51d:
s_cmp_lg_u32 s47, 0
s_branch .Lr51b
.Lr52:
s_cselect_b32 s47, 1, 0
s_mov_b32 s46, 0
s_getpc_b64 s[32:33]
s_branch .LrareA0
.Lr52d:
s_cmp_lg_u32 s47, 0
s_branch .Lr52b
.Lr53:
s_cselect_b32 s47, 1, 0
s_mov_b32 s46, 0
s_getpc_b64 s[32:33]
s_branch .LrareA1
.Lr53d:
s_cmp_lg_u32 s47, 0
s_branch .Lr53b
.Lr54:
s_cselect_b32 s47, 1, 0
s_mov_b32 s46, 0
s_getpc_b64 s[32:33]
s_branch .LrareV0
.Lr54d:
s_cmp_lg_u32 s47, 0
s_branch .Lr54b
.Lr55:
s_cselect_b32 s47, 1, 0
s_mov_b32 s46, 0
s_getpc_b64 s[32:33]
s_branch .LrareV1
.Lr55d:
s_cmp_lg_u32 s47, 0
s_branch .Lr55b
.Lr56:
s_cselect_b32 s47, 1, 0
s_mov_b32 s46, 0
s_getpc_b64 s[32:33]
s_branch .LrareV0
.Lr56d:
s_cmp_lg_u32 s47, 0
s_branch .Lr56b
.Lr57:
s_cselect_b32 s47, 1, 0
s_mov_b32 s46, 0
s_getpc_b64 s[32:33]
s_branch .LrareV1
.Lr57d:
s_cmp_lg_u32 s47, 0
s_branch .Lr57b
.Lr58:
s_cselect_b32 s47, 1, 0
s_mov_b32 s46, 0
s_getpc_b64 s[32:33]
s_branch .LrareV0
.Lr58d:
s_cmp_lg_u32 s47, 0
s_branch .Lr58b
.Lr59:
s_cselect_b32 s47, 1, 0
s_mov_b32 s46, 0
s_getpc_b64 s[32:33]
s_branch .LrareV1
.Lr59d:
s_cmp_lg_u32 s47, 0
s_branch .Lr59b
.Lr60:
s_cselect_b32 s47, 1, 0
s_mov_b32 s46, 0
s_getpc_b64 s[32:33]
s_branch .LrareV0
.Lr60d:
s_cmp_lg_u32 s47, 0
s_branch .Lr60b
.Lr61:
s_cselect_b32 s47, 1, 0
s_mov_b32 s46, 0
s_getpc_b64 s[32:33]
s_branch .LrareV1
.Lr61d:
s_cmp_lg_u32 s47, 0
s_branch .Lr61b
.Lr62:
s_cselect_b32 s47, 1, 0
s_mov_b32 s46, 0
s_getpc_b64 s[32:33]
s_branch .LrareV0
.Lr62d:
s_cmp_lg_u32 s47, 0
s_branch .Lr62b
.Lr63:
s_cselect_b32 s47, 1, 0
s_mov_b32 s46, 0
s_getpc_b64 s[32:33]
s_branch .LrareV1
.Lr63d:
s_cmp_lg_u32 s47, 0
s_branch .Lr63b
.Lr64:
s_cselect_b32 s47, 1, 0
s_mov_b32 s46, 0
s_getpc_b64 s[32:33]
s_branch .LrareV0
.Lr64d:
s_cmp_lg_u32 s47, 0
s_branch .Lr64b
.Lr65:
s_cselect_b32 s47, 1, 0
s_mov_b32 s46, 0
s_getpc_b64 s[32:33]
s_branch .LrareV1
.Lr65d:
s_cmp_lg_u32 s47, 0
s_branch .Lr65b
.Lr66:
s_cselect_b32 s47, 1, 0
s_mov_b32 s46, 0
s_getpc_b64 s[32:33]
s_branch .LrareV0
.Lr66d:
s_cmp_lg_u32 s47, 0
s_branch .Lr66b
.Lr67:
s_cselect_b32 s47, 1, 0
s_mov_b32 s46, 0
s_getpc_b64 s[32:33]
s_branch .LrareV1
.Lr67d:
s_cmp_lg_u32 s47, 0
s_branch .Lr67b
.Lr69:
s_cselect_b32 s47, 1, 0
s_mov_b32 s46, 0
s_getpc_b64 s[32:33]
s_branch .LrareV1
.Lr69d:
s_cmp_lg_u32 s47, 0
s_branch .Lr69b
.Lt70:
s_getpc_b64 s[30:31]
s_branch .Lsp0
s_branch .Lt70b
.Lr71:
s_cselect_b32 s47, 1, 0
s_cmp_lt_i32 s44, 0
s_cbranch_scc0 .Lr71c
v_mov_b32 v12, 0xf149f2ca
s_branch .Lr71d
.Lr71c:
s_mov_b32 s46, 0xf149f2ca
s_getpc_b64 s[32:33]
s_branch .LrareA0
.Lr71d:
s_cmp_lg_u32 s47, 0
s_branch .Lr71b
.Ls71:
s_getpc_b64 s[30:31]
s_branch .Lsp0
s_branch .Ls71b
.Lr72:
s_cselect_b32 s47, 1, 0
s_mov_b32 s46, 0
s_getpc_b64 s[32:33]
s_branch .LrareV1
v_mov_b32 v80, 0
v_mov_b32 v81, 0
v_mov_b32 v82, 0
v_mov_b32 v83, 0
v_mov_b32 v84, 0
v_mov_b32 v85, 0
v_mov_b32 v86, 0
v_mov_b32 v87, 0
v_mov_b32 v88, 0
v_mov_b32 v89, 0
v_mov_b32 v90, 0
v_mov_b32 v91, 0
v_mov_b32 v92, 0
v_mov_b32 v93, 0
v_mov_b32 v94, 0
v_mov_b32 v95, 0
.Lr72d:
s_cmp_lg_u32 s47, 0
s_branch .Lr72b
.Ls72:
s_getpc_b64 s[30:31]
s_branch .Lsp1
s_branch .Ls72b
.Lr73:
s_cselect_b32 s47, 1, 0
s_mov_b32 s46, 0
s_getpc_b64 s[32:33]
s_branch .LrareA0
.Lr73d:
s_cmp_lg_u32 s47, 0
s_branch .Lr73b
.Ls73:
s_getpc_b64 s[30:31]
s_branch .Lsp0
s_branch .Ls73b
.Lr74:
s_cselect_b32 s47, 1, 0
s_cmp_lt_i32 s45, 0
s_cbranch_scc0 .Lr74c
v_mov_b32 v13, 0xf149f2ca
s_branch .Lr74d
.Lr74c:
s_mov_b32 s46, 0xf149f2ca
s_getpc_b64 s[32:33]
s_branch .LrareA1
.Lr74d:
s_cmp_lg_u32 s47, 0
s_branch .Lr74b
.Ls74:
s_getpc_b64 s[30:31]
s_branch .Lsp1
s_branch .Ls74b
.Lr75:
s_cselect_b32 s47, 1, 0
s_mov_b32 s46, 0
s_getpc_b64 s[32:33]
s_branch .LrareA0
.Lr75d:
s_cmp_lg_u32 s47, 0
s_branch .Lr75b
.Lr76:
s_cselect_b32 s47, 1, 0
s_mov_b32 s46, 0
s_getpc_b64 s[32:33]
s_branch .LrareA1
.Lr76d:
s_cmp_lg_u32 s47, 0
s_branch .Lr76b
.Lr77:
s_cselect_b32 s47, 1, 0
s_mov_b32 s46, 0
s_getpc_b64 s[32:33]
s_branch .LrareA0
.Lr77d:
s_cmp_lg_u32 s47, 0
s_branch .Lr77b
.Lr78:
s_cselect_b32 s47, 1, 0
s_mov_b32 s46, 0
s_getpc_b64 s[32:33]
s_branch .LrareA1
.Lr78d:
s_cmp_lg_u32 s47, 0
s_branch .Lr78b
.Lr79:
s_cselect_b32 s47, 1, 0
s_mov_b32 s46, 0
s_getpc_b64 s[32:33]
s_branch .LrareA0
.Lr79d:
s_cmp_lg_u32 s47, 0
s_branch .Lr79b
.Lr80:
s_cselect_b32 s47, 1, 0
s_mov_b32 s46, 0
s_getpc_b64 s[32:33]
s_branch .LrareA1
.Lr80d:
s_cmp_lg_u32 s47, 0
s_branch .Lr80b
.Lr81:
s_cselect_b32 s47, 1, 0
s_mov_b32 s46, 0
s_getpc_b64 s[32:33]
s_branch .LrareA0
.Lr81d:
s_cmp_lg_u32 s47, 0
s_branch .Lr81b
.Lr82:
s_cselect_b32 s47, 1, 0
s_mov_b32 s46, 0
s_getpc_b64 s[32:33]
s_branch .LrareA1
.Lr82d:
s_cmp_lg_u32 s47, 0
s_branch .Lr82b
.Lr84:
s_cselect_b32 s47, 1, 0
s_mov_b32 s46, 0
s_getpc_b64 s[32:33]
s_branch .LrareA1
.Lr84d:
s_cmp_lg_u32 s47, 0
s_branch .Lr84b
.Lr85:
s_cselect_b32 s47, 1, 0
s_mov_b32 s46, 0
s_getpc_b64 s[32:33]
s_branch .LrareV0
.Lr85d:
s_cmp_lg_u32 s47, 0
s_branch .Lr85b
.Ls85:
s_getpc_b64 s[30:31]
s_branch .Lsp0
s_branch .Ls85b
.Lr86:
s_cselect_b32 s47, 1, 0
s_mov_b32 s46, 0
s_getpc_b64 s[32:33]
s_branch .LrareV1
.Lr86d:
s_cmp_lg_u32 s47, 0
s_branch .Lr86b
.Ls86:
s_getpc_b64 s[30:31]
s_branch .Lsp1
s_branch .Ls86b
.Lr87:
s_cselect_b32 s47, 1, 0
s_mov_b32 s46, 0
s_getpc_b64 s[32:33]
s_branch .LrareV0
.Lr87d:
s_cmp_lg_u32 s47, 0
s_branch .Lr87b
.Ls87:
s_getpc_b64 s[30:31]
s_branch .Lsp0
s_branch .Ls87b
.Lr88:
s_cselect_b32 s47, 1, 0
s_mov_b32 s46, 0
s_getpc_b64 s[32:33]
s_branch .LrareV1
.Lr88d:
s_cmp_lg_u32 s47, 0
s_branch .Lr88b
.Ls88:
s_getpc_b64 s[30:31]
s_branch .Lsp1
s_branch .Ls88b
.Lr89:
s_cselect_b32 s47, 1, 0
s_mov_b32 s46, 0
s_getpc_b64 s[32:33]
s_branch .LrareV0
.Lr89d:
s_cmp_lg_u32 s47, 0
s_branch .Lr89b
.Ls89:
s_getpc_b64 s[30:31]
s_branch .Lsp0
s_branch .Ls89b
.Lr90:
s_cselect_b32 s47, 1, 0
s_mov_b32 s46, 0
s_getpc_b64 s[32:33]
s_branch .LrareV1
.Lr90d:
s_cmp_lg_u32 s47, 0
s_branch .Lr90b
.Ls90:
s_getpc_b64 s[30:31]
s_branch .Lsp1
s_branch .Ls90b
.Lr91:
s_cselect_b32 s47, 1, 0
s_mov_b32 s46, 0
s_getpc_b64 s[32:33]
s_branch .LrareV0
.Lr91d:
s_cmp_lg_u32 s47, 0
s_branch .Lr91b
.Ls91:
s_getpc_b64 s[30:31]
s_branch .Lsp0
s_branch .Ls91b
.Lr92:
s_cselect_b32 s47, 1, 0
s_mov_b32 s46, 0
s_getpc_b64 s[32:33]
s_branch .LrareV1
.Lr92d:
s_cmp_lg_u32 s47, 0
s_branch .Lr92b
.Ls92:
s_getpc_b64 s[30:31]
s_branch .Lsp1
s_branch .Ls92b
.Lr93:
s_cselect_b32 s47, 1, 0
s_mov_b32 s46, 0
s_getpc_b64 s[32:33]
s_branch .LrareV0
.Lr93d:
s_cmp_lg_u32 s47, 0
s_branch .Lr93b
.Ls93:
s_getpc_b64 s[30:31]
s_branch .Lsp0
s_branch .Ls93b
.Lr94:
s_cselect_b32 s47, 1, 0
s_mov_b32 s46, 0
s_getpc_b64 s[32:33]
s_branch .LrareV1
.Lr94d:
s_cmp_lg_u32 s47, 0
s_branch .Lr94b
.Ls94:
s_getpc_b64 s[30:31]
s_branch .Lsp1
s_branch .Ls94b
.Lr95:
s_cselect_b32 s47, 1, 0
s_mov_b32 s46, 0
s_getpc_b64 s[32:33]
s_branch .LrareV0
.Lr95d:
s_cmp_lg_u32 s47, 0
s_branch .Lr95b
.Ls95:
s_getpc_b64 s[30:31]
s_branch .Lsp0
s_branch .Ls95b
.Lr96:
s_cselect_b32 s47, 1, 0
s_mov_b32 s46, 0
s_getpc_b64 s[32:33]
s_branch .LrareV1
.Lr96d:
s_cmp_lg_u32 s47, 0
s_branch .Lr96b
.Ls96:
s_getpc_b64 s[30:31]
s_branch .Lsp1
s_branch .Ls96b
.Lr97:
s_cselect_b32 s47, 1, 0
s_mov_b32 s46, 0
s_getpc_b64 s[32:33]
s_branch .LrareV0
.Lr97d:
s_cmp_lg_u32 s47, 0
s_branch .Lr97b
.Ls97:
s_getpc_b64 s[30:31]
s_branch .Lsp0
s_branch .Ls97b
.Lr98:
s_cselect_b32 s47, 1, 0
s_mov_b32 s46, 0
s_getpc_b64 s[32:33]
s_branch .LrareV1
.Lr98d:
s_cmp_lg_u32 s47, 0
s_branch .Lr98b
.Ls98:
s_getpc_b64 s[30:31]
s_branch .Lsp1
s_branch .Ls98b
.Lr99:
s_cselect_b32 s47, 1, 0
s_mov_b32 s46, 0
s_getpc_b64 s[32:33]
s_branch .LrareV0
.Lr99d:
s_cmp_lg_u32 s47, 0
s_branch .Lr99b
.Ls99:
s_getpc_b64 s[30:31]
s_branch .Lsp0
s_branch .Ls99b
.Lr100:
s_cselect_b32 s47, 1, 0
s_mov_b32 s46, 0
s_getpc_b64 s[32:33]
s_branch .LrareV1
.Lr100d:
s_cmp_lg_u32 s47, 0
s_branch .Lr100b
.Ls100:
s_getpc_b64 s[30:31]
s_branch .Lsp1
s_branch .Ls100b
.Lr101:
s_cselect_b32 s47, 1, 0
s_mov_b32 s46, 0
s_getpc_b64 s[32:33]
s_branch .LrareV0
.Lr101d:
s_cmp_lg_u32 s47, 0
s_branch .Lr101b
.Ls101:
s_getpc_b64 s[30:31]
s_branch .Lsp0
s_branch .Ls101b
.Lr102:
s_cselect_b32 s47, 1, 0
s_mov_b32 s46, 0
s_getpc_b64 s[32:33]
s_branch .LrareV1
.Lr102d:
s_cmp_lg_u32 s47, 0
s_branch .Lr102b
.Ls102:
s_getpc_b64 s[30:31]
s_branch .Lsp1
s_branch .Ls102b
.Lr103:
s_cselect_b32 s47, 1, 0
s_mov_b32 s46, 0
s_getpc_b64 s[32:33]
s_branch .LrareV0
.Lr103d:
s_cmp_lg_u32 s47, 0
s_branch .Lr103b
.Ls103:
s_getpc_b64 s[30:31]
s_branch .Lsp0
s_branch .Ls103b
.Lr104:
s_cselect_b32 s47, 1, 0
s_mov_b32 s46, 0
s_getpc_b64 s[32:33]
s_branch .LrareV1
.Lr104d:
s_cmp_lg_u32 s47, 0
s_branch .Lr104b
.Ls104:
s_getpc_b64 s[30:31]
s_branch .Lsp1
s_branch .Ls104b
.Lr105:
s_cselect_b32 s47, 1, 0
s_mov_b32 s46, 0
s_getpc_b64 s[32:33]
s_branch .LrareV0
.Lr105d:
s_cmp_lg_u32 s47, 0
s_branch .Lr105b
.Ls105:
s_getpc_b64 s[30:31]
s_branch .Lsp0
s_branch .Ls105b
.Lr106:
s_cselect_b32 s47, 1, 0
s_mov_b32 s46, 0
s_getpc_b64 s[32:33]
s_branch .LrareV1
.Lr106d:
s_cmp_lg_u32 s47, 0
s_branch .Lr106b
.Ls106:
s_getpc_b64 s[30:31]
s_branch .Lsp1
s_branch .Ls106b
.Lr107:
s_cselect_b32 s47, 1, 0
s_mov_b32 s46, 0
s_getpc_b64 s[32:33]
s_branch .LrareV0
.Lr107d:
s_cmp_lg_u32 s47, 0
s_branch .Lr107b
.Ls107:
s_getpc_b64 s[30:31]
s_branch .Lsp0
s_branch .Ls107b
.Lr108:
s_cselect_b32 s47, 1, 0
s_mov_b32 s46, 0
s_getpc_b64 s[32:33]
s_branch .LrareV1
.Lr108d:
s_cmp_lg_u32 s47, 0
s_branch .Lr108b
.Ls108:
s_getpc_b64 s[30:31]
s_branch .Lsp1
s_branch .Ls108b
.Lr109:
s_cselect_b32 s47, 1, 0
s_mov_b32 s46, 0
s_getpc_b64 s[32:33]
s_branch .LrareV0
.Lr109d:
s_cmp_lg_u32 s47, 0
s_branch .Lr109b
.Ls109:
s_getpc_b64 s[30:31]
s_branch .Lsp0
s_branch .Ls109b
.Lr110:
s_cselect_b32 s47, 1, 0
s_mov_b32 s46, 0
s_getpc_b64 s[32:33]
s_branch .LrareV1
.Lr110d:
s_cmp_lg_u32 s47, 0
s_branch .Lr110b
.Ls110:
s_getpc_b64 s[30:31]
s_branch .Lsp1
s_branch .Ls110b
.Lr111:
s_cselect_b32 s47, 1, 0
s_mov_b32 s46, 0
s_getpc_b64 s[32:33]
s_branch .LrareV0
.Lr111d:
s_cmp_lg_u32 s47, 0
s_branch .Lr111b
.Ls111:
s_getpc_b64 s[30:31]
s_branch .Lsp0
s_branch .Ls111b
.Lr112:
s_cselect_b32 s47, 1, 0
s_mov_b32 s46, 0
s_getpc_b64 s[32:33]
s_branch .LrareV1
.Lr112d:
s_cmp_lg_u32 s47, 0
s_branch .Lr112b
.Ls112:
s_getpc_b64 s[30:31]
s_branch .Lsp1
s_branch .Ls112b
.Lr113:
s_cselect_b32 s47, 1, 0
s_mov_b32 s46, 0
s_getpc_b64 s[32:33]
s_branch .LrareV0
.Lr113d:
s_cmp_lg_u32 s47, 0
s_branch .Lr113b
.Ls113:
s_getpc_b64 s[30:31]
s_branch .Lsp0
s_branch .Ls113b
.Lr114:
s_cselect_b32 s47, 1, 0
s_mov_b32 s46, 0
s_getpc_b64 s[32:33]
s_branch .LrareV1
.Lr114d:
s_cmp_lg_u32 s47, 0
s_branch .Lr114b
.Ls114:
s_getpc_b64 s[30:31]
s_branch .Lsp1
s_branch .Ls114b
.Lr115:
s_cselect_b32 s47, 1, 0
s_mov_b32 s46, 0
s_getpc_b64 s[32:33]
s_branch .LrareV0
.Lr115d:
s_cmp_lg_u32 s47, 0
s_branch .Lr115b
.Ls115:
s_getpc_b64 s[30:31]
s_branch .Lsp0
s_branch .Ls115b
.Lr116:
s_cselect_b32 s47, 1, 0
s_mov_b32 s46, 0
s_getpc_b64 s[32:33]
s_branch .LrareV1
.Lr116d:
s_cmp_lg_u32 s47, 0
s_branch .Lr116b
.Ls116:
s_getpc_b64 s[30:31]
s_branch .Lsp1
s_branch .Ls116b
.Lr117:
s_cselect_b32 s47, 1, 0
s_mov_b32 s46, 0
s_getpc_b64 s[32:33]
s_branch .LrareV0
.Lr117d:
s_cmp_lg_u32 s47, 0
s_branch .Lr117b
.Ls117:
s_getpc_b64 s[30:31]
s_branch .Lsp0
s_branch .Ls117b
.Lr118:
s_cselect_b32 s47, 1, 0
s_mov_b32 s46, 0
s_getpc_b64 s[32:33]
s_branch .LrareV1
.Lr118d:
s_cmp_lg_u32 s47, 0
s_branch .Lr118b
.Ls118:
s_getpc_b64 s[30:31]
s_branch .Lsp1
s_branch .Ls118b
.Lr119:
s_cselect_b32 s47, 1, 0
s_mov_b32 s46, 0
s_getpc_b64 s[32:33]
s_branch .LrareV0
.Lr119d:
s_cmp_lg_u32 s47, 0
s_branch .Lr119b
.Ls119:
s_getpc_b64 s[30:31]
s_branch .Lsp0
s_branch .Ls119b
.Lr120:
s_cselect_b32 s47, 1, 0
s_mov_b32 s46, 0
s_getpc_b64 s[32:33]
s_branch .LrareV1
.Lr120d:
s_cmp_lg_u32 s47, 0
s_branch .Lr120b
.Ls120:
s_getpc_b64 s[30:31]
s_branch .Lsp1
s_branch .Ls120b
.Lr121:
s_cselect_b32 s47, 1, 0
s_mov_b32 s46, 0
s_getpc_b64 s[32:33]
s_branch .LrareV0
.Lr121d:
s_cmp_lg_u32 s47, 0
s_branch .Lr121b
.Ls121:
s_getpc_b64 s[30:31]
s_branch .Lsp0
s_branch .Ls121b
.Lr122:
s_cselect_b32 s47, 1, 0
s_mov_b32 s46, 0
s_getpc_b64 s[32:33]
s_branch .LrareV1
.Lr122d:
s_cmp_lg_u32 s47, 0
s_branch .Lr122b
.Ls122:
s_getpc_b64 s[30:31]
s_branch .Lsp1
s_branch .Ls122b
.Lr123:
s_cselect_b32 s47, 1, 0
s_mov_b32 s46, 0
s_getpc_b64 s[32:33]
s_branch .LrareV0
.Lr123d:
s_cmp_lg_u32 s47, 0
s_branch .Lr123b
.Ls123:
s_getpc_b64 s[30:31]
s_branch .Lsp0
s_branch .Ls123b
.Lr124:
s_cselect_b32 s47, 1, 0
s_mov_b32 s46, 0
s_getpc_b64 s[32:33]
s_branch .LrareV1
.Lr124d:
s_cmp_lg_u32 s47, 0
s_branch .Lr124b
.Ls124:
s_getpc_b64 s[30:31]
s_branch .Lsp1
s_branch .Ls124b
.Lr125:
s_cselect_b32 s47, 1, 0
s_mov_b32 s46, 0
s_getpc_b64 s[32:33]
s_branch .LrareV0
.Lr125d:
s_cmp_lg_u32 s47, 0
s_branch .Lr125b
.Ls125:
s_getpc_b64 s[30:31]
s_branch .Lsp0
s_branch .Ls125b
.Lr126:
s_cselect_b32 s47, 1, 0
s_mov_b32 s46, 0
s_getpc_b64 s[32:33]
s_branch .LrareV1
.Lr126d:
s_cmp_lg_u32 s47, 0
s_branch .Lr126b
.Ls126:
s_getpc_b64 s[30:31]
s_branch .Lsp1
s_branch .Ls126b
.Lr127:
s_cselect_b32 s47, 1, 0
s_mov_b32 s46, 0
s_getpc_b64 s[32:33]
s_branch .LrareV0
.Lr127d:
s_cmp_lg_u32 s47, 0
s_branch .Lr127b
.Ls127:
s_getpc_b64 s[30:31]
s_branch .Lsp0
s_branch .Ls127b
.Lr128:
s_cselect_b32 s47, 1, 0
s_mov_b32 s46, 0
s_getpc_b64 s[32:33]
s_branch .LrareV1
.Lr128d:
s_cmp_lg_u32 s47, 0
s_branch .Lr128b
.Ls128:
s_getpc_b64 s[30:31]
s_branch .Lsp1
s_branch .Ls128b
.Lr129:
s_cselect_b32 s47, 1, 0
s_mov_b32 s46, 0
s_getpc_b64 s[32:33]
s_branch .LrareV0
.Lr129d:
s_cmp_lg_u32 s47, 0
s_branch .Lr129b
.Ls129:
s_getpc_b64 s[30:31]
s_branch .Lsp0
s_branch .Ls129b
.Lr130:
s_cselect_b32 s47, 1, 0
s_mov_b32 s46, 0
s_getpc_b64 s[32:33]
s_branch .LrareV1
.Lr130d:
s_cmp_lg_u32 s47, 0
s_branch .Lr130b
.Ls130:
s_getpc_b64 s[30:31]
s_branch .Lsp1
s_branch .Ls130b
.Lr131:
s_cselect_b32 s47, 1, 0
s_mov_b32 s46, 0
s_getpc_b64 s[32:33]
s_branch .LrareV0
.Lr131d:
s_cmp_lg_u32 s47, 0
s_branch .Lr131b
.Ls131:
s_getpc_b64 s[30:31]
s_branch .Lsp0
s_branch .Ls131b
.Lr132:
s_cselect_b32 s47, 1, 0
s_mov_b32 s46, 0
s_getpc_b64 s[32:33]
s_branch .LrareV1
.Lr132d:
s_cmp_lg_u32 s47, 0
s_branch .Lr132b
.Ls132:
s_getpc_b64 s[30:31]
s_branch .Lsp1
s_branch .Ls132b
.Lr133:
s_cselect_b32 s47, 1, 0
s_mov_b32 s46, 0
s_getpc_b64 s[32:33]
s_branch .LrareV0
.Lr133d:
s_cmp_lg_u32 s47, 0
s_branch .Lr133b
.Ls133:
s_getpc_b64 s[30:31]
s_branch .Lsp0
s_branch .Ls133b
.Lr134:
s_cselect_b32 s47, 1, 0
s_mov_b32 s46, 0
s_getpc_b64 s[32:33]
s_branch .LrareV1
.Lr134d:
s_cmp_lg_u32 s47, 0
s_branch .Lr134b
.Ls134:
s_getpc_b64 s[30:31]
s_branch .Lsp1
s_branch .Ls134b
.Lr135:
s_cselect_b32 s47, 1, 0
s_mov_b32 s46, 0
s_getpc_b64 s[32:33]
s_branch .LrareV0
.Lr135d:
s_cmp_lg_u32 s47, 0
s_branch .Lr135b
.Ls135:
s_getpc_b64 s[30:31]
s_branch .Lsp0
s_branch .Ls135b
.Lr136:
s_cselect_b32 s47, 1, 0
s_mov_b32 s46, 0
s_getpc_b64 s[32:33]
s_branch .LrareV1
.Lr136d:
s_cmp_lg_u32 s47, 0
s_branch .Lr136b
.Ls136:
s_getpc_b64 s[30:31]
s_branch .Lsp1
s_branch .Ls136b
.Lr137:
s_cselect_b32 s47, 1, 0
s_mov_b32 s46, 0
s_getpc_b64 s[32:33]
s_branch .LrareV0
.Lr137d:
s_cmp_lg_u32 s47, 0
s_branch .Lr137b
.Ls137:
s_getpc_b64 s[30:31]
s_branch .Lsp0
s_branch .Ls137b
.Lr138:
s_cselect_b32 s47, 1, 0
s_mov_b32 s46, 0
s_getpc_b64 s[32:33]
s_branch .LrareV1
.Lr138d:
s_cmp_lg_u32 s47, 0
s_branch .Lr138b
.Ls138:
s_getpc_b64 s[30:31]
s_branch .Lsp1
s_branch .Ls138b
.Lr139:
s_cselect_b32 s47, 1, 0
s_mov_b32 s46, 0
s_getpc_b64 s[32:33]
s_branch .LrareV0
.Lr139d:
s_cmp_lg_u32 s47, 0
s_branch .Lr139b
.Ls139:
s_getpc_b64 s[30:31]
s_branch .Lsp0
s_branch .Ls139b
.Lr140:
s_cselect_b32 s47, 1, 0
s_mov_b32 s46, 0
s_getpc_b64 s[32:33]
s_branch .LrareV1
.Lr140d:
s_cmp_lg_u32 s47, 0
s_branch .Lr140b
.Ls140:
s_getpc_b64 s[30:31]
s_branch .Lsp1
s_branch .Ls140b
.Lr141:
s_cselect_b32 s47, 1, 0
s_mov_b32 s46, 0
s_getpc_b64 s[32:33]
s_branch .LrareV0
.Lr141d:
s_cmp_lg_u32 s47, 0
s_branch .Lr141b
.Ls141:
s_getpc_b64 s[30:31]
s_branch .Lsp0
s_branch .Ls141b
.Lr142:
s_cselect_b32 s47, 1, 0
s_mov_b32 s46, 0
s_getpc_b64 s[32:33]
s_branch .LrareV1
.Lr142d:
s_cmp_lg_u32 s47, 0
s_branch .Lr142b
.Ls142:
s_getpc_b64 s[30:31]
s_branch .Lsp1
s_branch .Ls142b
.Lr143:
s_cselect_b32 s47, 1, 0
s_mov_b32 s46, 0
s_getpc_b64 s[32:33]
s_branch .LrareV0
.Lr143d:
s_cmp_lg_u32 s47, 0
s_branch .Lr143b
.Ls143:
s_getpc_b64 s[30:31]
s_branch .Lsp0
s_branch .Ls143b
.Lr144:
s_cselect_b32 s47, 1, 0
s_mov_b32 s46, 0
s_getpc_b64 s[32:33]
s_branch .LrareV1
.Lr144d:
s_cmp_lg_u32 s47, 0
s_branch .Lr144b
.Ls144:
s_getpc_b64 s[30:31]
s_branch .Lsp1
s_branch .Ls144b
.Lr145:
s_cselect_b32 s47, 1, 0
s_mov_b32 s46, 0
s_getpc_b64 s[32:33]
s_branch .LrareV0
.Lr145d:
s_cmp_lg_u32 s47, 0
s_branch .Lr145b
.Ls145:
s_getpc_b64 s[30:31]
s_branch .Lsp0
s_branch .Ls145b
.Lr146:
s_cselect_b32 s47, 1, 0
s_mov_b32 s46, 0
s_getpc_b64 s[32:33]
s_branch .LrareV1
.Lr146d:
s_cmp_lg_u32 s47, 0
s_branch .Lr146b
.Ls146:
s_getpc_b64 s[30:31]
s_branch .Lsp1
s_branch .Ls146b
.Lr147:
s_cselect_b32 s47, 1, 0
s_mov_b32 s46, 0
s_getpc_b64 s[32:33]
s_branch .LrareV0
.Lr147d:
s_cmp_lg_u32 s47, 0
s_branch .Lr147b
.Ls147:
s_getpc_b64 s[30:31]
s_branch .Lsp0
s_branch .Ls147b
.Lr148:
s_cselect_b32 s47, 1, 0
s_mov_b32 s46, 0
s_getpc_b64 s[32:33]
s_branch .LrareV1
.Lr148d:
s_cmp_lg_u32 s47, 0
s_branch .Lr148b
.Ls148:
s_getpc_b64 s[30:31]
s_branch .Lsp1
s_branch .Ls148b
.LspW:
buffer_load_dword v24, v6, s[16:19], s35 offen sc1
s_sleep 1
.LspWl:
buffer_load_dword v25, v6, s[16:19], s35 offen sc1
s_waitcnt vmcnt(1)
v_cmp_gt_u32 vcc, 1, v24
s_sub_u32 s47, s47, 1
s_max_i32 s47, s47, 0
s_nop 1
s_cbranch_vccz .LspWb
buffer_load_dword v24, v6, s[16:19], s35 offen sc1
s_waitcnt vmcnt(1)
v_cmp_gt_u32 vcc, 1, v25
s_sub_u32 s47, s47, 1
s_max_i32 s47, s47, 0
s_nop 1
s_cbranch_vccz .LspWb
s_cmp_eq_u32 s47, 0
s_cbranch_scc0 .LspWl
s_branch .LspWb
.LspA1:
s_waitcnt vmcnt(18)
v_cmp_gt_u32 vcc, 3, v27
s_nop 3
s_cbranch_vccz .LspAb
s_waitcnt vmcnt(9)
v_cmp_gt_u32 vcc, 3, v26
s_nop 3
s_cbranch_vccz .LspAb
s_waitcnt vmcnt(0)
v_cmp_gt_u32 vcc, 3, v25
s_nop 3
s_cbranch_vccz .LspAb
.LspA:
buffer_load_dword v25, v4, s[16:19], s31 offen sc1
s_sleep 1
.LspAl:
buffer_load_dword v26, v4, s[16:19], s31 offen sc1
s_waitcnt vmcnt(1)
v_cmp_gt_u32 vcc, 3, v25
s_sub_u32 s47, s47, 1
s_max_i32 s47, s47, 0
s_nop 1
s_cbranch_vccz .LspAb
buffer_load_dword v25, v4, s[16:19], s31 offen sc1
s_waitcnt vmcnt(1)
v_cmp_gt_u32 vcc, 3, v26
s_sub_u32 s47, s47, 1
s_max_i32 s47, s47, 0
s_nop 1
s_cbranch_vccz .LspAb
s_cmp_eq_u32 s47, 0
s_cbranch_scc0 .LspAl
s_branch .LspAb
.LspX:
s_sleep 1
buffer_load_dwordx4 v[16:19], v1, s[16:19], s38 offen sc1
s_sub_u32 s47, s47, 1
s_max_i32 s47, s47, 0
s_waitcnt vmcnt(0)
v_and_b32 v16, v16, v17
v_and_b32 v18, v18, v19
v_and_b32 v16, v16, v18
v_cmp_gt_u32 vcc, 3, v16
s_cmp_eq_u32 s47, 0
s_cbranch_scc1 .LspXb
s_nop 2
s_cbranch_vccnz .LspX
s_branch .LspXb
.LspB:
buffer_load_dword v40, v6, s[16:19], s49 offen sc1
s_sleep 1
.LspBl:
buffer_load_dword v41, v6, s[16:19], s49 offen sc1
s_waitcnt vmcnt(1)
v_cmp_gt_u32 vcc, 1, v40
s_sub_u32 s47, s47, 1
s_max_i32 s47, s47, 0
s_nop 1
s_cbranch_vccz .LspBb
buffer_load_dword v40, v6, s[16:19], s49 offen sc1
s_waitcnt vmcnt(1)
v_cmp_gt_u32 vcc, 1, v41
s_sub_u32 s47, s47, 1
s_max_i32 s47, s47, 0
s_nop 1
s_cbranch_vccz .LspBb
s_cmp_eq_u32 s47, 0
s_cbranch_scc0 .LspBl
s_branch .LspBb
.Lrd4:
s_cmp_eq_u32 s20, 0
s_cbranch_scc1 .Lrd4b
s_mov_b32 s20, 0
s_mov_b32 s21, 0x6306000
.Laf4:
buffer_load_dwordx4 v[72:75], v4, s[16:19], s21 offen offset:0 sc1
buffer_load_dwordx4 v[76:79], v4, s[16:19], s21 offen offset:1024 sc1
buffer_load_dwordx4 v[80:83], v4, s[16:19], s21 offen offset:2048 sc1
buffer_load_dwordx4 v[84:87], v4, s[16:19], s21 offen offset:3072 sc1
s_sub_u32 s47, s47, 1
s_max_i32 s47, s47, 0
s_waitcnt vmcnt(0)
v_min3_u32 v72, v72, v73, v74
v_min3_u32 v72, v72, v75, v76
v_min3_u32 v72, v72, v77, v78
v_min3_u32 v72, v72, v79, v80
v_min3_u32 v72, v72, v81, v82
v_min3_u32 v72, v72, v83, v84
v_min3_u32 v72, v72, v85, v86
v_min_u32 v72, v72, v87
v_cmp_gt_u32 vcc, 2, v72
s_cmp_eq_u32 s47, 0
s_cbranch_scc1 .Laf4x
s_nop 2
s_cbranch_vccnz .Laf4
.Laf4x:
s_sub_u32 s31, s31, 28672
s_sub_u32 s32, s32, 28672
s_sub_u32 s35, s35, 4096
v_mov_b32 v40, 2
s_branch .Lrd4s
.LspC:
buffer_load_dword v16, v6, s[16:19], s38 offen sc1
s_sleep 1
.LspCl:
buffer_load_dword v17, v6, s[16:19], s38 offen sc1
s_waitcnt vmcnt(1)
v_cmp_gt_u32 vcc, 1, v16
s_sub_u32 s47, s47, 1
s_max_i32 s47, s47, 0
s_nop 1
s_cbranch_vccz .LspCb
buffer_load_dword v16, v6, s[16:19], s38 offen sc1
s_waitcnt vmcnt(1)
v_cmp_gt_u32 vcc, 1, v17
s_sub_u32 s47, s47, 1
s_max_i32 s47, s47, 0
s_nop 1
s_cbranch_vccz .LspCb
s_cmp_eq_u32 s47, 0
s_cbranch_scc0 .LspCl
s_branch .LspCb
.LrareA0:
s_nop 7
s_nop 7
v_max3_f32 v16, v32, v33, v34
v_max3_f32 v16, v16, v35, v36
v_max3_f32 v16, v16, v37, v38
v_max3_f32 v16, v16, v39, v40
v_max3_f32 v16, v16, v41, v42
v_max3_f32 v16, v16, v43, v44
v_max3_f32 v16, v16, v45, v46
v_max_f32 v16, v16, v47
v_mov_b32 v17, v16
s_nop 1
v_permlane32_swap_b32 v16, v17
s_nop 1
v_max_f32 v16, v16, v17
v_max_f32 v16, s46, v16
v_max_f32 v17, 0, v16
v_exp_f32 v17, -v17
v_add_f32 v12, v12, v16
v_mul_f32 v8, v8, v17
v_sub_f32 v64, v64, v16
v_sub_f32 v32, v32, v16
v_sub_f32 v65, v65, v16
v_sub_f32 v33, v33, v16
v_sub_f32 v66, v66, v16
v_sub_f32 v34, v34, v16
v_sub_f32 v67, v67, v16
v_sub_f32 v35, v35, v16
v_sub_f32 v68, v68, v16
v_sub_f32 v36, v36, v16
v_sub_f32 v69, v69, v16
v_sub_f32 v37, v37, v16
v_sub_f32 v70, v70, v16
v_sub_f32 v38, v38, v16
v_sub_f32 v71, v71, v16
v_sub_f32 v39, v39, v16
v_sub_f32 v72, v72, v16
v_sub_f32 v40, v40, v16
v_sub_f32 v73, v73, v16
v_sub_f32 v41, v41, v16
v_sub_f32 v74, v74, v16
v_sub_f32 v42, v42, v16
v_sub_f32 v75, v75, v16
v_sub_f32 v43, v43, v16
v_sub_f32 v76, v76, v16
v_sub_f32 v44, v44, v16
v_sub_f32 v77, v77, v16
v_sub_f32 v45, v45, v16
v_sub_f32 v78, v78, v16
v_sub_f32 v46, v46, v16
v_sub_f32 v79, v79, v16
v_sub_f32 v47, v47, v16
v_accvgpr_read_b32 v18, a0
v_mul_f32 v18, v18, v17
v_accvgpr_write_b32 a0, v18
v_accvgpr_read_b32 v18, a1
v_mul_f32 v18, v18, v17
v_accvgpr_write_b32 a1, v18
v_accvgpr_read_b32 v18, a2
v_mul_f32 v18, v18, v17
v_accvgpr_write_b32 a2, v18
v_accvgpr_read_b32 v18, a3
v_mul_f32 v18, v18, v17
v_accvgpr_write_b32 a3, v18
v_accvgpr_read_b32 v18, a4
v_mul_f32 v18, v18, v17
v_accvgpr_write_b32 a4, v18
v_accvgpr_read_b32 v18, a5
v_mul_f32 v18, v18, v17
v_accvgpr_write_b32 a5, v18
v_accvgpr_read_b32 v18, a6
v_mul_f32 v18, v18, v17
v_accvgpr_write_b32 a6, v18
v_accvgpr_read_b32 v18, a7
v_mul_f32 v18, v18, v17
v_accvgpr_write_b32 a7, v18
v_accvgpr_read_b32 v18, a8
v_mul_f32 v18, v18, v17
v_accvgpr_write_b32 a8, v18
v_accvgpr_read_b32 v18, a9
v_mul_f32 v18, v18, v17
v_accvgpr_write_b32 a9, v18
v_accvgpr_read_b32 v18, a10
v_mul_f32 v18, v18, v17
v_accvgpr_write_b32 a10, v18
v_accvgpr_read_b32 v18, a11
v_mul_f32 v18, v18, v17
v_accvgpr_write_b32 a11, v18
v_accvgpr_read_b32 v18, a12
v_mul_f32 v18, v18, v17
v_accvgpr_write_b32 a12, v18
v_accvgpr_read_b32 v18, a13
v_mul_f32 v18, v18, v17
v_accvgpr_write_b32 a13, v18
v_accvgpr_read_b32 v18, a14
v_mul_f32 v18, v18, v17
v_accvgpr_write_b32 a14, v18
v_accvgpr_read_b32 v18, a15
v_mul_f32 v18, v18, v17
v_accvgpr_write_b32 a15, v18
v_accvgpr_read_b32 v18, a16
v_mul_f32 v18, v18, v17
v_accvgpr_write_b32 a16, v18
v_accvgpr_read_b32 v18, a17
v_mul_f32 v18, v18, v17
v_accvgpr_write_b32 a17, v18
v_accvgpr_read_b32 v18, a18
v_mul_f32 v18, v18, v17
v_accvgpr_write_b32 a18, v18
v_accvgpr_read_b32 v18, a19
v_mul_f32 v18, v18, v17
v_accvgpr_write_b32 a19, v18
v_accvgpr_read_b32 v18, a20
v_mul_f32 v18, v18, v17
v_accvgpr_write_b32 a20, v18
v_accvgpr_read_b32 v18, a21
v_mul_f32 v18, v18, v17
v_accvgpr_write_b32 a21, v18
v_accvgpr_read_b32 v18, a22
v_mul_f32 v18, v18, v17
v_accvgpr_write_b32 a22, v18
v_accvgpr_read_b32 v18, a23
v_mul_f32 v18, v18, v17
v_accvgpr_write_b32 a23, v18
v_accvgpr_read_b32 v18, a24
v_mul_f32 v18, v18, v17
v_accvgpr_write_b32 a24, v18
v_accvgpr_read_b32 v18, a25
v_mul_f32 v18, v18, v17
v_accvgpr_write_b32 a25, v18
v_accvgpr_read_b32 v18, a26
v_mul_f32 v18, v18, v17
v_accvgpr_write_b32 a26, v18
v_accvgpr_read_b32 v18, a27
v_mul_f32 v18, v18, v17
v_accvgpr_write_b32 a27, v18
v_accvgpr_read_b32 v18, a28
v_mul_f32 v18, v18, v17
v_accvgpr_write_b32 a28, v18
v_accvgpr_read_b32 v18, a29
v_mul_f32 v18, v18, v17
v_accvgpr_write_b32 a29, v18
v_accvgpr_read_b32 v18, a30
v_mul_f32 v18, v18, v17
v_accvgpr_write_b32 a30, v18
v_accvgpr_read_b32 v18, a31
v_mul_f32 v18, v18, v17
v_accvgpr_write_b32 a31, v18
v_accvgpr_read_b32 v18, a32
v_mul_f32 v18, v18, v17
v_accvgpr_write_b32 a32, v18
v_accvgpr_read_b32 v18, a33
v_mul_f32 v18, v18, v17
v_accvgpr_write_b32 a33, v18
v_accvgpr_read_b32 v18, a34
v_mul_f32 v18, v18, v17
v_accvgpr_write_b32 a34, v18
v_accvgpr_read_b32 v18, a35
v_mul_f32 v18, v18, v17
v_accvgpr_write_b32 a35, v18
v_accvgpr_read_b32 v18, a36
v_mul_f32 v18, v18, v17
v_accvgpr_write_b32 a36, v18
v_accvgpr_read_b32 v18, a37
v_mul_f32 v18, v18, v17
v_accvgpr_write_b32 a37, v18
v_accvgpr_read_b32 v18, a38
v_mul_f32 v18, v18, v17
v_accvgpr_write_b32 a38, v18
v_accvgpr_read_b32 v18, a39
v_mul_f32 v18, v18, v17
v_accvgpr_write_b32 a39, v18
v_accvgpr_read_b32 v18, a40
v_mul_f32 v18, v18, v17
v_accvgpr_write_b32 a40, v18
v_accvgpr_read_b32 v18, a41
v_mul_f32 v18, v18, v17
v_accvgpr_write_b32 a41, v18
v_accvgpr_read_b32 v18, a42
v_mul_f32 v18, v18, v17
v_accvgpr_write_b32 a42, v18
v_accvgpr_read_b32 v18, a43
v_mul_f32 v18, v18, v17
v_accvgpr_write_b32 a43, v18
v_accvgpr_read_b32 v18, a44
v_mul_f32 v18, v18, v17
v_accvgpr_write_b32 a44, v18
v_accvgpr_read_b32 v18, a45
v_mul_f32 v18, v18, v17
v_accvgpr_write_b32 a45, v18
v_accvgpr_read_b32 v18, a46
v_mul_f32 v18, v18, v17
v_accvgpr_write_b32 a46, v18
v_accvgpr_read_b32 v18, a47
v_mul_f32 v18, v18, v17
v_accvgpr_write_b32 a47, v18
v_accvgpr_read_b32 v18, a48
v_mul_f32 v18, v18, v17
v_accvgpr_write_b32 a48, v18
v_accvgpr_read_b32 v18, a49
v_mul_f32 v18, v18, v17
v_accvgpr_write_b32 a49, v18
v_accvgpr_read_b32 v18, a50
v_mul_f32 v18, v18, v17
v_accvgpr_write_b32 a50, v18
v_accvgpr_read_b32 v18, a51
v_mul_f32 v18, v18, v17
v_accvgpr_write_b32 a51, v18
v_accvgpr_read_b32 v18, a52
v_mul_f32 v18, v18, v17
v_accvgpr_write_b32 a52, v18
v_accvgpr_read_b32 v18, a53
v_mul_f32 v18, v18, v17
v_accvgpr_write_b32 a53, v18
v_accvgpr_read_b32 v18, a54
v_mul_f32 v18, v18, v17
v_accvgpr_write_b32 a54, v18
v_accvgpr_read_b32 v18, a55
v_mul_f32 v18, v18, v17
v_accvgpr_write_b32 a55, v18
v_accvgpr_read_b32 v18, a56
v_mul_f32 v18, v18, v17
v_accvgpr_write_b32 a56, v18
v_accvgpr_read_b32 v18, a57
v_mul_f32 v18, v18, v17
v_accvgpr_write_b32 a57, v18
v_accvgpr_read_b32 v18, a58
v_mul_f32 v18, v18, v17
v_accvgpr_write_b32 a58, v18
v_accvgpr_read_b32 v18, a59
v_mul_f32 v18, v18, v17
v_accvgpr_write_b32 a59, v18
v_accvgpr_read_b32 v18, a60
v_mul_f32 v18, v18, v17
v_accvgpr_write_b32 a60, v18
v_accvgpr_read_b32 v18, a61
v_mul_f32 v18, v18, v17
v_accvgpr_write_b32 a61, v18
v_accvgpr_read_b32 v18, a62
v_mul_f32 v18, v18, v17
v_accvgpr_write_b32 a62, v18
v_accvgpr_read_b32 v18, a63
v_mul_f32 v18, v18, v17
v_accvgpr_write_b32 a63, v18
v_exp_f32 v18, v32
v_exp_f32 v19, v33
s_nop 0
v_cvt_pk_bf16_f32 v96, v18, v19
v_exp_f32 v18, v34
v_exp_f32 v19, v35
s_nop 0
v_cvt_pk_bf16_f32 v97, v18, v19
v_exp_f32 v18, v36
v_exp_f32 v19, v37
s_nop 0
v_cvt_pk_bf16_f32 v98, v18, v19
v_exp_f32 v18, v38
v_exp_f32 v19, v39
s_nop 0
v_cvt_pk_bf16_f32 v99, v18, v19
v_exp_f32 v18, v40
v_exp_f32 v19, v41
s_nop 0
v_cvt_pk_bf16_f32 v100, v18, v19
v_exp_f32 v18, v42
v_exp_f32 v19, v43
s_nop 0
v_cvt_pk_bf16_f32 v101, v18, v19
v_exp_f32 v18, v44
v_exp_f32 v19, v45
s_nop 0
v_cvt_pk_bf16_f32 v102, v18, v19
v_exp_f32 v18, v46
v_exp_f32 v19, v47
s_nop 0
v_cvt_pk_bf16_f32 v103, v18, v19
s_nop 1
v_mfma_f32_16x16x32_bf16 v[24:27], v[28:31], v[96:99], 0
s_nop 7
v_mfma_f32_16x16x32_bf16 v[24:27], v[28:31], v[100:103], v[24:27]
s_nop 6
s_add_u32 s32, s32, 4
s_addc_u32 s33, s33, 0
s_setpc_b64 s[32:33]
.LrareA1:
s_nop 7
s_nop 7
v_max3_f32 v16, v48, v49, v50
v_max3_f32 v16, v16, v51, v52
v_max3_f32 v16, v16, v53, v54
v_max3_f32 v16, v16, v55, v56
v_max3_f32 v16, v16, v57, v58
v_max3_f32 v16, v16, v59, v60
v_max3_f32 v16, v16, v61, v62
v_max_f32 v16, v16, v63
v_mov_b32 v17, v16
s_nop 1
v_permlane32_swap_b32 v16, v17
s_nop 1
v_max_f32 v16, v16, v17
v_max_f32 v16, s46, v16
v_max_f32 v17, 0, v16
v_exp_f32 v17, -v17
v_add_f32 v13, v13, v16
v_mul_f32 v9, v9, v17
v_sub_f32 v80, v80, v16
v_sub_f32 v48, v48, v16
v_sub_f32 v81, v81, v16
v_sub_f32 v49, v49, v16
v_sub_f32 v82, v82, v16
v_sub_f32 v50, v50, v16
v_sub_f32 v83, v83, v16
v_sub_f32 v51, v51, v16
v_sub_f32 v84, v84, v16
v_sub_f32 v52, v52, v16
v_sub_f32 v85, v85, v16
v_sub_f32 v53, v53, v16
v_sub_f32 v86, v86, v16
v_sub_f32 v54, v54, v16
v_sub_f32 v87, v87, v16
v_sub_f32 v55, v55, v16
v_sub_f32 v88, v88, v16
v_sub_f32 v56, v56, v16
v_sub_f32 v89, v89, v16
v_sub_f32 v57, v57, v16
v_sub_f32 v90, v90, v16
v_sub_f32 v58, v58, v16
v_sub_f32 v91, v91, v16
v_sub_f32 v59, v59, v16
v_sub_f32 v92, v92, v16
v_sub_f32 v60, v60, v16
v_sub_f32 v93, v93, v16
v_sub_f32 v61, v61, v16
v_sub_f32 v94, v94, v16
v_sub_f32 v62, v62, v16
v_sub_f32 v95, v95, v16
v_sub_f32 v63, v63, v16
v_accvgpr_read_b32 v18, a64
v_mul_f32 v18, v18, v17
v_accvgpr_write_b32 a64, v18
v_accvgpr_read_b32 v18, a65
v_mul_f32 v18, v18, v17
v_accvgpr_write_b32 a65, v18
v_accvgpr_read_b32 v18, a66
v_mul_f32 v18, v18, v17
v_accvgpr_write_b32 a66, v18
v_accvgpr_read_b32 v18, a67
v_mul_f32 v18, v18, v17
v_accvgpr_write_b32 a67, v18
v_accvgpr_read_b32 v18, a68
v_mul_f32 v18, v18, v17
v_accvgpr_write_b32 a68, v18
v_accvgpr_read_b32 v18, a69
v_mul_f32 v18, v18, v17
v_accvgpr_write_b32 a69, v18
v_accvgpr_read_b32 v18, a70
v_mul_f32 v18, v18, v17
v_accvgpr_write_b32 a70, v18
v_accvgpr_read_b32 v18, a71
v_mul_f32 v18, v18, v17
v_accvgpr_write_b32 a71, v18
v_accvgpr_read_b32 v18, a72
v_mul_f32 v18, v18, v17
v_accvgpr_write_b32 a72, v18
v_accvgpr_read_b32 v18, a73
v_mul_f32 v18, v18, v17
v_accvgpr_write_b32 a73, v18
v_accvgpr_read_b32 v18, a74
v_mul_f32 v18, v18, v17
v_accvgpr_write_b32 a74, v18
v_accvgpr_read_b32 v18, a75
v_mul_f32 v18, v18, v17
v_accvgpr_write_b32 a75, v18
v_accvgpr_read_b32 v18, a76
v_mul_f32 v18, v18, v17
v_accvgpr_write_b32 a76, v18
v_accvgpr_read_b32 v18, a77
v_mul_f32 v18, v18, v17
v_accvgpr_write_b32 a77, v18
v_accvgpr_read_b32 v18, a78
v_mul_f32 v18, v18, v17
v_accvgpr_write_b32 a78, v18
v_accvgpr_read_b32 v18, a79
v_mul_f32 v18, v18, v17
v_accvgpr_write_b32 a79, v18
v_accvgpr_read_b32 v18, a80
v_mul_f32 v18, v18, v17
v_accvgpr_write_b32 a80, v18
v_accvgpr_read_b32 v18, a81
v_mul_f32 v18, v18, v17
v_accvgpr_write_b32 a81, v18
v_accvgpr_read_b32 v18, a82
v_mul_f32 v18, v18, v17
v_accvgpr_write_b32 a82, v18
v_accvgpr_read_b32 v18, a83
v_mul_f32 v18, v18, v17
v_accvgpr_write_b32 a83, v18
v_accvgpr_read_b32 v18, a84
v_mul_f32 v18, v18, v17
v_accvgpr_write_b32 a84, v18
v_accvgpr_read_b32 v18, a85
v_mul_f32 v18, v18, v17
v_accvgpr_write_b32 a85, v18
v_accvgpr_read_b32 v18, a86
v_mul_f32 v18, v18, v17
v_accvgpr_write_b32 a86, v18
v_accvgpr_read_b32 v18, a87
v_mul_f32 v18, v18, v17
v_accvgpr_write_b32 a87, v18
v_accvgpr_read_b32 v18, a88
v_mul_f32 v18, v18, v17
v_accvgpr_write_b32 a88, v18
v_accvgpr_read_b32 v18, a89
v_mul_f32 v18, v18, v17
v_accvgpr_write_b32 a89, v18
v_accvgpr_read_b32 v18, a90
v_mul_f32 v18, v18, v17
v_accvgpr_write_b32 a90, v18
v_accvgpr_read_b32 v18, a91
v_mul_f32 v18, v18, v17
v_accvgpr_write_b32 a91, v18
v_accvgpr_read_b32 v18, a92
v_mul_f32 v18, v18, v17
v_accvgpr_write_b32 a92, v18
v_accvgpr_read_b32 v18, a93
v_mul_f32 v18, v18, v17
v_accvgpr_write_b32 a93, v18
v_accvgpr_read_b32 v18, a94
v_mul_f32 v18, v18, v17
v_accvgpr_write_b32 a94, v18
v_accvgpr_read_b32 v18, a95
v_mul_f32 v18, v18, v17
v_accvgpr_write_b32 a95, v18
v_accvgpr_read_b32 v18, a96
v_mul_f32 v18, v18, v17
v_accvgpr_write_b32 a96, v18
v_accvgpr_read_b32 v18, a97
v_mul_f32 v18, v18, v17
v_accvgpr_write_b32 a97, v18
v_accvgpr_read_b32 v18, a98
v_mul_f32 v18, v18, v17
v_accvgpr_write_b32 a98, v18
v_accvgpr_read_b32 v18, a99
v_mul_f32 v18, v18, v17
v_accvgpr_write_b32 a99, v18
v_accvgpr_read_b32 v18, a100
v_mul_f32 v18, v18, v17
v_accvgpr_write_b32 a100, v18
v_accvgpr_read_b32 v18, a101
v_mul_f32 v18, v18, v17
v_accvgpr_write_b32 a101, v18
v_accvgpr_read_b32 v18, a102
v_mul_f32 v18, v18, v17
v_accvgpr_write_b32 a102, v18
v_accvgpr_read_b32 v18, a103
v_mul_f32 v18, v18, v17
v_accvgpr_write_b32 a103, v18
v_accvgpr_read_b32 v18, a104
v_mul_f32 v18, v18, v17
v_accvgpr_write_b32 a104, v18
v_accvgpr_read_b32 v18, a105
v_mul_f32 v18, v18, v17
v_accvgpr_write_b32 a105, v18
v_accvgpr_read_b32 v18, a106
v_mul_f32 v18, v18, v17
v_accvgpr_write_b32 a106, v18
v_accvgpr_read_b32 v18, a107
v_mul_f32 v18, v18, v17
v_accvgpr_write_b32 a107, v18
v_accvgpr_read_b32 v18, a108
v_mul_f32 v18, v18, v17
v_accvgpr_write_b32 a108, v18
v_accvgpr_read_b32 v18, a109
v_mul_f32 v18, v18, v17
v_accvgpr_write_b32 a109, v18
v_accvgpr_read_b32 v18, a110
v_mul_f32 v18, v18, v17
v_accvgpr_write_b32 a110, v18
v_accvgpr_read_b32 v18, a111
v_mul_f32 v18, v18, v17
v_accvgpr_write_b32 a111, v18
v_accvgpr_read_b32 v18, a112
v_mul_f32 v18, v18, v17
v_accvgpr_write_b32 a112, v18
v_accvgpr_read_b32 v18, a113
v_mul_f32 v18, v18, v17
v_accvgpr_write_b32 a113, v18
v_accvgpr_read_b32 v18, a114
v_mul_f32 v18, v18, v17
v_accvgpr_write_b32 a114, v18
v_accvgpr_read_b32 v18, a115
v_mul_f32 v18, v18, v17
v_accvgpr_write_b32 a115, v18
v_accvgpr_read_b32 v18, a116
v_mul_f32 v18, v18, v17
v_accvgpr_write_b32 a116, v18
v_accvgpr_read_b32 v18, a117
v_mul_f32 v18, v18, v17
v_accvgpr_write_b32 a117, v18
v_accvgpr_read_b32 v18, a118
v_mul_f32 v18, v18, v17
v_accvgpr_write_b32 a118, v18
v_accvgpr_read_b32 v18, a119
v_mul_f32 v18, v18, v17
v_accvgpr_write_b32 a119, v18
v_accvgpr_read_b32 v18, a120
v_mul_f32 v18, v18, v17
v_accvgpr_write_b32 a120, v18
v_accvgpr_read_b32 v18, a121
v_mul_f32 v18, v18, v17
v_accvgpr_write_b32 a121, v18
v_accvgpr_read_b32 v18, a122
v_mul_f32 v18, v18, v17
v_accvgpr_write_b32 a122, v18
v_accvgpr_read_b32 v18, a123
v_mul_f32 v18, v18, v17
v_accvgpr_write_b32 a123, v18
v_accvgpr_read_b32 v18, a124
v_mul_f32 v18, v18, v17
v_accvgpr_write_b32 a124, v18
v_accvgpr_read_b32 v18, a125
v_mul_f32 v18, v18, v17
v_accvgpr_write_b32 a125, v18
v_accvgpr_read_b32 v18, a126
v_mul_f32 v18, v18, v17
v_accvgpr_write_b32 a126, v18
v_accvgpr_read_b32 v18, a127
v_mul_f32 v18, v18, v17
v_accvgpr_write_b32 a127, v18
v_exp_f32 v18, v48
v_exp_f32 v19, v49
s_nop 0
v_cvt_pk_bf16_f32 v104, v18, v19
v_exp_f32 v18, v50
v_exp_f32 v19, v51
s_nop 0
v_cvt_pk_bf16_f32 v105, v18, v19
v_exp_f32 v18, v52
v_exp_f32 v19, v53
s_nop 0
v_cvt_pk_bf16_f32 v106, v18, v19
v_exp_f32 v18, v54
v_exp_f32 v19, v55
s_nop 0
v_cvt_pk_bf16_f32 v107, v18, v19
v_exp_f32 v18, v56
v_exp_f32 v19, v57
s_nop 0
v_cvt_pk_bf16_f32 v108, v18, v19
v_exp_f32 v18, v58
v_exp_f32 v19, v59
s_nop 0
v_cvt_pk_bf16_f32 v109, v18, v19
v_exp_f32 v18, v60
v_exp_f32 v19, v61
s_nop 0
v_cvt_pk_bf16_f32 v110, v18, v19
v_exp_f32 v18, v62
v_exp_f32 v19, v63
s_nop 0
v_cvt_pk_bf16_f32 v111, v18, v19
s_nop 1
v_mfma_f32_16x16x32_bf16 v[24:27], v[28:31], v[104:107], 0
s_nop 7
v_mfma_f32_16x16x32_bf16 v[24:27], v[28:31], v[108:111], v[24:27]
s_nop 6
s_add_u32 s32, s32, 4
s_addc_u32 s33, s33, 0
s_setpc_b64 s[32:33]
.LrareV0:
s_nop 7
s_nop 7
v_max3_f32 v16, v32, v33, v34
v_max3_f32 v16, v16, v35, v36
v_max3_f32 v16, v16, v37, v38
v_max3_f32 v16, v16, v39, v40
v_max3_f32 v16, v16, v41, v42
v_max3_f32 v16, v16, v43, v44
v_max3_f32 v16, v16, v45, v46
v_max_f32 v16, v16, v47
v_mov_b32 v17, v16
s_nop 1
v_permlane32_swap_b32 v16, v17
s_nop 1
v_max_f32 v16, v16, v17
v_max_f32 v16, s46, v16
v_max_f32 v17, 0, v16
v_exp_f32 v17, -v17
v_add_f32 v14, v14, v16
v_mul_f32 v10, v10, v17
v_sub_f32 v64, v64, v16
v_sub_f32 v32, v32, v16
v_sub_f32 v65, v65, v16
v_sub_f32 v33, v33, v16
v_sub_f32 v66, v66, v16
v_sub_f32 v34, v34, v16
v_sub_f32 v67, v67, v16
v_sub_f32 v35, v35, v16
v_sub_f32 v68, v68, v16
v_sub_f32 v36, v36, v16
v_sub_f32 v69, v69, v16
v_sub_f32 v37, v37, v16
v_sub_f32 v70, v70, v16
v_sub_f32 v38, v38, v16
v_sub_f32 v71, v71, v16
v_sub_f32 v39, v39, v16
v_sub_f32 v72, v72, v16
v_sub_f32 v40, v40, v16
v_sub_f32 v73, v73, v16
v_sub_f32 v41, v41, v16
v_sub_f32 v74, v74, v16
v_sub_f32 v42, v42, v16
v_sub_f32 v75, v75, v16
v_sub_f32 v43, v43, v16
v_sub_f32 v76, v76, v16
v_sub_f32 v44, v44, v16
v_sub_f32 v77, v77, v16
v_sub_f32 v45, v45, v16
v_sub_f32 v78, v78, v16
v_sub_f32 v46, v46, v16
v_sub_f32 v79, v79, v16
v_sub_f32 v47, v47, v16
v_mul_f32 v128, v128, v17
v_mul_f32 v129, v129, v17
v_mul_f32 v130, v130, v17
v_mul_f32 v131, v131, v17
v_mul_f32 v132, v132, v17
v_mul_f32 v133, v133, v17
v_mul_f32 v134, v134, v17
v_mul_f32 v135, v135, v17
v_mul_f32 v136, v136, v17
v_mul_f32 v137, v137, v17
v_mul_f32 v138, v138, v17
v_mul_f32 v139, v139, v17
v_mul_f32 v140, v140, v17
v_mul_f32 v141, v141, v17
v_mul_f32 v142, v142, v17
v_mul_f32 v143, v143, v17
v_mul_f32 v144, v144, v17
v_mul_f32 v145, v145, v17
v_mul_f32 v146, v146, v17
v_mul_f32 v147, v147, v17
v_mul_f32 v148, v148, v17
v_mul_f32 v149, v149, v17
v_mul_f32 v150, v150, v17
v_mul_f32 v151, v151, v17
v_mul_f32 v152, v152, v17
v_mul_f32 v153, v153, v17
v_mul_f32 v154, v154, v17
v_mul_f32 v155, v155, v17
v_mul_f32 v156, v156, v17
v_mul_f32 v157, v157, v17
v_mul_f32 v158, v158, v17
v_mul_f32 v159, v159, v17
v_mul_f32 v160, v160, v17
v_mul_f32 v161, v161, v17
v_mul_f32 v162, v162, v17
v_mul_f32 v163, v163, v17
v_mul_f32 v164, v164, v17
v_mul_f32 v165, v165, v17
v_mul_f32 v166, v166, v17
v_mul_f32 v167, v167, v17
v_mul_f32 v168, v168, v17
v_mul_f32 v169, v169, v17
v_mul_f32 v170, v170, v17
v_mul_f32 v171, v171, v17
v_mul_f32 v172, v172, v17
v_mul_f32 v173, v173, v17
v_mul_f32 v174, v174, v17
v_mul_f32 v175, v175, v17
v_mul_f32 v176, v176, v17
v_mul_f32 v177, v177, v17
v_mul_f32 v178, v178, v17
v_mul_f32 v179, v179, v17
v_mul_f32 v180, v180, v17
v_mul_f32 v181, v181, v17
v_mul_f32 v182, v182, v17
v_mul_f32 v183, v183, v17
v_mul_f32 v184, v184, v17
v_mul_f32 v185, v185, v17
v_mul_f32 v186, v186, v17
v_mul_f32 v187, v187, v17
v_mul_f32 v188, v188, v17
v_mul_f32 v189, v189, v17
v_mul_f32 v190, v190, v17
v_mul_f32 v191, v191, v17
v_exp_f32 v18, v32
v_exp_f32 v19, v33
s_nop 0
v_cvt_pk_bf16_f32 v96, v18, v19
v_exp_f32 v18, v34
v_exp_f32 v19, v35
s_nop 0
v_cvt_pk_bf16_f32 v97, v18, v19
v_exp_f32 v18, v36
v_exp_f32 v19, v37
s_nop 0
v_cvt_pk_bf16_f32 v98, v18, v19
v_exp_f32 v18, v38
v_exp_f32 v19, v39
s_nop 0
v_cvt_pk_bf16_f32 v99, v18, v19
v_exp_f32 v18, v40
v_exp_f32 v19, v41
s_nop 0
v_cvt_pk_bf16_f32 v100, v18, v19
v_exp_f32 v18, v42
v_exp_f32 v19, v43
s_nop 0
v_cvt_pk_bf16_f32 v101, v18, v19
v_exp_f32 v18, v44
v_exp_f32 v19, v45
s_nop 0
v_cvt_pk_bf16_f32 v102, v18, v19
v_exp_f32 v18, v46
v_exp_f32 v19, v47
s_nop 0
v_cvt_pk_bf16_f32 v103, v18, v19
s_nop 1
v_mfma_f32_16x16x32_bf16 v[24:27], v[28:31], v[96:99], 0
s_nop 7
v_mfma_f32_16x16x32_bf16 v[24:27], v[28:31], v[100:103], v[24:27]
s_nop 6
s_add_u32 s32, s32, 4
s_addc_u32 s33, s33, 0
s_setpc_b64 s[32:33]
.LrareV1:
s_nop 7
s_nop 7
v_max3_f32 v16, v48, v49, v50
v_max3_f32 v16, v16, v51, v52
v_max3_f32 v16, v16, v53, v54
v_max3_f32 v16, v16, v55, v56
v_max3_f32 v16, v16, v57, v58
v_max3_f32 v16, v16, v59, v60
v_max3_f32 v16, v16, v61, v62
v_max_f32 v16, v16, v63
v_mov_b32 v17, v16
s_nop 1
v_permlane32_swap_b32 v16, v17
s_nop 1
v_max_f32 v16, v16, v17
v_max_f32 v16, s46, v16
v_max_f32 v17, 0, v16
v_exp_f32 v17, -v17
v_add_f32 v15, v15, v16
v_mul_f32 v11, v11, v17
v_sub_f32 v80, v80, v16
v_sub_f32 v48, v48, v16
v_sub_f32 v81, v81, v16
v_sub_f32 v49, v49, v16
v_sub_f32 v82, v82, v16
v_sub_f32 v50, v50, v16
v_sub_f32 v83, v83, v16
v_sub_f32 v51, v51, v16
v_sub_f32 v84, v84, v16
v_sub_f32 v52, v52, v16
v_sub_f32 v85, v85, v16
v_sub_f32 v53, v53, v16
v_sub_f32 v86, v86, v16
v_sub_f32 v54, v54, v16
v_sub_f32 v87, v87, v16
v_sub_f32 v55, v55, v16
v_sub_f32 v88, v88, v16
v_sub_f32 v56, v56, v16
v_sub_f32 v89, v89, v16
v_sub_f32 v57, v57, v16
v_sub_f32 v90, v90, v16
v_sub_f32 v58, v58, v16
v_sub_f32 v91, v91, v16
v_sub_f32 v59, v59, v16
v_sub_f32 v92, v92, v16
v_sub_f32 v60, v60, v16
v_sub_f32 v93, v93, v16
v_sub_f32 v61, v61, v16
v_sub_f32 v94, v94, v16
v_sub_f32 v62, v62, v16
v_sub_f32 v95, v95, v16
v_sub_f32 v63, v63, v16
v_mul_f32 v192, v192, v17
v_mul_f32 v193, v193, v17
v_mul_f32 v194, v194, v17
v_mul_f32 v195, v195, v17
v_mul_f32 v196, v196, v17
v_mul_f32 v197, v197, v17
v_mul_f32 v198, v198, v17
v_mul_f32 v199, v199, v17
v_mul_f32 v200, v200, v17
v_mul_f32 v201, v201, v17
v_mul_f32 v202, v202, v17
v_mul_f32 v203, v203, v17
v_mul_f32 v204, v204, v17
v_mul_f32 v205, v205, v17
v_mul_f32 v206, v206, v17
v_mul_f32 v207, v207, v17
v_mul_f32 v208, v208, v17
v_mul_f32 v209, v209, v17
v_mul_f32 v210, v210, v17
v_mul_f32 v211, v211, v17
v_mul_f32 v212, v212, v17
v_mul_f32 v213, v213, v17
v_mul_f32 v214, v214, v17
v_mul_f32 v215, v215, v17
v_mul_f32 v216, v216, v17
v_mul_f32 v217, v217, v17
v_mul_f32 v218, v218, v17
v_mul_f32 v219, v219, v17
v_mul_f32 v220, v220, v17
v_mul_f32 v221, v221, v17
v_mul_f32 v222, v222, v17
v_mul_f32 v223, v223, v17
v_mul_f32 v224, v224, v17
v_mul_f32 v225, v225, v17
v_mul_f32 v226, v226, v17
v_mul_f32 v227, v227, v17
v_mul_f32 v228, v228, v17
v_mul_f32 v229, v229, v17
v_mul_f32 v230, v230, v17
v_mul_f32 v231, v231, v17
v_mul_f32 v232, v232, v17
v_mul_f32 v233, v233, v17
v_mul_f32 v234, v234, v17
v_mul_f32 v235, v235, v17
v_mul_f32 v236, v236, v17
v_mul_f32 v237, v237, v17
v_mul_f32 v238, v238, v17
v_mul_f32 v239, v239, v17
v_mul_f32 v240, v240, v17
v_mul_f32 v241, v241, v17
v_mul_f32 v242, v242, v17
v_mul_f32 v243, v243, v17
v_mul_f32 v244, v244, v17
v_mul_f32 v245, v245, v17
v_mul_f32 v246, v246, v17
v_mul_f32 v247, v247, v17
v_mul_f32 v248, v248, v17
v_mul_f32 v249, v249, v17
v_mul_f32 v250, v250, v17
v_mul_f32 v251, v251, v17
v_mul_f32 v252, v252, v17
v_mul_f32 v253, v253, v17
v_mul_f32 v254, v254, v17
v_mul_f32 v255, v255, v17
v_exp_f32 v18, v48
v_exp_f32 v19, v49
s_nop 0
v_cvt_pk_bf16_f32 v104, v18, v19
v_exp_f32 v18, v50
v_exp_f32 v19, v51
s_nop 0
v_cvt_pk_bf16_f32 v105, v18, v19
v_exp_f32 v18, v52
v_exp_f32 v19, v53
s_nop 0
v_cvt_pk_bf16_f32 v106, v18, v19
v_exp_f32 v18, v54
v_exp_f32 v19, v55
s_nop 0
v_cvt_pk_bf16_f32 v107, v18, v19
v_exp_f32 v18, v56
v_exp_f32 v19, v57
s_nop 0
v_cvt_pk_bf16_f32 v108, v18, v19
v_exp_f32 v18, v58
v_exp_f32 v19, v59
s_nop 0
v_cvt_pk_bf16_f32 v109, v18, v19
v_exp_f32 v18, v60
v_exp_f32 v19, v61
s_nop 0
v_cvt_pk_bf16_f32 v110, v18, v19
v_exp_f32 v18, v62
v_exp_f32 v19, v63
s_nop 0
v_cvt_pk_bf16_f32 v111, v18, v19
s_nop 1
v_mfma_f32_16x16x32_bf16 v[24:27], v[28:31], v[104:107], 0
s_nop 7
v_mfma_f32_16x16x32_bf16 v[24:27], v[28:31], v[108:111], v[24:27]
s_nop 6
s_add_u32 s32, s32, 4
s_addc_u32 s33, s33, 0
s_setpc_b64 s[32:33]
.Lsp0:
s_sub_u32 s41, s24, s25
s_ashr_i32 s41, s41, 13
s_add_i32 s47, s41, 2
s_sub_i32 s47, s47, s44
s_cmp_lt_i32 s47, 0
s_cbranch_scc1 .Lsp0_d
s_cmp_eq_i32 s47, 0
s_cbranch_scc1 .Lsp0_g
v_mov_b32 v64, v5
v_mov_b32 v65, v5
v_mov_b32 v66, v5
v_mov_b32 v67, v5
v_mov_b32 v68, v5
v_mov_b32 v69, v5
v_mov_b32 v70, v5
v_mov_b32 v71, v5
v_mov_b32 v72, v5
v_mov_b32 v73, v5
v_mov_b32 v74, v5
v_mov_b32 v75, v5
v_mov_b32 v76, v5
v_mov_b32 v77, v5
v_mov_b32 v78, v5
v_mov_b32 v79, v5
s_mov_b32 s42, -1
s_branch .Lsp0_d
.Lsp0_g:
v_cndmask_b32 v64, v64, v5, s[54:55]
v_cndmask_b32 v65, v65, v5, s[56:57]
v_cndmask_b32 v66, v66, v5, s[58:59]
v_cndmask_b32 v67, v67, v5, s[60:61]
v_cndmask_b32 v68, v68, v5, s[62:63]
v_cndmask_b32 v69, v69, v5, s[64:65]
v_cndmask_b32 v70, v70, v5, s[66:67]
v_cndmask_b32 v71, v71, v5, s[68:69]
v_cndmask_b32 v72, v72, v5, s[84:85]
v_cndmask_b32 v73, v73, v5, s[86:87]
v_cndmask_b32 v74, v74, v5, s[88:89]
v_cndmask_b32 v75, v75, v5, s[90:91]
v_cndmask_b32 v76, v76, v5, s[92:93]
v_cndmask_b32 v77, v77, v5, s[94:95]
v_cndmask_b32 v78, v78, v5, s[96:97]
v_cndmask_b32 v79, v79, v5, s[98:99]
s_cmp_eq_u32 s48, 32
s_cselect_b32 s42, s42, -1
.Lsp0_d:
s_add_u32 s30, s30, 4
s_addc_u32 s31, s31, 0
s_setpc_b64 s[30:31]
.Lsp1:
s_sub_u32 s41, s24, s25
s_ashr_i32 s41, s41, 13
s_add_i32 s47, s41, 2
s_sub_i32 s47, s47, s45
s_cmp_lt_i32 s47, 0
s_cbranch_scc1 .Lsp1_d
s_cmp_eq_i32 s47, 0
s_cbranch_scc1 .Lsp1_g
v_mov_b32 v80, v5
v_mov_b32 v81, v5
v_mov_b32 v82, v5
v_mov_b32 v83, v5
v_mov_b32 v84, v5
v_mov_b32 v85, v5
v_mov_b32 v86, v5
v_mov_b32 v87, v5
v_mov_b32 v88, v5
v_mov_b32 v89, v5
v_mov_b32 v90, v5
v_mov_b32 v91, v5
v_mov_b32 v92, v5
v_mov_b32 v93, v5
v_mov_b32 v94, v5
v_mov_b32 v95, v5
s_mov_b32 s43, -1
s_branch .Lsp1_d
.Lsp1_g:
v_cndmask_b32 v80, v80, v5, s[54:55]
v_cndmask_b32 v81, v81, v5, s[56:57]
v_cndmask_b32 v82, v82, v5, s[58:59]
v_cndmask_b32 v83, v83, v5, s[60:61]
v_cndmask_b32 v84, v84, v5, s[62:63]
v_cndmask_b32 v85, v85, v5, s[64:65]
v_cndmask_b32 v86, v86, v5, s[66:67]
v_cndmask_b32 v87, v87, v5, s[68:69]
v_cndmask_b32 v88, v88, v5, s[84:85]
v_cndmask_b32 v89, v89, v5, s[86:87]
v_cndmask_b32 v90, v90, v5, s[88:89]
v_cndmask_b32 v91, v91, v5, s[90:91]
v_cndmask_b32 v92, v92, v5, s[92:93]
v_cndmask_b32 v93, v93, v5, s[94:95]
v_cndmask_b32 v94, v94, v5, s[96:97]
v_cndmask_b32 v95, v95, v5, s[98:99]
s_cmp_eq_u32 s48, 32
s_cselect_b32 s43, s43, -1
.Lsp1_d:
s_add_u32 s30, s30, 4
s_addc_u32 s31, s31, 0
s_setpc_b64 s[30:31]
.Lend:
.Lk3s:
s_load_dwordx4 s[96:99], %14, 48
s_waitcnt vmcnt(8)
v_cmp_gt_u32 vcc, 1, v24
s_nop 3
s_cbranch_vccnz .LspW
.LspWb:
s_lshl_b32 s22, %2, 2
s_add_u32 s22, s22, s28
s_lshl_b32 s22, s22, 2
s_add_u32 s22, s22, 0x6306000
buffer_load_dwordx4 a[0:3], v2, s[16:19], s34 offen sc1
buffer_load_dwordx4 a[4:7], v3, s[16:19], s34 offen sc1
s_add_u32 s34, s34, 4096
buffer_load_dwordx4 a[8:11], v2, s[16:19], s34 offen sc1
buffer_load_dwordx4 a[12:15], v3, s[16:19], s34 offen sc1
s_add_u32 s34, s34, 4096
buffer_load_dwordx4 a[16:19], v2, s[16:19], s34 offen sc1
buffer_load_dwordx4 a[20:23], v3, s[16:19], s34 offen sc1
s_add_u32 s34, s34, 4096
buffer_load_dwordx4 a[24:27], v2, s[16:19], s34 offen sc1
buffer_load_dwordx4 a[28:31], v3, s[16:19], s34 offen sc1
s_add_u32 s34, s34, 4096
buffer_load_dwordx4 a[32:35], v2, s[16:19], s34 offen sc1
buffer_load_dwordx4 a[36:39], v3, s[16:19], s34 offen sc1
s_add_u32 s34, s34, 4096
buffer_load_dwordx4 a[40:43], v2, s[16:19], s34 offen sc1
buffer_load_dwordx4 a[44:47], v3, s[16:19], s34 offen sc1
s_waitcnt vmcnt(12)
s_barrier
buffer_store_dword v7, v5, s[16:19], s46 offen sc1
s_add_u32 s34, s34, 4096
buffer_load_dwordx4 a[48:51], v2, s[16:19], s34 offen sc1
buffer_load_dwordx4 a[52:55], v3, s[16:19], s34 offen sc1
s_add_u32 s34, s34, 4096
buffer_load_dwordx4 a[56:59], v2, s[16:19], s34 offen sc1
buffer_load_dwordx4 a[60:63], v3, s[16:19], s34 offen sc1
s_add_u32 s34, s34, 4096
buffer_load_dwordx4 a[64:67], v2, s[16:19], s34 offen sc1
buffer_load_dwordx4 a[68:71], v3, s[16:19], s34 offen sc1
s_add_u32 s34, s34, 4096
buffer_load_dwordx4 a[72:75], v2, s[16:19], s34 offen sc1
buffer_load_dwordx4 a[76:79], v3, s[16:19], s34 offen sc1
s_add_u32 s34, s34, 4096
buffer_load_dwordx4 a[80:83], v2, s[16:19], s34 offen sc1
buffer_load_dwordx4 a[84:87], v3, s[16:19], s34 offen sc1
s_add_u32 s34, s34, 4096
buffer_load_dwordx4 a[88:91], v2, s[16:19], s34 offen sc1
buffer_load_dwordx4 a[92:95], v3, s[16:19], s34 offen sc1
s_add_u32 s34, s34, 4096
buffer_load_dwordx4 a[96:99], v2, s[16:19], s34 offen sc1
buffer_load_dwordx4 a[100:103], v3, s[16:19], s34 offen sc1
s_add_u32 s34, s34, 4096
buffer_load_dwordx4 a[104:107], v2, s[16:19], s34 offen sc1
buffer_load_dwordx4 a[108:111], v3, s[16:19], s34 offen sc1
s_add_u32 s34, s34, 4096
buffer_load_dwordx4 a[112:115], v2, s[16:19], s34 offen sc1
buffer_load_dwordx4 a[116:119], v3, s[16:19], s34 offen sc1
s_add_u32 s34, s34, 4096
buffer_load_dwordx4 a[120:123], v2, s[16:19], s34 offen sc1
buffer_load_dwordx4 a[124:127], v3, s[16:19], s34 offen sc1
s_add_u32 s34, s34, 4096
buffer_load_dwordx4 a[128:131], v2, s[16:19], s34 offen sc1
buffer_load_dwordx4 a[132:135], v3, s[16:19], s34 offen sc1
s_add_u32 s34, s34, 4096
buffer_load_dwordx4 a[136:139], v2, s[16:19], s34 offen sc1
buffer_load_dwordx4 a[140:143], v3, s[16:19], s34 offen sc1
s_add_u32 s34, s34, 4096
buffer_load_dwordx4 a[144:147], v2, s[16:19], s34 offen sc1
buffer_load_dwordx4 a[148:151], v3, s[16:19], s34 offen sc1
s_add_u32 s34, s34, 4096
buffer_load_dwordx4 a[152:155], v2, s[16:19], s34 offen sc1
buffer_load_dwordx4 a[156:159], v3, s[16:19], s34 offen sc1
s_add_u32 s34, s34, 4096
buffer_load_dwordx4 a[160:163], v2, s[16:19], s34 offen sc1
buffer_load_dwordx4 a[164:167], v3, s[16:19], s34 offen sc1
s_add_u32 s34, s34, 4096
buffer_load_dwordx4 a[168:171], v2, s[16:19], s34 offen sc1
buffer_load_dwordx4 a[172:175], v3, s[16:19], s34 offen sc1
s_add_u32 s34, s34, 4096
buffer_load_dwordx4 a[176:179], v2, s[16:19], s34 offen sc1
buffer_load_dwordx4 a[180:183], v3, s[16:19], s34 offen sc1
s_add_u32 s34, s34, 4096
buffer_load_dwordx4 a[184:187], v2, s[16:19], s34 offen sc1
buffer_load_dwordx4 a[188:191], v3, s[16:19], s34 offen sc1
s_add_u32 s34, s34, 4096
buffer_load_dwordx4 a[192:195], v2, s[16:19], s34 offen sc1
buffer_load_dwordx4 a[196:199], v3, s[16:19], s34 offen sc1
s_add_u32 s34, s34, 4096
buffer_load_dwordx4 a[200:203], v2, s[16:19], s34 offen sc1
buffer_load_dwordx4 a[204:207], v3, s[16:19], s34 offen sc1
buffer_load_dword v28, v4, s[16:19], s31 offen sc1
s_add_u32 s34, s34, 4096
buffer_load_dwordx4 a[208:211], v2, s[16:19], s34 offen sc1
buffer_load_dwordx4 a[212:215], v3, s[16:19], s34 offen sc1
s_add_u32 s34, s34, 4096
buffer_load_dwordx4 a[216:219], v2, s[16:19], s34 offen sc1
buffer_load_dwordx4 a[220:223], v3, s[16:19], s34 offen sc1
s_add_u32 s34, s34, 4096
buffer_load_dwordx4 a[224:227], v2, s[16:19], s34 offen sc1
buffer_load_dwordx4 a[228:231], v3, s[16:19], s34 offen sc1
s_add_u32 s34, s34, 4096
buffer_load_dwordx4 a[232:235], v2, s[16:19], s34 offen sc1
buffer_load_dwordx4 a[236:239], v3, s[16:19], s34 offen sc1
buffer_load_dword v27, v4, s[16:19], s31 offen sc1
s_add_u32 s34, s34, 4096
buffer_load_dwordx4 a[240:243], v2, s[16:19], s34 offen sc1
buffer_load_dwordx4 a[244:247], v3, s[16:19], s34 offen sc1
s_add_u32 s34, s34, 4096
buffer_load_dwordx4 a[248:251], v2, s[16:19], s34 offen sc1
buffer_load_dwordx4 a[252:255], v3, s[16:19], s34 offen sc1
s_add_u32 s34, s34, 4096
buffer_load_dwordx4 v[104:107], v2, s[16:19], s34 offen sc1
buffer_load_dwordx4 v[108:111], v3, s[16:19], s34 offen sc1
s_add_u32 s34, s34, 4096
buffer_load_dwordx4 v[112:115], v2, s[16:19], s34 offen sc1
buffer_load_dwordx4 v[116:119], v3, s[16:19], s34 offen sc1
buffer_load_dword v26, v4, s[16:19], s31 offen sc1
s_add_u32 s34, s34, 4096
buffer_load_dwordx4 v[120:123], v2, s[16:19], s34 offen sc1
buffer_load_dwordx4 v[124:127], v3, s[16:19], s34 offen sc1
s_add_u32 s34, s34, 4096
buffer_load_dwordx4 v[128:131], v2, s[16:19], s34 offen sc1
buffer_load_dwordx4 v[132:135], v3, s[16:19], s34 offen sc1
s_add_u32 s34, s34, 4096
buffer_load_dwordx4 v[136:139], v2, s[16:19], s34 offen sc1
buffer_load_dwordx4 v[140:143], v3, s[16:19], s34 offen sc1
s_add_u32 s34, s34, 4096
buffer_load_dwordx4 v[144:147], v2, s[16:19], s34 offen sc1
buffer_load_dwordx4 v[148:151], v3, s[16:19], s34 offen sc1
buffer_load_dword v25, v4, s[16:19], s31 offen sc1
s_waitcnt lgkmcnt(0)
s_mov_b32 s24, s96
s_and_b32 s25, s97, 0xffff
s_mov_b32 s26, 0x200000
s_mov_b32 s27, 0x20000
s_waitcnt vmcnt(27)
v_cmp_gt_u32 vcc, 3, v28
s_nop 3
s_cbranch_vccnz .LspA1
.LspAb:
buffer_load_dwordx4 v[152:155], v1, s[16:19], s32 offen offset:0 sc1
buffer_load_dwordx4 v[156:159], v1, s[16:19], s33 offen offset:0 sc1
buffer_load_dwordx4 v[160:163], v1, s[16:19], s32 offen offset:1024 sc1
buffer_load_dwordx4 v[164:167], v1, s[16:19], s33 offen offset:1024 sc1
buffer_load_dwordx4 v[168:171], v1, s[16:19], s32 offen offset:2048 sc1
buffer_load_dwordx4 v[172:175], v1, s[16:19], s33 offen offset:2048 sc1
buffer_load_dwordx4 v[176:179], v1, s[16:19], s32 offen offset:3072 sc1
buffer_load_dwordx4 v[180:183], v1, s[16:19], s33 offen offset:3072 sc1
s_add_u32 s32, s32, 4096
s_add_u32 s33, s33, 4096
buffer_load_dwordx4 v[184:187], v1, s[16:19], s32 offen offset:0 sc1
buffer_load_dwordx4 v[188:191], v1, s[16:19], s33 offen offset:0 sc1
buffer_load_dwordx4 v[192:195], v1, s[16:19], s32 offen offset:1024 sc1
buffer_load_dwordx4 v[196:199], v1, s[16:19], s33 offen offset:1024 sc1
buffer_load_dwordx4 v[200:203], v1, s[16:19], s32 offen offset:2048 sc1
buffer_load_dwordx4 v[204:207], v1, s[16:19], s33 offen offset:2048 sc1
buffer_load_dwordx4 v[208:211], v1, s[16:19], s32 offen offset:3072 sc1
buffer_load_dwordx4 v[212:215], v1, s[16:19], s33 offen offset:3072 sc1
s_add_u32 s32, s32, 4096
s_add_u32 s33, s33, 4096
buffer_load_dwordx4 v[216:219], v1, s[16:19], s32 offen offset:0 sc1
buffer_load_dwordx4 v[220:223], v1, s[16:19], s33 offen offset:0 sc1
buffer_load_dwordx4 v[224:227], v1, s[16:19], s32 offen offset:1024 sc1
buffer_load_dwordx4 v[228:231], v1, s[16:19], s33 offen offset:1024 sc1
buffer_load_dwordx4 v[232:235], v1, s[16:19], s32 offen offset:2048 sc1
buffer_load_dwordx4 v[236:239], v1, s[16:19], s33 offen offset:2048 sc1
s_and_b32 s29, %2, 7
s_lshl_b32 s29, s29, 7
s_lshr_b32 s30, %2, 3
s_lshl_b32 s31, s30, 2
s_add_u32 s46, s29, s31
s_add_u32 s46, s46, 0x6302000
s_waitcnt vmcnt(21)
v_mfma_f32_32x32x16_bf16 v[32:47], a[0:3], v[152:155], 0
s_waitcnt vmcnt(20)
v_mfma_f32_32x32x16_bf16 v[48:63], a[0:3], v[156:159], 0
v_mfma_f32_32x32x16_bf16 v[64:79], a[4:7], v[152:155], 0
v_mfma_f32_32x32x16_bf16 v[80:95], a[4:7], v[156:159], 0
buffer_load_dwordx4 v[240:243], v1, s[16:19], s32 offen offset:3072 sc1
buffer_load_dwordx4 v[244:247], v1, s[16:19], s33 offen offset:3072 sc1
s_add_u32 s34, s34, 4096
buffer_load_dwordx4 v[248:251], v2, s[16:19], s34 offen sc1
buffer_load_dwordx4 v[252:255], v3, s[16:19], s34 offen sc1
s_waitcnt vmcnt(23)
v_mfma_f32_32x32x16_bf16 v[32:47], a[8:11], v[160:163], v[32:47]
s_waitcnt vmcnt(22)
v_mfma_f32_32x32x16_bf16 v[48:63], a[8:11], v[164:167], v[48:63]
v_mfma_f32_32x32x16_bf16 v[64:79], a[12:15], v[160:163], v[64:79]
v_mfma_f32_32x32x16_bf16 v[80:95], a[12:15], v[164:167], v[80:95]
s_add_u32 s32, s32, 4096
s_add_u32 s33, s33, 4096
buffer_load_dwordx4 a[0:3], v1, s[16:19], s32 offen offset:0 sc1
buffer_load_dwordx4 a[4:7], v1, s[16:19], s33 offen offset:0 sc1
s_add_u32 s34, s34, 4096
buffer_load_dwordx4 v[152:155], v2, s[16:19], s34 offen sc1
buffer_load_dwordx4 v[156:159], v3, s[16:19], s34 offen sc1
s_waitcnt vmcnt(25)
v_mfma_f32_32x32x16_bf16 v[32:47], a[16:19], v[168:171], v[32:47]
s_waitcnt vmcnt(24)
v_mfma_f32_32x32x16_bf16 v[48:63], a[16:19], v[172:175], v[48:63]
v_mfma_f32_32x32x16_bf16 v[64:79], a[20:23], v[168:171], v[64:79]
v_mfma_f32_32x32x16_bf16 v[80:95], a[20:23], v[172:175], v[80:95]
buffer_load_dwordx4 a[8:11], v1, s[16:19], s32 offen offset:1024 sc1
buffer_load_dwordx4 a[12:15], v1, s[16:19], s33 offen offset:1024 sc1
s_waitcnt vmcnt(25)
v_mfma_f32_32x32x16_bf16 v[32:47], a[24:27], v[176:179], v[32:47]
s_waitcnt vmcnt(24)
v_mfma_f32_32x32x16_bf16 v[48:63], a[24:27], v[180:183], v[48:63]
v_mfma_f32_32x32x16_bf16 v[64:79], a[28:31], v[176:179], v[64:79]
v_mfma_f32_32x32x16_bf16 v[80:95], a[28:31], v[180:183], v[80:95]
buffer_load_dwordx4 v[160:163], v1, s[16:19], s32 offen offset:2048 sc1
buffer_load_dwordx4 v[164:167], v1, s[16:19], s33 offen offset:2048 sc1
s_add_u32 s34, s34, 4096
buffer_load_dwordx4 a[16:19], v2, s[16:19], s34 offen sc1
buffer_load_dwordx4 a[20:23], v3, s[16:19], s34 offen sc1
s_waitcnt vmcnt(27)
v_mfma_f32_32x32x16_bf16 v[32:47], a[32:35], v[184:187], v[32:47]
s_waitcnt vmcnt(26)
v_mfma_f32_32x32x16_bf16 v[48:63], a[32:35], v[188:191], v[48:63]
v_mfma_f32_32x32x16_bf16 v[64:79], a[36:39], v[184:187], v[64:79]
v_mfma_f32_32x32x16_bf16 v[80:95], a[36:39], v[188:191], v[80:95]
buffer_load_dwordx4 v[168:171], v1, s[16:19], s32 offen offset:3072 sc1
buffer_load_dwordx4 v[172:175], v1, s[16:19], s33 offen offset:3072 sc1
s_add_u32 s34, s34, 4096
buffer_load_dwordx4 a[24:27], v2, s[16:19], s34 offen sc1
buffer_load_dwordx4 a[28:31], v3, s[16:19], s34 offen sc1
s_waitcnt vmcnt(29)
v_mfma_f32_32x32x16_bf16 v[32:47], a[40:43], v[192:195], v[32:47]
s_waitcnt vmcnt(28)
v_mfma_f32_32x32x16_bf16 v[48:63], a[40:43], v[196:199], v[48:63]
v_mfma_f32_32x32x16_bf16 v[64:79], a[44:47], v[192:195], v[64:79]
v_mfma_f32_32x32x16_bf16 v[80:95], a[44:47], v[196:199], v[80:95]
s_add_u32 s32, s32, 4096
s_add_u32 s33, s33, 4096
buffer_load_dwordx4 v[176:179], v1, s[16:19], s32 offen offset:0 sc1
buffer_load_dwordx4 v[180:183], v1, s[16:19], s33 offen offset:0 sc1
s_waitcnt vmcnt(29)
v_mfma_f32_32x32x16_bf16 v[32:47], a[48:51], v[200:203], v[32:47]
s_waitcnt vmcnt(28)
v_mfma_f32_32x32x16_bf16 v[48:63], a[48:51], v[204:207], v[48:63]
v_mfma_f32_32x32x16_bf16 v[64:79], a[52:55], v[200:203], v[64:79]
v_mfma_f32_32x32x16_bf16 v[80:95], a[52:55], v[204:207], v[80:95]
buffer_load_dwordx4 a[32:35], v1, s[16:19], s32 offen offset:1024 sc1
buffer_load_dwordx4 a[36:39], v1, s[16:19], s33 offen offset:1024 sc1
s_add_u32 s34, s34, 4096
buffer_load_dwordx4 v[184:187], v2, s[16:19], s34 offen sc1
buffer_load_dwordx4 v[188:191], v3, s[16:19], s34 offen sc1
s_waitcnt vmcnt(31)
v_mfma_f32_32x32x16_bf16 v[32:47], a[56:59], v[208:211], v[32:47]
s_waitcnt vmcnt(30)
v_mfma_f32_32x32x16_bf16 v[48:63], a[56:59], v[212:215], v[48:63]
v_mfma_f32_32x32x16_bf16 v[64:79], a[60:63], v[208:211], v[64:79]
v_mfma_f32_32x32x16_bf16 v[80:95], a[60:63], v[212:215], v[80:95]
buffer_load_dwordx4 a[40:43], v1, s[16:19], s32 offen offset:2048 sc1
buffer_load_dwordx4 a[44:47], v1, s[16:19], s33 offen offset:2048 sc1
s_add_u32 s34, s34, 4096
buffer_load_dwordx4 v[192:195], v2, s[16:19], s34 offen sc1
buffer_load_dwordx4 v[196:199], v3, s[16:19], s34 offen sc1
s_waitcnt vmcnt(33)
v_mfma_f32_32x32x16_bf16 v[32:47], a[64:67], v[216:219], v[32:47]
s_waitcnt vmcnt(32)
v_mfma_f32_32x32x16_bf16 v[48:63], a[64:67], v[220:223], v[48:63]
v_mfma_f32_32x32x16_bf16 v[64:79], a[68:71], v[216:219], v[64:79]
v_mfma_f32_32x32x16_bf16 v[80:95], a[68:71], v[220:223], v[80:95]
buffer_load_dwordx4 a[48:51], v1, s[16:19], s32 offen offset:3072 sc1
buffer_load_dwordx4 a[52:55], v1, s[16:19], s33 offen offset:3072 sc1
s_waitcnt vmcnt(33)
v_mfma_f32_32x32x16_bf16 v[32:47], a[72:75], v[224:227], v[32:47]
s_waitcnt vmcnt(32)
v_mfma_f32_32x32x16_bf16 v[48:63], a[72:75], v[228:231], v[48:63]
v_mfma_f32_32x32x16_bf16 v[64:79], a[76:79], v[224:227], v[64:79]
v_mfma_f32_32x32x16_bf16 v[80:95], a[76:79], v[228:231], v[80:95]
s_add_u32 s32, s32, 4096
s_add_u32 s33, s33, 4096
buffer_load_dwordx4 v[200:203], v1, s[16:19], s32 offen offset:0 sc1
buffer_load_dwordx4 v[204:207], v1, s[16:19], s33 offen offset:0 sc1
s_add_u32 s34, s34, 4096
buffer_load_dwordx4 a[56:59], v2, s[16:19], s34 offen sc1
buffer_load_dwordx4 a[60:63], v3, s[16:19], s34 offen sc1
s_waitcnt vmcnt(35)
v_mfma_f32_32x32x16_bf16 v[32:47], a[80:83], v[232:235], v[32:47]
s_waitcnt vmcnt(34)
v_mfma_f32_32x32x16_bf16 v[48:63], a[80:83], v[236:239], v[48:63]
v_mfma_f32_32x32x16_bf16 v[64:79], a[84:87], v[232:235], v[64:79]
v_mfma_f32_32x32x16_bf16 v[80:95], a[84:87], v[236:239], v[80:95]
buffer_load_dwordx4 v[208:211], v1, s[16:19], s32 offen offset:1024 sc1
buffer_load_dwordx4 v[212:215], v1, s[16:19], s33 offen offset:1024 sc1
s_add_u32 s34, s34, 4096
buffer_load_dwordx4 a[64:67], v2, s[16:19], s34 offen sc1
buffer_load_dwordx4 a[68:71], v3, s[16:19], s34 offen sc1
s_waitcnt vmcnt(37)
v_mfma_f32_32x32x16_bf16 v[32:47], a[88:91], v[240:243], v[32:47]
s_waitcnt vmcnt(36)
v_mfma_f32_32x32x16_bf16 v[48:63], a[88:91], v[244:247], v[48:63]
v_mfma_f32_32x32x16_bf16 v[64:79], a[92:95], v[240:243], v[64:79]
v_mfma_f32_32x32x16_bf16 v[80:95], a[92:95], v[244:247], v[80:95]
buffer_load_dwordx4 v[216:219], v1, s[16:19], s32 offen offset:2048 sc1
buffer_load_dwordx4 v[220:223], v1, s[16:19], s33 offen offset:2048 sc1
s_waitcnt vmcnt(35)
v_mfma_f32_32x32x16_bf16 v[32:47], a[96:99], a[0:3], v[32:47]
s_waitcnt vmcnt(34)
v_mfma_f32_32x32x16_bf16 v[48:63], a[96:99], a[4:7], v[48:63]
v_mfma_f32_32x32x16_bf16 v[64:79], a[100:103], a[0:3], v[64:79]
v_mfma_f32_32x32x16_bf16 v[80:95], a[100:103], a[4:7], v[80:95]
buffer_load_dwordx4 a[72:75], v1, s[16:19], s32 offen offset:3072 sc1
buffer_load_dwordx4 a[76:79], v1, s[16:19], s33 offen offset:3072 sc1
s_add_u32 s34, s34, 4096
buffer_load_dwordx4 v[224:227], v2, s[16:19], s34 offen sc1
buffer_load_dwordx4 v[228:231], v3, s[16:19], s34 offen sc1
s_waitcnt vmcnt(35)
v_mfma_f32_32x32x16_bf16 v[32:47], a[104:107], a[8:11], v[32:47]
s_waitcnt vmcnt(34)
v_mfma_f32_32x32x16_bf16 v[48:63], a[104:107], a[12:15], v[48:63]
v_mfma_f32_32x32x16_bf16 v[64:79], a[108:111], a[8:11], v[64:79]
v_mfma_f32_32x32x16_bf16 v[80:95], a[108:111], a[12:15], v[80:95]
s_add_u32 s32, s32, 4096
s_add_u32 s33, s33, 4096
buffer_load_dwordx4 a[80:83], v1, s[16:19], s32 offen offset:0 sc1
buffer_load_dwordx4 a[84:87], v1, s[16:19], s33 offen offset:0 sc1
s_add_u32 s34, s34, 4096
buffer_load_dwordx4 v[232:235], v2, s[16:19], s34 offen sc1
buffer_load_dwordx4 v[236:239], v3, s[16:19], s34 offen sc1
s_waitcnt vmcnt(37)
v_mfma_f32_32x32x16_bf16 v[32:47], a[112:115], v[160:163], v[32:47]
s_waitcnt vmcnt(36)
v_mfma_f32_32x32x16_bf16 v[48:63], a[112:115], v[164:167], v[48:63]
v_mfma_f32_32x32x16_bf16 v[64:79], a[116:119], v[160:163], v[64:79]
v_mfma_f32_32x32x16_bf16 v[80:95], a[116:119], v[164:167], v[80:95]
buffer_load_dwordx4 a[88:91], v1, s[16:19], s32 offen offset:1024 sc1
buffer_load_dwordx4 a[92:95], v1, s[16:19], s33 offen offset:1024 sc1
s_waitcnt vmcnt(35)
v_mfma_f32_32x32x16_bf16 v[32:47], a[120:123], v[168:171], v[32:47]
s_waitcnt vmcnt(34)
v_mfma_f32_32x32x16_bf16 v[48:63], a[120:123], v[172:175], v[48:63]
v_mfma_f32_32x32x16_bf16 v[64:79], a[124:127], v[168:171], v[64:79]
v_mfma_f32_32x32x16_bf16 v[80:95], a[124:127], v[172:175], v[80:95]
buffer_load_dwordx4 v[240:243], v1, s[16:19], s32 offen offset:2048 sc1
buffer_load_dwordx4 v[244:247], v1, s[16:19], s33 offen offset:2048 sc1
s_add_u32 s34, s34, 4096
buffer_load_dwordx4 a[96:99], v2, s[16:19], s34 offen sc1
buffer_load_dwordx4 a[100:103], v3, s[16:19], s34 offen sc1
s_waitcnt vmcnt(35)
v_mfma_f32_32x32x16_bf16 v[32:47], a[128:131], v[176:179], v[32:47]
s_waitcnt vmcnt(34)
v_mfma_f32_32x32x16_bf16 v[48:63], a[128:131], v[180:183], v[48:63]
v_mfma_f32_32x32x16_bf16 v[64:79], a[132:135], v[176:179], v[64:79]
v_mfma_f32_32x32x16_bf16 v[80:95], a[132:135], v[180:183], v[80:95]
buffer_load_dwordx4 a[0:3], v1, s[16:19], s32 offen offset:3072 sc1
buffer_load_dwordx4 a[4:7], v1, s[16:19], s33 offen offset:3072 sc1
s_add_u32 s34, s34, 4096
buffer_load_dwordx4 a[104:107], v2, s[16:19], s34 offen sc1
buffer_load_dwordx4 a[108:111], v3, s[16:19], s34 offen sc1
s_waitcnt vmcnt(37)
v_mfma_f32_32x32x16_bf16 v[32:47], a[136:139], a[32:35], v[32:47]
s_waitcnt vmcnt(36)
v_mfma_f32_32x32x16_bf16 v[48:63], a[136:139], a[36:39], v[48:63]
v_mfma_f32_32x32x16_bf16 v[64:79], a[140:143], a[32:35], v[64:79]
v_mfma_f32_32x32x16_bf16 v[80:95], a[140:143], a[36:39], v[80:95]
s_add_u32 s32, s32, 4096
s_add_u32 s33, s33, 4096
buffer_load_dwordx4 a[8:11], v1, s[16:19], s32 offen offset:0 sc1
buffer_load_dwordx4 a[12:15], v1, s[16:19], s33 offen offset:0 sc1
s_waitcnt vmcnt(35)
v_mfma_f32_32x32x16_bf16 v[32:47], a[144:147], a[40:43], v[32:47]
s_waitcnt vmcnt(34)
v_mfma_f32_32x32x16_bf16 v[48:63], a[144:147], a[44:47], v[48:63]
v_mfma_f32_32x32x16_bf16 v[64:79], a[148:151], a[40:43], v[64:79]
v_mfma_f32_32x32x16_bf16 v[80:95], a[148:151], a[44:47], v[80:95]
buffer_load_dwordx4 a[112:115], v1, s[16:19], s32 offen offset:1024 sc1
buffer_load_dwordx4 a[116:119], v1, s[16:19], s33 offen offset:1024 sc1
s_add_u32 s34, s34, 4096
buffer_load_dwordx4 v[160:163], v2, s[16:19], s34 offen sc1
buffer_load_dwordx4 v[164:167], v3, s[16:19], s34 offen sc1
s_waitcnt vmcnt(35)
v_mfma_f32_32x32x16_bf16 v[32:47], a[152:155], a[48:51], v[32:47]
s_waitcnt vmcnt(34)
v_mfma_f32_32x32x16_bf16 v[48:63], a[152:155], a[52:55], v[48:63]
v_mfma_f32_32x32x16_bf16 v[64:79], a[156:159], a[48:51], v[64:79]
v_mfma_f32_32x32x16_bf16 v[80:95], a[156:159], a[52:55], v[80:95]
buffer_load_dwordx4 a[120:123], v1, s[16:19], s32 offen offset:2048 sc1
buffer_load_dwordx4 a[124:127], v1, s[16:19], s33 offen offset:2048 sc1
s_waitcnt vmcnt(35)
v_mfma_f32_32x32x16_bf16 v[32:47], a[160:163], v[200:203], v[32:47]
s_waitcnt vmcnt(34)
v_mfma_f32_32x32x16_bf16 v[48:63], a[160:163], v[204:207], v[48:63]
v_mfma_f32_32x32x16_bf16 v[64:79], a[164:167], v[200:203], v[64:79]
v_mfma_f32_32x32x16_bf16 v[80:95], a[164:167], v[204:207], v[80:95]
buffer_load_dwordx4 v[168:171], v1, s[16:19], s32 offen offset:3072 sc1
buffer_load_dwordx4 v[172:175], v1, s[16:19], s33 offen offset:3072 sc1
s_add_u32 s34, s34, 4096
buffer_load_dwordx4 a[128:131], v2, s[16:19], s34 offen sc1
buffer_load_dwordx4 a[132:135], v3, s[16:19], s34 offen sc1
s_waitcnt vmcnt(35)
v_mfma_f32_32x32x16_bf16 v[32:47], a[168:171], v[208:211], v[32:47]
s_waitcnt vmcnt(34)
v_mfma_f32_32x32x16_bf16 v[48:63], a[168:171], v[212:215], v[48:63]
v_mfma_f32_32x32x16_bf16 v[64:79], a[172:175], v[208:211], v[64:79]
v_mfma_f32_32x32x16_bf16 v[80:95], a[172:175], v[212:215], v[80:95]
s_add_u32 s32, s32, 4096
s_add_u32 s33, s33, 4096
buffer_load_dwordx4 v[176:179], v1, s[16:19], s32 offen offset:0 sc1
buffer_load_dwordx4 v[180:183], v1, s[16:19], s33 offen offset:0 sc1
s_add_u32 s34, s34, 4096
buffer_load_dwordx4 a[136:139], v2, s[16:19], s34 offen sc1
buffer_load_dwordx4 a[140:143], v3, s[16:19], s34 offen sc1
s_waitcnt vmcnt(35)
v_mfma_f32_32x32x16_bf16 v[32:47], a[176:179], v[216:219], v[32:47]
s_waitcnt vmcnt(34)
v_mfma_f32_32x32x16_bf16 v[48:63], a[176:179], v[220:223], v[48:63]
v_mfma_f32_32x32x16_bf16 v[64:79], a[180:183], v[216:219], v[64:79]
v_mfma_f32_32x32x16_bf16 v[80:95], a[180:183], v[220:223], v[80:95]
buffer_load_dwordx4 a[32:35], v1, s[16:19], s32 offen offset:1024 sc1
buffer_load_dwordx4 a[36:39], v1, s[16:19], s33 offen offset:1024 sc1
s_waitcnt vmcnt(35)
v_mfma_f32_32x32x16_bf16 v[32:47], a[184:187], a[72:75], v[32:47]
s_waitcnt vmcnt(34)
v_mfma_f32_32x32x16_bf16 v[48:63], a[184:187], a[76:79], v[48:63]
v_mfma_f32_32x32x16_bf16 v[64:79], a[188:191], a[72:75], v[64:79]
v_mfma_f32_32x32x16_bf16 v[80:95], a[188:191], a[76:79], v[80:95]
buffer_load_dwordx4 a[144:147], v1, s[16:19], s32 offen offset:2048 sc1
buffer_load_dwordx4 a[148:151], v1, s[16:19], s33 offen offset:2048 sc1
s_add_u32 s34, s34, 4096
buffer_load_dwordx4 a[40:43], v2, s[16:19], s34 offen sc1
buffer_load_dwordx4 a[44:47], v3, s[16:19], s34 offen sc1
s_waitcnt vmcnt(35)
v_mfma_f32_32x32x16_bf16 v[32:47], a[192:195], a[80:83], v[32:47]
s_waitcnt vmcnt(34)
v_mfma_f32_32x32x16_bf16 v[48:63], a[192:195], a[84:87], v[48:63]
v_mfma_f32_32x32x16_bf16 v[64:79], a[196:199], a[80:83], v[64:79]
v_mfma_f32_32x32x16_bf16 v[80:95], a[196:199], a[84:87], v[80:95]
buffer_load_dwordx4 a[152:155], v1, s[16:19], s32 offen offset:3072 sc1
buffer_load_dwordx4 a[156:159], v1, s[16:19], s33 offen offset:3072 sc1
s_add_u32 s34, s34, 4096
buffer_load_dwordx4 a[48:51], v2, s[16:19], s34 offen sc1
buffer_load_dwordx4 a[52:55], v3, s[16:19], s34 offen sc1
s_waitcnt vmcnt(35)
v_mfma_f32_32x32x16_bf16 v[32:47], a[200:203], a[88:91], v[32:47]
s_waitcnt vmcnt(34)
v_mfma_f32_32x32x16_bf16 v[48:63], a[200:203], a[92:95], v[48:63]
v_mfma_f32_32x32x16_bf16 v[64:79], a[204:207], a[88:91], v[64:79]
v_mfma_f32_32x32x16_bf16 v[80:95], a[204:207], a[92:95], v[80:95]
s_add_u32 s32, s32, 4096
s_add_u32 s33, s33, 4096
buffer_load_dwordx4 a[160:163], v1, s[16:19], s32 offen offset:0 sc1
buffer_load_dwordx4 a[164:167], v1, s[16:19], s33 offen offset:0 sc1
s_waitcnt vmcnt(35)
v_mfma_f32_32x32x16_bf16 v[32:47], a[208:211], v[240:243], v[32:47]
s_waitcnt vmcnt(34)
v_mfma_f32_32x32x16_bf16 v[48:63], a[208:211], v[244:247], v[48:63]
v_mfma_f32_32x32x16_bf16 v[64:79], a[212:215], v[240:243], v[64:79]
v_mfma_f32_32x32x16_bf16 v[80:95], a[212:215], v[244:247], v[80:95]
buffer_load_dwordx4 v[200:203], v1, s[16:19], s32 offen offset:1024 sc1
buffer_load_dwordx4 v[204:207], v1, s[16:19], s33 offen offset:1024 sc1
s_add_u32 s34, s34, 4096
buffer_load_dwordx4 a[168:171], v2, s[16:19], s34 offen sc1
buffer_load_dwordx4 a[172:175], v3, s[16:19], s34 offen sc1
s_waitcnt vmcnt(35)
v_mfma_f32_32x32x16_bf16 v[32:47], a[216:219], a[0:3], v[32:47]
s_waitcnt vmcnt(34)
v_mfma_f32_32x32x16_bf16 v[48:63], a[216:219], a[4:7], v[48:63]
v_mfma_f32_32x32x16_bf16 v[64:79], a[220:223], a[0:3], v[64:79]
v_mfma_f32_32x32x16_bf16 v[80:95], a[220:223], a[4:7], v[80:95]
buffer_load_dwordx4 v[208:211], v1, s[16:19], s32 offen offset:2048 sc1
buffer_load_dwordx4 v[212:215], v1, s[16:19], s33 offen offset:2048 sc1
s_add_u32 s34, s34, 4096
buffer_load_dwordx4 a[176:179], v2, s[16:19], s34 offen sc1
buffer_load_dwordx4 a[180:183], v3, s[16:19], s34 offen sc1
s_waitcnt vmcnt(35)
v_mfma_f32_32x32x16_bf16 v[32:47], a[224:227], a[8:11], v[32:47]
s_waitcnt vmcnt(34)
v_mfma_f32_32x32x16_bf16 v[48:63], a[224:227], a[12:15], v[48:63]
v_mfma_f32_32x32x16_bf16 v[64:79], a[228:231], a[8:11], v[64:79]
v_mfma_f32_32x32x16_bf16 v[80:95], a[228:231], a[12:15], v[80:95]
buffer_load_dwordx4 v[216:219], v1, s[16:19], s32 offen offset:3072 sc1
buffer_load_dwordx4 v[220:223], v1, s[16:19], s33 offen offset:3072 sc1
s_waitcnt vmcnt(35)
v_mfma_f32_32x32x16_bf16 v[32:47], a[232:235], a[112:115], v[32:47]
s_waitcnt vmcnt(34)
v_mfma_f32_32x32x16_bf16 v[48:63], a[232:235], a[116:119], v[48:63]
v_mfma_f32_32x32x16_bf16 v[64:79], a[236:239], a[112:115], v[64:79]
v_mfma_f32_32x32x16_bf16 v[80:95], a[236:239], a[116:119], v[80:95]
s_add_u32 s32, s32, 4096
s_add_u32 s33, s33, 4096
buffer_load_dwordx4 a[184:187], v1, s[16:19], s32 offen offset:0 sc1
buffer_load_dwordx4 a[188:191], v1, s[16:19], s33 offen offset:0 sc1
s_add_u32 s34, s34, 4096
buffer_load_dwordx4 a[72:75], v2, s[16:19], s34 offen sc1
buffer_load_dwordx4 a[76:79], v3, s[16:19], s34 offen sc1
s_waitcnt vmcnt(35)
v_mfma_f32_32x32x16_bf16 v[32:47], a[240:243], a[120:123], v[32:47]
s_waitcnt vmcnt(34)
v_mfma_f32_32x32x16_bf16 v[48:63], a[240:243], a[124:127], v[48:63]
v_mfma_f32_32x32x16_bf16 v[64:79], a[244:247], a[120:123], v[64:79]
v_mfma_f32_32x32x16_bf16 v[80:95], a[244:247], a[124:127], v[80:95]
buffer_load_dwordx4 a[192:195], v1, s[16:19], s32 offen offset:1024 sc1
buffer_load_dwordx4 a[196:199], v1, s[16:19], s33 offen offset:1024 sc1
s_add_u32 s34, s34, 4096
buffer_load_dwordx4 a[80:83], v2, s[16:19], s34 offen sc1
buffer_load_dwordx4 a[84:87], v3, s[16:19], s34 offen sc1
s_waitcnt vmcnt(37)
v_mfma_f32_32x32x16_bf16 v[32:47], a[248:251], v[168:171], v[32:47]
s_waitcnt vmcnt(36)
v_mfma_f32_32x32x16_bf16 v[48:63], a[248:251], v[172:175], v[48:63]
v_mfma_f32_32x32x16_bf16 v[64:79], a[252:255], v[168:171], v[64:79]
v_mfma_f32_32x32x16_bf16 v[80:95], a[252:255], v[172:175], v[80:95]
buffer_load_dwordx4 a[200:203], v1, s[16:19], s32 offen offset:2048 sc1
buffer_load_dwordx4 a[204:207], v1, s[16:19], s33 offen offset:2048 sc1
s_waitcnt vmcnt(35)
v_mfma_f32_32x32x16_bf16 v[32:47], v[104:107], v[176:179], v[32:47]
s_waitcnt vmcnt(34)
v_mfma_f32_32x32x16_bf16 v[48:63], v[104:107], v[180:183], v[48:63]
v_mfma_f32_32x32x16_bf16 v[64:79], v[108:111], v[176:179], v[64:79]
v_mfma_f32_32x32x16_bf16 v[80:95], v[108:111], v[180:183], v[80:95]
buffer_load_dwordx4 a[88:91], v1, s[16:19], s32 offen offset:3072 sc1
buffer_load_dwordx4 a[92:95], v1, s[16:19], s33 offen offset:3072 sc1
s_add_u32 s34, s34, 4096
buffer_load_dwordx4 a[208:211], v2, s[16:19], s34 offen sc1
buffer_load_dwordx4 a[212:215], v3, s[16:19], s34 offen sc1
s_waitcnt vmcnt(35)
v_mfma_f32_32x32x16_bf16 v[32:47], v[112:115], a[32:35], v[32:47]
s_waitcnt vmcnt(34)
v_mfma_f32_32x32x16_bf16 v[48:63], v[112:115], a[36:39], v[48:63]
v_mfma_f32_32x32x16_bf16 v[64:79], v[116:119], a[32:35], v[64:79]
v_mfma_f32_32x32x16_bf16 v[80:95], v[116:119], a[36:39], v[80:95]
s_add_u32 s32, s32, 4096
s_add_u32 s33, s33, 4096
buffer_load_dwordx4 v[240:243], v1, s[16:19], s32 offen offset:0 sc1
buffer_load_dwordx4 v[244:247], v1, s[16:19], s33 offen offset:0 sc1
s_add_u32 s34, s34, 4096
buffer_load_dwordx4 a[216:219], v2, s[16:19], s34 offen sc1
buffer_load_dwordx4 a[220:223], v3, s[16:19], s34 offen sc1
s_waitcnt vmcnt(37)
v_mfma_f32_32x32x16_bf16 v[32:47], v[120:123], a[144:147], v[32:47]
s_waitcnt vmcnt(36)
v_mfma_f32_32x32x16_bf16 v[48:63], v[120:123], a[148:151], v[48:63]
v_mfma_f32_32x32x16_bf16 v[64:79], v[124:127], a[144:147], v[64:79]
v_mfma_f32_32x32x16_bf16 v[80:95], v[124:127], a[148:151], v[80:95]
buffer_load_dwordx4 a[0:3], v1, s[16:19], s32 offen offset:1024 sc1
buffer_load_dwordx4 a[4:7], v1, s[16:19], s33 offen offset:1024 sc1
s_waitcnt vmcnt(35)
v_mfma_f32_32x32x16_bf16 v[32:47], v[128:131], a[152:155], v[32:47]
s_waitcnt vmcnt(34)
v_mfma_f32_32x32x16_bf16 v[48:63], v[128:131], a[156:159], v[48:63]
v_mfma_f32_32x32x16_bf16 v[64:79], v[132:135], a[152:155], v[64:79]
v_mfma_f32_32x32x16_bf16 v[80:95], v[132:135], a[156:159], v[80:95]
buffer_load_dwordx4 a[224:227], v1, s[16:19], s32 offen offset:2048 sc1
buffer_load_dwordx4 a[228:231], v1, s[16:19], s33 offen offset:2048 sc1
s_add_u32 s34, s34, 4096
buffer_load_dwordx4 a[8:11], v2, s[16:19], s34 offen sc1
buffer_load_dwordx4 a[12:15], v3, s[16:19], s34 offen sc1
s_waitcnt vmcnt(35)
v_mfma_f32_32x32x16_bf16 v[32:47], v[136:139], a[160:163], v[32:47]
s_waitcnt vmcnt(34)
v_mfma_f32_32x32x16_bf16 v[48:63], v[136:139], a[164:167], v[48:63]
v_mfma_f32_32x32x16_bf16 v[64:79], v[140:143], a[160:163], v[64:79]
v_mfma_f32_32x32x16_bf16 v[80:95], v[140:143], a[164:167], v[80:95]
buffer_load_dwordx4 a[232:235], v1, s[16:19], s32 offen offset:3072 sc1
buffer_load_dwordx4 a[236:239], v1, s[16:19], s33 offen offset:3072 sc1
s_add_u32 s34, s34, 4096
buffer_load_dwordx4 a[112:115], v2, s[16:19], s34 offen sc1
buffer_load_dwordx4 a[116:119], v3, s[16:19], s34 offen sc1
s_waitcnt vmcnt(37)
v_mfma_f32_32x32x16_bf16 v[32:47], v[144:147], v[200:203], v[32:47]
s_waitcnt vmcnt(36)
v_mfma_f32_32x32x16_bf16 v[48:63], v[144:147], v[204:207], v[48:63]
v_mfma_f32_32x32x16_bf16 v[64:79], v[148:151], v[200:203], v[64:79]
v_mfma_f32_32x32x16_bf16 v[80:95], v[148:151], v[204:207], v[80:95]
s_add_u32 s32, s32, 4096
s_add_u32 s33, s33, 4096
buffer_load_dwordx4 a[240:243], v1, s[16:19], s32 offen offset:0 sc1
buffer_load_dwordx4 a[244:247], v1, s[16:19], s33 offen offset:0 sc1
s_waitcnt vmcnt(35)
v_mfma_f32_32x32x16_bf16 v[32:47], v[248:251], v[208:211], v[32:47]
s_waitcnt vmcnt(34)
v_mfma_f32_32x32x16_bf16 v[48:63], v[248:251], v[212:215], v[48:63]
v_mfma_f32_32x32x16_bf16 v[64:79], v[252:255], v[208:211], v[64:79]
v_mfma_f32_32x32x16_bf16 v[80:95], v[252:255], v[212:215], v[80:95]
buffer_load_dwordx4 a[120:123], v1, s[16:19], s32 offen offset:1024 sc1
buffer_load_dwordx4 a[124:127], v1, s[16:19], s33 offen offset:1024 sc1
s_add_u32 s34, s34, 4096
buffer_load_dwordx4 a[248:251], v2, s[16:19], s34 offen sc1
buffer_load_dwordx4 a[252:255], v3, s[16:19], s34 offen sc1
s_waitcnt vmcnt(35)
v_mfma_f32_32x32x16_bf16 v[32:47], v[152:155], v[216:219], v[32:47]
s_waitcnt vmcnt(34)
v_mfma_f32_32x32x16_bf16 v[48:63], v[152:155], v[220:223], v[48:63]
v_mfma_f32_32x32x16_bf16 v[64:79], v[156:159], v[216:219], v[64:79]
v_mfma_f32_32x32x16_bf16 v[80:95], v[156:159], v[220:223], v[80:95]
buffer_load_dwordx4 v[168:171], v1, s[16:19], s32 offen offset:2048 sc1
buffer_load_dwordx4 v[172:175], v1, s[16:19], s33 offen offset:2048 sc1
s_waitcnt vmcnt(35)
v_mfma_f32_32x32x16_bf16 v[32:47], a[16:19], a[184:187], v[32:47]
s_waitcnt vmcnt(34)
v_mfma_f32_32x32x16_bf16 v[48:63], a[16:19], a[188:191], v[48:63]
v_mfma_f32_32x32x16_bf16 v[64:79], a[20:23], a[184:187], v[64:79]
v_mfma_f32_32x32x16_bf16 v[80:95], a[20:23], a[188:191], v[80:95]
buffer_load_dwordx4 v[104:107], v1, s[16:19], s32 offen offset:3072 sc1
buffer_load_dwordx4 v[108:111], v1, s[16:19], s33 offen offset:3072 sc1
s_waitcnt vmcnt(33)
v_mfma_f32_32x32x16_bf16 v[32:47], a[24:27], a[192:195], v[32:47]
s_waitcnt vmcnt(32)
v_mfma_f32_32x32x16_bf16 v[48:63], a[24:27], a[196:199], v[48:63]
v_mfma_f32_32x32x16_bf16 v[64:79], a[28:31], a[192:195], v[64:79]
v_mfma_f32_32x32x16_bf16 v[80:95], a[28:31], a[196:199], v[80:95]
s_add_u32 s32, s32, 4096
s_add_u32 s33, s33, 4096
buffer_load_dwordx4 v[176:179], v1, s[16:19], s32 offen offset:0 sc1
buffer_load_dwordx4 v[180:183], v1, s[16:19], s33 offen offset:0 sc1
s_waitcnt vmcnt(31)
v_mfma_f32_32x32x16_bf16 v[32:47], v[184:187], a[200:203], v[32:47]
s_waitcnt vmcnt(30)
v_mfma_f32_32x32x16_bf16 v[48:63], v[184:187], a[204:207], v[48:63]
v_mfma_f32_32x32x16_bf16 v[64:79], v[188:191], a[200:203], v[64:79]
v_mfma_f32_32x32x16_bf16 v[80:95], v[188:191], a[204:207], v[80:95]
buffer_load_dwordx4 v[112:115], v1, s[16:19], s32 offen offset:1024 sc1
buffer_load_dwordx4 v[116:119], v1, s[16:19], s33 offen offset:1024 sc1
s_waitcnt vmcnt(31)
v_mfma_f32_32x32x16_bf16 v[32:47], v[192:195], a[88:91], v[32:47]
s_waitcnt vmcnt(30)
v_mfma_f32_32x32x16_bf16 v[48:63], v[192:195], a[92:95], v[48:63]
v_mfma_f32_32x32x16_bf16 v[64:79], v[196:199], a[88:91], v[64:79]
v_mfma_f32_32x32x16_bf16 v[80:95], v[196:199], a[92:95], v[80:95]
buffer_load_dwordx4 a[32:35], v1, s[16:19], s32 offen offset:2048 sc1
buffer_load_dwordx4 a[36:39], v1, s[16:19], s33 offen offset:2048 sc1
s_waitcnt vmcnt(29)
v_mfma_f32_32x32x16_bf16 v[32:47], a[56:59], v[240:243], v[32:47]
s_waitcnt vmcnt(28)
v_mfma_f32_32x32x16_bf16 v[48:63], a[56:59], v[244:247], v[48:63]
v_mfma_f32_32x32x16_bf16 v[64:79], a[60:63], v[240:243], v[64:79]
v_mfma_f32_32x32x16_bf16 v[80:95], a[60:63], v[244:247], v[80:95]
buffer_load_dwordx4 v[120:123], v1, s[16:19], s32 offen offset:3072 sc1
buffer_load_dwordx4 v[124:127], v1, s[16:19], s33 offen offset:3072 sc1
s_waitcnt vmcnt(27)
v_mfma_f32_32x32x16_bf16 v[32:47], a[64:67], a[0:3], v[32:47]
s_waitcnt vmcnt(26)
v_mfma_f32_32x32x16_bf16 v[48:63], a[64:67], a[4:7], v[48:63]
v_mfma_f32_32x32x16_bf16 v[64:79], a[68:71], a[0:3], v[64:79]
v_mfma_f32_32x32x16_bf16 v[80:95], a[68:71], a[4:7], v[80:95]
s_add_u32 s32, s32, 4096
s_add_u32 s33, s33, 4096
buffer_load_dwordx4 a[144:147], v1, s[16:19], s32 offen offset:0 sc1
buffer_load_dwordx4 a[148:151], v1, s[16:19], s33 offen offset:0 sc1
s_waitcnt vmcnt(27)
v_mfma_f32_32x32x16_bf16 v[32:47], v[224:227], a[224:227], v[32:47]
s_waitcnt vmcnt(26)
v_mfma_f32_32x32x16_bf16 v[48:63], v[224:227], a[228:231], v[48:63]
v_mfma_f32_32x32x16_bf16 v[64:79], v[228:231], a[224:227], v[64:79]
v_mfma_f32_32x32x16_bf16 v[80:95], v[228:231], a[228:231], v[80:95]
buffer_load_dwordx4 v[128:131], v1, s[16:19], s32 offen offset:1024 sc1
buffer_load_dwordx4 v[132:135], v1, s[16:19], s33 offen offset:1024 sc1
s_waitcnt vmcnt(25)
v_mfma_f32_32x32x16_bf16 v[32:47], v[232:235], a[232:235], v[32:47]
s_waitcnt vmcnt(24)
v_mfma_f32_32x32x16_bf16 v[48:63], v[232:235], a[236:239], v[48:63]
v_mfma_f32_32x32x16_bf16 v[64:79], v[236:239], a[232:235], v[64:79]
v_mfma_f32_32x32x16_bf16 v[80:95], v[236:239], a[236:239], v[80:95]
buffer_load_dwordx4 a[152:155], v1, s[16:19], s32 offen offset:2048 sc1
buffer_load_dwordx4 a[156:159], v1, s[16:19], s33 offen offset:2048 sc1
buffer_load_dwordx4 v[16:19], v1, s[16:19], s38 offen sc1
s_waitcnt vmcnt(24)
v_mfma_f32_32x32x16_bf16 v[32:47], a[96:99], a[240:243], v[32:47]
s_waitcnt vmcnt(23)
v_mfma_f32_32x32x16_bf16 v[48:63], a[96:99], a[244:247], v[48:63]
v_mfma_f32_32x32x16_bf16 v[64:79], a[100:103], a[240:243], v[64:79]
v_mfma_f32_32x32x16_bf16 v[80:95], a[100:103], a[244:247], v[80:95]
buffer_load_dwordx4 v[136:139], v1, s[16:19], s32 offen offset:3072 sc1
buffer_load_dwordx4 v[140:143], v1, s[16:19], s33 offen offset:3072 sc1
v_and_b32 v8, 31, v0
v_lshlrev_b32 v8, 10, v8
v_bfe_u32 v9, v0, 5, 1
v_lshl_add_u32 v8, v9, 5, v8
s_and_b32 s29, s28, 1
s_lshl_b32 s30, s36, 1
s_add_u32 s29, s29, s30
s_lshl_b32 s29, s29, 15
s_lshl_b32 s30, s37, 7
s_add_u32 s29, s29, s30
s_and_b32 s30, s28, 2
s_lshl_b32 s30, s30, 5
s_add_u32 s29, s29, s30
buffer_load_dwordx4 v[96:99], v8, s[24:27], s29 offen offset:0
buffer_load_dwordx4 v[100:103], v8, s[24:27], s29 offen offset:16
s_bfe_u32 s48, %2, 0x20003
s_getpc_b64 s[50:51]
.Lpfk3:
s_add_u32 s50, s50, .Lk4s-.Lpfk3
s_addc_u32 s51, s51, 0
s_mul_i32 s48, s48, 22
v_min_u32 v20, 21, v0
v_add_lshl_u32 v20, v20, s48, 7
v_min_u32 v20, .Lk4e-.Lk4s-4, v20
global_load_dword v21, v20, s[50:51]
s_waitcnt vmcnt(27)
v_mfma_f32_32x32x16_bf16 v[32:47], a[104:107], a[120:123], v[32:47]
s_waitcnt vmcnt(26)
v_mfma_f32_32x32x16_bf16 v[48:63], a[104:107], a[124:127], v[48:63]
v_mfma_f32_32x32x16_bf16 v[64:79], a[108:111], a[120:123], v[64:79]
v_mfma_f32_32x32x16_bf16 v[80:95], a[108:111], a[124:127], v[80:95]
s_add_u32 s32, s32, 4096
s_add_u32 s33, s33, 4096
buffer_load_dwordx4 a[160:163], v1, s[16:19], s32 offen offset:0 sc1
buffer_load_dwordx4 a[164:167], v1, s[16:19], s33 offen offset:0 sc1
s_waitcnt vmcnt(25)
v_mfma_f32_32x32x16_bf16 v[32:47], v[160:163], v[168:171], v[32:47]
s_waitcnt vmcnt(24)
v_mfma_f32_32x32x16_bf16 v[48:63], v[160:163], v[172:175], v[48:63]
v_mfma_f32_32x32x16_bf16 v[64:79], v[164:167], v[168:171], v[64:79]
v_mfma_f32_32x32x16_bf16 v[80:95], v[164:167], v[172:175], v[80:95]
buffer_load_dwordx4 v[144:147], v1, s[16:19], s32 offen offset:1024 sc1
buffer_load_dwordx4 v[148:151], v1, s[16:19], s33 offen offset:1024 sc1
s_waitcnt vmcnt(25)
v_mfma_f32_32x32x16_bf16 v[32:47], a[128:131], v[104:107], v[32:47]
s_waitcnt vmcnt(24)
v_mfma_f32_32x32x16_bf16 v[48:63], a[128:131], v[108:111], v[48:63]
v_mfma_f32_32x32x16_bf16 v[64:79], a[132:135], v[104:107], v[64:79]
v_mfma_f32_32x32x16_bf16 v[80:95], a[132:135], v[108:111], v[80:95]
buffer_load_dwordx4 v[200:203], v1, s[16:19], s32 offen offset:2048 sc1
buffer_load_dwordx4 v[204:207], v1, s[16:19], s33 offen offset:2048 sc1
s_waitcnt vmcnt(25)
v_mfma_f32_32x32x16_bf16 v[32:47], a[136:139], v[176:179], v[32:47]
s_waitcnt vmcnt(24)
v_mfma_f32_32x32x16_bf16 v[48:63], a[136:139], v[180:183], v[48:63]
v_mfma_f32_32x32x16_bf16 v[64:79], a[140:143], v[176:179], v[64:79]
v_mfma_f32_32x32x16_bf16 v[80:95], a[140:143], v[180:183], v[80:95]
buffer_load_dwordx4 v[248:251], v1, s[16:19], s32 offen offset:3072 sc1
buffer_load_dwordx4 v[252:255], v1, s[16:19], s33 offen offset:3072 sc1
s_waitcnt vmcnt(25)
v_mfma_f32_32x32x16_bf16 v[32:47], a[40:43], v[112:115], v[32:47]
s_waitcnt vmcnt(24)
v_mfma_f32_32x32x16_bf16 v[48:63], a[40:43], v[116:119], v[48:63]
v_mfma_f32_32x32x16_bf16 v[64:79], a[44:47], v[112:115], v[64:79]
v_mfma_f32_32x32x16_bf16 v[80:95], a[44:47], v[116:119], v[80:95]
s_waitcnt vmcnt(23)
v_mfma_f32_32x32x16_bf16 v[32:47], a[48:51], a[32:35], v[32:47]
s_waitcnt vmcnt(22)
v_mfma_f32_32x32x16_bf16 v[48:63], a[48:51], a[36:39], v[48:63]
v_mfma_f32_32x32x16_bf16 v[64:79], a[52:55], a[32:35], v[64:79]
v_mfma_f32_32x32x16_bf16 v[80:95], a[52:55], a[36:39], v[80:95]
s_waitcnt vmcnt(21)
v_mfma_f32_32x32x16_bf16 v[32:47], a[168:171], v[120:123], v[32:47]
s_waitcnt vmcnt(20)
v_mfma_f32_32x32x16_bf16 v[48:63], a[168:171], v[124:127], v[48:63]
v_mfma_f32_32x32x16_bf16 v[64:79], a[172:175], v[120:123], v[64:79]
v_mfma_f32_32x32x16_bf16 v[80:95], a[172:175], v[124:127], v[80:95]
s_waitcnt vmcnt(19)
v_mfma_f32_32x32x16_bf16 v[32:47], a[176:179], a[144:147], v[32:47]
s_waitcnt vmcnt(18)
v_mfma_f32_32x32x16_bf16 v[48:63], a[176:179], a[148:151], v[48:63]
v_mfma_f32_32x32x16_bf16 v[64:79], a[180:183], a[144:147], v[64:79]
v_mfma_f32_32x32x16_bf16 v[80:95], a[180:183], a[148:151], v[80:95]
s_waitcnt vmcnt(13)
v_and_b32 v16, v16, v17
v_and_b32 v18, v18, v19
v_and_b32 v16, v16, v18
v_cmp_gt_u32 vcc, 3, v16
s_nop 3
s_cbranch_vccnz .LspX
.LspXb:
s_add_u32 m0, s44, 0
s_nop 0
buffer_load_dwordx4 v1, s[40:43], m0 offen sc1 lds
s_add_u32 m0, s44, 4096
s_nop 0
buffer_load_dwordx4 v1, s[40:43], m0 offen sc1 lds
s_add_u32 m0, s44, 8192
s_nop 0
buffer_load_dwordx4 v1, s[40:43], m0 offen sc1 lds
v_mfma_f32_32x32x16_bf16 v[32:47], a[72:75], v[128:131], v[32:47]
v_mfma_f32_32x32x16_bf16 v[48:63], a[72:75], v[132:135], v[48:63]
v_mfma_f32_32x32x16_bf16 v[64:79], a[76:79], v[128:131], v[64:79]
v_mfma_f32_32x32x16_bf16 v[80:95], a[76:79], v[132:135], v[80:95]
s_add_u32 m0, s44, 12288
s_nop 0
buffer_load_dwordx4 v1, s[40:43], m0 offen sc1 lds
s_add_u32 m0, s44, 114688
s_nop 0
buffer_load_dwordx4 v1, s[40:43], m0 offen sc1 lds
s_add_u32 m0, s44, 118784
s_nop 0
buffer_load_dwordx4 v1, s[40:43], m0 offen sc1 lds
v_mfma_f32_32x32x16_bf16 v[32:47], a[80:83], a[152:155], v[32:47]
v_mfma_f32_32x32x16_bf16 v[48:63], a[80:83], a[156:159], v[48:63]
v_mfma_f32_32x32x16_bf16 v[64:79], a[84:87], a[152:155], v[64:79]
v_mfma_f32_32x32x16_bf16 v[80:95], a[84:87], a[156:159], v[80:95]
s_add_u32 m0, s44, 16384
s_nop 0
buffer_load_dwordx4 v1, s[40:43], m0 offen sc1 lds
s_add_u32 m0, s44, 20480
s_nop 0
buffer_load_dwordx4 v1, s[40:43], m0 offen sc1 lds
s_add_u32 m0, s44, 24576
s_nop 0
buffer_load_dwordx4 v1, s[40:43], m0 offen sc1 lds
s_waitcnt vmcnt(21)
v_mfma_f32_32x32x16_bf16 v[32:47], a[208:211], v[136:139], v[32:47]
s_waitcnt vmcnt(20)
v_mfma_f32_32x32x16_bf16 v[48:63], a[208:211], v[140:143], v[48:63]
v_mfma_f32_32x32x16_bf16 v[64:79], a[212:215], v[136:139], v[64:79]
v_mfma_f32_32x32x16_bf16 v[80:95], a[212:215], v[140:143], v[80:95]
s_add_u32 m0, s44, 28672
s_nop 0
buffer_load_dwordx4 v1, s[40:43], m0 offen sc1 lds
s_add_u32 m0, s44, 32768
s_nop 0
buffer_load_dwordx4 v1, s[40:43], m0 offen sc1 lds
s_add_u32 m0, s44, 36864
s_nop 0
buffer_load_dwordx4 v1, s[40:43], m0 offen sc1 lds
s_waitcnt vmcnt(19)
v_mfma_f32_32x32x16_bf16 v[32:47], a[216:219], a[160:163], v[32:47]
s_waitcnt vmcnt(18)
v_mfma_f32_32x32x16_bf16 v[48:63], a[216:219], a[164:167], v[48:63]
v_mfma_f32_32x32x16_bf16 v[64:79], a[220:223], a[160:163], v[64:79]
v_mfma_f32_32x32x16_bf16 v[80:95], a[220:223], a[164:167], v[80:95]
s_add_u32 m0, s44, 40960
s_nop 0
buffer_load_dwordx4 v1, s[40:43], m0 offen sc1 lds
s_add_u32 m0, s44, 45056
s_nop 0
buffer_load_dwordx4 v1, s[40:43], m0 offen sc1 lds
s_add_u32 m0, s44, 49152
s_nop 0
buffer_load_dwordx4 v1, s[40:43], m0 offen sc1 lds
s_waitcnt vmcnt(20)
v_mfma_f32_32x32x16_bf16 v[32:47], a[8:11], v[144:147], v[32:47]
s_waitcnt vmcnt(19)
v_mfma_f32_32x32x16_bf16 v[48:63], a[8:11], v[148:151], v[48:63]
v_mfma_f32_32x32x16_bf16 v[64:79], a[12:15], v[144:147], v[64:79]
v_mfma_f32_32x32x16_bf16 v[80:95], a[12:15], v[148:151], v[80:95]
s_add_u32 m0, s44, 53248
s_nop 0
buffer_load_dwordx4 v1, s[40:43], m0 offen sc1 lds
s_add_u32 m0, s44, 57344
s_nop 0
buffer_load_dwordx4 v1, s[40:43], m0 offen sc1 lds
s_add_u32 m0, s44, 61440
s_nop 0
buffer_load_dwordx4 v1, s[40:43], m0 offen sc1 lds
s_waitcnt vmcnt(21)
v_mfma_f32_32x32x16_bf16 v[32:47], a[112:115], v[200:203], v[32:47]
s_waitcnt vmcnt(20)
v_mfma_f32_32x32x16_bf16 v[48:63], a[112:115], v[204:207], v[48:63]
v_mfma_f32_32x32x16_bf16 v[64:79], a[116:119], v[200:203], v[64:79]
v_mfma_f32_32x32x16_bf16 v[80:95], a[116:119], v[204:207], v[80:95]
s_add_u32 m0, s44, 65536
s_nop 0
buffer_load_dwordx4 v1, s[40:43], m0 offen sc1 lds
s_add_u32 m0, s44, 69632
s_nop 0
buffer_load_dwordx4 v1, s[40:43], m0 offen sc1 lds
s_waitcnt vmcnt(21)
v_mfma_f32_32x32x16_bf16 v[32:47], a[248:251], v[248:251], v[32:47]
s_waitcnt vmcnt(20)
v_mfma_f32_32x32x16_bf16 v[48:63], a[248:251], v[252:255], v[48:63]
v_mfma_f32_32x32x16_bf16 v[64:79], a[252:255], v[248:251], v[64:79]
v_mfma_f32_32x32x16_bf16 v[80:95], a[252:255], v[252:255], v[80:95]
s_lshl_b32 s29, s28, 12
s_add_u32 s29, s29, 131072
v_add_u32 v10, s29, v1
v_xor_b32 v11, 4096, v10
v_xor_b32 v12, 8192, v10
s_and_b32 s29, s28, 1
s_lshl_b32 s29, s29, 12
s_and_b32 s30, s28, 2
s_lshl_b32 s30, s30, 13
s_add_u32 s29, s29, s30
s_add_u32 s30, s29, 106496
v_add_u32 v13, s30, v1
s_sub_u32 s30, 126976, s29
v_add_u32 v14, s30, v1
ds_write_b128 v11, v[48:51] offset:0
ds_write_b128 v11, v[52:55] offset:1024
ds_write_b128 v11, v[56:59] offset:2048
ds_write_b128 v11, v[60:63] offset:3072
ds_write_b128 v12, v[64:67] offset:16384
ds_write_b128 v12, v[68:71] offset:17408
ds_write_b128 v12, v[72:75] offset:18432
ds_write_b128 v12, v[76:79] offset:19456
ds_write_b128 v14, v[80:83] offset:0
ds_write_b128 v14, v[84:87] offset:1024
ds_write_b128 v14, v[88:91] offset:2048
ds_write_b128 v14, v[92:95] offset:3072
v_permlane32_swap_b32 v96, v98
v_permlane32_swap_b32 v97, v99
v_permlane32_swap_b32 v100, v102
v_permlane32_swap_b32 v101, v103
v_lshlrev_b32 v8, 16, v96
v_and_b32 v9, 0xffff0000, v96
v_add_f32 v32, v32, v8
v_add_f32 v33, v33, v9
v_lshlrev_b32 v8, 16, v97
v_and_b32 v9, 0xffff0000, v97
v_add_f32 v34, v34, v8
v_add_f32 v35, v35, v9
v_lshlrev_b32 v8, 16, v100
v_and_b32 v9, 0xffff0000, v100
v_add_f32 v36, v36, v8
v_add_f32 v37, v37, v9
v_lshlrev_b32 v8, 16, v101
v_and_b32 v9, 0xffff0000, v101
v_add_f32 v38, v38, v8
v_add_f32 v39, v39, v9
v_lshlrev_b32 v8, 16, v98
v_and_b32 v9, 0xffff0000, v98
v_add_f32 v40, v40, v8
v_add_f32 v41, v41, v9
v_lshlrev_b32 v8, 16, v99
v_and_b32 v9, 0xffff0000, v99
v_add_f32 v42, v42, v8
v_add_f32 v43, v43, v9
v_lshlrev_b32 v8, 16, v102
v_and_b32 v9, 0xffff0000, v102
v_add_f32 v44, v44, v8
v_add_f32 v45, v45, v9
v_lshlrev_b32 v8, 16, v103
v_and_b32 v9, 0xffff0000, v103
v_add_f32 v46, v46, v8
v_add_f32 v47, v47, v9
s_and_b32 s29, s28, 1
s_lshl_b32 s30, s36, 1
s_add_u32 s31, s29, s30
s_lshl_b32 s29, s31, 5
s_lshl_b32 s30, s37, 2
s_add_u32 s29, s29, s30
s_and_b32 s30, s28, 2
s_add_u32 s29, s29, s30
s_lshl_b32 s29, s29, 10
s_add_u32 s29, s29, 0x4000000
s_waitcnt lgkmcnt(0)
s_barrier
ds_read_b128 v[128:131], v10 offset:0
ds_read_b128 v[132:135], v10 offset:1024
ds_read_b128 v[136:139], v10 offset:2048
ds_read_b128 v[140:143], v10 offset:3072
ds_read_b128 v[144:147], v10 offset:16384
ds_read_b128 v[148:151], v10 offset:17408
ds_read_b128 v[152:155], v10 offset:18432
ds_read_b128 v[156:159], v10 offset:19456
ds_read_b128 v[160:163], v13 offset:0
ds_read_b128 v[164:167], v13 offset:1024
ds_read_b128 v[168:171], v13 offset:2048
ds_read_b128 v[172:175], v13 offset:3072
v_and_b32 v8, 15, v0
v_lshlrev_b32 v8, 4, v8
v_bfe_u32 v9, v0, 4, 1
v_lshl_add_u32 v8, v9, 11, v8
v_bfe_u32 v9, v0, 5, 1
v_lshl_add_u32 v8, v9, 2, v8
s_lshl_b32 s31, s31, 12
s_lshl_b32 s30, s37, 8
s_add_u32 s31, s31, s30
s_and_b32 s30, s28, 2
s_lshl_b32 s30, s30, 2
s_add_u32 s31, s31, s30
s_add_u32 s31, s31, 0x5500000
v_mov_b32 v192, 0
v_mov_b32 v193, 0
v_mov_b32 v14, 1
s_waitcnt lgkmcnt(11)
v_pk_add_f32 v[32:33], v[32:33], v[128:129]
v_pk_add_f32 v[34:35], v[34:35], v[130:131]
s_waitcnt lgkmcnt(10)
v_pk_add_f32 v[36:37], v[36:37], v[132:133]
v_pk_add_f32 v[38:39], v[38:39], v[134:135]
s_waitcnt lgkmcnt(9)
v_pk_add_f32 v[40:41], v[40:41], v[136:137]
v_pk_add_f32 v[42:43], v[42:43], v[138:139]
s_waitcnt lgkmcnt(8)
v_pk_add_f32 v[44:45], v[44:45], v[140:141]
v_pk_add_f32 v[46:47], v[46:47], v[142:143]
s_waitcnt lgkmcnt(7)
v_pk_add_f32 v[32:33], v[32:33], v[144:145]
v_pk_add_f32 v[34:35], v[34:35], v[146:147]
s_waitcnt lgkmcnt(6)
v_pk_add_f32 v[36:37], v[36:37], v[148:149]
v_pk_add_f32 v[38:39], v[38:39], v[150:151]
s_waitcnt lgkmcnt(5)
v_pk_add_f32 v[40:41], v[40:41], v[152:153]
v_pk_add_f32 v[42:43], v[42:43], v[154:155]
s_waitcnt lgkmcnt(4)
v_pk_add_f32 v[44:45], v[44:45], v[156:157]
v_pk_add_f32 v[46:47], v[46:47], v[158:159]
s_waitcnt lgkmcnt(3)
v_pk_add_f32 v[32:33], v[32:33], v[160:161]
v_pk_add_f32 v[34:35], v[34:35], v[162:163]
s_waitcnt lgkmcnt(2)
v_pk_add_f32 v[36:37], v[36:37], v[164:165]
v_pk_add_f32 v[38:39], v[38:39], v[166:167]
s_waitcnt lgkmcnt(1)
v_pk_add_f32 v[40:41], v[40:41], v[168:169]
v_pk_add_f32 v[42:43], v[42:43], v[170:171]
s_waitcnt lgkmcnt(0)
v_pk_add_f32 v[44:45], v[44:45], v[172:173]
v_pk_add_f32 v[46:47], v[46:47], v[174:175]
v_cvt_pk_bf16_f32 v176, v32, v33
v_cvt_pk_bf16_f32 v177, v34, v35
v_cvt_pk_bf16_f32 v178, v36, v37
v_cvt_pk_bf16_f32 v179, v38, v39
v_cvt_pk_bf16_f32 v180, v40, v41
v_cvt_pk_bf16_f32 v181, v42, v43
v_cvt_pk_bf16_f32 v182, v44, v45
v_cvt_pk_bf16_f32 v183, v46, v47
buffer_store_dwordx4 v[176:179], v1, s[16:19], s29 offen offset:0 sc1
buffer_store_dwordx4 v[180:183], v1, s[16:19], s29 offen offset:1024 sc1
v_lshlrev_b32 v11, 16, v176
v_and_b32 v12, 0xffff0000, v176
v_add_f32 v192, v192, v11
v_fmac_f32 v193, v11, v11
v_add_f32 v192, v192, v12
v_fmac_f32 v193, v12, v12
v_lshlrev_b32 v11, 16, v177
v_and_b32 v12, 0xffff0000, v177
v_add_f32 v192, v192, v11
v_fmac_f32 v193, v11, v11
v_add_f32 v192, v192, v12
v_fmac_f32 v193, v12, v12
v_lshlrev_b32 v11, 16, v178
v_and_b32 v12, 0xffff0000, v178
v_add_f32 v192, v192, v11
v_fmac_f32 v193, v11, v11
v_add_f32 v192, v192, v12
v_fmac_f32 v193, v12, v12
v_lshlrev_b32 v11, 16, v179
v_and_b32 v12, 0xffff0000, v179
v_add_f32 v192, v192, v11
v_fmac_f32 v193, v11, v11
v_add_f32 v192, v192, v12
v_fmac_f32 v193, v12, v12
v_lshlrev_b32 v11, 16, v180
v_and_b32 v12, 0xffff0000, v180
v_add_f32 v192, v192, v11
v_fmac_f32 v193, v11, v11
v_add_f32 v192, v192, v12
v_fmac_f32 v193, v12, v12
v_lshlrev_b32 v11, 16, v181
v_and_b32 v12, 0xffff0000, v181
v_add_f32 v192, v192, v11
v_fmac_f32 v193, v11, v11
v_add_f32 v192, v192, v12
v_fmac_f32 v193, v12, v12
v_lshlrev_b32 v11, 16, v182
v_and_b32 v12, 0xffff0000, v182
v_add_f32 v192, v192, v11
v_fmac_f32 v193, v11, v11
v_add_f32 v192, v192, v12
v_fmac_f32 v193, v12, v12
v_lshlrev_b32 v11, 16, v183
v_and_b32 v12, 0xffff0000, v183
v_add_f32 v192, v192, v11
v_fmac_f32 v193, v11, v11
v_add_f32 v192, v192, v12
v_fmac_f32 v193, v12, v12
s_nop 1
v_permlane32_swap_b32 v192, v193
s_nop 1
v_add_f32 v192, v192, v193
buffer_store_dword v192, v8, s[16:19], s31 offen sc1
s_barrier
buffer_store_dword v7, v5, s[16:19], s46 offen sc1
.Lk3e:
.Lk4s:
s_and_b32 s29, %2, 1
s_lshl_b32 s29, s29, 2
s_bfe_u32 s30, %2, 0x20003
s_add_u32 s29, s29, s30
s_lshl_b32 s39, s29, 7
s_add_u32 s38, s38, 8192
s_add_u32 s49, s38, s39
v_mov_b32 v6, v4
v_mov_b32 v7, v5
v_lshrrev_b32 v248, 6, v0
v_and_b32 v4, 63, v0
v_readfirstlane_b32 s24, v248
s_bfe_u32 s25, %2, 0x20001
s_lshl_b32 s25, s25, 3
s_lshr_b32 s26, %2, 5
s_add_u32 s29, s25, s26
v_lshlrev_b32 v4, 4, v4
s_lshl_b32 s30, s24, 10
s_and_b32 s25, %2, 1
s_lshl_b32 s25, s25, 2
s_bfe_u32 s26, %2, 0x20003
s_add_u32 s28, s25, s26
v_and_b32 v248, 15, v0
v_lshlrev_b32 v248, 4, v248
v_bfe_u32 v249, v0, 4, 2
v_lshl_add_u32 v1, v249, 9, v248
s_lshl_b32 s25, s28, 2
s_add_u32 s25, s25, s24
s_lshl_b32 s31, s25, 16
s_add_u32 s31, s31, 0x4000000
s_add_u32 s32, s31, 0x8000
s_lshl_b32 s35, s25, 13
s_add_u32 s35, s35, 0x5500000
s_mul_i32 s34, s25, 229376
s_lshr_b32 s26, s29, 1
s_mul_i32 s26, s26, 7168
s_add_u32 s34, s34, s26
s_add_u32 s34, s34, 0x4200000
s_add_u32 s26, s30, 122880
v_add_u32 v5, s26, v248
s_and_b32 s25, s29, 1
s_lshl_b32 s26, s25, 12
s_add_u32 s27, s26, 2048
s_lshl_b32 s25, s25, 9
s_add_u32 s25, s25, 2048
v_add_u32 v2, s27, v1
v_add_u32 v248, s25, v1
v_cmp_eq_u32 vcc, 2, v249
s_nop 1
v_cndmask_b32 v2, v2, v248, vcc
v_cmp_eq_u32 vcc, 3, v249
v_mov_b32 v248, 0x10000000
s_nop 0
v_cndmask_b32 v2, v2, v248, vcc
s_add_u32 s29, s34, s26
s_mov_b32 s36, 0x3b000000
v_add_u32 v250, 0x10000, v4
buffer_load_dword v40, v6, s[16:19], s49 offen sc1
s_add_u32 m0, s44, 73728
s_nop 0
buffer_load_dwordx4 v4, s[40:43], m0 offen sc1 lds
s_add_u32 m0, s44, 77824
s_nop 0
buffer_load_dwordx4 v4, s[40:43], m0 offen sc1 lds
s_add_u32 m0, s44, 81920
s_nop 0
buffer_load_dwordx4 v4, s[40:43], m0 offen sc1 lds
s_add_u32 m0, s44, 86016
s_nop 0
buffer_load_dwordx4 v4, s[40:43], m0 offen sc1 lds
s_add_u32 m0, s44, 90112
s_nop 0
buffer_load_dwordx4 v4, s[40:43], m0 offen sc1 lds
s_add_u32 m0, s44, 94208
s_nop 0
buffer_load_dwordx4 v4, s[40:43], m0 offen sc1 lds
s_add_u32 m0, s44, 98304
s_nop 0
buffer_load_dwordx4 v4, s[40:43], m0 offen sc1 lds
s_add_u32 m0, s44, 102400
s_nop 0
buffer_load_dwordx4 v4, s[40:43], m0 offen sc1 lds
s_add_u32 m0, s44, 106496
s_nop 0
buffer_load_dwordx4 v4, s[40:43], m0 offen sc1 lds
s_add_u32 m0, s44, 110592
s_nop 0
buffer_load_dwordx4 v4, s[40:43], m0 offen sc1 lds
s_mov_b32 s20, 1
s_waitcnt vmcnt(10)
v_cmp_gt_u32 vcc, 1, v40
s_nop 3
s_cbranch_vccnz .LspB
.LspBb:
.Lrd4s:
buffer_load_dwordx4 a[0:3], v1, s[16:19], s31 offen offset:0 sc1
buffer_load_dwordx4 a[4:7], v1, s[16:19], s31 offen offset:256 sc1
buffer_load_dwordx4 a[16:19], v1, s[16:19], s31 offen offset:2048 sc1
buffer_load_dwordx4 a[20:23], v1, s[16:19], s31 offen offset:2304 sc1
s_add_u32 s31, s31, 4096
buffer_load_dwordx4 a[32:35], v1, s[16:19], s31 offen offset:0 sc1
buffer_load_dwordx4 a[36:39], v1, s[16:19], s31 offen offset:256 sc1
buffer_load_dwordx4 a[48:51], v1, s[16:19], s31 offen offset:2048 sc1
buffer_load_dwordx4 a[52:55], v1, s[16:19], s31 offen offset:2304 sc1
v_min_u32 v42, 16, v4
v_lshlrev_b32 v42, 23, v42
v_mov_b32 v43, 2
buffer_store_dword v43, v42, s[16:19], s22 offen sc1
s_waitcnt vmcnt(9)
s_cmp_eq_u32 s20, 0
s_cbranch_scc1 .Lrd4n
s_barrier
.Lrd4n:
ds_read_b128 v[8:11], v4 offset:0
ds_read_b128 v[12:15], v4 offset:1024
ds_read_b128 v[16:19], v4 offset:2048
ds_read_b128 v[20:23], v4 offset:3072
ds_read_b128 v[24:27], v4 offset:4096
ds_read_b128 v[28:31], v4 offset:5120
ds_read_b128 v[80:83], v250 offset:49152
ds_read_b128 v[96:99], v250 offset:50176
ds_read_b128 v[112:115], v250 offset:51200
ds_read_b128 v[128:131], v250 offset:52224
ds_read_b128 v[144:147], v250 offset:53248
ds_read_b128 v[160:163], v250 offset:54272
ds_read_b128 v[176:179], v250 offset:55296
v_mov_b32 v198, 0
v_mov_b32 v199, 0
s_waitcnt lgkmcnt(6)
v_pk_add_f32 v[80:81], v[80:81], v[82:83]
s_waitcnt lgkmcnt(5)
v_pk_add_f32 v[96:97], v[96:97], v[98:99]
s_waitcnt lgkmcnt(4)
v_pk_add_f32 v[112:113], v[112:113], v[114:115]
s_waitcnt lgkmcnt(3)
v_pk_add_f32 v[128:129], v[128:129], v[130:131]
s_waitcnt lgkmcnt(2)
v_pk_add_f32 v[144:145], v[144:145], v[146:147]
s_waitcnt lgkmcnt(1)
v_pk_add_f32 v[160:161], v[160:161], v[162:163]
s_waitcnt lgkmcnt(0)
v_pk_add_f32 v[176:177], v[176:177], v[178:179]
v_add_f32 v80, v80, v81
v_add_f32 v96, v96, v97
v_add_f32 v112, v112, v113
v_add_f32 v128, v128, v129
v_add_f32 v144, v144, v145
v_add_f32 v160, v160, v161
v_add_f32 v176, v176, v177
v_cvt_pk_bf16_f32 v184, v80, v80
v_cvt_pk_bf16_f32 v186, v96, v96
v_cvt_pk_bf16_f32 v188, v112, v112
v_cvt_pk_bf16_f32 v190, v128, v128
v_cvt_pk_bf16_f32 v192, v144, v144
v_cvt_pk_bf16_f32 v194, v160, v160
v_cvt_pk_bf16_f32 v196, v176, v176
v_lshlrev_b32 v81, 16, v184
v_lshlrev_b32 v97, 16, v186
v_lshlrev_b32 v113, 16, v188
v_lshlrev_b32 v129, 16, v190
v_lshlrev_b32 v145, 16, v192
v_lshlrev_b32 v161, 16, v194
v_lshlrev_b32 v177, 16, v196
v_sub_f32 v80, v80, v81
v_sub_f32 v96, v96, v97
v_sub_f32 v112, v112, v113
v_sub_f32 v128, v128, v129
v_sub_f32 v144, v144, v145
v_sub_f32 v160, v160, v161
v_sub_f32 v176, v176, v177
v_cvt_pk_bf16_f32 v185, v80, v80
v_cvt_pk_bf16_f32 v187, v96, v96
v_cvt_pk_bf16_f32 v189, v112, v112
v_cvt_pk_bf16_f32 v191, v128, v128
v_cvt_pk_bf16_f32 v193, v144, v144
v_cvt_pk_bf16_f32 v195, v160, v160
v_cvt_pk_bf16_f32 v197, v176, v176
s_waitcnt vmcnt(8)
v_mfma_f32_16x16x32_bf16 v[72:75], v[8:11], a[0:3], 0
ds_read_b128 v[32:35], v4 offset:6144
s_waitcnt vmcnt(7)
v_mfma_f32_16x16x32_bf16 v[76:79], v[8:11], a[4:7], 0
s_add_u32 s31, s31, 4096
buffer_load_dwordx4 a[64:67], v1, s[16:19], s31 offen offset:0 sc1
v_mfma_f32_16x16x32_bf16 v[88:91], v[12:15], a[0:3], 0
ds_read_b128 v[36:39], v4 offset:7168
v_mfma_f32_16x16x32_bf16 v[92:95], v[12:15], a[4:7], 0
v_mfma_f32_16x16x32_bf16 v[104:107], v[16:19], a[0:3], 0
ds_read_b128 v[8:11], v4 offset:8192
buffer_load_dwordx4 a[68:71], v1, s[16:19], s31 offen offset:256 sc1
v_mfma_f32_16x16x32_bf16 v[108:111], v[16:19], a[4:7], 0
v_mfma_f32_16x16x32_bf16 v[120:123], v[20:23], a[0:3], 0
ds_read_b128 v[12:15], v4 offset:9216
v_mfma_f32_16x16x32_bf16 v[124:127], v[20:23], a[4:7], 0
buffer_load_dwordx4 v[200:203], v4, s[16:19], s35 offen offset:0 sc1
v_mfma_f32_16x16x32_bf16 v[136:139], v[24:27], a[0:3], 0
ds_read_b128 v[16:19], v4 offset:10240
v_mfma_f32_16x16x32_bf16 v[140:143], v[24:27], a[4:7], 0
v_mfma_f32_16x16x32_bf16 v[152:155], v[28:31], a[0:3], 0
ds_read_b128 v[20:23], v4 offset:11264
buffer_load_dwordx4 v[204:207], v4, s[16:19], s35 offen offset:1024 sc1
v_mfma_f32_16x16x32_bf16 v[156:159], v[28:31], a[4:7], 0
s_waitcnt lgkmcnt(5)
v_mfma_f32_16x16x32_bf16 v[168:171], v[32:35], a[0:3], 0
ds_read_b128 v[24:27], v4 offset:12288
v_mfma_f32_16x16x32_bf16 v[172:175], v[32:35], a[4:7], 0
buffer_load_dwordx4 v[208:211], v4, s[16:19], s35 offen offset:2048 sc1
s_waitcnt vmcnt(11) lgkmcnt(5)
v_mfma_f32_16x16x32_bf16 v[72:75], v[36:39], a[16:19], v[72:75]
ds_read_b128 v[28:31], v4 offset:13312
s_waitcnt vmcnt(10)
v_mfma_f32_16x16x32_bf16 v[76:79], v[36:39], a[20:23], v[76:79]
s_waitcnt lgkmcnt(5)
v_mfma_f32_16x16x32_bf16 v[88:91], v[8:11], a[16:19], v[88:91]
ds_read_b128 v[32:35], v4 offset:14336
buffer_load_dwordx4 v[212:215], v4, s[16:19], s35 offen offset:3072 sc1
v_mfma_f32_16x16x32_bf16 v[92:95], v[8:11], a[20:23], v[92:95]
s_waitcnt lgkmcnt(5)
v_mfma_f32_16x16x32_bf16 v[104:107], v[12:15], a[16:19], v[104:107]
ds_read_b128 v[36:39], v4 offset:15360
v_mfma_f32_16x16x32_bf16 v[108:111], v[12:15], a[20:23], v[108:111]
s_add_u32 s35, s35, 4096
buffer_load_dwordx4 v[216:219], v4, s[16:19], s35 offen offset:0 sc1
s_waitcnt lgkmcnt(5)
v_mfma_f32_16x16x32_bf16 v[120:123], v[16:19], a[16:19], v[120:123]
ds_read_b128 v[8:11], v4 offset:16384
v_mfma_f32_16x16x32_bf16 v[124:127], v[16:19], a[20:23], v[124:127]
s_waitcnt lgkmcnt(5)
v_mfma_f32_16x16x32_bf16 v[136:139], v[20:23], a[16:19], v[136:139]
ds_read_b128 v[12:15], v4 offset:17408
buffer_load_dwordx4 v[220:223], v4, s[16:19], s35 offen offset:1024 sc1
v_mfma_f32_16x16x32_bf16 v[140:143], v[20:23], a[20:23], v[140:143]
s_waitcnt lgkmcnt(5)
v_mfma_f32_16x16x32_bf16 v[152:155], v[24:27], a[16:19], v[152:155]
ds_read_b128 v[16:19], v4 offset:18432
v_mfma_f32_16x16x32_bf16 v[156:159], v[24:27], a[20:23], v[156:159]
buffer_load_dwordx4 v[224:227], v4, s[16:19], s35 offen offset:2048 sc1
s_waitcnt lgkmcnt(5)
v_mfma_f32_16x16x32_bf16 v[168:171], v[28:31], a[16:19], v[168:171]
ds_read_b128 v[20:23], v4 offset:19456
v_mfma_f32_16x16x32_bf16 v[172:175], v[28:31], a[20:23], v[172:175]
s_waitcnt vmcnt(13) lgkmcnt(5)
v_mfma_f32_16x16x32_bf16 v[72:75], v[32:35], a[32:35], v[72:75]
ds_read_b128 v[24:27], v4 offset:20480
buffer_load_dwordx4 v[228:231], v4, s[16:19], s35 offen offset:3072 sc1
s_waitcnt vmcnt(13)
v_mfma_f32_16x16x32_bf16 v[76:79], v[32:35], a[36:39], v[76:79]
s_waitcnt lgkmcnt(5)
v_mfma_f32_16x16x32_bf16 v[88:91], v[36:39], a[32:35], v[88:91]
ds_read_b128 v[28:31], v4 offset:21504
v_mfma_f32_16x16x32_bf16 v[92:95], v[36:39], a[36:39], v[92:95]
buffer_load_dwordx4 a[80:83], v1, s[16:19], s31 offen offset:2048 sc1
s_waitcnt lgkmcnt(5)
v_mfma_f32_16x16x32_bf16 v[104:107], v[8:11], a[32:35], v[104:107]
ds_read_b128 v[32:35], v4 offset:22528
v_mfma_f32_16x16x32_bf16 v[108:111], v[8:11], a[36:39], v[108:111]
s_waitcnt lgkmcnt(5)
v_mfma_f32_16x16x32_bf16 v[120:123], v[12:15], a[32:35], v[120:123]
ds_read_b128 v[36:39], v4 offset:23552
buffer_load_dwordx4 a[84:87], v1, s[16:19], s31 offen offset:2304 sc1
v_mfma_f32_16x16x32_bf16 v[124:127], v[12:15], a[36:39], v[124:127]
s_waitcnt lgkmcnt(5)
v_mfma_f32_16x16x32_bf16 v[136:139], v[16:19], a[32:35], v[136:139]
ds_read_b128 v[8:11], v4 offset:24576
v_mfma_f32_16x16x32_bf16 v[140:143], v[16:19], a[36:39], v[140:143]
s_add_u32 s31, s31, 4096
buffer_load_dwordx4 a[96:99], v1, s[16:19], s31 offen offset:0 sc1
s_waitcnt lgkmcnt(5)
v_mfma_f32_16x16x32_bf16 v[152:155], v[20:23], a[32:35], v[152:155]
ds_read_b128 v[12:15], v4 offset:25600
v_mfma_f32_16x16x32_bf16 v[156:159], v[20:23], a[36:39], v[156:159]
s_waitcnt lgkmcnt(5)
v_mfma_f32_16x16x32_bf16 v[168:171], v[24:27], a[32:35], v[168:171]
ds_read_b128 v[16:19], v4 offset:26624
buffer_load_dwordx4 a[100:103], v1, s[16:19], s31 offen offset:256 sc1
v_mfma_f32_16x16x32_bf16 v[172:175], v[24:27], a[36:39], v[172:175]
s_waitcnt vmcnt(16) lgkmcnt(5)
v_mfma_f32_16x16x32_bf16 v[72:75], v[28:31], a[48:51], v[72:75]
ds_read_b128 v[20:23], v4 offset:27648
s_waitcnt vmcnt(15)
v_mfma_f32_16x16x32_bf16 v[76:79], v[28:31], a[52:55], v[76:79]
buffer_load_dwordx4 a[112:115], v1, s[16:19], s31 offen offset:2048 sc1
s_waitcnt lgkmcnt(5)
v_mfma_f32_16x16x32_bf16 v[88:91], v[32:35], a[48:51], v[88:91]
ds_read_b128 v[24:27], v4 offset:28672
v_mfma_f32_16x16x32_bf16 v[92:95], v[32:35], a[52:55], v[92:95]
s_waitcnt lgkmcnt(5)
v_mfma_f32_16x16x32_bf16 v[104:107], v[36:39], a[48:51], v[104:107]
ds_read_b128 v[28:31], v4 offset:29696
buffer_load_dwordx4 a[116:119], v1, s[16:19], s31 offen offset:2304 sc1
v_mfma_f32_16x16x32_bf16 v[108:111], v[36:39], a[52:55], v[108:111]
s_waitcnt lgkmcnt(5)
v_mfma_f32_16x16x32_bf16 v[120:123], v[8:11], a[48:51], v[120:123]
ds_read_b128 v[32:35], v4 offset:30720
v_mfma_f32_16x16x32_bf16 v[124:127], v[8:11], a[52:55], v[124:127]
s_add_u32 s31, s31, 4096
buffer_load_dwordx4 a[128:131], v1, s[16:19], s31 offen offset:0 sc1
s_waitcnt lgkmcnt(5)
v_mfma_f32_16x16x32_bf16 v[136:139], v[12:15], a[48:51], v[136:139]
ds_read_b128 v[36:39], v4 offset:31744
v_mfma_f32_16x16x32_bf16 v[140:143], v[12:15], a[52:55], v[140:143]
s_waitcnt lgkmcnt(5)
v_mfma_f32_16x16x32_bf16 v[152:155], v[16:19], a[48:51], v[152:155]
ds_read_b128 v[8:11], v4 offset:32768
buffer_load_dwordx4 a[132:135], v1, s[16:19], s31 offen offset:256 sc1
v_mfma_f32_16x16x32_bf16 v[156:159], v[16:19], a[52:55], v[156:159]
s_waitcnt lgkmcnt(5)
v_mfma_f32_16x16x32_bf16 v[168:171], v[20:23], a[48:51], v[168:171]
ds_read_b128 v[12:15], v4 offset:33792
v_mfma_f32_16x16x32_bf16 v[172:175], v[20:23], a[52:55], v[172:175]
buffer_load_dwordx4 a[144:147], v1, s[16:19], s31 offen offset:2048 sc1
s_waitcnt vmcnt(18) lgkmcnt(5)
v_mfma_f32_16x16x32_bf16 v[72:75], v[24:27], a[64:67], v[72:75]
ds_read_b128 v[16:19], v4 offset:34816
s_waitcnt vmcnt(17)
v_mfma_f32_16x16x32_bf16 v[76:79], v[24:27], a[68:71], v[76:79]
s_waitcnt lgkmcnt(5)
v_mfma_f32_16x16x32_bf16 v[88:91], v[28:31], a[64:67], v[88:91]
ds_read_b128 v[20:23], v4 offset:35840
buffer_load_dwordx4 a[148:151], v1, s[16:19], s31 offen offset:2304 sc1
v_mfma_f32_16x16x32_bf16 v[92:95], v[28:31], a[68:71], v[92:95]
s_waitcnt lgkmcnt(5)
v_mfma_f32_16x16x32_bf16 v[104:107], v[32:35], a[64:67], v[104:107]
ds_read_b128 v[24:27], v4 offset:36864
v_mfma_f32_16x16x32_bf16 v[108:111], v[32:35], a[68:71], v[108:111]
s_add_u32 s31, s31, 4096
buffer_load_dwordx4 a[160:163], v1, s[16:19], s31 offen offset:0 sc1
s_waitcnt lgkmcnt(5)
v_mfma_f32_16x16x32_bf16 v[120:123], v[36:39], a[64:67], v[120:123]
ds_read_b128 v[28:31], v4 offset:37888
v_mfma_f32_16x16x32_bf16 v[124:127], v[36:39], a[68:71], v[124:127]
s_waitcnt lgkmcnt(5)
v_mfma_f32_16x16x32_bf16 v[136:139], v[8:11], a[64:67], v[136:139]
ds_read_b128 v[32:35], v4 offset:38912
buffer_load_dwordx4 a[164:167], v1, s[16:19], s31 offen offset:256 sc1
v_mfma_f32_16x16x32_bf16 v[140:143], v[8:11], a[68:71], v[140:143]
s_waitcnt lgkmcnt(5)
v_mfma_f32_16x16x32_bf16 v[152:155], v[12:15], a[64:67], v[152:155]
ds_read_b128 v[36:39], v4 offset:39936
v_mfma_f32_16x16x32_bf16 v[156:159], v[12:15], a[68:71], v[156:159]
buffer_load_dwordx4 a[176:179], v1, s[16:19], s31 offen offset:2048 sc1
s_waitcnt lgkmcnt(5)
v_mfma_f32_16x16x32_bf16 v[168:171], v[16:19], a[64:67], v[168:171]
ds_read_b128 v[8:11], v4 offset:40960
v_mfma_f32_16x16x32_bf16 v[172:175], v[16:19], a[68:71], v[172:175]
s_waitcnt vmcnt(12) lgkmcnt(5)
v_mfma_f32_16x16x32_bf16 v[72:75], v[20:23], a[80:83], v[72:75]
ds_read_b128 v[12:15], v4 offset:41984
buffer_load_dwordx4 a[180:183], v1, s[16:19], s31 offen offset:2304 sc1
s_waitcnt vmcnt(12)
v_mfma_f32_16x16x32_bf16 v[76:79], v[20:23], a[84:87], v[76:79]
s_waitcnt lgkmcnt(5)
v_mfma_f32_16x16x32_bf16 v[88:91], v[24:27], a[80:83], v[88:91]
ds_read_b128 v[16:19], v4 offset:43008
v_mfma_f32_16x16x32_bf16 v[92:95], v[24:27], a[84:87], v[92:95]
s_add_u32 s31, s31, 4096
buffer_load_dwordx4 a[192:195], v1, s[16:19], s31 offen offset:0 sc1
s_waitcnt lgkmcnt(5)
v_mfma_f32_16x16x32_bf16 v[104:107], v[28:31], a[80:83], v[104:107]
ds_read_b128 v[20:23], v4 offset:44032
v_mfma_f32_16x16x32_bf16 v[108:111], v[28:31], a[84:87], v[108:111]
s_waitcnt lgkmcnt(5)
v_mfma_f32_16x16x32_bf16 v[120:123], v[32:35], a[80:83], v[120:123]
ds_read_b128 v[24:27], v4 offset:45056
buffer_load_dwordx4 a[196:199], v1, s[16:19], s31 offen offset:256 sc1
v_mfma_f32_16x16x32_bf16 v[124:127], v[32:35], a[84:87], v[124:127]
s_waitcnt lgkmcnt(5)
v_mfma_f32_16x16x32_bf16 v[136:139], v[36:39], a[80:83], v[136:139]
ds_read_b128 v[28:31], v4 offset:46080
v_mfma_f32_16x16x32_bf16 v[140:143], v[36:39], a[84:87], v[140:143]
buffer_load_dwordx4 a[208:211], v1, s[16:19], s31 offen offset:2048 sc1
s_waitcnt lgkmcnt(5)
v_mfma_f32_16x16x32_bf16 v[152:155], v[8:11], a[80:83], v[152:155]
ds_read_b128 v[32:35], v4 offset:47104
v_mfma_f32_16x16x32_bf16 v[156:159], v[8:11], a[84:87], v[156:159]
s_waitcnt lgkmcnt(5)
v_mfma_f32_16x16x32_bf16 v[168:171], v[12:15], a[80:83], v[168:171]
ds_read_b128 v[36:39], v4 offset:48128
buffer_load_dwordx4 a[212:215], v1, s[16:19], s31 offen offset:2304 sc1
v_mfma_f32_16x16x32_bf16 v[172:175], v[12:15], a[84:87], v[172:175]
s_waitcnt vmcnt(15) lgkmcnt(5)
v_mfma_f32_16x16x32_bf16 v[72:75], v[16:19], a[96:99], v[72:75]
ds_read_b128 v[8:11], v4 offset:49152
s_waitcnt vmcnt(14)
v_mfma_f32_16x16x32_bf16 v[76:79], v[16:19], a[100:103], v[76:79]
s_add_u32 s31, s31, 4096
buffer_load_dwordx4 a[224:227], v1, s[16:19], s31 offen offset:0 sc1
s_waitcnt lgkmcnt(5)
v_mfma_f32_16x16x32_bf16 v[88:91], v[20:23], a[96:99], v[88:91]
ds_read_b128 v[12:15], v4 offset:50176
v_mfma_f32_16x16x32_bf16 v[92:95], v[20:23], a[100:103], v[92:95]
s_waitcnt lgkmcnt(5)
v_mfma_f32_16x16x32_bf16 v[104:107], v[24:27], a[96:99], v[104:107]
ds_read_b128 v[16:19], v4 offset:51200
buffer_load_dwordx4 a[228:231], v1, s[16:19], s31 offen offset:256 sc1
v_mfma_f32_16x16x32_bf16 v[108:111], v[24:27], a[100:103], v[108:111]
s_waitcnt lgkmcnt(5)
v_mfma_f32_16x16x32_bf16 v[120:123], v[28:31], a[96:99], v[120:123]
ds_read_b128 v[20:23], v4 offset:52224
v_mfma_f32_16x16x32_bf16 v[124:127], v[28:31], a[100:103], v[124:127]
buffer_load_dwordx4 a[240:243], v1, s[16:19], s31 offen offset:2048 sc1
s_waitcnt lgkmcnt(5)
v_mfma_f32_16x16x32_bf16 v[136:139], v[32:35], a[96:99], v[136:139]
ds_read_b128 v[24:27], v4 offset:53248
v_mfma_f32_16x16x32_bf16 v[140:143], v[32:35], a[100:103], v[140:143]
s_waitcnt lgkmcnt(5)
v_mfma_f32_16x16x32_bf16 v[152:155], v[36:39], a[96:99], v[152:155]
ds_read_b128 v[28:31], v4 offset:54272
buffer_load_dwordx4 a[244:247], v1, s[16:19], s31 offen offset:2304 sc1
v_mfma_f32_16x16x32_bf16 v[156:159], v[36:39], a[100:103], v[156:159]
s_waitcnt lgkmcnt(5)
v_mfma_f32_16x16x32_bf16 v[168:171], v[8:11], a[96:99], v[168:171]
ds_read_b128 v[32:35], v4 offset:55296
v_mfma_f32_16x16x32_bf16 v[172:175], v[8:11], a[100:103], v[172:175]
buffer_load_dwordx4 a[8:11], v1, s[16:19], s32 offen offset:0 sc1
s_waitcnt vmcnt(18) lgkmcnt(5)
v_mfma_f32_16x16x32_bf16 v[72:75], v[12:15], a[112:115], v[72:75]
ds_read_b128 v[36:39], v4 offset:56320
s_waitcnt vmcnt(17)
v_mfma_f32_16x16x32_bf16 v[76:79], v[12:15], a[116:119], v[76:79]
s_waitcnt lgkmcnt(5)
v_mfma_f32_16x16x32_bf16 v[88:91], v[16:19], a[112:115], v[88:91]
ds_read_b128 v[8:11], v4 offset:57344
buffer_load_dwordx4 a[12:15], v1, s[16:19], s32 offen offset:256 sc1
v_mfma_f32_16x16x32_bf16 v[92:95], v[16:19], a[116:119], v[92:95]
s_waitcnt lgkmcnt(5)
v_mfma_f32_16x16x32_bf16 v[104:107], v[20:23], a[112:115], v[104:107]
ds_read_b128 v[12:15], v4 offset:58368
v_mfma_f32_16x16x32_bf16 v[108:111], v[20:23], a[116:119], v[108:111]
buffer_load_dwordx4 a[24:27], v1, s[16:19], s32 offen offset:2048 sc1
s_waitcnt lgkmcnt(5)
v_mfma_f32_16x16x32_bf16 v[120:123], v[24:27], a[112:115], v[120:123]
ds_read_b128 v[16:19], v4 offset:59392
v_mfma_f32_16x16x32_bf16 v[124:127], v[24:27], a[116:119], v[124:127]
s_waitcnt lgkmcnt(5)
v_mfma_f32_16x16x32_bf16 v[136:139], v[28:31], a[112:115], v[136:139]
ds_read_b128 v[20:23], v4 offset:60416
buffer_load_dwordx4 a[28:31], v1, s[16:19], s32 offen offset:2304 sc1
v_mfma_f32_16x16x32_bf16 v[140:143], v[28:31], a[116:119], v[140:143]
s_waitcnt lgkmcnt(5)
v_mfma_f32_16x16x32_bf16 v[152:155], v[32:35], a[112:115], v[152:155]
ds_read_b128 v[24:27], v4 offset:61440
v_mfma_f32_16x16x32_bf16 v[156:159], v[32:35], a[116:119], v[156:159]
s_add_u32 s32, s32, 4096
buffer_load_dwordx4 a[40:43], v1, s[16:19], s32 offen offset:0 sc1
s_waitcnt lgkmcnt(5)
v_mfma_f32_16x16x32_bf16 v[168:171], v[36:39], a[112:115], v[168:171]
ds_read_b128 v[28:31], v4 offset:62464
v_mfma_f32_16x16x32_bf16 v[172:175], v[36:39], a[116:119], v[172:175]
s_waitcnt vmcnt(20) lgkmcnt(5)
v_mfma_f32_16x16x32_bf16 v[72:75], v[8:11], a[128:131], v[72:75]
ds_read_b128 v[32:35], v4 offset:63488
buffer_load_dwordx4 a[44:47], v1, s[16:19], s32 offen offset:256 sc1
s_waitcnt vmcnt(20)
v_mfma_f32_16x16x32_bf16 v[76:79], v[8:11], a[132:135], v[76:79]
s_waitcnt lgkmcnt(5)
v_mfma_f32_16x16x32_bf16 v[88:91], v[12:15], a[128:131], v[88:91]
ds_read_b128 v[36:39], v4 offset:64512
v_mfma_f32_16x16x32_bf16 v[92:95], v[12:15], a[132:135], v[92:95]
buffer_load_dwordx4 a[56:59], v1, s[16:19], s32 offen offset:2048 sc1
s_waitcnt lgkmcnt(5)
v_mfma_f32_16x16x32_bf16 v[104:107], v[16:19], a[128:131], v[104:107]
ds_read_b128 v[8:11], v250 offset:0
v_mfma_f32_16x16x32_bf16 v[108:111], v[16:19], a[132:135], v[108:111]
s_waitcnt lgkmcnt(5)
v_mfma_f32_16x16x32_bf16 v[120:123], v[20:23], a[128:131], v[120:123]
ds_read_b128 v[12:15], v250 offset:1024
buffer_load_dwordx4 a[60:63], v1, s[16:19], s32 offen offset:2304 sc1
v_mfma_f32_16x16x32_bf16 v[124:127], v[20:23], a[132:135], v[124:127]
s_waitcnt lgkmcnt(5)
v_mfma_f32_16x16x32_bf16 v[136:139], v[24:27], a[128:131], v[136:139]
ds_read_b128 v[16:19], v250 offset:2048
v_mfma_f32_16x16x32_bf16 v[140:143], v[24:27], a[132:135], v[140:143]
s_add_u32 s32, s32, 4096
buffer_load_dwordx4 a[72:75], v1, s[16:19], s32 offen offset:0 sc1
s_waitcnt lgkmcnt(5)
v_mfma_f32_16x16x32_bf16 v[152:155], v[28:31], a[128:131], v[152:155]
ds_read_b128 v[20:23], v250 offset:3072
v_mfma_f32_16x16x32_bf16 v[156:159], v[28:31], a[132:135], v[156:159]
s_waitcnt lgkmcnt(5)
v_mfma_f32_16x16x32_bf16 v[168:171], v[32:35], a[128:131], v[168:171]
ds_read_b128 v[24:27], v250 offset:4096
buffer_load_dwordx4 a[76:79], v1, s[16:19], s32 offen offset:256 sc1
v_mfma_f32_16x16x32_bf16 v[172:175], v[32:35], a[132:135], v[172:175]
s_waitcnt vmcnt(23) lgkmcnt(5)
v_mfma_f32_16x16x32_bf16 v[72:75], v[36:39], a[144:147], v[72:75]
ds_read_b128 v[28:31], v250 offset:5120
v_add_f32 v200, v200, v202
s_waitcnt vmcnt(22)
v_mfma_f32_16x16x32_bf16 v[76:79], v[36:39], a[148:151], v[76:79]
buffer_load_dwordx4 a[88:91], v1, s[16:19], s32 offen offset:2048 sc1
v_add_f32 v201, v201, v203
s_waitcnt lgkmcnt(5)
v_mfma_f32_16x16x32_bf16 v[88:91], v[8:11], a[144:147], v[88:91]
ds_read_b128 v[32:35], v250 offset:6144
v_add_f32 v204, v204, v206
v_mfma_f32_16x16x32_bf16 v[92:95], v[8:11], a[148:151], v[92:95]
v_add_f32 v205, v205, v207
v_add_f32 v200, v200, v204
s_waitcnt lgkmcnt(5)
v_mfma_f32_16x16x32_bf16 v[104:107], v[12:15], a[144:147], v[104:107]
ds_read_b128 v[36:39], v250 offset:7168
buffer_load_dwordx4 a[92:95], v1, s[16:19], s32 offen offset:2304 sc1
v_mfma_f32_16x16x32_bf16 v[108:111], v[12:15], a[148:151], v[108:111]
v_add_f32 v201, v201, v205
v_add_f32 v208, v208, v210
s_waitcnt lgkmcnt(5)
v_mfma_f32_16x16x32_bf16 v[120:123], v[16:19], a[144:147], v[120:123]
ds_read_b128 v[8:11], v250 offset:8192
v_add_f32 v209, v209, v211
v_mfma_f32_16x16x32_bf16 v[124:127], v[16:19], a[148:151], v[124:127]
s_add_u32 s32, s32, 4096
buffer_load_dwordx4 a[104:107], v1, s[16:19], s32 offen offset:0 sc1
s_waitcnt lgkmcnt(5)
v_mfma_f32_16x16x32_bf16 v[136:139], v[20:23], a[144:147], v[136:139]
ds_read_b128 v[12:15], v250 offset:9216
v_add_f32 v212, v212, v214
v_mfma_f32_16x16x32_bf16 v[140:143], v[20:23], a[148:151], v[140:143]
v_add_f32 v213, v213, v215
v_add_f32 v208, v208, v212
s_waitcnt lgkmcnt(5)
v_mfma_f32_16x16x32_bf16 v[152:155], v[24:27], a[144:147], v[152:155]
ds_read_b128 v[16:19], v250 offset:10240
buffer_load_dwordx4 a[108:111], v1, s[16:19], s32 offen offset:256 sc1
v_mfma_f32_16x16x32_bf16 v[156:159], v[24:27], a[148:151], v[156:159]
v_add_f32 v209, v209, v213
v_add_f32 v216, v216, v218
s_waitcnt lgkmcnt(5)
v_mfma_f32_16x16x32_bf16 v[168:171], v[28:31], a[144:147], v[168:171]
ds_read_b128 v[20:23], v250 offset:11264
v_add_f32 v217, v217, v219
v_mfma_f32_16x16x32_bf16 v[172:175], v[28:31], a[148:151], v[172:175]
buffer_load_dwordx4 a[120:123], v1, s[16:19], s32 offen offset:2048 sc1
v_add_f32 v220, v220, v222
s_waitcnt vmcnt(26) lgkmcnt(5)
v_mfma_f32_16x16x32_bf16 v[72:75], v[32:35], a[160:163], v[72:75]
ds_read_b128 v[24:27], v250 offset:12288
v_add_f32 v221, v221, v223
s_waitcnt vmcnt(25)
v_mfma_f32_16x16x32_bf16 v[76:79], v[32:35], a[164:167], v[76:79]
v_add_f32 v216, v216, v220
v_add_f32 v217, v217, v221
s_waitcnt lgkmcnt(5)
v_mfma_f32_16x16x32_bf16 v[88:91], v[36:39], a[160:163], v[88:91]
ds_read_b128 v[28:31], v250 offset:13312
buffer_load_dwordx4 a[124:127], v1, s[16:19], s32 offen offset:2304 sc1
v_mfma_f32_16x16x32_bf16 v[92:95], v[36:39], a[164:167], v[92:95]
v_add_f32 v224, v224, v226
v_add_f32 v225, v225, v227
s_waitcnt lgkmcnt(5)
v_mfma_f32_16x16x32_bf16 v[104:107], v[8:11], a[160:163], v[104:107]
ds_read_b128 v[32:35], v250 offset:14336
v_add_f32 v228, v228, v230
v_mfma_f32_16x16x32_bf16 v[108:111], v[8:11], a[164:167], v[108:111]
s_add_u32 s32, s32, 4096
buffer_load_dwordx4 a[136:139], v1, s[16:19], s32 offen offset:0 sc1
s_waitcnt lgkmcnt(5)
v_mfma_f32_16x16x32_bf16 v[120:123], v[12:15], a[160:163], v[120:123]
ds_read_b128 v[36:39], v250 offset:15360
v_add_f32 v229, v229, v231
v_mfma_f32_16x16x32_bf16 v[124:127], v[12:15], a[164:167], v[124:127]
v_add_f32 v224, v224, v228
v_add_f32 v225, v225, v229
s_waitcnt lgkmcnt(5)
v_mfma_f32_16x16x32_bf16 v[136:139], v[16:19], a[160:163], v[136:139]
ds_read_b128 v[8:11], v250 offset:16384
buffer_load_dwordx4 a[140:143], v1, s[16:19], s32 offen offset:256 sc1
v_mfma_f32_16x16x32_bf16 v[140:143], v[16:19], a[164:167], v[140:143]
v_permlane32_swap_b32 v200, v216
v_permlane32_swap_b32 v208, v224
s_waitcnt lgkmcnt(5)
v_mfma_f32_16x16x32_bf16 v[152:155], v[20:23], a[160:163], v[152:155]
ds_read_b128 v[12:15], v250 offset:17408
v_permlane32_swap_b32 v201, v217
v_mfma_f32_16x16x32_bf16 v[156:159], v[20:23], a[164:167], v[156:159]
buffer_load_dwordx4 a[152:155], v1, s[16:19], s32 offen offset:2048 sc1
v_permlane32_swap_b32 v209, v225
s_waitcnt lgkmcnt(5)
v_mfma_f32_16x16x32_bf16 v[168:171], v[24:27], a[160:163], v[168:171]
ds_read_b128 v[16:19], v250 offset:18432
v_add_f32 v200, v200, v216
v_mfma_f32_16x16x32_bf16 v[172:175], v[24:27], a[164:167], v[172:175]
v_add_f32 v208, v208, v224
v_add_f32 v201, v201, v217
s_waitcnt vmcnt(28) lgkmcnt(5)
v_mfma_f32_16x16x32_bf16 v[72:75], v[28:31], a[176:179], v[72:75]
ds_read_b128 v[20:23], v250 offset:19456
buffer_load_dwordx4 a[156:159], v1, s[16:19], s32 offen offset:2304 sc1
s_waitcnt vmcnt(28)
v_mfma_f32_16x16x32_bf16 v[76:79], v[28:31], a[180:183], v[76:79]
v_add_f32 v209, v209, v225
s_nop 0
s_waitcnt lgkmcnt(5)
v_mfma_f32_16x16x32_bf16 v[88:91], v[32:35], a[176:179], v[88:91]
ds_read_b128 v[24:27], v250 offset:20480
v_permlane16_swap_b32 v200, v208
v_mfma_f32_16x16x32_bf16 v[92:95], v[32:35], a[180:183], v[92:95]
s_add_u32 s32, s32, 4096
buffer_load_dwordx4 a[168:171], v1, s[16:19], s32 offen offset:0 sc1
s_waitcnt lgkmcnt(5)
v_mfma_f32_16x16x32_bf16 v[104:107], v[36:39], a[176:179], v[104:107]
ds_read_b128 v[28:31], v250 offset:21504
v_permlane16_swap_b32 v201, v209
v_mfma_f32_16x16x32_bf16 v[108:111], v[36:39], a[180:183], v[108:111]
v_add_f32 v200, v200, v208
v_add_f32 v201, v201, v209
s_waitcnt lgkmcnt(5)
v_mfma_f32_16x16x32_bf16 v[120:123], v[8:11], a[176:179], v[120:123]
ds_read_b128 v[32:35], v250 offset:22528
buffer_load_dwordx4 a[172:175], v1, s[16:19], s32 offen offset:256 sc1
v_mfma_f32_16x16x32_bf16 v[124:127], v[8:11], a[180:183], v[124:127]
v_mul_f32 v200, 0xbb000000, v200
v_mul_f32 v202, v200, v200
s_waitcnt lgkmcnt(5)
v_mfma_f32_16x16x32_bf16 v[136:139], v[12:15], a[176:179], v[136:139]
ds_read_b128 v[36:39], v250 offset:23552
v_fma_f32 v201, v201, s36, -v202
v_mfma_f32_16x16x32_bf16 v[140:143], v[12:15], a[180:183], v[140:143]
buffer_load_dwordx4 a[184:187], v1, s[16:19], s32 offen offset:2048 sc1
v_cmp_u_f32 s[40:41], v201, v201
s_waitcnt lgkmcnt(5)
v_mfma_f32_16x16x32_bf16 v[152:155], v[16:19], a[176:179], v[152:155]
ds_read_b128 v[8:11], v250 offset:24576
v_max_f32 v201, 0, v201
v_mfma_f32_16x16x32_bf16 v[156:159], v[16:19], a[180:183], v[156:159]
v_add_f32 v201, 0x3727c5ac, v201
v_sqrt_f32 v209, v201
s_waitcnt lgkmcnt(5)
v_mfma_f32_16x16x32_bf16 v[168:171], v[20:23], a[176:179], v[168:171]
ds_read_b128 v[12:15], v250 offset:25600
buffer_load_dwordx4 a[188:191], v1, s[16:19], s32 offen offset:2304 sc1
v_mfma_f32_16x16x32_bf16 v[172:175], v[20:23], a[180:183], v[172:175]
v_rsq_f32 v208, v201
v_mov_b32 v206, 0
s_waitcnt vmcnt(31) lgkmcnt(5)
v_mfma_f32_16x16x32_bf16 v[72:75], v[24:27], a[192:195], v[72:75]
ds_read_b128 v[16:19], v250 offset:26624
v_mov_b32 v207, 0
s_waitcnt vmcnt(30)
v_mfma_f32_16x16x32_bf16 v[76:79], v[24:27], a[196:199], v[76:79]
s_add_u32 s32, s32, 4096
buffer_load_dwordx4 a[200:203], v1, s[16:19], s32 offen offset:0 sc1
s_waitcnt lgkmcnt(5)
v_mfma_f32_16x16x32_bf16 v[88:91], v[28:31], a[192:195], v[88:91]
ds_read_b128 v[20:23], v250 offset:27648
v_cvt_pk_bf16_f32 v202, v200, v200
v_mfma_f32_16x16x32_bf16 v[92:95], v[28:31], a[196:199], v[92:95]
v_mul_f32 v208, 0xbfb8aa3b, v208
v_lshlrev_b32 v202, 16, v202
s_waitcnt lgkmcnt(5)
v_mfma_f32_16x16x32_bf16 v[104:107], v[32:35], a[192:195], v[104:107]
ds_read_b128 v[24:27], v250 offset:28672
buffer_load_dwordx4 a[204:207], v1, s[16:19], s32 offen offset:256 sc1
v_mfma_f32_16x16x32_bf16 v[108:111], v[32:35], a[196:199], v[108:111]
v_sub_f32 v202, v200, v202
v_cvt_pk_bf16_f32 v204, v200, v202
s_waitcnt lgkmcnt(5)
v_mfma_f32_16x16x32_bf16 v[120:123], v[36:39], a[192:195], v[120:123]
ds_read_b128 v[28:31], v250 offset:29696
v_cvt_pk_bf16_f32 v205, v200, v202
v_mfma_f32_16x16x32_bf16 v[124:127], v[36:39], a[196:199], v[124:127]
buffer_load_dwordx4 a[216:219], v1, s[16:19], s32 offen offset:2048 sc1
v_add_u32 v202, s30, v250
s_waitcnt lgkmcnt(5)
v_mfma_f32_16x16x32_bf16 v[136:139], v[8:11], a[192:195], v[136:139]
ds_read_b128 v[32:35], v250 offset:30720
ds_write_b128 v202, v[204:207] offset:57344
v_mfma_f32_16x16x32_bf16 v[140:143], v[8:11], a[196:199], v[140:143]
ds_write_b64 v202, v[208:209] offset:61440
ds_read_b128 v[232:235], v5 offset:0
s_waitcnt lgkmcnt(8)
v_mfma_f32_16x16x32_bf16 v[152:155], v[12:15], a[192:195], v[152:155]
ds_read_b128 v[36:39], v250 offset:31744
buffer_load_dwordx4 a[220:223], v1, s[16:19], s32 offen offset:2304 sc1
v_mfma_f32_16x16x32_bf16 v[156:159], v[12:15], a[196:199], v[156:159]
ds_read_b128 v[236:239], v5 offset:256
ds_read_b128 v[240:243], v5 offset:512
s_waitcnt lgkmcnt(10)
v_mfma_f32_16x16x32_bf16 v[168:171], v[16:19], a[192:195], v[168:171]
ds_read_b128 v[8:11], v250 offset:32768
ds_read_b128 v[244:247], v5 offset:768
v_mfma_f32_16x16x32_bf16 v[172:175], v[16:19], a[196:199], v[172:175]
s_add_u32 s32, s32, 4096
buffer_load_dwordx4 a[232:235], v1, s[16:19], s32 offen offset:0 sc1
s_waitcnt vmcnt(34) lgkmcnt(11)
v_mfma_f32_16x16x32_bf16 v[72:75], v[20:23], a[208:211], v[72:75]
ds_read_b128 v[12:15], v250 offset:33792
ds_read_b64 v[216:217], v5 offset:4096
s_waitcnt vmcnt(33)
v_mfma_f32_16x16x32_bf16 v[76:79], v[20:23], a[212:215], v[76:79]
ds_read_b64 v[218:219], v5 offset:4352
ds_read_b64 v[220:221], v5 offset:4608
s_waitcnt lgkmcnt(14)
v_mfma_f32_16x16x32_bf16 v[88:91], v[24:27], a[208:211], v[88:91]
ds_read_b128 v[16:19], v250 offset:34816
buffer_load_dwordx4 a[236:239], v1, s[16:19], s32 offen offset:256 sc1
v_mfma_f32_16x16x32_bf16 v[92:95], v[24:27], a[212:215], v[92:95]
ds_read_b64 v[222:223], v5 offset:4864
s_waitcnt lgkmcnt(15)
v_mfma_f32_16x16x32_bf16 v[104:107], v[28:31], a[208:211], v[104:107]
ds_read_b128 v[20:23], v250 offset:35840
v_mfma_f32_16x16x32_bf16 v[108:111], v[28:31], a[212:215], v[108:111]
buffer_load_dwordx4 a[248:251], v1, s[16:19], s32 offen offset:2048 sc1
s_waitcnt lgkmcnt(15)
v_mfma_f32_16x16x32_bf16 v[120:123], v[32:35], a[208:211], v[120:123]
ds_read_b128 v[24:27], v250 offset:36864
v_mfma_f32_16x16x32_bf16 v[124:127], v[32:35], a[212:215], v[124:127]
s_waitcnt lgkmcnt(12)
v_mfma_f32_16x16x32_bf16 v[136:139], v[36:39], a[208:211], v[136:139]
ds_read_b128 v[28:31], v250 offset:37888
buffer_load_dwordx4 a[252:255], v1, s[16:19], s32 offen offset:2304 sc1
v_mfma_f32_16x16x32_bf16 v[140:143], v[36:39], a[212:215], v[140:143]
s_waitcnt lgkmcnt(10)
v_mfma_f32_16x16x32_bf16 v[152:155], v[8:11], a[208:211], v[152:155]
ds_read_b128 v[32:35], v250 offset:38912
v_mfma_f32_16x16x32_bf16 v[80:83], v[184:187], v[240:243], 0
v_mfma_f32_16x16x32_bf16 v[156:159], v[8:11], a[212:215], v[156:159]
s_waitcnt lgkmcnt(10)
v_mfma_f32_16x16x32_bf16 v[84:87], v[184:187], v[244:247], 0
s_waitcnt lgkmcnt(9)
v_mfma_f32_16x16x32_bf16 v[168:171], v[12:15], a[208:211], v[168:171]
ds_read_b128 v[36:39], v250 offset:39936
v_mfma_f32_16x16x32_bf16 v[96:99], v[186:189], v[240:243], 0
v_mfma_f32_16x16x32_bf16 v[172:175], v[12:15], a[212:215], v[172:175]
v_mfma_f32_16x16x32_bf16 v[100:103], v[186:189], v[244:247], 0
s_waitcnt vmcnt(35) lgkmcnt(6)
v_mfma_f32_16x16x32_bf16 v[72:75], v[16:19], a[224:227], v[72:75]
ds_read_b128 v[8:11], v250 offset:40960
v_mfma_f32_16x16x32_bf16 v[112:115], v[188:191], v[240:243], 0
s_waitcnt vmcnt(34)
v_mfma_f32_16x16x32_bf16 v[76:79], v[16:19], a[228:231], v[76:79]
v_mfma_f32_16x16x32_bf16 v[116:119], v[188:191], v[244:247], 0
s_waitcnt lgkmcnt(5)
v_mfma_f32_16x16x32_bf16 v[88:91], v[20:23], a[224:227], v[88:91]
ds_read_b128 v[12:15], v250 offset:41984
v_mfma_f32_16x16x32_bf16 v[128:131], v[190:193], v[240:243], 0
v_mfma_f32_16x16x32_bf16 v[92:95], v[20:23], a[228:231], v[92:95]
v_mfma_f32_16x16x32_bf16 v[132:135], v[190:193], v[244:247], 0
s_waitcnt lgkmcnt(5)
v_mfma_f32_16x16x32_bf16 v[104:107], v[24:27], a[224:227], v[104:107]
ds_read_b128 v[16:19], v250 offset:43008
v_mfma_f32_16x16x32_bf16 v[144:147], v[192:195], v[240:243], 0
v_mfma_f32_16x16x32_bf16 v[108:111], v[24:27], a[228:231], v[108:111]
v_mfma_f32_16x16x32_bf16 v[148:151], v[192:195], v[244:247], 0
s_waitcnt lgkmcnt(5)
v_mfma_f32_16x16x32_bf16 v[120:123], v[28:31], a[224:227], v[120:123]
ds_read_b128 v[20:23], v250 offset:44032
v_mfma_f32_16x16x32_bf16 v[160:163], v[194:197], v[240:243], 0
v_mfma_f32_16x16x32_bf16 v[124:127], v[28:31], a[228:231], v[124:127]
v_mfma_f32_16x16x32_bf16 v[164:167], v[194:197], v[244:247], 0
s_waitcnt lgkmcnt(5)
v_mfma_f32_16x16x32_bf16 v[136:139], v[32:35], a[224:227], v[136:139]
ds_read_b128 v[24:27], v250 offset:45056
v_mfma_f32_16x16x32_bf16 v[176:179], v[196:199], v[240:243], 0
v_mfma_f32_16x16x32_bf16 v[140:143], v[32:35], a[228:231], v[140:143]
v_mfma_f32_16x16x32_bf16 v[180:183], v[196:199], v[244:247], 0
s_waitcnt lgkmcnt(5)
v_mfma_f32_16x16x32_bf16 v[152:155], v[36:39], a[224:227], v[152:155]
ds_read_b128 v[28:31], v250 offset:46080
v_mfma_f32_16x16x32_bf16 v[72:75], v[184:187], v[232:235], v[72:75]
v_mfma_f32_16x16x32_bf16 v[156:159], v[36:39], a[228:231], v[156:159]
v_mfma_f32_16x16x32_bf16 v[76:79], v[184:187], v[236:239], v[76:79]
s_waitcnt lgkmcnt(5)
v_mfma_f32_16x16x32_bf16 v[168:171], v[8:11], a[224:227], v[168:171]
ds_read_b128 v[32:35], v250 offset:47104
v_mfma_f32_16x16x32_bf16 v[88:91], v[186:189], v[232:235], v[88:91]
v_mfma_f32_16x16x32_bf16 v[172:175], v[8:11], a[228:231], v[172:175]
v_mfma_f32_16x16x32_bf16 v[92:95], v[186:189], v[236:239], v[92:95]
s_waitcnt vmcnt(33) lgkmcnt(5)
v_mfma_f32_16x16x32_bf16 v[72:75], v[12:15], a[240:243], v[72:75]
ds_read_b128 v[36:39], v250 offset:48128
v_mfma_f32_16x16x32_bf16 v[104:107], v[188:191], v[232:235], v[104:107]
s_waitcnt vmcnt(32)
v_mfma_f32_16x16x32_bf16 v[76:79], v[12:15], a[244:247], v[76:79]
v_mfma_f32_16x16x32_bf16 v[108:111], v[188:191], v[236:239], v[108:111]
s_waitcnt lgkmcnt(5)
v_mfma_f32_16x16x32_bf16 v[88:91], v[16:19], a[240:243], v[88:91]
ds_read_b128 v[8:11], v4 offset:0
v_mfma_f32_16x16x32_bf16 v[120:123], v[190:193], v[232:235], v[120:123]
v_mfma_f32_16x16x32_bf16 v[92:95], v[16:19], a[244:247], v[92:95]
v_mfma_f32_16x16x32_bf16 v[124:127], v[190:193], v[236:239], v[124:127]
s_waitcnt lgkmcnt(5)
v_mfma_f32_16x16x32_bf16 v[104:107], v[20:23], a[240:243], v[104:107]
ds_read_b128 v[12:15], v4 offset:1024
v_mfma_f32_16x16x32_bf16 v[136:139], v[192:195], v[232:235], v[136:139]
v_mfma_f32_16x16x32_bf16 v[108:111], v[20:23], a[244:247], v[108:111]
v_mfma_f32_16x16x32_bf16 v[140:143], v[192:195], v[236:239], v[140:143]
s_waitcnt lgkmcnt(5)
v_mfma_f32_16x16x32_bf16 v[120:123], v[24:27], a[240:243], v[120:123]
ds_read_b128 v[16:19], v4 offset:2048
v_mfma_f32_16x16x32_bf16 v[152:155], v[194:197], v[232:235], v[152:155]
v_mfma_f32_16x16x32_bf16 v[124:127], v[24:27], a[244:247], v[124:127]
v_mfma_f32_16x16x32_bf16 v[156:159], v[194:197], v[236:239], v[156:159]
s_waitcnt lgkmcnt(5)
v_mfma_f32_16x16x32_bf16 v[136:139], v[28:31], a[240:243], v[136:139]
ds_read_b128 v[20:23], v4 offset:3072
v_mfma_f32_16x16x32_bf16 v[168:171], v[196:199], v[232:235], v[168:171]
v_mfma_f32_16x16x32_bf16 v[140:143], v[28:31], a[244:247], v[140:143]
v_mfma_f32_16x16x32_bf16 v[172:175], v[196:199], v[236:239], v[172:175]
s_waitcnt lgkmcnt(5)
v_mfma_f32_16x16x32_bf16 v[152:155], v[32:35], a[240:243], v[152:155]
ds_read_b128 v[24:27], v4 offset:4096
v_mfma_f32_16x16x32_bf16 v[156:159], v[32:35], a[244:247], v[156:159]
s_waitcnt lgkmcnt(5)
v_mfma_f32_16x16x32_bf16 v[168:171], v[36:39], a[240:243], v[168:171]
ds_read_b128 v[28:31], v4 offset:5120
v_mfma_f32_16x16x32_bf16 v[172:175], v[36:39], a[244:247], v[172:175]
s_waitcnt vmcnt(31) lgkmcnt(5)
v_mfma_f32_16x16x32_bf16 v[80:83], v[8:11], a[8:11], v[80:83]
ds_read_b128 v[32:35], v4 offset:6144
v_mul_f32 v72, v72, v216
s_waitcnt vmcnt(30)
v_mfma_f32_16x16x32_bf16 v[84:87], v[8:11], a[12:15], v[84:87]
v_mul_f32 v73, v73, v216
v_mul_f32 v76, v76, v218
s_waitcnt lgkmcnt(5)
v_mfma_f32_16x16x32_bf16 v[96:99], v[12:15], a[8:11], v[96:99]
ds_read_b128 v[36:39], v4 offset:7168
v_mul_f32 v77, v77, v218
v_mfma_f32_16x16x32_bf16 v[100:103], v[12:15], a[12:15], v[100:103]
v_exp_f32 v72, v72
s_waitcnt lgkmcnt(5)
v_mfma_f32_16x16x32_bf16 v[112:115], v[16:19], a[8:11], v[112:115]
ds_read_b128 v[8:11], v4 offset:8192
v_fma_f32 v72, v72, v217, v217
v_mfma_f32_16x16x32_bf16 v[116:119], v[16:19], a[12:15], v[116:119]
v_exp_f32 v73, v73
s_waitcnt lgkmcnt(5)
v_mfma_f32_16x16x32_bf16 v[128:131], v[20:23], a[8:11], v[128:131]
ds_read_b128 v[12:15], v4 offset:9216
v_fma_f32 v73, v73, v217, v217
v_mfma_f32_16x16x32_bf16 v[132:135], v[20:23], a[12:15], v[132:135]
v_exp_f32 v76, v76
s_waitcnt lgkmcnt(5)
v_mfma_f32_16x16x32_bf16 v[144:147], v[24:27], a[8:11], v[144:147]
ds_read_b128 v[16:19], v4 offset:10240
v_fma_f32 v76, v76, v219, v219
v_mfma_f32_16x16x32_bf16 v[148:151], v[24:27], a[12:15], v[148:151]
v_exp_f32 v77, v77
s_waitcnt lgkmcnt(5)
v_mfma_f32_16x16x32_bf16 v[160:163], v[28:31], a[8:11], v[160:163]
ds_read_b128 v[20:23], v4 offset:11264
v_fma_f32 v77, v77, v219, v219
v_mfma_f32_16x16x32_bf16 v[164:167], v[28:31], a[12:15], v[164:167]
v_rcp_f32 v72, v72
s_waitcnt lgkmcnt(5)
v_mfma_f32_16x16x32_bf16 v[176:179], v[32:35], a[8:11], v[176:179]
ds_read_b128 v[24:27], v4 offset:12288
v_mul_f32 v72, v72, v74
v_mfma_f32_16x16x32_bf16 v[180:183], v[32:35], a[12:15], v[180:183]
v_rcp_f32 v73, v73
s_waitcnt vmcnt(29) lgkmcnt(5)
v_mfma_f32_16x16x32_bf16 v[80:83], v[36:39], a[24:27], v[80:83]
ds_read_b128 v[28:31], v4 offset:13312
v_mul_f32 v73, v73, v75
s_waitcnt vmcnt(28)
v_mfma_f32_16x16x32_bf16 v[84:87], v[36:39], a[28:31], v[84:87]
v_rcp_f32 v76, v76
s_waitcnt lgkmcnt(5)
v_mfma_f32_16x16x32_bf16 v[96:99], v[8:11], a[24:27], v[96:99]
ds_read_b128 v[32:35], v4 offset:14336
v_mul_f32 v76, v76, v78
v_mfma_f32_16x16x32_bf16 v[100:103], v[8:11], a[28:31], v[100:103]
s_add_u32 s48, s45, 0
buffer_load_dwordx4 a[0:3], v4, s[16:19], s48 offen offset:0 sc1
buffer_load_dwordx4 a[4:7], v4, s[16:19], s48 offen offset:1024 sc1
s_waitcnt lgkmcnt(5)
v_mfma_f32_16x16x32_bf16 v[112:115], v[12:15], a[24:27], v[112:115]
ds_read_b128 v[36:39], v4 offset:15360
v_cvt_pk_bf16_f32 v40, v72, v73
v_mfma_f32_16x16x32_bf16 v[116:119], v[12:15], a[28:31], v[116:119]
v_rcp_f32 v77, v77
s_waitcnt lgkmcnt(5)
v_mfma_f32_16x16x32_bf16 v[128:131], v[16:19], a[24:27], v[128:131]
ds_read_b128 v[8:11], v4 offset:16384
v_mul_f32 v77, v77, v79
v_mfma_f32_16x16x32_bf16 v[132:135], v[16:19], a[28:31], v[132:135]
v_mul_f32 v88, v88, v216
v_cvt_pk_bf16_f32 v48, v76, v77
s_waitcnt lgkmcnt(5)
v_mfma_f32_16x16x32_bf16 v[144:147], v[20:23], a[24:27], v[144:147]
ds_read_b128 v[12:15], v4 offset:17408
v_mul_f32 v89, v89, v216
v_mfma_f32_16x16x32_bf16 v[148:151], v[20:23], a[28:31], v[148:151]
v_mul_f32 v92, v92, v218
v_mul_f32 v93, v93, v218
s_waitcnt lgkmcnt(5)
v_mfma_f32_16x16x32_bf16 v[160:163], v[24:27], a[24:27], v[160:163]
ds_read_b128 v[16:19], v4 offset:18432
v_mfma_f32_16x16x32_bf16 v[164:167], v[24:27], a[28:31], v[164:167]
v_exp_f32 v88, v88
s_waitcnt lgkmcnt(5)
v_mfma_f32_16x16x32_bf16 v[176:179], v[28:31], a[24:27], v[176:179]
ds_read_b128 v[20:23], v4 offset:19456
v_fma_f32 v88, v88, v217, v217
v_mfma_f32_16x16x32_bf16 v[180:183], v[28:31], a[28:31], v[180:183]
v_exp_f32 v89, v89
s_waitcnt vmcnt(29) lgkmcnt(5)
v_mfma_f32_16x16x32_bf16 v[80:83], v[32:35], a[40:43], v[80:83]
ds_read_b128 v[24:27], v4 offset:20480
v_fma_f32 v89, v89, v217, v217
s_waitcnt vmcnt(28)
v_mfma_f32_16x16x32_bf16 v[84:87], v[32:35], a[44:47], v[84:87]
v_exp_f32 v92, v92
s_waitcnt lgkmcnt(5)
v_mfma_f32_16x16x32_bf16 v[96:99], v[36:39], a[40:43], v[96:99]
ds_read_b128 v[28:31], v4 offset:21504
v_fma_f32 v92, v92, v219, v219
v_mfma_f32_16x16x32_bf16 v[100:103], v[36:39], a[44:47], v[100:103]
v_exp_f32 v93, v93
s_waitcnt lgkmcnt(5)
v_mfma_f32_16x16x32_bf16 v[112:115], v[8:11], a[40:43], v[112:115]
ds_read_b128 v[32:35], v4 offset:22528
v_fma_f32 v93, v93, v219, v219
v_mfma_f32_16x16x32_bf16 v[116:119], v[8:11], a[44:47], v[116:119]
s_add_u32 s48, s45, 4096
buffer_load_dwordx4 a[16:19], v4, s[16:19], s48 offen offset:0 sc1
buffer_load_dwordx4 a[20:23], v4, s[16:19], s48 offen offset:1024 sc1
s_waitcnt lgkmcnt(5)
v_mfma_f32_16x16x32_bf16 v[128:131], v[12:15], a[40:43], v[128:131]
ds_read_b128 v[36:39], v4 offset:23552
v_mul_f32 v104, v104, v216
v_mfma_f32_16x16x32_bf16 v[132:135], v[12:15], a[44:47], v[132:135]
v_rcp_f32 v88, v88
s_waitcnt lgkmcnt(5)
v_mfma_f32_16x16x32_bf16 v[144:147], v[16:19], a[40:43], v[144:147]
ds_read_b128 v[8:11], v4 offset:24576
v_mul_f32 v88, v88, v90
v_mfma_f32_16x16x32_bf16 v[148:151], v[16:19], a[44:47], v[148:151]
v_rcp_f32 v89, v89
s_waitcnt lgkmcnt(5)
v_mfma_f32_16x16x32_bf16 v[160:163], v[20:23], a[40:43], v[160:163]
ds_read_b128 v[12:15], v4 offset:25600
v_mul_f32 v89, v89, v91
v_mfma_f32_16x16x32_bf16 v[164:167], v[20:23], a[44:47], v[164:167]
v_rcp_f32 v92, v92
s_waitcnt lgkmcnt(5)
v_mfma_f32_16x16x32_bf16 v[176:179], v[24:27], a[40:43], v[176:179]
ds_read_b128 v[16:19], v4 offset:26624
v_mul_f32 v92, v92, v94
v_mfma_f32_16x16x32_bf16 v[180:183], v[24:27], a[44:47], v[180:183]
v_rcp_f32 v93, v93
s_waitcnt vmcnt(29) lgkmcnt(5)
v_mfma_f32_16x16x32_bf16 v[80:83], v[28:31], a[56:59], v[80:83]
ds_read_b128 v[20:23], v4 offset:27648
v_mul_f32 v93, v93, v95
s_waitcnt vmcnt(28)
v_mfma_f32_16x16x32_bf16 v[84:87], v[28:31], a[60:63], v[84:87]
v_cvt_pk_bf16_f32 v41, v88, v89
v_cvt_pk_bf16_f32 v49, v92, v93
s_waitcnt lgkmcnt(5)
v_mfma_f32_16x16x32_bf16 v[96:99], v[32:35], a[56:59], v[96:99]
ds_read_b128 v[24:27], v4 offset:28672
v_mul_f32 v105, v105, v216
v_mfma_f32_16x16x32_bf16 v[100:103], v[32:35], a[60:63], v[100:103]
v_mul_f32 v108, v108, v218
v_mul_f32 v109, v109, v218
s_waitcnt lgkmcnt(5)
v_mfma_f32_16x16x32_bf16 v[112:115], v[36:39], a[56:59], v[112:115]
ds_read_b128 v[28:31], v4 offset:29696
v_mfma_f32_16x16x32_bf16 v[116:119], v[36:39], a[60:63], v[116:119]
v_exp_f32 v104, v104
s_bfe_u32 s48, %2, 0x10003
s_getpc_b64 s[50:51]
.Lpfk4:
s_add_u32 s50, s50, .Lk5s-.Lpfk4
s_addc_u32 s51, s51, 0
s_mul_i32 s48, s48, 20
v_min_u32 v254, 19, v0
v_add_lshl_u32 v254, v254, s48, 7
v_min_u32 v254, .Lk5e-.Lk5s-4, v254
global_load_dword v255, v254, s[50:51]
s_waitcnt lgkmcnt(5)
v_mfma_f32_16x16x32_bf16 v[128:131], v[8:11], a[56:59], v[128:131]
ds_read_b128 v[32:35], v4 offset:30720
v_fma_f32 v104, v104, v217, v217
v_mfma_f32_16x16x32_bf16 v[132:135], v[8:11], a[60:63], v[132:135]
s_add_u32 s48, s45, 8192
buffer_load_dwordx4 a[32:35], v4, s[16:19], s48 offen offset:0 sc1
buffer_load_dwordx4 a[36:39], v4, s[16:19], s48 offen offset:1024 sc1
s_waitcnt lgkmcnt(5)
v_mfma_f32_16x16x32_bf16 v[144:147], v[12:15], a[56:59], v[144:147]
ds_read_b128 v[36:39], v4 offset:31744
v_mfma_f32_16x16x32_bf16 v[148:151], v[12:15], a[60:63], v[148:151]
v_exp_f32 v105, v105
s_waitcnt lgkmcnt(5)
v_mfma_f32_16x16x32_bf16 v[160:163], v[16:19], a[56:59], v[160:163]
ds_read_b128 v[8:11], v4 offset:32768
v_fma_f32 v105, v105, v217, v217
v_mfma_f32_16x16x32_bf16 v[164:167], v[16:19], a[60:63], v[164:167]
v_exp_f32 v108, v108
s_waitcnt lgkmcnt(5)
v_mfma_f32_16x16x32_bf16 v[176:179], v[20:23], a[56:59], v[176:179]
ds_read_b128 v[12:15], v4 offset:33792
v_fma_f32 v108, v108, v219, v219
v_mfma_f32_16x16x32_bf16 v[180:183], v[20:23], a[60:63], v[180:183]
v_exp_f32 v109, v109
s_waitcnt vmcnt(30) lgkmcnt(5)
v_mfma_f32_16x16x32_bf16 v[80:83], v[24:27], a[72:75], v[80:83]
ds_read_b128 v[16:19], v4 offset:34816
v_fma_f32 v109, v109, v219, v219
s_waitcnt vmcnt(29)
v_mfma_f32_16x16x32_bf16 v[84:87], v[24:27], a[76:79], v[84:87]
v_rcp_f32 v104, v104
s_waitcnt lgkmcnt(5)
v_mfma_f32_16x16x32_bf16 v[96:99], v[28:31], a[72:75], v[96:99]
ds_read_b128 v[20:23], v4 offset:35840
v_mul_f32 v104, v104, v106
v_mfma_f32_16x16x32_bf16 v[100:103], v[28:31], a[76:79], v[100:103]
v_rcp_f32 v105, v105
s_waitcnt lgkmcnt(5)
v_mfma_f32_16x16x32_bf16 v[112:115], v[32:35], a[72:75], v[112:115]
ds_read_b128 v[24:27], v4 offset:36864
v_mul_f32 v105, v105, v107
v_mfma_f32_16x16x32_bf16 v[116:119], v[32:35], a[76:79], v[116:119]
v_rcp_f32 v108, v108
s_waitcnt lgkmcnt(5)
v_mfma_f32_16x16x32_bf16 v[128:131], v[36:39], a[72:75], v[128:131]
ds_read_b128 v[28:31], v4 offset:37888
v_mul_f32 v108, v108, v110
v_mfma_f32_16x16x32_bf16 v[132:135], v[36:39], a[76:79], v[132:135]
v_rcp_f32 v109, v109
s_waitcnt lgkmcnt(5)
v_mfma_f32_16x16x32_bf16 v[144:147], v[8:11], a[72:75], v[144:147]
ds_read_b128 v[32:35], v4 offset:38912
v_mul_f32 v109, v109, v111
v_mfma_f32_16x16x32_bf16 v[148:151], v[8:11], a[76:79], v[148:151]
s_add_u32 s48, s45, 12288
buffer_load_dwordx4 a[48:51], v4, s[16:19], s48 offen offset:0 sc1
buffer_load_dwordx4 a[52:55], v4, s[16:19], s48 offen offset:1024 sc1
s_waitcnt lgkmcnt(5)
v_mfma_f32_16x16x32_bf16 v[160:163], v[12:15], a[72:75], v[160:163]
ds_read_b128 v[36:39], v4 offset:39936
v_cvt_pk_bf16_f32 v42, v104, v105
v_mfma_f32_16x16x32_bf16 v[164:167], v[12:15], a[76:79], v[164:167]
v_cvt_pk_bf16_f32 v50, v108, v109
v_mul_f32 v120, v120, v216
s_waitcnt lgkmcnt(5)
v_mfma_f32_16x16x32_bf16 v[176:179], v[16:19], a[72:75], v[176:179]
ds_read_b128 v[8:11], v4 offset:40960
v_mul_f32 v121, v121, v216
v_mfma_f32_16x16x32_bf16 v[180:183], v[16:19], a[76:79], v[180:183]
v_mul_f32 v124, v124, v218
v_mul_f32 v125, v125, v218
s_waitcnt vmcnt(30) lgkmcnt(5)
v_mfma_f32_16x16x32_bf16 v[80:83], v[20:23], a[88:91], v[80:83]
ds_read_b128 v[12:15], v4 offset:41984
s_waitcnt vmcnt(29)
v_mfma_f32_16x16x32_bf16 v[84:87], v[20:23], a[92:95], v[84:87]
v_exp_f32 v120, v120
s_waitcnt lgkmcnt(5)
v_mfma_f32_16x16x32_bf16 v[96:99], v[24:27], a[88:91], v[96:99]
ds_read_b128 v[16:19], v4 offset:43008
v_fma_f32 v120, v120, v217, v217
v_mfma_f32_16x16x32_bf16 v[100:103], v[24:27], a[92:95], v[100:103]
v_exp_f32 v121, v121
s_waitcnt lgkmcnt(5)
v_mfma_f32_16x16x32_bf16 v[112:115], v[28:31], a[88:91], v[112:115]
ds_read_b128 v[20:23], v4 offset:44032
v_fma_f32 v121, v121, v217, v217
v_mfma_f32_16x16x32_bf16 v[116:119], v[28:31], a[92:95], v[116:119]
v_exp_f32 v124, v124
s_waitcnt lgkmcnt(5)
v_mfma_f32_16x16x32_bf16 v[128:131], v[32:35], a[88:91], v[128:131]
ds_read_b128 v[24:27], v4 offset:45056
v_fma_f32 v124, v124, v219, v219
v_mfma_f32_16x16x32_bf16 v[132:135], v[32:35], a[92:95], v[132:135]
v_exp_f32 v125, v125
s_waitcnt lgkmcnt(5)
v_mfma_f32_16x16x32_bf16 v[144:147], v[36:39], a[88:91], v[144:147]
ds_read_b128 v[28:31], v4 offset:46080
v_fma_f32 v125, v125, v219, v219
v_mfma_f32_16x16x32_bf16 v[148:151], v[36:39], a[92:95], v[148:151]
v_rcp_f32 v120, v120
s_waitcnt lgkmcnt(5)
v_mfma_f32_16x16x32_bf16 v[160:163], v[8:11], a[88:91], v[160:163]
ds_read_b128 v[32:35], v4 offset:47104
v_mul_f32 v120, v120, v122
v_mfma_f32_16x16x32_bf16 v[164:167], v[8:11], a[92:95], v[164:167]
s_add_u32 s48, s45, 16384
buffer_load_dwordx4 a[64:67], v4, s[16:19], s48 offen offset:0 sc1
buffer_load_dwordx4 a[68:71], v4, s[16:19], s48 offen offset:1024 sc1
s_waitcnt lgkmcnt(5)
v_mfma_f32_16x16x32_bf16 v[176:179], v[12:15], a[88:91], v[176:179]
ds_read_b128 v[36:39], v4 offset:48128
s_add_u32 s26, s29, 0
v_mfma_f32_16x16x32_bf16 v[180:183], v[12:15], a[92:95], v[180:183]
v_rcp_f32 v121, v121
s_waitcnt vmcnt(30) lgkmcnt(5)
v_mfma_f32_16x16x32_bf16 v[80:83], v[16:19], a[104:107], v[80:83]
ds_read_b128 v[8:11], v4 offset:49152
v_mul_f32 v121, v121, v123
s_waitcnt vmcnt(29)
v_mfma_f32_16x16x32_bf16 v[84:87], v[16:19], a[108:111], v[84:87]
v_rcp_f32 v124, v124
s_waitcnt lgkmcnt(5)
v_mfma_f32_16x16x32_bf16 v[96:99], v[20:23], a[104:107], v[96:99]
ds_read_b128 v[12:15], v4 offset:50176
v_mul_f32 v124, v124, v126
v_mfma_f32_16x16x32_bf16 v[100:103], v[20:23], a[108:111], v[100:103]
v_rcp_f32 v125, v125
s_waitcnt lgkmcnt(5)
v_mfma_f32_16x16x32_bf16 v[112:115], v[24:27], a[104:107], v[112:115]
ds_read_b128 v[16:19], v4 offset:51200
v_mul_f32 v125, v125, v127
v_mfma_f32_16x16x32_bf16 v[116:119], v[24:27], a[108:111], v[116:119]
v_cvt_pk_bf16_f32 v43, v120, v121
v_cvt_pk_bf16_f32 v51, v124, v125
s_waitcnt lgkmcnt(5)
v_mfma_f32_16x16x32_bf16 v[128:131], v[28:31], a[104:107], v[128:131]
ds_read_b128 v[20:23], v4 offset:52224
buffer_store_dwordx4 v[40:43], v1, s[16:19], s26 offen offset:0 sc1
v_mfma_f32_16x16x32_bf16 v[132:135], v[28:31], a[108:111], v[132:135]
buffer_store_dwordx4 v[48:51], v1, s[16:19], s26 offen offset:256 sc1
v_mul_f32 v136, v136, v216
s_waitcnt lgkmcnt(5)
v_mfma_f32_16x16x32_bf16 v[144:147], v[32:35], a[104:107], v[144:147]
ds_read_b128 v[24:27], v4 offset:53248
v_mul_f32 v137, v137, v216
v_mfma_f32_16x16x32_bf16 v[148:151], v[32:35], a[108:111], v[148:151]
v_mul_f32 v140, v140, v218
v_mul_f32 v141, v141, v218
s_waitcnt lgkmcnt(5)
v_mfma_f32_16x16x32_bf16 v[160:163], v[36:39], a[104:107], v[160:163]
ds_read_b128 v[28:31], v4 offset:54272
v_mfma_f32_16x16x32_bf16 v[164:167], v[36:39], a[108:111], v[164:167]
v_exp_f32 v136, v136
s_waitcnt lgkmcnt(5)
v_mfma_f32_16x16x32_bf16 v[176:179], v[8:11], a[104:107], v[176:179]
ds_read_b128 v[32:35], v4 offset:55296
v_fma_f32 v136, v136, v217, v217
v_mfma_f32_16x16x32_bf16 v[180:183], v[8:11], a[108:111], v[180:183]
s_add_u32 s48, s45, 20480
buffer_load_dwordx4 a[80:83], v4, s[16:19], s48 offen offset:0 sc1
buffer_load_dwordx4 a[84:87], v4, s[16:19], s48 offen offset:1024 sc1
s_waitcnt vmcnt(32) lgkmcnt(5)
v_mfma_f32_16x16x32_bf16 v[80:83], v[12:15], a[120:123], v[80:83]
ds_read_b128 v[36:39], v4 offset:56320
s_waitcnt vmcnt(31)
v_mfma_f32_16x16x32_bf16 v[84:87], v[12:15], a[124:127], v[84:87]
v_exp_f32 v137, v137
s_waitcnt lgkmcnt(5)
v_mfma_f32_16x16x32_bf16 v[96:99], v[16:19], a[120:123], v[96:99]
ds_read_b128 v[8:11], v4 offset:57344
v_fma_f32 v137, v137, v217, v217
v_mfma_f32_16x16x32_bf16 v[100:103], v[16:19], a[124:127], v[100:103]
v_exp_f32 v140, v140
s_waitcnt lgkmcnt(5)
v_mfma_f32_16x16x32_bf16 v[112:115], v[20:23], a[120:123], v[112:115]
ds_read_b128 v[12:15], v4 offset:58368
v_fma_f32 v140, v140, v219, v219
v_mfma_f32_16x16x32_bf16 v[116:119], v[20:23], a[124:127], v[116:119]
v_exp_f32 v141, v141
s_waitcnt lgkmcnt(5)
v_mfma_f32_16x16x32_bf16 v[128:131], v[24:27], a[120:123], v[128:131]
ds_read_b128 v[16:19], v4 offset:59392
v_fma_f32 v141, v141, v219, v219
v_mfma_f32_16x16x32_bf16 v[132:135], v[24:27], a[124:127], v[132:135]
v_rcp_f32 v136, v136
s_waitcnt lgkmcnt(5)
v_mfma_f32_16x16x32_bf16 v[144:147], v[28:31], a[120:123], v[144:147]
ds_read_b128 v[20:23], v4 offset:60416
v_mul_f32 v136, v136, v138
v_mfma_f32_16x16x32_bf16 v[148:151], v[28:31], a[124:127], v[148:151]
v_rcp_f32 v137, v137
s_waitcnt lgkmcnt(5)
v_mfma_f32_16x16x32_bf16 v[160:163], v[32:35], a[120:123], v[160:163]
ds_read_b128 v[24:27], v4 offset:61440
v_mul_f32 v137, v137, v139
v_mfma_f32_16x16x32_bf16 v[164:167], v[32:35], a[124:127], v[164:167]
v_rcp_f32 v140, v140
s_waitcnt lgkmcnt(5)
v_mfma_f32_16x16x32_bf16 v[176:179], v[36:39], a[120:123], v[176:179]
ds_read_b128 v[28:31], v4 offset:62464
v_mul_f32 v140, v140, v142
v_mfma_f32_16x16x32_bf16 v[180:183], v[36:39], a[124:127], v[180:183]
v_rcp_f32 v141, v141
s_waitcnt vmcnt(30) lgkmcnt(5)
v_mfma_f32_16x16x32_bf16 v[80:83], v[8:11], a[136:139], v[80:83]
ds_read_b128 v[32:35], v4 offset:63488
v_mul_f32 v141, v141, v143
s_waitcnt vmcnt(29)
v_mfma_f32_16x16x32_bf16 v[84:87], v[8:11], a[140:143], v[84:87]
s_add_u32 s48, s45, 24576
buffer_load_dwordx4 a[96:99], v4, s[16:19], s48 offen offset:0 sc1
buffer_load_dwordx4 a[100:103], v4, s[16:19], s48 offen offset:1024 sc1
s_waitcnt lgkmcnt(5)
v_mfma_f32_16x16x32_bf16 v[96:99], v[12:15], a[136:139], v[96:99]
ds_read_b128 v[36:39], v4 offset:64512
v_cvt_pk_bf16_f32 v44, v136, v137
v_mfma_f32_16x16x32_bf16 v[100:103], v[12:15], a[140:143], v[100:103]
v_cvt_pk_bf16_f32 v52, v140, v141
v_cvt_pk_bf16_f32 v138, v136, v137
s_waitcnt lgkmcnt(5)
v_mfma_f32_16x16x32_bf16 v[112:115], v[16:19], a[136:139], v[112:115]
ds_read_b128 v[8:11], v250 offset:0
v_cvt_pk_bf16_f32 v142, v140, v141
v_mfma_f32_16x16x32_bf16 v[116:119], v[16:19], a[140:143], v[116:119]
v_mul_f32 v152, v152, v216
v_mul_f32 v153, v153, v216
s_waitcnt lgkmcnt(5)
v_mfma_f32_16x16x32_bf16 v[128:131], v[20:23], a[136:139], v[128:131]
ds_read_b128 v[12:15], v250 offset:1024
v_mul_f32 v156, v156, v218
v_mfma_f32_16x16x32_bf16 v[132:135], v[20:23], a[140:143], v[132:135]
v_mul_f32 v157, v157, v218
s_waitcnt lgkmcnt(5)
v_mfma_f32_16x16x32_bf16 v[144:147], v[24:27], a[136:139], v[144:147]
ds_read_b128 v[16:19], v250 offset:2048
v_mfma_f32_16x16x32_bf16 v[148:151], v[24:27], a[140:143], v[148:151]
v_exp_f32 v152, v152
s_waitcnt lgkmcnt(5)
v_mfma_f32_16x16x32_bf16 v[160:163], v[28:31], a[136:139], v[160:163]
ds_read_b128 v[20:23], v250 offset:3072
v_fma_f32 v152, v152, v217, v217
v_mfma_f32_16x16x32_bf16 v[164:167], v[28:31], a[140:143], v[164:167]
v_exp_f32 v153, v153
s_waitcnt lgkmcnt(5)
v_mfma_f32_16x16x32_bf16 v[176:179], v[32:35], a[136:139], v[176:179]
ds_read_b128 v[24:27], v250 offset:4096
v_fma_f32 v153, v153, v217, v217
v_mfma_f32_16x16x32_bf16 v[180:183], v[32:35], a[140:143], v[180:183]
v_exp_f32 v156, v156
s_waitcnt vmcnt(30) lgkmcnt(5)
v_mfma_f32_16x16x32_bf16 v[80:83], v[36:39], a[152:155], v[80:83]
ds_read_b128 v[28:31], v250 offset:5120
v_fma_f32 v156, v156, v219, v219
s_waitcnt vmcnt(29)
v_mfma_f32_16x16x32_bf16 v[84:87], v[36:39], a[156:159], v[84:87]
v_exp_f32 v157, v157
s_waitcnt lgkmcnt(5)
v_mfma_f32_16x16x32_bf16 v[96:99], v[8:11], a[152:155], v[96:99]
ds_read_b128 v[32:35], v250 offset:6144
v_fma_f32 v157, v157, v219, v219
v_mfma_f32_16x16x32_bf16 v[100:103], v[8:11], a[156:159], v[100:103]
s_add_u32 s48, s45, 28672
buffer_load_dwordx4 a[112:115], v4, s[16:19], s48 offen offset:0 sc1
buffer_load_dwordx4 a[116:119], v4, s[16:19], s48 offen offset:1024 sc1
s_waitcnt lgkmcnt(5)
v_mfma_f32_16x16x32_bf16 v[112:115], v[12:15], a[152:155], v[112:115]
ds_read_b128 v[36:39], v250 offset:13312
v_mul_f32 v168, v168, v216
v_mfma_f32_16x16x32_bf16 v[116:119], v[12:15], a[156:159], v[116:119]
v_rcp_f32 v152, v152
s_waitcnt lgkmcnt(5)
v_mfma_f32_16x16x32_bf16 v[128:131], v[16:19], a[152:155], v[128:131]
ds_read_b128 v[8:11], v250 offset:20480
v_mul_f32 v152, v152, v154
v_mfma_f32_16x16x32_bf16 v[132:135], v[16:19], a[156:159], v[132:135]
v_rcp_f32 v153, v153
s_waitcnt lgkmcnt(5)
v_mfma_f32_16x16x32_bf16 v[144:147], v[20:23], a[152:155], v[144:147]
ds_read_b128 v[12:15], v250 offset:27648
v_mul_f32 v153, v153, v155
v_mfma_f32_16x16x32_bf16 v[148:151], v[20:23], a[156:159], v[148:151]
v_rcp_f32 v156, v156
s_waitcnt lgkmcnt(5)
v_mfma_f32_16x16x32_bf16 v[160:163], v[24:27], a[152:155], v[160:163]
ds_read_b128 v[16:19], v250 offset:34816
v_mul_f32 v156, v156, v158
v_mfma_f32_16x16x32_bf16 v[164:167], v[24:27], a[156:159], v[164:167]
v_rcp_f32 v157, v157
s_waitcnt lgkmcnt(5)
v_mfma_f32_16x16x32_bf16 v[176:179], v[28:31], a[152:155], v[176:179]
ds_read_b128 v[20:23], v250 offset:41984
v_mul_f32 v157, v157, v159
v_mfma_f32_16x16x32_bf16 v[180:183], v[28:31], a[156:159], v[180:183]
v_cvt_pk_bf16_f32 v45, v152, v153
v_cvt_pk_bf16_f32 v53, v156, v157
s_waitcnt vmcnt(30) lgkmcnt(5)
v_mfma_f32_16x16x32_bf16 v[80:83], v[32:35], a[168:171], v[80:83]
ds_read_b128 v[24:27], v250 offset:7168
v_cvt_pk_bf16_f32 v154, v152, v153
s_waitcnt vmcnt(29)
v_mfma_f32_16x16x32_bf16 v[84:87], v[32:35], a[172:175], v[84:87]
v_cvt_pk_bf16_f32 v158, v156, v157
v_mul_f32 v169, v169, v216
s_waitcnt vmcnt(28) lgkmcnt(5)
v_mfma_f32_16x16x32_bf16 v[80:83], v[36:39], a[184:187], v[80:83]
ds_read_b128 v[28:31], v250 offset:14336
v_mul_f32 v172, v172, v218
s_waitcnt vmcnt(27)
v_mfma_f32_16x16x32_bf16 v[84:87], v[36:39], a[188:191], v[84:87]
v_mul_f32 v173, v173, v218
s_waitcnt vmcnt(26) lgkmcnt(5)
v_mfma_f32_16x16x32_bf16 v[80:83], v[8:11], a[200:203], v[80:83]
ds_read_b128 v[32:35], v250 offset:21504
s_waitcnt vmcnt(25)
v_mfma_f32_16x16x32_bf16 v[84:87], v[8:11], a[204:207], v[84:87]
s_add_u32 s48, s45, 32768
buffer_load_dwordx4 a[128:131], v4, s[16:19], s48 offen offset:0 sc1
buffer_load_dwordx4 a[132:135], v4, s[16:19], s48 offen offset:1024 sc1
s_waitcnt vmcnt(26) lgkmcnt(5)
v_mfma_f32_16x16x32_bf16 v[80:83], v[12:15], a[216:219], v[80:83]
ds_read_b128 v[36:39], v250 offset:28672
s_waitcnt vmcnt(25)
v_mfma_f32_16x16x32_bf16 v[84:87], v[12:15], a[220:223], v[84:87]
v_exp_f32 v168, v168
s_waitcnt vmcnt(24) lgkmcnt(5)
v_mfma_f32_16x16x32_bf16 v[80:83], v[16:19], a[232:235], v[80:83]
ds_read_b128 v[8:11], v250 offset:35840
v_fma_f32 v168, v168, v217, v217
s_waitcnt vmcnt(23)
v_mfma_f32_16x16x32_bf16 v[84:87], v[16:19], a[236:239], v[84:87]
v_exp_f32 v169, v169
s_waitcnt vmcnt(22) lgkmcnt(5)
v_mfma_f32_16x16x32_bf16 v[80:83], v[20:23], a[248:251], v[80:83]
ds_read_b128 v[12:15], v250 offset:43008
v_fma_f32 v169, v169, v217, v217
s_waitcnt vmcnt(21)
v_mfma_f32_16x16x32_bf16 v[84:87], v[20:23], a[252:255], v[84:87]
v_exp_f32 v172, v172
s_waitcnt lgkmcnt(5)
v_mfma_f32_16x16x32_bf16 v[96:99], v[24:27], a[168:171], v[96:99]
ds_read_b128 v[16:19], v250 offset:8192
v_fma_f32 v172, v172, v219, v219
v_mfma_f32_16x16x32_bf16 v[100:103], v[24:27], a[172:175], v[100:103]
v_exp_f32 v173, v173
s_waitcnt lgkmcnt(5)
v_mfma_f32_16x16x32_bf16 v[96:99], v[28:31], a[184:187], v[96:99]
ds_read_b128 v[20:23], v250 offset:15360
v_fma_f32 v173, v173, v219, v219
v_mfma_f32_16x16x32_bf16 v[100:103], v[28:31], a[188:191], v[100:103]
v_rcp_f32 v168, v168
s_waitcnt lgkmcnt(5)
v_mfma_f32_16x16x32_bf16 v[96:99], v[32:35], a[200:203], v[96:99]
ds_read_b128 v[24:27], v250 offset:22528
v_mul_f32 v168, v168, v170
v_mfma_f32_16x16x32_bf16 v[100:103], v[32:35], a[204:207], v[100:103]
v_rcp_f32 v169, v169
s_waitcnt lgkmcnt(5)
v_mfma_f32_16x16x32_bf16 v[96:99], v[36:39], a[216:219], v[96:99]
ds_read_b128 v[28:31], v250 offset:29696
v_mul_f32 v169, v169, v171
v_mfma_f32_16x16x32_bf16 v[100:103], v[36:39], a[220:223], v[100:103]
v_rcp_f32 v172, v172
s_waitcnt lgkmcnt(5)
v_mfma_f32_16x16x32_bf16 v[96:99], v[8:11], a[232:235], v[96:99]
ds_read_b128 v[32:35], v250 offset:36864
v_mul_f32 v172, v172, v174
v_mfma_f32_16x16x32_bf16 v[100:103], v[8:11], a[236:239], v[100:103]
s_add_u32 s48, s45, 36864
buffer_load_dwordx4 a[144:147], v4, s[16:19], s48 offen offset:0 sc1
buffer_load_dwordx4 a[148:151], v4, s[16:19], s48 offen offset:1024 sc1
s_waitcnt lgkmcnt(5)
v_mfma_f32_16x16x32_bf16 v[96:99], v[12:15], a[248:251], v[96:99]
ds_read_b128 v[36:39], v250 offset:44032
v_cvt_pk_bf16_f32 v46, v168, v169
v_mfma_f32_16x16x32_bf16 v[100:103], v[12:15], a[252:255], v[100:103]
v_rcp_f32 v173, v173
s_waitcnt lgkmcnt(5)
v_mfma_f32_16x16x32_bf16 v[112:115], v[16:19], a[168:171], v[112:115]
ds_read_b128 v[8:11], v250 offset:9216
v_mul_f32 v173, v173, v175
v_mfma_f32_16x16x32_bf16 v[116:119], v[16:19], a[172:175], v[116:119]
v_cvt_pk_bf16_f32 v170, v168, v169
v_cvt_pk_bf16_f32 v54, v172, v173
s_waitcnt lgkmcnt(5)
v_mfma_f32_16x16x32_bf16 v[112:115], v[20:23], a[184:187], v[112:115]
ds_read_b128 v[12:15], v250 offset:16384
v_cvt_pk_bf16_f32 v174, v172, v173
v_mfma_f32_16x16x32_bf16 v[116:119], v[20:23], a[188:191], v[116:119]
s_add_u32 s26, s34, 0
v_permlane16_swap_b32 v138, v154
s_waitcnt lgkmcnt(5)
v_mfma_f32_16x16x32_bf16 v[112:115], v[24:27], a[200:203], v[112:115]
ds_read_b128 v[16:19], v250 offset:23552
v_permlane16_swap_b32 v142, v158
v_mfma_f32_16x16x32_bf16 v[116:119], v[24:27], a[204:207], v[116:119]
v_permlane16_swap_b32 v170, v47
v_permlane16_swap_b32 v174, v55
s_waitcnt lgkmcnt(5)
v_mfma_f32_16x16x32_bf16 v[112:115], v[28:31], a[216:219], v[112:115]
ds_read_b128 v[20:23], v250 offset:30720
v_permlane32_swap_b32 v154, v47
v_mfma_f32_16x16x32_bf16 v[116:119], v[28:31], a[220:223], v[116:119]
v_permlane32_swap_b32 v158, v55
buffer_store_dwordx4 v[44:47], v2, s[16:19], s26 offen offset:0 sc1
s_waitcnt lgkmcnt(5)
v_mfma_f32_16x16x32_bf16 v[112:115], v[32:35], a[232:235], v[112:115]
ds_read_b128 v[24:27], v250 offset:37888
buffer_store_dwordx4 v[52:55], v2, s[16:19], s26 offen offset:256 sc1
v_mfma_f32_16x16x32_bf16 v[116:119], v[32:35], a[236:239], v[116:119]
v_mul_f32 v80, v80, v220
v_mul_f32 v81, v81, v220
s_waitcnt lgkmcnt(5)
v_mfma_f32_16x16x32_bf16 v[112:115], v[36:39], a[248:251], v[112:115]
ds_read_b128 v[28:31], v250 offset:45056
v_mul_f32 v84, v84, v222
v_mfma_f32_16x16x32_bf16 v[116:119], v[36:39], a[252:255], v[116:119]
v_mul_f32 v85, v85, v222
s_waitcnt lgkmcnt(5)
v_mfma_f32_16x16x32_bf16 v[128:131], v[8:11], a[168:171], v[128:131]
ds_read_b128 v[32:35], v250 offset:10240
v_mfma_f32_16x16x32_bf16 v[132:135], v[8:11], a[172:175], v[132:135]
s_add_u32 s48, s45, 40960
buffer_load_dwordx4 a[160:163], v4, s[16:19], s48 offen offset:0 sc1
buffer_load_dwordx4 a[164:167], v4, s[16:19], s48 offen offset:1024 sc1
s_waitcnt lgkmcnt(5)
v_mfma_f32_16x16x32_bf16 v[128:131], v[12:15], a[184:187], v[128:131]
ds_read_b128 v[36:39], v250 offset:17408
v_mfma_f32_16x16x32_bf16 v[132:135], v[12:15], a[188:191], v[132:135]
v_exp_f32 v80, v80
s_waitcnt lgkmcnt(5)
v_mfma_f32_16x16x32_bf16 v[128:131], v[16:19], a[200:203], v[128:131]
ds_read_b128 v[8:11], v250 offset:24576
v_fma_f32 v80, v80, v221, v221
v_mfma_f32_16x16x32_bf16 v[132:135], v[16:19], a[204:207], v[132:135]
v_exp_f32 v81, v81
s_waitcnt lgkmcnt(5)
v_mfma_f32_16x16x32_bf16 v[128:131], v[20:23], a[216:219], v[128:131]
ds_read_b128 v[12:15], v250 offset:31744
v_fma_f32 v81, v81, v221, v221
v_mfma_f32_16x16x32_bf16 v[132:135], v[20:23], a[220:223], v[132:135]
v_exp_f32 v84, v84
s_waitcnt lgkmcnt(5)
v_mfma_f32_16x16x32_bf16 v[128:131], v[24:27], a[232:235], v[128:131]
ds_read_b128 v[16:19], v250 offset:38912
v_fma_f32 v84, v84, v223, v223
v_mfma_f32_16x16x32_bf16 v[132:135], v[24:27], a[236:239], v[132:135]
v_exp_f32 v85, v85
s_waitcnt lgkmcnt(5)
v_mfma_f32_16x16x32_bf16 v[128:131], v[28:31], a[248:251], v[128:131]
ds_read_b128 v[20:23], v250 offset:46080
v_fma_f32 v85, v85, v223, v223
v_mfma_f32_16x16x32_bf16 v[132:135], v[28:31], a[252:255], v[132:135]
v_rcp_f32 v80, v80
s_waitcnt lgkmcnt(5)
v_mfma_f32_16x16x32_bf16 v[144:147], v[32:35], a[168:171], v[144:147]
ds_read_b128 v[24:27], v250 offset:11264
v_mul_f32 v80, v80, v82
v_mfma_f32_16x16x32_bf16 v[148:151], v[32:35], a[172:175], v[148:151]
v_rcp_f32 v81, v81
s_waitcnt lgkmcnt(5)
v_mfma_f32_16x16x32_bf16 v[144:147], v[36:39], a[184:187], v[144:147]
ds_read_b128 v[28:31], v250 offset:18432
v_mul_f32 v81, v81, v83
v_mfma_f32_16x16x32_bf16 v[148:151], v[36:39], a[188:191], v[148:151]
v_rcp_f32 v84, v84
s_waitcnt lgkmcnt(5)
v_mfma_f32_16x16x32_bf16 v[144:147], v[8:11], a[200:203], v[144:147]
ds_read_b128 v[32:35], v250 offset:25600
v_mul_f32 v84, v84, v86
v_mfma_f32_16x16x32_bf16 v[148:151], v[8:11], a[204:207], v[148:151]
s_add_u32 s48, s45, 45056
buffer_load_dwordx4 a[176:179], v4, s[16:19], s48 offen offset:0 sc1
buffer_load_dwordx4 a[180:183], v4, s[16:19], s48 offen offset:1024 sc1
s_waitcnt lgkmcnt(5)
v_mfma_f32_16x16x32_bf16 v[144:147], v[12:15], a[216:219], v[144:147]
ds_read_b128 v[36:39], v250 offset:32768
v_cvt_pk_bf16_f32 v56, v80, v81
v_mfma_f32_16x16x32_bf16 v[148:151], v[12:15], a[220:223], v[148:151]
v_rcp_f32 v85, v85
s_waitcnt lgkmcnt(5)
v_mfma_f32_16x16x32_bf16 v[144:147], v[16:19], a[232:235], v[144:147]
ds_read_b128 v[8:11], v250 offset:39936
v_mul_f32 v85, v85, v87
v_mfma_f32_16x16x32_bf16 v[148:151], v[16:19], a[236:239], v[148:151]
v_mul_f32 v96, v96, v220
v_cvt_pk_bf16_f32 v64, v84, v85
s_waitcnt lgkmcnt(5)
v_mfma_f32_16x16x32_bf16 v[144:147], v[20:23], a[248:251], v[144:147]
ds_read_b128 v[12:15], v250 offset:47104
v_mul_f32 v97, v97, v220
v_mfma_f32_16x16x32_bf16 v[148:151], v[20:23], a[252:255], v[148:151]
v_mul_f32 v100, v100, v222
v_mul_f32 v101, v101, v222
s_waitcnt lgkmcnt(5)
v_mfma_f32_16x16x32_bf16 v[160:163], v[24:27], a[168:171], v[160:163]
ds_read_b128 v[16:19], v250 offset:12288
v_mfma_f32_16x16x32_bf16 v[164:167], v[24:27], a[172:175], v[164:167]
v_exp_f32 v96, v96
s_waitcnt lgkmcnt(5)
v_mfma_f32_16x16x32_bf16 v[160:163], v[28:31], a[184:187], v[160:163]
ds_read_b128 v[20:23], v250 offset:19456
v_fma_f32 v96, v96, v221, v221
v_mfma_f32_16x16x32_bf16 v[164:167], v[28:31], a[188:191], v[164:167]
v_exp_f32 v97, v97
s_waitcnt lgkmcnt(5)
v_mfma_f32_16x16x32_bf16 v[160:163], v[32:35], a[200:203], v[160:163]
ds_read_b128 v[24:27], v250 offset:26624
v_fma_f32 v97, v97, v221, v221
v_mfma_f32_16x16x32_bf16 v[164:167], v[32:35], a[204:207], v[164:167]
v_exp_f32 v100, v100
s_waitcnt lgkmcnt(5)
v_mfma_f32_16x16x32_bf16 v[160:163], v[36:39], a[216:219], v[160:163]
ds_read_b128 v[28:31], v250 offset:33792
v_fma_f32 v100, v100, v223, v223
v_mfma_f32_16x16x32_bf16 v[164:167], v[36:39], a[220:223], v[164:167]
v_exp_f32 v101, v101
s_waitcnt lgkmcnt(5)
v_mfma_f32_16x16x32_bf16 v[160:163], v[8:11], a[232:235], v[160:163]
ds_read_b128 v[32:35], v250 offset:40960
v_fma_f32 v101, v101, v223, v223
v_mfma_f32_16x16x32_bf16 v[164:167], v[8:11], a[236:239], v[164:167]
s_add_u32 s48, s45, 49152
buffer_load_dwordx4 a[192:195], v4, s[16:19], s48 offen offset:0 sc1
buffer_load_dwordx4 a[196:199], v4, s[16:19], s48 offen offset:1024 sc1
s_waitcnt lgkmcnt(5)
v_mfma_f32_16x16x32_bf16 v[160:163], v[12:15], a[248:251], v[160:163]
ds_read_b128 v[36:39], v250 offset:48128
v_mul_f32 v112, v112, v220
v_mfma_f32_16x16x32_bf16 v[164:167], v[12:15], a[252:255], v[164:167]
v_rcp_f32 v96, v96
s_waitcnt lgkmcnt(5)
v_mfma_f32_16x16x32_bf16 v[176:179], v[16:19], a[168:171], v[176:179]
v_rcp_f32 v97, v97
v_mfma_f32_16x16x32_bf16 v[180:183], v[16:19], a[172:175], v[180:183]
v_rcp_f32 v100, v100
s_waitcnt lgkmcnt(4)
v_mfma_f32_16x16x32_bf16 v[176:179], v[20:23], a[184:187], v[176:179]
v_rcp_f32 v101, v101
v_mfma_f32_16x16x32_bf16 v[180:183], v[20:23], a[188:191], v[180:183]
v_mul_f32 v96, v96, v98
v_mul_f32 v97, v97, v99
s_waitcnt lgkmcnt(3)
v_mfma_f32_16x16x32_bf16 v[176:179], v[24:27], a[200:203], v[176:179]
v_mul_f32 v100, v100, v102
v_mul_f32 v101, v101, v103
v_mfma_f32_16x16x32_bf16 v[180:183], v[24:27], a[204:207], v[180:183]
v_cvt_pk_bf16_f32 v57, v96, v97
v_cvt_pk_bf16_f32 v65, v100, v101
s_waitcnt lgkmcnt(2)
v_mfma_f32_16x16x32_bf16 v[176:179], v[28:31], a[216:219], v[176:179]
v_mul_f32 v113, v113, v220
v_mul_f32 v116, v116, v222
v_mfma_f32_16x16x32_bf16 v[180:183], v[28:31], a[220:223], v[180:183]
v_mul_f32 v117, v117, v222
s_waitcnt lgkmcnt(1)
v_mfma_f32_16x16x32_bf16 v[176:179], v[32:35], a[232:235], v[176:179]
v_exp_f32 v112, v112
v_mfma_f32_16x16x32_bf16 v[180:183], v[32:35], a[236:239], v[180:183]
v_exp_f32 v113, v113
s_waitcnt lgkmcnt(0)
v_mfma_f32_16x16x32_bf16 v[176:179], v[36:39], a[248:251], v[176:179]
v_exp_f32 v116, v116
v_mfma_f32_16x16x32_bf16 v[180:183], v[36:39], a[252:255], v[180:183]
v_exp_f32 v117, v117
v_fma_f32 v112, v112, v221, v221
v_fma_f32 v113, v113, v221, v221
v_fma_f32 v116, v116, v223, v223
v_fma_f32 v117, v117, v223, v223
v_rcp_f32 v112, v112
v_rcp_f32 v113, v113
v_rcp_f32 v116, v116
v_rcp_f32 v117, v117
v_mul_f32 v112, v112, v114
v_mul_f32 v113, v113, v115
v_mul_f32 v116, v116, v118
v_mul_f32 v117, v117, v119
v_cvt_pk_bf16_f32 v58, v112, v113
v_mul_f32 v128, v128, v220
v_cvt_pk_bf16_f32 v66, v116, v117
v_mul_f32 v129, v129, v220
v_mul_f32 v132, v132, v222
v_mul_f32 v133, v133, v222
v_exp_f32 v128, v128
v_exp_f32 v129, v129
v_exp_f32 v132, v132
v_exp_f32 v133, v133
v_fma_f32 v128, v128, v221, v221
v_fma_f32 v129, v129, v221, v221
v_fma_f32 v132, v132, v223, v223
v_fma_f32 v133, v133, v223, v223
v_rcp_f32 v128, v128
v_rcp_f32 v129, v129
v_rcp_f32 v132, v132
v_rcp_f32 v133, v133
v_mul_f32 v128, v128, v130
v_mul_f32 v129, v129, v131
v_mul_f32 v132, v132, v134
v_mul_f32 v133, v133, v135
v_cvt_pk_bf16_f32 v59, v128, v129
s_add_u32 s26, s29, 114688
v_cvt_pk_bf16_f32 v67, v132, v133
v_mul_f32 v144, v144, v220
buffer_store_dwordx4 v[56:59], v1, s[16:19], s26 offen offset:0 sc1
buffer_store_dwordx4 v[64:67], v1, s[16:19], s26 offen offset:256 sc1
v_mul_f32 v145, v145, v220
v_mul_f32 v148, v148, v222
v_mul_f32 v149, v149, v222
v_exp_f32 v144, v144
v_exp_f32 v145, v145
v_exp_f32 v148, v148
v_exp_f32 v149, v149
v_fma_f32 v144, v144, v221, v221
v_fma_f32 v145, v145, v221, v221
v_fma_f32 v148, v148, v223, v223
v_fma_f32 v149, v149, v223, v223
v_rcp_f32 v144, v144
v_rcp_f32 v145, v145
v_rcp_f32 v148, v148
v_rcp_f32 v149, v149
v_mul_f32 v144, v144, v146
v_mul_f32 v145, v145, v147
v_mul_f32 v148, v148, v150
v_mul_f32 v149, v149, v151
v_cvt_pk_bf16_f32 v60, v144, v145
v_cvt_pk_bf16_f32 v146, v144, v145
v_cvt_pk_bf16_f32 v68, v148, v149
v_cvt_pk_bf16_f32 v150, v148, v149
v_mul_f32 v160, v160, v220
v_mul_f32 v161, v161, v220
v_mul_f32 v164, v164, v222
v_mul_f32 v165, v165, v222
v_exp_f32 v160, v160
v_exp_f32 v161, v161
v_exp_f32 v164, v164
v_exp_f32 v165, v165
v_fma_f32 v160, v160, v221, v221
v_fma_f32 v161, v161, v221, v221
v_fma_f32 v164, v164, v223, v223
v_fma_f32 v165, v165, v223, v223
v_rcp_f32 v160, v160
v_rcp_f32 v161, v161
v_rcp_f32 v164, v164
v_rcp_f32 v165, v165
v_mul_f32 v160, v160, v162
v_mul_f32 v161, v161, v163
v_mul_f32 v164, v164, v166
v_mul_f32 v165, v165, v167
v_cvt_pk_bf16_f32 v61, v160, v161
v_cvt_pk_bf16_f32 v162, v160, v161
v_cvt_pk_bf16_f32 v69, v164, v165
v_cvt_pk_bf16_f32 v166, v164, v165
v_mul_f32 v176, v176, v220
v_mul_f32 v177, v177, v220
v_mul_f32 v180, v180, v222
v_mul_f32 v181, v181, v222
v_exp_f32 v176, v176
v_exp_f32 v177, v177
v_exp_f32 v180, v180
v_exp_f32 v181, v181
v_fma_f32 v176, v176, v221, v221
v_fma_f32 v177, v177, v221, v221
v_fma_f32 v180, v180, v223, v223
v_fma_f32 v181, v181, v223, v223
v_rcp_f32 v176, v176
v_rcp_f32 v177, v177
v_rcp_f32 v180, v180
v_rcp_f32 v181, v181
v_mul_f32 v176, v176, v178
v_mul_f32 v177, v177, v179
v_mul_f32 v180, v180, v182
v_mul_f32 v181, v181, v183
v_cvt_pk_bf16_f32 v62, v176, v177
v_cvt_pk_bf16_f32 v178, v176, v177
v_cvt_pk_bf16_f32 v70, v180, v181
v_cvt_pk_bf16_f32 v182, v180, v181
s_add_u32 s26, s34, 114688
v_permlane16_swap_b32 v146, v162
v_permlane16_swap_b32 v150, v166
v_permlane16_swap_b32 v178, v63
v_permlane16_swap_b32 v182, v71
s_nop 0
s_nop 0
v_permlane32_swap_b32 v162, v63
v_permlane32_swap_b32 v166, v71
s_nop 0
buffer_store_dwordx4 v[60:63], v2, s[16:19], s26 offen offset:0 sc1
buffer_store_dwordx4 v[68:71], v2, s[16:19], s26 offen offset:256 sc1
v_cmp_u_f32 vcc, v72, v76
v_cmp_u_f32 s[42:43], v80, v84
s_nop 1
s_or_b64 vcc, vcc, s[40:41]
s_or_b64 vcc, vcc, s[42:43]
s_cbranch_scc1 .Lrd4
.Lrd4b:
.Lk4e:
s_bfe_u32 s29, %2, 0x20001
s_lshl_b32 s29, s29, 3
s_lshr_b32 s30, %2, 5
s_add_u32 s29, s29, s30
s_lshl_b32 s29, s29, 2
s_add_u32 s46, s29, s39
s_add_u32 s46, s46, 0x6304000
s_and_b32 s29, %2, 7
s_lshl_b32 s29, s29, 7
s_add_u32 s38, s38, s29
s_add_u32 s38, s38, 8192
v_mov_b32 v14, 1
.Lk5s:
v_lshrrev_b32 v8, 6, v0
v_and_b32 v1, 63, v0
v_readfirstlane_b32 s28, v8
s_and_b32 s29, %2, 7
s_lshl_b32 s29, s29, 2
s_bfe_u32 s30, %2, 0x20003
s_add_u32 s36, s29, s30
s_lshr_b32 s37, %2, 5
v_lshlrev_b32 v1, 4, v1
s_mul_i32 s32, s36, 229376
s_mul_i32 s29, s28, 28672
s_add_u32 s32, s32, s29
s_add_u32 s32, s32, 0x4200000
s_add_u32 s33, s32, 114688
s_mov_b32 s24, s98
s_and_b32 s25, s99, 0xffff
s_mov_b32 s26, 0x200000
s_mov_b32 s27, 0x20000
s_add_u32 s48, s45, 53248
buffer_load_dwordx4 a[208:211], v4, s[16:19], s48 offen offset:0 sc1
buffer_load_dwordx4 a[212:215], v4, s[16:19], s48 offen offset:1024 sc1
s_add_u32 s48, s45, 57344
buffer_load_dwordx4 a[224:227], v4, s[16:19], s48 offen offset:0 sc1
buffer_load_dwordx4 a[228:231], v4, s[16:19], s48 offen offset:1024 sc1
s_add_u32 s48, s45, 61440
buffer_load_dwordx4 a[240:243], v4, s[16:19], s48 offen offset:0 sc1
buffer_load_dwordx4 a[244:247], v4, s[16:19], s48 offen offset:1024 sc1
s_add_u32 s48, s45, 106496
buffer_load_dwordx4 v[200:203], v4, s[16:19], s48 offen offset:0 sc1
buffer_load_dwordx4 v[204:207], v4, s[16:19], s48 offen offset:1024 sc1
s_waitcnt vmcnt(8)
s_barrier
buffer_store_dword v14, v7, s[16:19], s46 offen sc1
s_add_u32 s48, s45, 110592
buffer_load_dwordx4 v[208:211], v4, s[16:19], s48 offen offset:0 sc1
buffer_load_dwordx4 v[212:215], v4, s[16:19], s48 offen offset:1024 sc1
s_add_u32 s48, s45, 65536
buffer_load_dwordx4 a[8:11], v4, s[16:19], s48 offen offset:0 sc1
buffer_load_dwordx4 a[12:15], v4, s[16:19], s48 offen offset:1024 sc1
s_add_u32 s48, s45, 69632
buffer_load_dwordx4 a[24:27], v4, s[16:19], s48 offen offset:0 sc1
buffer_load_dwordx4 a[28:31], v4, s[16:19], s48 offen offset:1024 sc1
s_add_u32 s48, s45, 73728
buffer_load_dwordx4 a[40:43], v4, s[16:19], s48 offen offset:0 sc1
buffer_load_dwordx4 a[44:47], v4, s[16:19], s48 offen offset:1024 sc1
s_add_u32 s48, s45, 77824
buffer_load_dwordx4 a[56:59], v4, s[16:19], s48 offen offset:0 sc1
buffer_load_dwordx4 a[60:63], v4, s[16:19], s48 offen offset:1024 sc1
s_add_u32 s48, s45, 81920
buffer_load_dwordx4 a[72:75], v4, s[16:19], s48 offen offset:0 sc1
buffer_load_dwordx4 a[76:79], v4, s[16:19], s48 offen offset:1024 sc1
s_add_u32 s48, s45, 86016
buffer_load_dwordx4 a[88:91], v4, s[16:19], s48 offen offset:0 sc1
buffer_load_dwordx4 a[92:95], v4, s[16:19], s48 offen offset:1024 sc1
s_add_u32 s48, s45, 90112
buffer_load_dwordx4 a[104:107], v4, s[16:19], s48 offen offset:0 sc1
buffer_load_dwordx4 a[108:111], v4, s[16:19], s48 offen offset:1024 sc1
s_add_u32 s48, s45, 94208
buffer_load_dwordx4 a[120:123], v4, s[16:19], s48 offen offset:0 sc1
buffer_load_dwordx4 a[124:127], v4, s[16:19], s48 offen offset:1024 sc1
s_add_u32 s48, s45, 98304
buffer_load_dwordx4 a[136:139], v4, s[16:19], s48 offen offset:0 sc1
buffer_load_dwordx4 a[140:143], v4, s[16:19], s48 offen offset:1024 sc1
s_add_u32 s48, s45, 102400
buffer_load_dwordx4 a[152:155], v4, s[16:19], s48 offen offset:0 sc1
buffer_load_dwordx4 a[156:159], v4, s[16:19], s48 offen offset:1024 sc1
s_and_b32 s29, s28, 1
s_lshl_b32 s30, s36, 1
s_add_u32 s29, s29, s30
s_lshl_b32 s29, s29, 5
s_lshl_b32 s30, s37, 2
s_add_u32 s29, s29, s30
s_and_b32 s30, s28, 2
s_add_u32 s29, s29, s30
s_lshl_b32 s29, s29, 10
s_add_u32 s29, s29, 0x4000000
buffer_load_dwordx4 v[96:99], v1, s[16:19], s29 offen offset:0 sc1
buffer_load_dwordx4 v[100:103], v1, s[16:19], s29 offen offset:1024 sc1
buffer_load_dword v16, v6, s[16:19], s38 offen sc1
s_waitcnt vmcnt(0)
v_cmp_gt_u32 vcc, 1, v16
s_nop 3
s_cbranch_vccnz .LspC
.LspCb:
buffer_load_dwordx4 a[168:171], v1, s[16:19], s32 offen offset:0 sc1
buffer_load_dwordx4 a[172:175], v1, s[16:19], s33 offen offset:0 sc1
buffer_load_dwordx4 a[184:187], v1, s[16:19], s32 offen offset:1024 sc1
buffer_load_dwordx4 a[188:191], v1, s[16:19], s33 offen offset:1024 sc1
buffer_load_dwordx4 a[200:203], v1, s[16:19], s32 offen offset:2048 sc1
buffer_load_dwordx4 a[204:207], v1, s[16:19], s33 offen offset:2048 sc1
buffer_load_dwordx4 a[216:219], v1, s[16:19], s32 offen offset:3072 sc1
buffer_load_dwordx4 a[220:223], v1, s[16:19], s33 offen offset:3072 sc1
s_add_u32 s32, s32, 4096
s_add_u32 s33, s33, 4096
buffer_load_dwordx4 a[232:235], v1, s[16:19], s32 offen offset:0 sc1
buffer_load_dwordx4 a[236:239], v1, s[16:19], s33 offen offset:0 sc1
buffer_load_dwordx4 a[248:251], v1, s[16:19], s32 offen offset:1024 sc1
buffer_load_dwordx4 a[252:255], v1, s[16:19], s33 offen offset:1024 sc1
s_waitcnt vmcnt(11)
v_mfma_f32_32x32x16_bf16 v[32:47], a[0:3], a[168:171], 0
v_mfma_f32_32x32x16_bf16 v[64:79], a[4:7], a[168:171], 0
s_waitcnt vmcnt(10)
v_mfma_f32_32x32x16_bf16 v[48:63], a[0:3], a[172:175], 0
v_mfma_f32_32x32x16_bf16 v[80:95], a[4:7], a[172:175], 0
buffer_load_dwordx4 a[168:171], v1, s[16:19], s32 offen offset:2048 sc1
buffer_load_dwordx4 a[172:175], v1, s[16:19], s33 offen offset:2048 sc1
s_waitcnt vmcnt(11)
v_mfma_f32_32x32x16_bf16 v[32:47], a[16:19], a[184:187], v[32:47]
v_mfma_f32_32x32x16_bf16 v[64:79], a[20:23], a[184:187], v[64:79]
s_waitcnt vmcnt(10)
v_mfma_f32_32x32x16_bf16 v[48:63], a[16:19], a[188:191], v[48:63]
v_mfma_f32_32x32x16_bf16 v[80:95], a[20:23], a[188:191], v[80:95]
buffer_load_dwordx4 a[184:187], v1, s[16:19], s32 offen offset:3072 sc1
buffer_load_dwordx4 a[188:191], v1, s[16:19], s33 offen offset:3072 sc1
s_waitcnt vmcnt(11)
v_mfma_f32_32x32x16_bf16 v[32:47], a[32:35], a[200:203], v[32:47]
v_mfma_f32_32x32x16_bf16 v[64:79], a[36:39], a[200:203], v[64:79]
s_waitcnt vmcnt(10)
v_mfma_f32_32x32x16_bf16 v[48:63], a[32:35], a[204:207], v[48:63]
v_mfma_f32_32x32x16_bf16 v[80:95], a[36:39], a[204:207], v[80:95]
s_add_u32 s32, s32, 4096
s_add_u32 s33, s33, 4096
buffer_load_dwordx4 a[200:203], v1, s[16:19], s32 offen offset:0 sc1
buffer_load_dwordx4 a[204:207], v1, s[16:19], s33 offen offset:0 sc1
s_waitcnt vmcnt(11)
v_mfma_f32_32x32x16_bf16 v[32:47], a[48:51], a[216:219], v[32:47]
v_mfma_f32_32x32x16_bf16 v[64:79], a[52:55], a[216:219], v[64:79]
s_waitcnt vmcnt(10)
v_mfma_f32_32x32x16_bf16 v[48:63], a[48:51], a[220:223], v[48:63]
v_mfma_f32_32x32x16_bf16 v[80:95], a[52:55], a[220:223], v[80:95]
buffer_load_dwordx4 a[216:219], v1, s[16:19], s32 offen offset:1024 sc1
buffer_load_dwordx4 a[220:223], v1, s[16:19], s33 offen offset:1024 sc1
s_waitcnt vmcnt(11)
v_mfma_f32_32x32x16_bf16 v[32:47], a[64:67], a[232:235], v[32:47]
v_mfma_f32_32x32x16_bf16 v[64:79], a[68:71], a[232:235], v[64:79]
s_waitcnt vmcnt(10)
v_mfma_f32_32x32x16_bf16 v[48:63], a[64:67], a[236:239], v[48:63]
v_mfma_f32_32x32x16_bf16 v[80:95], a[68:71], a[236:239], v[80:95]
buffer_load_dwordx4 a[232:235], v1, s[16:19], s32 offen offset:2048 sc1
buffer_load_dwordx4 a[236:239], v1, s[16:19], s33 offen offset:2048 sc1
s_waitcnt vmcnt(11)
v_mfma_f32_32x32x16_bf16 v[32:47], a[80:83], a[248:251], v[32:47]
v_mfma_f32_32x32x16_bf16 v[64:79], a[84:87], a[248:251], v[64:79]
s_waitcnt vmcnt(10)
v_mfma_f32_32x32x16_bf16 v[48:63], a[80:83], a[252:255], v[48:63]
v_mfma_f32_32x32x16_bf16 v[80:95], a[84:87], a[252:255], v[80:95]
buffer_load_dwordx4 a[248:251], v1, s[16:19], s32 offen offset:3072 sc1
buffer_load_dwordx4 a[252:255], v1, s[16:19], s33 offen offset:3072 sc1
s_waitcnt vmcnt(11)
v_mfma_f32_32x32x16_bf16 v[32:47], a[96:99], a[168:171], v[32:47]
v_mfma_f32_32x32x16_bf16 v[64:79], a[100:103], a[168:171], v[64:79]
s_waitcnt vmcnt(10)
v_mfma_f32_32x32x16_bf16 v[48:63], a[96:99], a[172:175], v[48:63]
v_mfma_f32_32x32x16_bf16 v[80:95], a[100:103], a[172:175], v[80:95]
s_add_u32 s32, s32, 4096
s_add_u32 s33, s33, 4096
buffer_load_dwordx4 a[168:171], v1, s[16:19], s32 offen offset:0 sc1
buffer_load_dwordx4 a[172:175], v1, s[16:19], s33 offen offset:0 sc1
s_waitcnt vmcnt(11)
v_mfma_f32_32x32x16_bf16 v[32:47], a[112:115], a[184:187], v[32:47]
v_mfma_f32_32x32x16_bf16 v[64:79], a[116:119], a[184:187], v[64:79]
s_waitcnt vmcnt(10)
v_mfma_f32_32x32x16_bf16 v[48:63], a[112:115], a[188:191], v[48:63]
v_mfma_f32_32x32x16_bf16 v[80:95], a[116:119], a[188:191], v[80:95]
buffer_load_dwordx4 a[184:187], v1, s[16:19], s32 offen offset:1024 sc1
buffer_load_dwordx4 a[188:191], v1, s[16:19], s33 offen offset:1024 sc1
s_waitcnt vmcnt(11)
v_mfma_f32_32x32x16_bf16 v[32:47], a[128:131], a[200:203], v[32:47]
v_mfma_f32_32x32x16_bf16 v[64:79], a[132:135], a[200:203], v[64:79]
s_waitcnt vmcnt(10)
v_mfma_f32_32x32x16_bf16 v[48:63], a[128:131], a[204:207], v[48:63]
v_mfma_f32_32x32x16_bf16 v[80:95], a[132:135], a[204:207], v[80:95]
buffer_load_dwordx4 a[200:203], v1, s[16:19], s32 offen offset:2048 sc1
buffer_load_dwordx4 a[204:207], v1, s[16:19], s33 offen offset:2048 sc1
s_waitcnt vmcnt(11)
v_mfma_f32_32x32x16_bf16 v[32:47], a[144:147], a[216:219], v[32:47]
v_mfma_f32_32x32x16_bf16 v[64:79], a[148:151], a[216:219], v[64:79]
s_waitcnt vmcnt(10)
v_mfma_f32_32x32x16_bf16 v[48:63], a[144:147], a[220:223], v[48:63]
v_mfma_f32_32x32x16_bf16 v[80:95], a[148:151], a[220:223], v[80:95]
buffer_load_dwordx4 a[216:219], v1, s[16:19], s32 offen offset:3072 sc1
buffer_load_dwordx4 a[220:223], v1, s[16:19], s33 offen offset:3072 sc1
s_waitcnt vmcnt(11)
v_mfma_f32_32x32x16_bf16 v[32:47], a[160:163], a[232:235], v[32:47]
v_mfma_f32_32x32x16_bf16 v[64:79], a[164:167], a[232:235], v[64:79]
s_waitcnt vmcnt(10)
v_mfma_f32_32x32x16_bf16 v[48:63], a[160:163], a[236:239], v[48:63]
v_mfma_f32_32x32x16_bf16 v[80:95], a[164:167], a[236:239], v[80:95]
s_add_u32 s32, s32, 4096
s_add_u32 s33, s33, 4096
buffer_load_dwordx4 a[232:235], v1, s[16:19], s32 offen offset:0 sc1
buffer_load_dwordx4 a[236:239], v1, s[16:19], s33 offen offset:0 sc1
s_waitcnt vmcnt(11)
v_mfma_f32_32x32x16_bf16 v[32:47], a[176:179], a[248:251], v[32:47]
v_mfma_f32_32x32x16_bf16 v[64:79], a[180:183], a[248:251], v[64:79]
s_waitcnt vmcnt(10)
v_mfma_f32_32x32x16_bf16 v[48:63], a[176:179], a[252:255], v[48:63]
v_mfma_f32_32x32x16_bf16 v[80:95], a[180:183], a[252:255], v[80:95]
buffer_load_dwordx4 a[248:251], v1, s[16:19], s32 offen offset:1024 sc1
buffer_load_dwordx4 a[252:255], v1, s[16:19], s33 offen offset:1024 sc1
s_waitcnt vmcnt(11)
v_mfma_f32_32x32x16_bf16 v[32:47], a[192:195], a[168:171], v[32:47]
v_mfma_f32_32x32x16_bf16 v[64:79], a[196:199], a[168:171], v[64:79]
s_waitcnt vmcnt(10)
v_mfma_f32_32x32x16_bf16 v[48:63], a[192:195], a[172:175], v[48:63]
v_mfma_f32_32x32x16_bf16 v[80:95], a[196:199], a[172:175], v[80:95]
buffer_load_dwordx4 a[168:171], v1, s[16:19], s32 offen offset:2048 sc1
buffer_load_dwordx4 a[172:175], v1, s[16:19], s33 offen offset:2048 sc1
s_waitcnt vmcnt(11)
v_mfma_f32_32x32x16_bf16 v[32:47], a[208:211], a[184:187], v[32:47]
v_mfma_f32_32x32x16_bf16 v[64:79], a[212:215], a[184:187], v[64:79]
s_waitcnt vmcnt(10)
v_mfma_f32_32x32x16_bf16 v[48:63], a[208:211], a[188:191], v[48:63]
v_mfma_f32_32x32x16_bf16 v[80:95], a[212:215], a[188:191], v[80:95]
buffer_load_dwordx4 a[184:187], v1, s[16:19], s32 offen offset:3072 sc1
buffer_load_dwordx4 a[188:191], v1, s[16:19], s33 offen offset:3072 sc1
s_waitcnt vmcnt(11)
v_mfma_f32_32x32x16_bf16 v[32:47], a[224:227], a[200:203], v[32:47]
v_mfma_f32_32x32x16_bf16 v[64:79], a[228:231], a[200:203], v[64:79]
s_waitcnt vmcnt(10)
v_mfma_f32_32x32x16_bf16 v[48:63], a[224:227], a[204:207], v[48:63]
v_mfma_f32_32x32x16_bf16 v[80:95], a[228:231], a[204:207], v[80:95]
s_add_u32 s32, s32, 4096
s_add_u32 s33, s33, 4096
buffer_load_dwordx4 a[200:203], v1, s[16:19], s32 offen offset:0 sc1
buffer_load_dwordx4 a[204:207], v1, s[16:19], s33 offen offset:0 sc1
s_waitcnt vmcnt(11)
v_mfma_f32_32x32x16_bf16 v[32:47], a[240:243], a[216:219], v[32:47]
v_mfma_f32_32x32x16_bf16 v[64:79], a[244:247], a[216:219], v[64:79]
s_waitcnt vmcnt(10)
v_mfma_f32_32x32x16_bf16 v[48:63], a[240:243], a[220:223], v[48:63]
v_mfma_f32_32x32x16_bf16 v[80:95], a[244:247], a[220:223], v[80:95]
buffer_load_dwordx4 a[216:219], v1, s[16:19], s32 offen offset:1024 sc1
buffer_load_dwordx4 a[220:223], v1, s[16:19], s33 offen offset:1024 sc1
s_waitcnt vmcnt(11)
v_mfma_f32_32x32x16_bf16 v[32:47], a[8:11], a[232:235], v[32:47]
v_mfma_f32_32x32x16_bf16 v[64:79], a[12:15], a[232:235], v[64:79]
s_waitcnt vmcnt(10)
v_mfma_f32_32x32x16_bf16 v[48:63], a[8:11], a[236:239], v[48:63]
v_mfma_f32_32x32x16_bf16 v[80:95], a[12:15], a[236:239], v[80:95]
buffer_load_dwordx4 a[232:235], v1, s[16:19], s32 offen offset:2048 sc1
buffer_load_dwordx4 a[236:239], v1, s[16:19], s33 offen offset:2048 sc1
s_waitcnt vmcnt(11)
v_mfma_f32_32x32x16_bf16 v[32:47], a[24:27], a[248:251], v[32:47]
v_mfma_f32_32x32x16_bf16 v[64:79], a[28:31], a[248:251], v[64:79]
s_waitcnt vmcnt(10)
v_mfma_f32_32x32x16_bf16 v[48:63], a[24:27], a[252:255], v[48:63]
v_mfma_f32_32x32x16_bf16 v[80:95], a[28:31], a[252:255], v[80:95]
buffer_load_dwordx4 a[248:251], v1, s[16:19], s32 offen offset:3072 sc1
buffer_load_dwordx4 a[252:255], v1, s[16:19], s33 offen offset:3072 sc1
s_waitcnt vmcnt(11)
v_mfma_f32_32x32x16_bf16 v[32:47], a[40:43], a[168:171], v[32:47]
v_mfma_f32_32x32x16_bf16 v[64:79], a[44:47], a[168:171], v[64:79]
s_waitcnt vmcnt(10)
v_mfma_f32_32x32x16_bf16 v[48:63], a[40:43], a[172:175], v[48:63]
v_mfma_f32_32x32x16_bf16 v[80:95], a[44:47], a[172:175], v[80:95]
s_add_u32 s32, s32, 4096
s_add_u32 s33, s33, 4096
buffer_load_dwordx4 a[168:171], v1, s[16:19], s32 offen offset:0 sc1
buffer_load_dwordx4 a[172:175], v1, s[16:19], s33 offen offset:0 sc1
s_waitcnt vmcnt(11)
v_mfma_f32_32x32x16_bf16 v[32:47], a[56:59], a[184:187], v[32:47]
v_mfma_f32_32x32x16_bf16 v[64:79], a[60:63], a[184:187], v[64:79]
s_waitcnt vmcnt(10)
v_mfma_f32_32x32x16_bf16 v[48:63], a[56:59], a[188:191], v[48:63]
v_mfma_f32_32x32x16_bf16 v[80:95], a[60:63], a[188:191], v[80:95]
buffer_load_dwordx4 a[184:187], v1, s[16:19], s32 offen offset:1024 sc1
buffer_load_dwordx4 a[188:191], v1, s[16:19], s33 offen offset:1024 sc1
s_waitcnt vmcnt(11)
v_mfma_f32_32x32x16_bf16 v[32:47], a[72:75], a[200:203], v[32:47]
v_mfma_f32_32x32x16_bf16 v[64:79], a[76:79], a[200:203], v[64:79]
s_waitcnt vmcnt(10)
v_mfma_f32_32x32x16_bf16 v[48:63], a[72:75], a[204:207], v[48:63]
v_mfma_f32_32x32x16_bf16 v[80:95], a[76:79], a[204:207], v[80:95]
buffer_load_dwordx4 a[200:203], v1, s[16:19], s32 offen offset:2048 sc1
buffer_load_dwordx4 a[204:207], v1, s[16:19], s33 offen offset:2048 sc1
s_waitcnt vmcnt(11)
v_mfma_f32_32x32x16_bf16 v[32:47], a[88:91], a[216:219], v[32:47]
v_mfma_f32_32x32x16_bf16 v[64:79], a[92:95], a[216:219], v[64:79]
s_waitcnt vmcnt(10)
v_mfma_f32_32x32x16_bf16 v[48:63], a[88:91], a[220:223], v[48:63]
v_mfma_f32_32x32x16_bf16 v[80:95], a[92:95], a[220:223], v[80:95]
buffer_load_dwordx4 a[216:219], v1, s[16:19], s32 offen offset:3072 sc1
buffer_load_dwordx4 a[220:223], v1, s[16:19], s33 offen offset:3072 sc1
s_waitcnt vmcnt(11)
v_mfma_f32_32x32x16_bf16 v[32:47], a[104:107], a[232:235], v[32:47]
v_mfma_f32_32x32x16_bf16 v[64:79], a[108:111], a[232:235], v[64:79]
s_waitcnt vmcnt(10)
v_mfma_f32_32x32x16_bf16 v[48:63], a[104:107], a[236:239], v[48:63]
v_mfma_f32_32x32x16_bf16 v[80:95], a[108:111], a[236:239], v[80:95]
s_waitcnt vmcnt(9)
v_mfma_f32_32x32x16_bf16 v[32:47], a[120:123], a[248:251], v[32:47]
v_mfma_f32_32x32x16_bf16 v[64:79], a[124:127], a[248:251], v[64:79]
s_waitcnt vmcnt(8)
v_mfma_f32_32x32x16_bf16 v[48:63], a[120:123], a[252:255], v[48:63]
v_mfma_f32_32x32x16_bf16 v[80:95], a[124:127], a[252:255], v[80:95]
s_waitcnt vmcnt(7)
v_mfma_f32_32x32x16_bf16 v[32:47], a[136:139], a[168:171], v[32:47]
v_mfma_f32_32x32x16_bf16 v[64:79], a[140:143], a[168:171], v[64:79]
s_waitcnt vmcnt(6)
v_mfma_f32_32x32x16_bf16 v[48:63], a[136:139], a[172:175], v[48:63]
v_mfma_f32_32x32x16_bf16 v[80:95], a[140:143], a[172:175], v[80:95]
s_waitcnt vmcnt(5)
v_mfma_f32_32x32x16_bf16 v[32:47], a[152:155], a[184:187], v[32:47]
v_mfma_f32_32x32x16_bf16 v[64:79], a[156:159], a[184:187], v[64:79]
s_waitcnt vmcnt(4)
v_mfma_f32_32x32x16_bf16 v[48:63], a[152:155], a[188:191], v[48:63]
v_mfma_f32_32x32x16_bf16 v[80:95], a[156:159], a[188:191], v[80:95]
s_waitcnt vmcnt(3)
v_mfma_f32_32x32x16_bf16 v[32:47], v[200:203], a[200:203], v[32:47]
v_mfma_f32_32x32x16_bf16 v[64:79], v[204:207], a[200:203], v[64:79]
s_waitcnt vmcnt(2)
v_mfma_f32_32x32x16_bf16 v[48:63], v[200:203], a[204:207], v[48:63]
v_mfma_f32_32x32x16_bf16 v[80:95], v[204:207], a[204:207], v[80:95]
s_waitcnt vmcnt(1)
v_mfma_f32_32x32x16_bf16 v[32:47], v[208:211], a[216:219], v[32:47]
v_mfma_f32_32x32x16_bf16 v[64:79], v[212:215], a[216:219], v[64:79]
s_waitcnt vmcnt(0)
v_mfma_f32_32x32x16_bf16 v[48:63], v[208:211], a[220:223], v[48:63]
v_mfma_f32_32x32x16_bf16 v[80:95], v[212:215], a[220:223], v[80:95]
v_and_b32 v8, 31, v0
v_mul_u32_u24 v8, 272, v8
v_bfe_u32 v9, v0, 5, 1
v_lshl_add_u32 v8, v9, 4, v8
s_mul_i32 s29, s28, 17408
v_add_u32 v10, s29, v8
s_waitcnt vmcnt(0)
s_nop 7
s_cmp_lg_u32 s28, 0
s_cbranch_scc1 .Lres1_0
v_lshlrev_b32 v11, 16, v96
v_and_b32 v12, 0xffff0000, v96
v_add_f32 v32, v32, v11
v_add_f32 v33, v33, v12
v_lshlrev_b32 v11, 16, v97
v_and_b32 v12, 0xffff0000, v97
v_add_f32 v34, v34, v11
v_add_f32 v35, v35, v12
v_lshlrev_b32 v11, 16, v98
v_and_b32 v12, 0xffff0000, v98
v_add_f32 v36, v36, v11
v_add_f32 v37, v37, v12
v_lshlrev_b32 v11, 16, v99
v_and_b32 v12, 0xffff0000, v99
v_add_f32 v38, v38, v11
v_add_f32 v39, v39, v12
v_lshlrev_b32 v11, 16, v100
v_and_b32 v12, 0xffff0000, v100
v_add_f32 v40, v40, v11
v_add_f32 v41, v41, v12
v_lshlrev_b32 v11, 16, v101
v_and_b32 v12, 0xffff0000, v101
v_add_f32 v42, v42, v11
v_add_f32 v43, v43, v12
v_lshlrev_b32 v11, 16, v102
v_and_b32 v12, 0xffff0000, v102
v_add_f32 v44, v44, v11
v_add_f32 v45, v45, v12
v_lshlrev_b32 v11, 16, v103
v_and_b32 v12, 0xffff0000, v103
v_add_f32 v46, v46, v11
v_add_f32 v47, v47, v12
.Lres1_0:
s_cmp_lg_u32 s28, 1
s_cbranch_scc1 .Lres1_1
v_lshlrev_b32 v11, 16, v96
v_and_b32 v12, 0xffff0000, v96
v_add_f32 v48, v48, v11
v_add_f32 v49, v49, v12
v_lshlrev_b32 v11, 16, v97
v_and_b32 v12, 0xffff0000, v97
v_add_f32 v50, v50, v11
v_add_f32 v51, v51, v12
v_lshlrev_b32 v11, 16, v98
v_and_b32 v12, 0xffff0000, v98
v_add_f32 v52, v52, v11
v_add_f32 v53, v53, v12
v_lshlrev_b32 v11, 16, v99
v_and_b32 v12, 0xffff0000, v99
v_add_f32 v54, v54, v11
v_add_f32 v55, v55, v12
v_lshlrev_b32 v11, 16, v100
v_and_b32 v12, 0xffff0000, v100
v_add_f32 v56, v56, v11
v_add_f32 v57, v57, v12
v_lshlrev_b32 v11, 16, v101
v_and_b32 v12, 0xffff0000, v101
v_add_f32 v58, v58, v11
v_add_f32 v59, v59, v12
v_lshlrev_b32 v11, 16, v102
v_and_b32 v12, 0xffff0000, v102
v_add_f32 v60, v60, v11
v_add_f32 v61, v61, v12
v_lshlrev_b32 v11, 16, v103
v_and_b32 v12, 0xffff0000, v103
v_add_f32 v62, v62, v11
v_add_f32 v63, v63, v12
.Lres1_1:
s_cmp_lg_u32 s28, 2
s_cbranch_scc1 .Lres1_2
v_lshlrev_b32 v11, 16, v96
v_and_b32 v12, 0xffff0000, v96
v_add_f32 v64, v64, v11
v_add_f32 v65, v65, v12
v_lshlrev_b32 v11, 16, v97
v_and_b32 v12, 0xffff0000, v97
v_add_f32 v66, v66, v11
v_add_f32 v67, v67, v12
v_lshlrev_b32 v11, 16, v98
v_and_b32 v12, 0xffff0000, v98
v_add_f32 v68, v68, v11
v_add_f32 v69, v69, v12
v_lshlrev_b32 v11, 16, v99
v_and_b32 v12, 0xffff0000, v99
v_add_f32 v70, v70, v11
v_add_f32 v71, v71, v12
v_lshlrev_b32 v11, 16, v100
v_and_b32 v12, 0xffff0000, v100
v_add_f32 v72, v72, v11
v_add_f32 v73, v73, v12
v_lshlrev_b32 v11, 16, v101
v_and_b32 v12, 0xffff0000, v101
v_add_f32 v74, v74, v11
v_add_f32 v75, v75, v12
v_lshlrev_b32 v11, 16, v102
v_and_b32 v12, 0xffff0000, v102
v_add_f32 v76, v76, v11
v_add_f32 v77, v77, v12
v_lshlrev_b32 v11, 16, v103
v_and_b32 v12, 0xffff0000, v103
v_add_f32 v78, v78, v11
v_add_f32 v79, v79, v12
.Lres1_2:
s_cmp_lg_u32 s28, 3
s_cbranch_scc1 .Lres1_3
v_lshlrev_b32 v11, 16, v96
v_and_b32 v12, 0xffff0000, v96
v_add_f32 v80, v80, v11
v_add_f32 v81, v81, v12
v_lshlrev_b32 v11, 16, v97
v_and_b32 v12, 0xffff0000, v97
v_add_f32 v82, v82, v11
v_add_f32 v83, v83, v12
v_lshlrev_b32 v11, 16, v98
v_and_b32 v12, 0xffff0000, v98
v_add_f32 v84, v84, v11
v_add_f32 v85, v85, v12
v_lshlrev_b32 v11, 16, v99
v_and_b32 v12, 0xffff0000, v99
v_add_f32 v86, v86, v11
v_add_f32 v87, v87, v12
v_lshlrev_b32 v11, 16, v100
v_and_b32 v12, 0xffff0000, v100
v_add_f32 v88, v88, v11
v_add_f32 v89, v89, v12
v_lshlrev_b32 v11, 16, v101
v_and_b32 v12, 0xffff0000, v101
v_add_f32 v90, v90, v11
v_add_f32 v91, v91, v12
v_lshlrev_b32 v11, 16, v102
v_and_b32 v12, 0xffff0000, v102
v_add_f32 v92, v92, v11
v_add_f32 v93, v93, v12
v_lshlrev_b32 v11, 16, v103
v_and_b32 v12, 0xffff0000, v103
v_add_f32 v94, v94, v11
v_add_f32 v95, v95, v12
.Lres1_3:
ds_write_b128 v10, v[32:35] offset:0
ds_write_b128 v10, v[36:39] offset:32
ds_write_b128 v10, v[40:43] offset:64
ds_write_b128 v10, v[44:47] offset:96
ds_write_b128 v10, v[48:51] offset:8704
ds_write_b128 v10, v[52:55] offset:8736
ds_write_b128 v10, v[56:59] offset:8768
ds_write_b128 v10, v[60:63] offset:8800
ds_write_b128 v10, v[64:67] offset:128
ds_write_b128 v10, v[68:71] offset:160
ds_write_b128 v10, v[72:75] offset:192
ds_write_b128 v10, v[76:79] offset:224
ds_write_b128 v10, v[80:83] offset:8832
ds_write_b128 v10, v[84:87] offset:8864
ds_write_b128 v10, v[88:91] offset:8896
ds_write_b128 v10, v[92:95] offset:8928
s_waitcnt lgkmcnt(0)
s_barrier
v_bfe_u32 v8, v0, 3, 3
v_mul_u32_u24 v9, 272, v8
v_and_b32 v10, 7, v0
v_lshl_add_u32 v9, v10, 5, v9
s_mul_i32 s29, s28, 4352
v_add_u32 v9, s29, v9
v_add_u32 v11, 34816, v9
v_lshlrev_b32 v8, 10, v8
v_lshl_add_u32 v8, v10, 4, v8
s_lshl_b32 s29, s36, 2
s_add_u32 s29, s29, s28
s_lshl_b32 s29, s29, 14
s_lshl_b32 s30, s37, 7
s_add_u32 s29, s29, s30
s_add_u32 s30, s29, 8192
ds_read_b128 v[128:131], v9 offset:0
ds_read_b128 v[132:135], v9 offset:16
ds_read_b128 v[136:139], v9 offset:17408
ds_read_b128 v[140:143], v9 offset:17424
ds_read_b128 v[144:147], v11 offset:0
ds_read_b128 v[148:151], v11 offset:16
ds_read_b128 v[152:155], v11 offset:17408
ds_read_b128 v[156:159], v11 offset:17424
ds_read_b128 v[160:163], v9 offset:2176
ds_read_b128 v[164:167], v9 offset:2192
ds_read_b128 v[168:171], v9 offset:19584
ds_read_b128 v[172:175], v9 offset:19600
ds_read_b128 v[176:179], v11 offset:2176
ds_read_b128 v[180:183], v11 offset:2192
ds_read_b128 v[184:187], v11 offset:19584
ds_read_b128 v[188:191], v11 offset:19600
s_waitcnt lgkmcnt(13)
v_pk_add_f32 v[128:129], v[128:129], v[136:137]
v_pk_add_f32 v[130:131], v[130:131], v[138:139]
s_waitcnt lgkmcnt(12)
v_pk_add_f32 v[132:133], v[132:133], v[140:141]
v_pk_add_f32 v[134:135], v[134:135], v[142:143]
s_waitcnt lgkmcnt(11)
v_pk_add_f32 v[128:129], v[128:129], v[144:145]
v_pk_add_f32 v[130:131], v[130:131], v[146:147]
s_waitcnt lgkmcnt(10)
v_pk_add_f32 v[132:133], v[132:133], v[148:149]
v_pk_add_f32 v[134:135], v[134:135], v[150:151]
s_waitcnt lgkmcnt(9)
v_pk_add_f32 v[128:129], v[128:129], v[152:153]
v_pk_add_f32 v[130:131], v[130:131], v[154:155]
s_waitcnt lgkmcnt(8)
v_pk_add_f32 v[132:133], v[132:133], v[156:157]
v_pk_add_f32 v[134:135], v[134:135], v[158:159]
v_cvt_pk_bf16_f32 v136, v128, v129
v_cvt_pk_bf16_f32 v137, v130, v131
v_cvt_pk_bf16_f32 v138, v132, v133
v_cvt_pk_bf16_f32 v139, v134, v135
buffer_store_dwordx4 v[136:139], v8, s[24:27], s29 offen
s_waitcnt lgkmcnt(5)
v_pk_add_f32 v[160:161], v[160:161], v[168:169]
v_pk_add_f32 v[162:163], v[162:163], v[170:171]
s_waitcnt lgkmcnt(4)
v_pk_add_f32 v[164:165], v[164:165], v[172:173]
v_pk_add_f32 v[166:167], v[166:167], v[174:175]
s_waitcnt lgkmcnt(3)
v_pk_add_f32 v[160:161], v[160:161], v[176:177]
v_pk_add_f32 v[162:163], v[162:163], v[178:179]
s_waitcnt lgkmcnt(2)
v_pk_add_f32 v[164:165], v[164:165], v[180:181]
v_pk_add_f32 v[166:167], v[166:167], v[182:183]
s_waitcnt lgkmcnt(1)
v_pk_add_f32 v[160:161], v[160:161], v[184:185]
v_pk_add_f32 v[162:163], v[162:163], v[186:187]
s_waitcnt lgkmcnt(0)
v_pk_add_f32 v[164:165], v[164:165], v[188:189]
v_pk_add_f32 v[166:167], v[166:167], v[190:191]
v_cvt_pk_bf16_f32 v168, v160, v161
v_cvt_pk_bf16_f32 v169, v162, v163
v_cvt_pk_bf16_f32 v170, v164, v165
v_cvt_pk_bf16_f32 v171, v166, v167
buffer_store_dwordx4 v[168:171], v8, s[24:27], s30 offen
.Lk5e:
)ASM"
    :: "s"((unsigned)(size_t)ws), "s"((unsigned)((size_t)ws >> 32)), "s"(blockIdx.x), "s"((unsigned)(size_t)wq), "s"((unsigned)((size_t)wq >> 32)), "v"(threadIdx.x),
       "s"((unsigned)(size_t)wo), "s"((unsigned)((size_t)wo >> 32)), "s"((unsigned)(size_t)wd), "s"((unsigned)((size_t)wd >> 32)), "s"((unsigned)(size_t)wg), "s"((unsigned)((size_t)wg >> 32)), "s"((unsigned)(size_t)wu), "s"((unsigned)((size_t)wu >> 32)), "s"((uint64_t)__builtin_amdgcn_kernarg_segment_ptr()) :
      "v1", "v2", "v3", "v4", "v5", "v6", "v7", "v8", "v9", "v10", "v11", "v12", "v13", "v14", "v15", "v16",
      "v17", "v18", "v19", "v20", "v21", "v22", "v23", "v24", "v25", "v26", "v27", "v28", "v29", "v30", "v31", "v32",
      "v33", "v34", "v35", "v36", "v37", "v38", "v39", "v40", "v41", "v42", "v43", "v44", "v45", "v46", "v47", "v48",
      "v49", "v50", "v51", "v52", "v53", "v54", "v55", "v56", "v57", "v58", "v59", "v60", "v61", "v62", "v63", "v64",
      "v65", "v66", "v67", "v68", "v69", "v70", "v71", "v72", "v73", "v74", "v75", "v76", "v77", "v78", "v79", "v80",
      "v81", "v82", "v83", "v84", "v85", "v86", "v87", "v88", "v89", "v90", "v91", "v92", "v93", "v94", "v95", "v96",
      "v97", "v98", "v99", "v100", "v101", "v102", "v103", "v104", "v105", "v106", "v107", "v108", "v109", "v110", "v111", "v112",
      "v113", "v114", "v115", "v116", "v117", "v118", "v119", "v120", "v121", "v122", "v123", "v124", "v125", "v126", "v127", "v128",
      "v129", "v130", "v131", "v132", "v133", "v134", "v135", "v136", "v137", "v138", "v139", "v140", "v141", "v142", "v143", "v144",
      "v145", "v146", "v147", "v148", "v149", "v150", "v151", "v152", "v153", "v154", "v155", "v156", "v157", "v158", "v159", "v160",
      "v161", "v162", "v163", "v164", "v165", "v166", "v167", "v168", "v169", "v170", "v171", "v172", "v173", "v174", "v175", "v176",
      "v177", "v178", "v179", "v180", "v181", "v182", "v183", "v184", "v185", "v186", "v187", "v188", "v189", "v190", "v191", "v192",
      "v193", "v194", "v195", "v196", "v197", "v198", "v199", "v200", "v201", "v202", "v203", "v204", "v205", "v206", "v207", "v208",
      "v209", "v210", "v211", "v212", "v213", "v214", "v215", "v216", "v217", "v218", "v219", "v220", "v221", "v222", "v223", "v224",
      "v225", "v226", "v227", "v228", "v229", "v230", "v231", "v232", "v233", "v234", "v235", "v236", "v237", "v238", "v239", "v240",
      "v241", "v242", "v243", "v244", "v245", "v246", "v247", "v248", "v249", "v250", "v251", "v252", "v253", "v254", "v255", "a0",
      "a1", "a2", "a3", "a4", "a5", "a6", "a7", "a8", "a9", "a10", "a11", "a12", "a13", "a14", "a15", "a16",
      "a17", "a18", "a19", "a20", "a21", "a22", "a23", "a24", "a25", "a26", "a27", "a28", "a29", "a30", "a31", "a32",
      "a33", "a34", "a35", "a36", "a37", "a38", "a39", "a40", "a41", "a42", "a43", "a44", "a45", "a46", "a47", "a48",
      "a49", "a50", "a51", "a52", "a53", "a54", "a55", "a56", "a57", "a58", "a59", "a60", "a61", "a62", "a63", "a64",
      "a65", "a66", "a67", "a68", "a69", "a70", "a71", "a72", "a73", "a74", "a75", "a76", "a77", "a78", "a79", "a80",
      "a81", "a82", "a83", "a84", "a85", "a86", "a87", "a88", "a89", "a90", "a91", "a92", "a93", "a94", "a95", "a96",
      "a97", "a98", "a99", "a100", "a101", "a102", "a103", "a104", "a105", "a106", "a107", "a108", "a109", "a110", "a111", "a112",
      "a113", "a114", "a115", "a116", "a117", "a118", "a119", "a120", "a121", "a122", "a123", "a124", "a125", "a126", "a127", "a128",
      "a129", "a130", "a131", "a132", "a133", "a134", "a135", "a136", "a137", "a138", "a139", "a140", "a141", "a142", "a143", "a144",
      "a145", "a146", "a147", "a148", "a149", "a150", "a151", "a152", "a153", "a154", "a155", "a156", "a157", "a158", "a159", "a160",
      "a161", "a162", "a163", "a164", "a165", "a166", "a167", "a168", "a169", "a170", "a171", "a172", "a173", "a174", "a175", "a176",
      "a177", "a178", "a179", "a180", "a181", "a182", "a183", "a184", "a185", "a186", "a187", "a188", "a189", "a190", "a191", "a192",
      "a193", "a194", "a195", "a196", "a197", "a198", "a199", "a200", "a201", "a202", "a203", "a204", "a205", "a206", "a207", "a208",
      "a209", "a210", "a211", "a212", "a213", "a214", "a215", "a216", "a217", "a218", "a219", "a220", "a221", "a222", "a223", "a224",
      "a225", "a226", "a227", "a228", "a229", "a230", "a231", "a232", "a233", "a234", "a235", "a236", "a237", "a238", "a239", "a240",
      "a241", "a242", "a243", "a244", "a245", "a246", "a247", "a248", "a249", "a250", "a251", "a252", "a253", "a254", "a255", "s16",
      "s17", "s18", "s19", "s20", "s21", "s22", "s23", "s24", "s25", "s26", "s27", "s28", "s29", "s30", "s31", "s32",
      "s33", "s34", "s35", "s36", "s37", "s38", "s39", "s40", "s41", "s42", "s43", "s44", "s45", "s46", "s47", "s48",
      "s49", "s50", "s51", "s52", "s53", "s54", "s55", "s56", "s57", "s58", "s59", "s60", "s61", "s62", "s63", "s64",
      "s65", "s66", "s67", "s68", "s69", "s70", "s71", "s72", "s73", "s74", "s75", "s76", "s77", "s78", "s79", "s80",
      "s81", "s82", "s83", "s84", "s85", "s86", "s87", "s88", "s89", "s90", "s91", "s92", "s93", "s94", "s95", "s96",
      "s97", "s98", "s99", "s100", "s101", "vcc", "scc", "m0", "memory");
}
extern "C" void kernel_launch(void* const* d_in, const int* in_sizes, int n_in, void* d_out, int out_size, void* d_ws,
                              size_t ws_size, hipStream_t stream) {
  const uint16_t* x = (const uint16_t*)d_in[0];
  const uint16_t* ckv = (const uint16_t*)d_in[1];
  const uint16_t* wq = (const uint16_t*)d_in[2];
  const uint16_t* wuk = (const uint16_t*)d_in[3];
  const uint16_t* wuv = (const uint16_t*)d_in[4];
  const uint16_t* wo = (const uint16_t*)d_in[5];
  const uint16_t* wg = (const uint16_t*)d_in[7];
  const uint16_t* wu = (const uint16_t*)d_in[8];
  const uint16_t* wd = (const uint16_t*)d_in[9];
  char* ws = (char*)d_ws;
  uint16_t* out = (uint16_t*)d_out;
  (void)hipFuncSetAttribute((const void*)k1_kv, hipFuncAttributeMaxDynamicSharedMemorySize, 163840);
  (void)hipFuncSetAttribute((const void*)k2_attn, hipFuncAttributeMaxDynamicSharedMemorySize, 163840);
  k1_kv<<<256, 256, 163840, stream>>>(ws, ckv, wuk, wuv, wq, x);
  k2_attn<<<256, 256, 163840, stream>>>(ws, wq, wo, wd, wg, wu, x, out);
}
```
